```python
import torch

def _softmax(x, axis=-1):
    return torch.softmax(x, dim=axis)
def _gelu(x):
    return torch.nn.functional.gelu(x, approximate="tanh")
def _silu(x):
    return torch.nn.functional.silu(x)
def _sigmoid(x):
    return torch.sigmoid(x)
def _layernorm(x, axis=-1, eps=1e-5):
    m = x.mean(dim=axis, keepdim=True)
    v = x.var(dim=axis, keepdim=True, unbiased=False)
    return (x - m) / torch.sqrt(v + eps)
def _scatter_add(tgt, idx, src, axis=0):
    return tgt.clone().index_add_(axis, idx, src)
def _segment_sum(src, seg, n):
    out = torch.zeros((n,) + src.shape[1:], dtype=src.dtype)
    return out.index_add_(0, seg, src)
def _pad(x, pw):
    flat = tuple(v for pair in reversed(pw) for v in pair)
    return torch.nn.functional.pad(x, flat)

kernel_name = 'recurrent_scan__mha_block_cached__mixer_channel_mlp__prompt'

def setup_inputs():

    _w = (lambda a: (lambda t: t.to(torch.bfloat16) if t.is_floating_point() else t)(torch.as_tensor(a)))
    def _A_setup():
        B, Sq, D = 1, 2048, 512
        H, Sk, Dh = 32, 2048, 128
        return {
            "x": _w((lambda *s: torch.randn(*s, dtype=torch.bfloat16) * 0.05)(B, Sq, D)),
            "k_cache": _w((lambda *s: torch.randn(*s, dtype=torch.bfloat16) * 1.6)(B, H, Sk, Dh)),
            "v_cache": _w((lambda *s: torch.randn(*s, dtype=torch.bfloat16) * 0.8)(B, H, Sk, Dh)),
            "wq": _w((lambda *s: torch.randn(*s, dtype=torch.bfloat16) * 1.6)(D, H * Dh) * (D ** -0.5)),
            "wo": _w((lambda *s: torch.randn(*s, dtype=torch.bfloat16) * 0.8)(H * Dh, D) * ((H * Dh) ** -0.5)),
        }
    _A_in = _A_setup()

    def _B_setup():
        B, S, D, F = 1, 2048, 512, 1792
        return {
            "x": _w((lambda *s: torch.randn(*s, dtype=torch.bfloat16) * 0.05)(B, S, D)),
            "w1": _w((lambda *s: torch.randn(*s, dtype=torch.bfloat16) * 1.6)(D, F) * (D ** -0.5)),
            "b1": _w((lambda *s: torch.randn(*s, dtype=torch.bfloat16) * 0.8)(F)),
            "w2": _w((lambda *s: torch.randn(*s, dtype=torch.bfloat16) * 0.2)(F, D) * (F ** -0.5)),
            "b2": _w((lambda *s: torch.randn(*s, dtype=torch.bfloat16) * 0.2)(D)),
        }
    _B_in = _B_setup()

    return {
        "A_x": _A_in["x"],
        "A_k_cache": _A_in["k_cache"],
        "A_v_cache": _A_in["v_cache"],
        "A_wq": _A_in["wq"],
        "A_wo": _A_in["wo"],
        "B_x": _B_in["x"],
        "B_w1": _B_in["w1"],
        "B_b1": _B_in["b1"],
        "B_w2": _B_in["w2"],
        "B_b2": _B_in["b2"],
    }

def reference(A_x, A_k_cache, A_v_cache, A_wq, A_wo, B_x, B_w1, B_b1, B_w2, B_b2):
    def _A_ref(x, k_cache, v_cache, wq, wo):
        B, Sq, D = x.shape
        H, Dh = 32, 128
        q = (lambda x, *d: x.permute(*d))((x @ wq).reshape(B, Sq, H, Dh), 0, 2, 1, 3)
        scores = torch.einsum("bhqd,bhkd->bhqk", q, k_cache) * (Dh ** -0.5)
        p = _softmax(scores, axis=-1)
        o = torch.einsum("bhqk,bhkd->bhqd", p, v_cache)
        return (lambda x, *d: x.permute(*d))(o, 0, 2, 1, 3).reshape(B, Sq, H * Dh) @ wo

    def _B_ref(x, w1, b1, w2, b2):
        y = _layernorm(x, axis=-1)
        h = _gelu(torch.einsum("bsd,df->bsf", y, w1) + b1)
        return torch.einsum("bsf,fd->bsd", h, w2) + b2

    x = A_x
    xn = _layernorm(x, axis=-1)
    s = _A_ref(xn, A_k_cache, A_v_cache, A_wq, A_wo)
    g = _silu(_B_ref(xn, B_w1, B_b1, B_w2, B_b2))
    return x + s * g
```

```cpp
#include <hip/hip_runtime.h>
#include <hip/hip_bf16.h>

typedef unsigned short u16;
typedef unsigned u32x4 __attribute__((ext_vector_type(4)));
typedef unsigned u32x2 __attribute__((ext_vector_type(2)));
typedef float f32x4 __attribute__((ext_vector_type(4)));
typedef float f32x2 __attribute__((ext_vector_type(2)));
typedef __bf16 bf16x2 __attribute__((ext_vector_type(2)));

constexpr size_t WS_TOK = 65536, WS_LNF = 98304, WS_LNA = 59899904, WS_XF = 131072, WS_HF = 2228224, WS_OF = 9568256, WS_END = 59932672;

__device__ __forceinline__ float bf_lo(unsigned w) { return __uint_as_float(w << 16); }
__device__ __forceinline__ float bf_hi(unsigned w) { return __uint_as_float(w & 0xffff0000u); }
__device__ __forceinline__ unsigned pk_bf16(float lo, float hi) { f32x2 v = {lo, hi}; return __builtin_bit_cast(unsigned, __builtin_convertvector(v, bf16x2)); }
__device__ __forceinline__ float xgroup_sum(float v) {
  auto a = __builtin_amdgcn_permlane32_swap(__float_as_uint(v), __float_as_uint(v), false, false);
  v = __uint_as_float(a[0]) + __uint_as_float(a[1]);
  auto b = __builtin_amdgcn_permlane16_swap(__float_as_uint(v), __float_as_uint(v), false, false);
  return __uint_as_float(b[0]) + __uint_as_float(b[1]);
}

template <int CTRL> __device__ __forceinline__ float dpp_f(float v) { return __int_as_float(__builtin_amdgcn_mov_dpp(__float_as_int(v), CTRL, 0xF, 0xF, true)); }
__device__ __forceinline__ float wave_sum(float s) {
  s += dpp_f<0xB1>(s); s += dpp_f<0x4E>(s); s += dpp_f<0x141>(s); s += dpp_f<0x140>(s);
  return xgroup_sum(s);
}
typedef __attribute__((address_space(1))) unsigned gu32;
__device__ __forceinline__ unsigned ld_agent(const char* p) { return __hip_atomic_load((gu32*)p, __ATOMIC_RELAXED, __HIP_MEMORY_SCOPE_AGENT); }
__device__ __forceinline__ void st_agent(const char* p, unsigned v) { __hip_atomic_store((gu32*)p, v, __ATOMIC_RELAXED, __HIP_MEMORY_SCOPE_AGENT); }

__device__ __forceinline__ void ln_row(const u32x4 raw, const int lane, u32x4& piece, f32x4& tok) {
  const float QS = 0.12751743082459868f;
  float v[8];
#pragma unroll
  for (int k = 0; k < 4; ++k) { v[2 * k] = bf_lo(raw[k]); v[2 * k + 1] = bf_hi(raw[k]); }
  const float m1 = wave_sum(((v[0] + v[1]) + (v[2] + v[3])) + ((v[4] + v[5]) + (v[6] + v[7]))) * (1.f / 512.f);
  float q = 0.f;
#pragma unroll
  for (int k = 0; k < 8; ++k) { v[k] -= m1; q += v[k] * v[k]; }
  const float r1 = QS * __builtin_amdgcn_rsqf(wave_sum(q) * (1.f / 512.f) + 1e-5f);
  unsigned pk[4];
  q = 0.f;
#pragma unroll
  for (int k = 0; k < 4; ++k) { pk[k] = pk_bf16(v[2 * k] * r1, v[2 * k + 1] * r1); v[2 * k] = bf_lo(pk[k]); v[2 * k + 1] = bf_hi(pk[k]); q += v[2 * k] + v[2 * k + 1]; }
  const bool hi = (lane >> 1) & 1;
  const unsigned s0 = hi ? pk[0] : pk[2], s1 = hi ? pk[1] : pk[3];
  const unsigned r0 = (unsigned)__builtin_amdgcn_mov_dpp((int)s0, 0x4E, 0xF, 0xF, true), r2 = (unsigned)__builtin_amdgcn_mov_dpp((int)s1, 0x4E, 0xF, 0xF, true);
  if (hi) piece = u32x4{r0, r2, pk[2], pk[3]}; else piece = u32x4{pk[0], pk[1], r0, r2};
  const float m2 = wave_sum(q) * (1.f / 512.f);
  q = 0.f;
#pragma unroll
  for (int k = 0; k < 8; ++k) { const float d = v[k] - m2; q += d * d; }
  const float u = wave_sum(q) * (1.f / 512.f) * (1.f / (QS * QS)) + 1e-5f, rs = __builtin_amdgcn_rsqf(u);
  tok = f32x4{rs * (1.f / QS), 0.f, QS * u * rs, 0.f};
}

__global__ __launch_bounds__(256, 1) void k_main(const u16* __restrict__ K, const u16* __restrict__ V, const u16* __restrict__ wq,
                                                 const u16* __restrict__ w1, const u16* __restrict__ b1, char* __restrict__ ws,
                                                 const u16* __restrict__ x, const u16* __restrict__ wo, const u16* __restrict__ w2,
                                                 const u16* __restrict__ b2, u16* __restrict__ out) {
  __shared__ __attribute__((aligned(16))) char lds[163840];
  const unsigned bid = blockIdx.x, xcd = bid & 7, j = bid >> 3;
  const unsigned h = xcd * 4 + (j & 3), qb = j >> 2;
  const unsigned w = __builtin_amdgcn_readfirstlane(threadIdx.x >> 6);
  const unsigned long long ka = (unsigned long long)__builtin_amdgcn_kernarg_segment_ptr();
  const int lane = threadIdx.x & 63;
  asm volatile("s_getpc_b64 s[40:41]\n\ts_mov_b32 s42, 0x8000\n\ts_mov_b32 s43, 0x20000\n\ts_mov_b32 m0, 42240\n\ts_lshl_b32 s44, %[w], 13\n\t"
               "buffer_load_dword %[o], s[40:43], s44 offen lds"
               :: [o] "v"(lane * 128), [w] "s"(w) : "memory", "scc", "s40", "s41", "s42", "s43", "s44");
  const unsigned row = 256 * qb + 8 * h + 2 * w;
  const u32x4 raw0 = *(const u32x4*)(x + (size_t)row * 512 + lane * 8), raw1 = *(const u32x4*)(x + (size_t)(row + 1) * 512 + lane * 8);
  const char* F = ws + WS_LNF + (size_t)qb * 32 * 64;
  const char* A = ws + WS_LNA + (size_t)qb * 32 * 128;
  const unsigned pl = 8 * w + (lane & 7);
  const unsigned old_p = ld_agent(F + pl * 64), old_m = ld_agent(F + h * 64);
  const unsigned req = old_p + 1, mine = old_m + 1;
  if (lane < 8) st_agent(A + pl * 128 + h * 4, req);
  if (w >= 2) {
    asm volatile(
    "s_load_dwordx4 s[60:63], %[ka], 16\n\t"
    "s_getpc_b64 s[96:97]\n\t"
    "s_mov_b32 s98, 0x10000\n\t"
    "s_mov_b32 s99, 0x20000\n\t"
    "v_mbcnt_lo_u32_b32 v0, -1, 0\n\t"
    "v_mbcnt_hi_u32_b32 v0, -1, v0\n\t"
    "v_lshlrev_b32 v9, 7, v0\n\t"
    "s_min_u32 s78, %[h], 27\n\t"
    "v_lshrrev_b32 v7, 3, v0\n\t"
    "v_and_b32 v8, 7, v0\n\t"
    "v_lshrrev_b32 v3, 1, v8\n\t"
    "v_bfe_u32 v4, v7, 1, 2\n\t"
    "v_xor_b32 v3, v3, v4\n\t"
    "v_lshlrev_b32 v3, 5, v3\n\t"
    "v_and_b32 v4, 1, v8\n\t"
    "v_lshl_add_u32 v3, v4, 4, v3\n\t"
    "v_mul_u32_u24 v4, 3584, v7\n\t"
    "v_add_u32 v10, v3, v4\n\t"
    "v_and_b32 v1, 15, v0\n\t"
    "v_lshrrev_b32 v2, 4, v0\n\t"
    "v_lshlrev_b32 v3, 2, v2\n\t"
    "v_xor_b32 v3, v3, v1\n\t"
    "v_lshlrev_b32 v3, 4, v3\n\t"
    "v_lshl_add_u32 v11, v2, 13, v3\n\t"
    "s_waitcnt lgkmcnt(0)\n\t"
    "s_mov_b64 s[64:65], s[60:61]\n\t"
    "s_mov_b64 s[60:61], s[62:63]\n\t"
    "s_mov_b32 s62, 1835008\n\t"
    "s_mov_b32 s63, 0x20000\n\t"
    "s_mov_b32 s66, 4194304\n\t"
    "s_mov_b32 s67, 0x20000\n\t"
    "s_sub_u32 s50, %[w], 2\n\t"
    "s_lshl_b32 s50, s50, 1\n\t"
    "s_add_u32 s50, s50, 0\n\t"
    "s_lshl_b32 s94, s50, 12\n\t"
    "s_lshl_b32 s95, s50, 13\n\t"
    "s_mul_i32 s86, s50, 114688\n\t"
    "s_lshl_b32 s76, s78, 7\n\t"
    "s_add_u32 s86, s86, s76\n\t"
    "s_lshl_b32 s87, s50, 18\n\t"
    "s_lshl_b32 s76, %[h], 8\n\t"
    "s_add_u32 s87, s87, s76\n\t"
    "s_add_u32 m0, s94, 98304\n\t"
    "s_add_u32 s76, s86, 0\n\t"
    "buffer_load_dwordx4 v10, s[60:63], s76 offen lds\n\t"
    "s_add_u32 m0, s94, 99328\n\t"
    "s_add_u32 s76, s86, 28672\n\t"
    "buffer_load_dwordx4 v10, s[60:63], s76 offen lds\n\t"
    "s_add_u32 m0, s94, 100352\n\t"
    "s_add_u32 s76, s86, 57344\n\t"
    "buffer_load_dwordx4 v10, s[60:63], s76 offen lds\n\t"
    "s_add_u32 m0, s94, 101376\n\t"
    "s_add_u32 s76, s86, 86016\n\t"
    "buffer_load_dwordx4 v10, s[60:63], s76 offen lds\n\t"
    "s_add_u32 m0, s95, 0\n\t"
    "s_add_u32 s76, s87, 0\n\t"
    "buffer_load_dwordx4 v11, s[64:67], s76 offen lds\n\t"
    "s_add_u32 m0, s95, 1024\n\t"
    "s_add_u32 s76, s87, 32768\n\t"
    "buffer_load_dwordx4 v11, s[64:67], s76 offen lds\n\t"
    "s_add_u32 m0, s95, 2048\n\t"
    "s_add_u32 s76, s87, 65536\n\t"
    "buffer_load_dwordx4 v11, s[64:67], s76 offen lds\n\t"
    "s_add_u32 m0, s95, 3072\n\t"
    "s_add_u32 s76, s87, 98304\n\t"
    "buffer_load_dwordx4 v11, s[64:67], s76 offen lds\n\t"
    "s_add_u32 m0, s95, 4096\n\t"
    "s_add_u32 s76, s87, 131072\n\t"
    "buffer_load_dwordx4 v11, s[64:67], s76 offen lds\n\t"
    "s_add_u32 m0, s95, 5120\n\t"
    "s_add_u32 s76, s87, 163840\n\t"
    "buffer_load_dwordx4 v11, s[64:67], s76 offen lds\n\t"
    "s_add_u32 m0, s95, 6144\n\t"
    "s_add_u32 s76, s87, 196608\n\t"
    "buffer_load_dwordx4 v11, s[64:67], s76 offen lds\n\t"
    "s_add_u32 m0, s95, 7168\n\t"
    "s_add_u32 s76, s87, 229376\n\t"
    "buffer_load_dwordx4 v11, s[64:67], s76 offen lds\n\t"
    "s_lshr_b32 s76, %[bid], 3\n\t"
    "s_lshl_b32 s76, s76, 2\n\t"
    "s_add_u32 s76, s76, s50\n\t"
    "s_lshl_b32 s76, s76, 13\n\t"
    "s_mov_b32 m0, 41984\n\t"
    "s_nop 0\n\t"
    "buffer_load_dword v9, s[96:99], s76 offen lds\n\t"
    "s_sub_u32 s50, %[w], 2\n\t"
    "s_lshl_b32 s50, s50, 1\n\t"
    "s_add_u32 s50, s50, 1\n\t"
    "s_lshl_b32 s94, s50, 12\n\t"
    "s_lshl_b32 s95, s50, 13\n\t"
    "s_mul_i32 s86, s50, 114688\n\t"
    "s_lshl_b32 s76, s78, 7\n\t"
    "s_add_u32 s86, s86, s76\n\t"
    "s_lshl_b32 s87, s50, 18\n\t"
    "s_lshl_b32 s76, %[h], 8\n\t"
    "s_add_u32 s87, s87, s76\n\t"
    "s_add_u32 m0, s94, 98304\n\t"
    "s_add_u32 s76, s86, 0\n\t"
    "buffer_load_dwordx4 v10, s[60:63], s76 offen lds\n\t"
    "s_add_u32 m0, s94, 99328\n\t"
    "s_add_u32 s76, s86, 28672\n\t"
    "buffer_load_dwordx4 v10, s[60:63], s76 offen lds\n\t"
    "s_add_u32 m0, s94, 100352\n\t"
    "s_add_u32 s76, s86, 57344\n\t"
    "buffer_load_dwordx4 v10, s[60:63], s76 offen lds\n\t"
    "s_add_u32 m0, s94, 101376\n\t"
    "s_add_u32 s76, s86, 86016\n\t"
    "buffer_load_dwordx4 v10, s[60:63], s76 offen lds\n\t"
    "s_add_u32 m0, s95, 0\n\t"
    "s_add_u32 s76, s87, 0\n\t"
    "buffer_load_dwordx4 v11, s[64:67], s76 offen lds\n\t"
    "s_add_u32 m0, s95, 1024\n\t"
    "s_add_u32 s76, s87, 32768\n\t"
    "buffer_load_dwordx4 v11, s[64:67], s76 offen lds\n\t"
    "s_add_u32 m0, s95, 2048\n\t"
    "s_add_u32 s76, s87, 65536\n\t"
    "buffer_load_dwordx4 v11, s[64:67], s76 offen lds\n\t"
    "s_add_u32 m0, s95, 3072\n\t"
    "s_add_u32 s76, s87, 98304\n\t"
    "buffer_load_dwordx4 v11, s[64:67], s76 offen lds\n\t"
    "s_add_u32 m0, s95, 4096\n\t"
    "s_add_u32 s76, s87, 131072\n\t"
    "buffer_load_dwordx4 v11, s[64:67], s76 offen lds\n\t"
    "s_add_u32 m0, s95, 5120\n\t"
    "s_add_u32 s76, s87, 163840\n\t"
    "buffer_load_dwordx4 v11, s[64:67], s76 offen lds\n\t"
    "s_add_u32 m0, s95, 6144\n\t"
    "s_add_u32 s76, s87, 196608\n\t"
    "buffer_load_dwordx4 v11, s[64:67], s76 offen lds\n\t"
    "s_add_u32 m0, s95, 7168\n\t"
    "s_add_u32 s76, s87, 229376\n\t"
    "buffer_load_dwordx4 v11, s[64:67], s76 offen lds\n\t"
    "s_lshr_b32 s76, %[bid], 3\n\t"
    "s_lshl_b32 s76, s76, 2\n\t"
    "s_add_u32 s76, s76, s50\n\t"
    "s_lshl_b32 s76, s76, 13\n\t"
    "s_mov_b32 m0, 41984\n\t"
    "s_nop 0\n\t"
    "buffer_load_dword v9, s[96:99], s76 offen lds\n\t"
      :
      : [ka] "s"(ka), [w] "s"(w), [h] "s"(h), [bid] "s"(bid)
      : "memory", "scc", "v0","v1","v2","v3","v4","v5","v6","v7","v8","v9","v10","v11","v12","v13","v14","v15","s40","s41","s42","s43","s44","s45","s46","s47","s48","s49","s50","s51","s52","s53","s54","s55","s56","s57","s58","s59","s60","s61","s62","s63","s64","s65","s66","s67","s68","s69","s70","s71","s72","s73","s74","s75","s76","s77","s78","s79","s80","s81","s82","s83","s84","s85","s86","s87","s88","s89","s90","s91","s92","s93","s94","s95","s96","s97","s98","s99","s100","s101");
  }
  {
    u32x4 p0, p1; f32x4 t0, t1;
    ln_row(raw0, lane, p0, t0);
    ln_row(raw1, lane, p1, t1);
    char* sc = lds + 32768;
    const int pi = 4 * (lane >> 2) + 2 * (lane & 1) + ((lane >> 1) & 1);
    *(u32x4*)(sc + (2 * w) * 1040 + pi * 16) = p0;
    *(u32x4*)(sc + (2 * w + 1) * 1040 + pi * 16) = p1;
    if (lane == 0) { *(f32x4*)(sc + 8320 + (2 * w) * 16) = t0; *(f32x4*)(sc + 8320 + (2 * w + 1) * 16) = t1; }
    __syncthreads();
    const __amdgpu_buffer_rsrc_t r = __builtin_amdgcn_make_buffer_rsrc(ws, 0, (int)WS_END, 0x00020000);
    const unsigned tb = 16 * qb + (h >> 1), i = lane & 7;
    unsigned seen = 0;
    if (w < 2) {
#pragma unroll
      for (int s = 0; s < 4; ++s) {
        const unsigned p = 32 * w + 8 * s + (lane >> 3);
        const u32x4 d = *(const u32x4*)(sc + i * 1040 + p * 16);
        __builtin_amdgcn_raw_buffer_store_b128(d, r, (p & 3) * 256 + (8 * (h & 1) + i) * 16 + (p >> 2) * 1024, (unsigned)WS_XF + tb * 16384, 16);
      }
      if (w == 0) {
        if (lane < 8) {
          const u32x4 d = *(const u32x4*)(sc + 8320 + lane * 16);
          __builtin_amdgcn_raw_buffer_store_b128(d, r, lane * 16, (unsigned)WS_TOK + (256 * qb + 8 * h) * 16, 16);
        }
        seen = ld_agent(A + h * 128 + (lane & 31) * 4);
      } else if (lane < 16) {
        const unsigned area = lane >> 2;
        const unsigned off = area == 0 ? (h < 4 ? 0 + (qb * 4 + h) * 64 : 0x40000000u) : area == 1 ? 4096 + (qb * 32 + h) * 64
                           : area == 2 ? (h < 16 ? 20480 + (qb * 16 + h) * 64 : 0x40000000u) : 36864 + (qb * 32 + h) * 64;
        __builtin_amdgcn_raw_buffer_store_b128(u32x4{0u, 0u, 0u, 0u}, r, off + (lane & 3) * 16, 0, 16);
      }
      asm volatile("s_waitcnt vmcnt(0)" ::: "memory");
    }
    __syncthreads();
    if (w == 0) {
      for (unsigned spin = 0; spin < 0x8000 && !__all(seen == mine); ++spin) {
        __builtin_amdgcn_s_sleep(1);
        seen = ld_agent(A + h * 128 + (lane & 31) * 4);
      }
      if (lane == 0) st_agent(F + h * 64, mine);
    }
  }
  unsigned reqv = req;
  asm volatile(
    "s_mov_b32 s50, %[w]\n\t"
    "s_load_dwordx4 s[40:43], %[ka], 0\n\t"
    "s_load_dwordx4 s[60:63], %[ka], 16\n\t"
    "s_load_dwordx4 s[68:71], %[ka], 32\n\t"
    "s_min_u32 s78, %[h], 27\n\t"
    "s_lshl_b32 s94, s50, 12\n\t"
    "s_lshl_b32 s95, s50, 13\n\t"
    "s_lshl_b32 s79, %[qb], 4\n\t"
    "s_lshl_b32 s76, s50, 2\n\t"
    "s_add_u32 s79, s79, s76\n\t"
    "s_lshl_b32 s90, s79, 8\n\t"
    "s_add_u32 s90, s90, 65536\n\t"
    "s_lshl_b32 s91, s78, 8\n\t"
    "s_add_u32 s91, s91, 98304\n\t"
    "s_add_u32 s76, s79, 0\n\t"
    "s_lshl_b32 s76, s76, 14\n\t"
    "s_add_u32 s80, s76, 131072\n\t"
    "s_add_u32 s76, s79, 1\n\t"
    "s_lshl_b32 s76, s76, 14\n\t"
    "s_add_u32 s81, s76, 131072\n\t"
    "s_add_u32 s76, s79, 2\n\t"
    "s_lshl_b32 s76, s76, 14\n\t"
    "s_add_u32 s82, s76, 131072\n\t"
    "s_add_u32 s76, s79, 3\n\t"
    "s_lshl_b32 s76, s76, 14\n\t"
    "s_add_u32 s83, s76, 131072\n\t"
    "s_mul_i32 s84, s79, 57344\n\t"
    "s_lshl_b32 s76, s78, 11\n\t"
    "s_add_u32 s84, s84, s76\n\t"
    "s_lshl_b32 s85, s79, 17\n\t"
    "s_lshl_b32 s76, %[h], 12\n\t"
    "s_add_u32 s85, s85, s76\n\t"
    "s_add_u32 s85, s85, 9568256\n\t"
    "s_mul_i32 s86, s50, 114688\n\t"
    "s_lshl_b32 s76, s78, 7\n\t"
    "s_add_u32 s86, s86, s76\n\t"
    "s_lshl_b32 s87, s50, 18\n\t"
    "s_lshl_b32 s76, %[h], 8\n\t"
    "s_add_u32 s87, s87, s76\n\t"
    "s_mov_b32 s48, s94\n\t"
    "s_mov_b32 s51, s94\n\t"
    "s_add_u32 s52, s94, 0\n\t"
    "s_add_u32 s53, s94, 16384\n\t"
    "s_add_u32 s54, s94, 32768\n\t"
    "s_add_u32 s55, s94, 49152\n\t"
    "s_add_u32 s56, s94, 65536\n\t"
    "s_add_u32 s57, s94, 81920\n\t"
    "s_add_u32 s58, s94, 98304\n\t"
    "s_add_u32 s59, s94, 114688\n\t"
    "v_mbcnt_lo_u32_b32 v0, -1, 0\n\t"
    "v_mbcnt_hi_u32_b32 v0, -1, v0\n\t"
    "v_lshlrev_b32 v255, 4, v0\n\t"
    "v_and_b32 v1, 15, v0\n\t"
    "v_lshrrev_b32 v2, 4, v0\n\t"
    "v_lshlrev_b32 v11, 4, v1\n\t"
    "v_lshlrev_b32 v12, 4, v2\n\t"
    "v_lshlrev_b32 v13, 3, v2\n\t"
    "v_lshrrev_b32 v7, 3, v0\n\t"
    "v_and_b32 v8, 7, v0\n\t"
    "v_lshrrev_b32 v3, 1, v8\n\t"
    "v_bfe_u32 v4, v7, 1, 2\n\t"
    "v_xor_b32 v3, v3, v4\n\t"
    "v_lshlrev_b32 v3, 5, v3\n\t"
    "v_and_b32 v4, 1, v8\n\t"
    "v_lshl_add_u32 v3, v4, 4, v3\n\t"
    "v_mul_u32_u24 v4, 3584, v7\n\t"
    "v_add_u32 v230, v3, v4\n\t"
    "s_waitcnt lgkmcnt(0)\n\t"
    "s_mov_b64 s[88:89], s[68:69]\n\t"
    "s_mov_b64 s[68:69], s[70:71]\n\t"
    "s_mov_b32 s70, 59932672\n\t"
    "s_mov_b32 s71, 0x20000\n\t"
    "s_lshl_b32 s76, s78, 7\n\t"
    "s_add_u32 s88, s88, s76\n\t"
    "s_addc_u32 s89, s89, 0\n\t"
    "s_mov_b64 s[64:65], s[60:61]\n\t"
    "s_mov_b64 s[60:61], s[62:63]\n\t"
    "s_mov_b32 s62, 1835008\n\t"
    "s_mov_b32 s63, 0x20000\n\t"
    "s_mov_b32 s66, 4194304\n\t"
    "s_mov_b32 s67, 0x20000\n\t"
    "s_lshl_b32 s76, %[h], 19\n\t"
    "s_add_u32 s44, s42, s76\n\t"
    "s_addc_u32 s45, s43, 0\n\t"
    "s_add_u32 s40, s40, s76\n\t"
    "s_addc_u32 s41, s41, 0\n\t"
    "s_mov_b32 s42, 0x80000\n\t"
    "s_mov_b32 s43, 0x20000\n\t"
    "s_mov_b32 s46, 0x80000\n\t"
    "s_mov_b32 s47, 0x20000\n\t"
    "s_add_u32 s72, s68, 2228224\n\t"
    "s_addc_u32 s73, s69, 0\n\t"
    "s_cmp_lt_u32 %[h], 28\n\t"
    "s_cselect_b32 s74, 7340032, 0\n\t"
    "s_mov_b32 s75, 0x20000\n\t"
    "global_load_dwordx2 v[48:49], v13, s[88:89] offset:0\n\t"
    "global_load_dwordx2 v[50:51], v13, s[88:89] offset:32\n\t"
    "global_load_dwordx2 v[52:53], v13, s[88:89] offset:64\n\t"
    "global_load_dwordx2 v[54:55], v13, s[88:89] offset:96\n\t"
    "v_lshrrev_b32 v5, 2, v1\n\t"
    "v_and_b32 v6, 3, v0\n\t"
    "v_or_b32 v3, 0, v2\n\t"
    "v_xor_b32 v3, v3, v1\n\t"
    "v_lshlrev_b32 v3, 4, v3\n\t"
    "v_lshl_add_u32 v224, v2, 8, v3\n\t"
    "v_or_b32 v3, 4, v2\n\t"
    "v_xor_b32 v3, v3, v1\n\t"
    "v_lshlrev_b32 v3, 4, v3\n\t"
    "v_lshl_add_u32 v225, v2, 8, v3\n\t"
    "v_or_b32 v3, 8, v2\n\t"
    "v_xor_b32 v3, v3, v1\n\t"
    "v_lshlrev_b32 v3, 4, v3\n\t"
    "v_lshl_add_u32 v226, v2, 8, v3\n\t"
    "v_or_b32 v3, 12, v2\n\t"
    "v_xor_b32 v3, v3, v1\n\t"
    "v_lshlrev_b32 v3, 4, v3\n\t"
    "v_lshl_add_u32 v227, v2, 8, v3\n\t"
    "v_lshrrev_b32 v3, 1, v1\n\t"
    "v_or_b32 v4, 0, v2\n\t"
    "v_xor_b32 v3, v3, v4\n\t"
    "v_lshlrev_b32 v3, 5, v3\n\t"
    "v_and_b32 v4, 1, v1\n\t"
    "v_lshl_add_u32 v3, v4, 4, v3\n\t"
    "v_lshl_add_u32 v228, v2, 8, v3\n\t"
    "v_lshrrev_b32 v3, 1, v1\n\t"
    "v_or_b32 v4, 4, v2\n\t"
    "v_xor_b32 v3, v3, v4\n\t"
    "v_lshlrev_b32 v3, 5, v3\n\t"
    "v_and_b32 v4, 1, v1\n\t"
    "v_lshl_add_u32 v3, v4, 4, v3\n\t"
    "v_lshl_add_u32 v229, v2, 8, v3\n\t"
    "v_lshlrev_b32 v3, 2, v2\n\t"
    "v_xor_b32 v3, v3, v1\n\t"
    "v_lshlrev_b32 v3, 4, v3\n\t"
    "v_lshl_add_u32 v231, v2, 13, v3\n\t"
    "v_lshl_add_u32 v4, v2, 2, v5\n\t"
    "v_bfe_u32 v3, v4, 1, 2\n\t"
    "v_lshlrev_b32 v9, 7, v4\n\t"
    "v_lshl_add_u32 v9, v6, 3, v9\n\t"
    "v_add_u32 v9, 98304, v9\n\t"
    "v_xor_b32 v10, 0, v3\n\t"
    "v_lshl_add_u32 v232, v10, 5, v9\n\t"
    "v_xor_b32 v10, 1, v3\n\t"
    "v_lshl_add_u32 v233, v10, 5, v9\n\t"
    "v_xor_b32 v10, 2, v3\n\t"
    "v_lshl_add_u32 v234, v10, 5, v9\n\t"
    "v_xor_b32 v10, 3, v3\n\t"
    "v_lshl_add_u32 v235, v10, 5, v9\n\t"
    "v_lshlrev_b32 v9, 8, v4\n\t"
    "v_lshl_add_u32 v9, v6, 4, v9\n\t"
    "v_xor_b32 v10, 0, v5\n\t"
    "v_lshl_add_u32 v236, v10, 6, v9\n\t"
    "v_add_u32 v240, 0x10000, v236\n\t"
    "v_xor_b32 v10, 1, v5\n\t"
    "v_lshl_add_u32 v237, v10, 6, v9\n\t"
    "v_add_u32 v241, 0x10000, v237\n\t"
    "v_xor_b32 v10, 2, v5\n\t"
    "v_lshl_add_u32 v238, v10, 6, v9\n\t"
    "v_add_u32 v242, 0x10000, v238\n\t"
    "v_xor_b32 v10, 3, v5\n\t"
    "v_lshl_add_u32 v239, v10, 6, v9\n\t"
    "v_add_u32 v243, 0x10000, v239\n\t"
    "v_mov_b32 v253, 0xbdd2d3e8\n\t"
    "v_bfe_u32 v3, v255, 4, 3\n\t"
    "v_lshlrev_b32 v3, 6, v3\n\t"
    "s_lshl_b32 s76, %[qb], 5\n\t"
    "s_lshl_b32 s77, s50, 3\n\t"
    "s_add_u32 s76, s76, s77\n\t"
    "s_lshl_b32 s76, s76, 6\n\t"
    "s_add_u32 s76, s76, 98304\n\t"
    "s_mov_b32 s77, 0x8000\n\t"
    ".Lln%=:\n\t"
    "buffer_load_dword v4, v3, s[68:71], s76 offen sc1\n\t"
    "s_waitcnt vmcnt(0)\n\t"
    "v_cmp_eq_u32 vcc, v4, %[req]\n\t"
    "s_nop 1\n\t"
    "s_cmp_eq_u64 vcc, -1\n\t"
    "s_cbranch_scc1 .Llnd%=\n\t"
    "s_sleep 1\n\t"
    "s_sub_u32 s77, s77, 1\n\t"
    "s_cmp_lg_u32 s77, 0\n\t"
    "s_cbranch_scc1 .Lln%=\n\t"
    ".Llnd%=:\n\t"
    "s_add_u32 s76, s90, 0\n\t"
    "buffer_load_dwordx4 v[16:19], v11, s[68:71], s76 offen sc1\n\t"
    "s_add_u32 s76, s90, 256\n\t"
    "buffer_load_dwordx4 v[20:23], v11, s[68:71], s76 offen sc1\n\t"
    "s_add_u32 s76, s90, 512\n\t"
    "buffer_load_dwordx4 v[24:27], v11, s[68:71], s76 offen sc1\n\t"
    "s_add_u32 s76, s90, 768\n\t"
    "buffer_load_dwordx4 v[28:31], v11, s[68:71], s76 offen sc1\n\t"
    "s_add_u32 s77, s80, 0\n\t"
    "buffer_load_dwordx4 a[0:3], v255, s[68:71], s77 offen offset:0 sc1\n\t"
    "buffer_load_dwordx4 a[4:7], v255, s[68:71], s77 offen offset:1024 sc1\n\t"
    "buffer_load_dwordx4 a[8:11], v255, s[68:71], s77 offen offset:2048 sc1\n\t"
    "buffer_load_dwordx4 a[12:15], v255, s[68:71], s77 offen offset:3072 sc1\n\t"
    "s_add_u32 s77, s81, 0\n\t"
    "buffer_load_dwordx4 a[64:67], v255, s[68:71], s77 offen offset:0 sc1\n\t"
    "buffer_load_dwordx4 a[68:71], v255, s[68:71], s77 offen offset:1024 sc1\n\t"
    "buffer_load_dwordx4 a[72:75], v255, s[68:71], s77 offen offset:2048 sc1\n\t"
    "buffer_load_dwordx4 a[76:79], v255, s[68:71], s77 offen offset:3072 sc1\n\t"
    "s_waitcnt vmcnt(8)\n\t"
    "v_lshlrev_b32 v56, 16, v48\n\t"
    "v_and_b32 v57, 0xffff0000, v48\n\t"
    "v_lshlrev_b32 v58, 16, v49\n\t"
    "v_and_b32 v59, 0xffff0000, v49\n\t"
    "s_add_u32 s77, s82, 0\n\t"
    "buffer_load_dwordx4 a[128:131], v255, s[68:71], s77 offen offset:0 sc1\n\t"
    "v_lshlrev_b32 v60, 16, v50\n\t"
    "v_and_b32 v61, 0xffff0000, v50\n\t"
    "v_lshlrev_b32 v62, 16, v51\n\t"
    "v_and_b32 v63, 0xffff0000, v51\n\t"
    "buffer_load_dwordx4 a[132:135], v255, s[68:71], s77 offen offset:1024 sc1\n\t"
    "v_lshlrev_b32 v64, 16, v52\n\t"
    "v_and_b32 v65, 0xffff0000, v52\n\t"
    "v_lshlrev_b32 v66, 16, v53\n\t"
    "v_and_b32 v67, 0xffff0000, v53\n\t"
    "buffer_load_dwordx4 a[136:139], v255, s[68:71], s77 offen offset:2048 sc1\n\t"
    "v_lshlrev_b32 v68, 16, v54\n\t"
    "v_and_b32 v69, 0xffff0000, v54\n\t"
    "v_lshlrev_b32 v70, 16, v55\n\t"
    "v_and_b32 v71, 0xffff0000, v55\n\t"
    "buffer_load_dwordx4 a[140:143], v255, s[68:71], s77 offen offset:3072 sc1\n\t"
    "v_mov_b32 v244, v16\n\t"
    "v_mov_b32 v245, v20\n\t"
    "v_mov_b32 v246, v24\n\t"
    "v_mov_b32 v247, v28\n\t"
    "v_mul_f32 v128, v56, v18\n\t"
    "v_mul_f32 v129, v57, v18\n\t"
    "v_mul_f32 v130, v58, v18\n\t"
    "v_mul_f32 v131, v59, v18\n\t"
    "s_add_u32 s77, s83, 0\n\t"
    "buffer_load_dwordx4 a[192:195], v255, s[68:71], s77 offen offset:0 sc1\n\t"
    "v_mul_f32 v136, v56, v22\n\t"
    "v_mul_f32 v137, v57, v22\n\t"
    "v_mul_f32 v138, v58, v22\n\t"
    "v_mul_f32 v139, v59, v22\n\t"
    "buffer_load_dwordx4 a[196:199], v255, s[68:71], s77 offen offset:1024 sc1\n\t"
    "v_mul_f32 v144, v56, v26\n\t"
    "v_mul_f32 v145, v57, v26\n\t"
    "v_mul_f32 v146, v58, v26\n\t"
    "v_mul_f32 v147, v59, v26\n\t"
    "buffer_load_dwordx4 a[200:203], v255, s[68:71], s77 offen offset:2048 sc1\n\t"
    "v_mul_f32 v152, v56, v30\n\t"
    "v_mul_f32 v153, v57, v30\n\t"
    "v_mul_f32 v154, v58, v30\n\t"
    "v_mul_f32 v155, v59, v30\n\t"
    "buffer_load_dwordx4 a[204:207], v255, s[68:71], s77 offen offset:3072 sc1\n\t"
    "s_waitcnt vmcnt(0)\n\t"
    "s_barrier\n\t"
    "ds_read_b64_tr_b16 v[192:193], v232 offset:0\n\t"
    "ds_read_b64_tr_b16 v[194:195], v232 offset:2048\n\t"
    "ds_read_b64_tr_b16 v[196:197], v232 offset:4096\n\t"
    "ds_read_b64_tr_b16 v[198:199], v232 offset:6144\n\t"
    "ds_read_b64_tr_b16 v[200:201], v232 offset:8192\n\t"
    "ds_read_b64_tr_b16 v[202:203], v232 offset:10240\n\t"
    "ds_read_b64_tr_b16 v[204:205], v232 offset:12288\n\t"
    "ds_read_b64_tr_b16 v[206:207], v232 offset:14336\n\t"
    "s_waitcnt lgkmcnt(0)\n\t"
    "v_mfma_f32_16x16x32_bf16 v[128:131], v[192:195], a[0:3], v[128:131]\n\t"
    "ds_read_b64_tr_b16 v[208:209], v233 offset:0\n\t"
    "v_mul_f32 v132, v60, v18\n\t"
    "v_mfma_f32_16x16x32_bf16 v[128:131], v[196:199], a[4:7], v[128:131]\n\t"
    "ds_read_b64_tr_b16 v[210:211], v233 offset:2048\n\t"
    "v_mul_f32 v133, v61, v18\n\t"
    "v_mfma_f32_16x16x32_bf16 v[128:131], v[200:203], a[8:11], v[128:131]\n\t"
    "ds_read_b64_tr_b16 v[212:213], v233 offset:4096\n\t"
    "v_mul_f32 v134, v62, v18\n\t"
    "v_mfma_f32_16x16x32_bf16 v[128:131], v[204:207], a[12:15], v[128:131]\n\t"
    "ds_read_b64_tr_b16 v[214:215], v233 offset:6144\n\t"
    "v_mul_f32 v135, v63, v18\n\t"
    "v_mfma_f32_16x16x32_bf16 v[136:139], v[192:195], a[64:67], v[136:139]\n\t"
    "ds_read_b64_tr_b16 v[216:217], v233 offset:8192\n\t"
    "v_mul_f32 v140, v60, v22\n\t"
    "v_mfma_f32_16x16x32_bf16 v[136:139], v[196:199], a[68:71], v[136:139]\n\t"
    "ds_read_b64_tr_b16 v[218:219], v233 offset:10240\n\t"
    "v_mul_f32 v141, v61, v22\n\t"
    "v_mfma_f32_16x16x32_bf16 v[136:139], v[200:203], a[72:75], v[136:139]\n\t"
    "ds_read_b64_tr_b16 v[220:221], v233 offset:12288\n\t"
    "v_mul_f32 v142, v62, v22\n\t"
    "v_mfma_f32_16x16x32_bf16 v[136:139], v[204:207], a[76:79], v[136:139]\n\t"
    "ds_read_b64_tr_b16 v[222:223], v233 offset:14336\n\t"
    "v_mul_f32 v143, v63, v22\n\t"
    "v_mfma_f32_16x16x32_bf16 v[144:147], v[192:195], a[128:131], v[144:147]\n\t"
    "s_add_u32 m0, s94, 114688\n\t"
    "s_add_u32 s76, s86, 458752\n\t"
    "buffer_load_dwordx4 v230, s[60:63], s76 offen lds\n\t"
    "v_mfma_f32_16x16x32_bf16 v[144:147], v[196:199], a[132:135], v[144:147]\n\t"
    "v_mul_f32 v148, v60, v26\n\t"
    "v_mul_f32 v149, v61, v26\n\t"
    "v_mfma_f32_16x16x32_bf16 v[144:147], v[200:203], a[136:139], v[144:147]\n\t"
    "v_mul_f32 v150, v62, v26\n\t"
    "v_mul_f32 v151, v63, v26\n\t"
    "v_mfma_f32_16x16x32_bf16 v[144:147], v[204:207], a[140:143], v[144:147]\n\t"
    "s_add_u32 m0, s94, 115712\n\t"
    "s_add_u32 s76, s86, 487424\n\t"
    "buffer_load_dwordx4 v230, s[60:63], s76 offen lds\n\t"
    "v_mfma_f32_16x16x32_bf16 v[152:155], v[192:195], a[192:195], v[152:155]\n\t"
    "v_mul_f32 v156, v60, v30\n\t"
    "v_mul_f32 v157, v61, v30\n\t"
    "v_mfma_f32_16x16x32_bf16 v[152:155], v[196:199], a[196:199], v[152:155]\n\t"
    "v_mul_f32 v158, v62, v30\n\t"
    "v_mul_f32 v159, v63, v30\n\t"
    "v_mfma_f32_16x16x32_bf16 v[152:155], v[200:203], a[200:203], v[152:155]\n\t"
    "s_add_u32 m0, s94, 116736\n\t"
    "s_add_u32 s76, s86, 516096\n\t"
    "buffer_load_dwordx4 v230, s[60:63], s76 offen lds\n\t"
    "v_mfma_f32_16x16x32_bf16 v[152:155], v[204:207], a[204:207], v[152:155]\n\t"
    "s_waitcnt lgkmcnt(0)\n\t"
    "v_mfma_f32_16x16x32_bf16 v[132:135], v[208:211], a[0:3], v[132:135]\n\t"
    "ds_read_b64_tr_b16 v[192:193], v234 offset:0\n\t"
    "v_mul_f32 v160, v64, v18\n\t"
    "v_mfma_f32_16x16x32_bf16 v[132:135], v[212:215], a[4:7], v[132:135]\n\t"
    "ds_read_b64_tr_b16 v[194:195], v234 offset:2048\n\t"
    "v_mul_f32 v161, v65, v18\n\t"
    "v_mfma_f32_16x16x32_bf16 v[132:135], v[216:219], a[8:11], v[132:135]\n\t"
    "ds_read_b64_tr_b16 v[196:197], v234 offset:4096\n\t"
    "v_mul_f32 v162, v66, v18\n\t"
    "v_mfma_f32_16x16x32_bf16 v[132:135], v[220:223], a[12:15], v[132:135]\n\t"
    "ds_read_b64_tr_b16 v[198:199], v234 offset:6144\n\t"
    "v_mul_f32 v163, v67, v18\n\t"
    "v_mfma_f32_16x16x32_bf16 v[140:143], v[208:211], a[64:67], v[140:143]\n\t"
    "ds_read_b64_tr_b16 v[200:201], v234 offset:8192\n\t"
    "v_mul_f32 v168, v64, v22\n\t"
    "v_mfma_f32_16x16x32_bf16 v[140:143], v[212:215], a[68:71], v[140:143]\n\t"
    "ds_read_b64_tr_b16 v[202:203], v234 offset:10240\n\t"
    "v_mul_f32 v169, v65, v22\n\t"
    "v_mfma_f32_16x16x32_bf16 v[140:143], v[216:219], a[72:75], v[140:143]\n\t"
    "ds_read_b64_tr_b16 v[204:205], v234 offset:12288\n\t"
    "v_mul_f32 v170, v66, v22\n\t"
    "v_mfma_f32_16x16x32_bf16 v[140:143], v[220:223], a[76:79], v[140:143]\n\t"
    "ds_read_b64_tr_b16 v[206:207], v234 offset:14336\n\t"
    "v_mul_f32 v171, v67, v22\n\t"
    "v_mfma_f32_16x16x32_bf16 v[148:151], v[208:211], a[128:131], v[148:151]\n\t"
    "s_add_u32 m0, s94, 117760\n\t"
    "s_add_u32 s76, s86, 544768\n\t"
    "buffer_load_dwordx4 v230, s[60:63], s76 offen lds\n\t"
    "v_mfma_f32_16x16x32_bf16 v[148:151], v[212:215], a[132:135], v[148:151]\n\t"
    "v_mul_f32 v176, v64, v26\n\t"
    "v_mul_f32 v177, v65, v26\n\t"
    "v_mfma_f32_16x16x32_bf16 v[148:151], v[216:219], a[136:139], v[148:151]\n\t"
    "v_mul_f32 v178, v66, v26\n\t"
    "v_mul_f32 v179, v67, v26\n\t"
    "v_mfma_f32_16x16x32_bf16 v[148:151], v[220:223], a[140:143], v[148:151]\n\t"
    "s_add_u32 s77, s80, 4096\n\t"
    "buffer_load_dwordx4 a[16:19], v255, s[68:71], s77 offen offset:0 sc1\n\t"
    "v_mfma_f32_16x16x32_bf16 v[156:159], v[208:211], a[192:195], v[156:159]\n\t"
    "v_mul_f32 v184, v64, v30\n\t"
    "v_mul_f32 v185, v65, v30\n\t"
    "v_mfma_f32_16x16x32_bf16 v[156:159], v[212:215], a[196:199], v[156:159]\n\t"
    "v_mul_f32 v186, v66, v30\n\t"
    "v_mul_f32 v187, v67, v30\n\t"
    "v_mfma_f32_16x16x32_bf16 v[156:159], v[216:219], a[200:203], v[156:159]\n\t"
    "buffer_load_dwordx4 a[20:23], v255, s[68:71], s77 offen offset:1024 sc1\n\t"
    "v_mfma_f32_16x16x32_bf16 v[156:159], v[220:223], a[204:207], v[156:159]\n\t"
    "s_waitcnt lgkmcnt(0)\n\t"
    "v_mfma_f32_16x16x32_bf16 v[160:163], v[192:195], a[0:3], v[160:163]\n\t"
    "ds_read_b64_tr_b16 v[208:209], v235 offset:0\n\t"
    "v_mul_f32 v164, v68, v18\n\t"
    "v_mfma_f32_16x16x32_bf16 v[160:163], v[196:199], a[4:7], v[160:163]\n\t"
    "ds_read_b64_tr_b16 v[210:211], v235 offset:2048\n\t"
    "v_mul_f32 v165, v69, v18\n\t"
    "v_mfma_f32_16x16x32_bf16 v[160:163], v[200:203], a[8:11], v[160:163]\n\t"
    "ds_read_b64_tr_b16 v[212:213], v235 offset:4096\n\t"
    "v_mul_f32 v166, v70, v18\n\t"
    "v_mfma_f32_16x16x32_bf16 v[160:163], v[204:207], a[12:15], v[160:163]\n\t"
    "ds_read_b64_tr_b16 v[214:215], v235 offset:6144\n\t"
    "v_mul_f32 v167, v71, v18\n\t"
    "v_mfma_f32_16x16x32_bf16 v[168:171], v[192:195], a[64:67], v[168:171]\n\t"
    "ds_read_b64_tr_b16 v[216:217], v235 offset:8192\n\t"
    "v_mul_f32 v172, v68, v22\n\t"
    "v_mfma_f32_16x16x32_bf16 v[168:171], v[196:199], a[68:71], v[168:171]\n\t"
    "ds_read_b64_tr_b16 v[218:219], v235 offset:10240\n\t"
    "v_mul_f32 v173, v69, v22\n\t"
    "v_mfma_f32_16x16x32_bf16 v[168:171], v[200:203], a[72:75], v[168:171]\n\t"
    "ds_read_b64_tr_b16 v[220:221], v235 offset:12288\n\t"
    "v_mul_f32 v174, v70, v22\n\t"
    "v_mfma_f32_16x16x32_bf16 v[168:171], v[204:207], a[76:79], v[168:171]\n\t"
    "ds_read_b64_tr_b16 v[222:223], v235 offset:14336\n\t"
    "v_mul_f32 v175, v71, v22\n\t"
    "v_mfma_f32_16x16x32_bf16 v[176:179], v[192:195], a[128:131], v[176:179]\n\t"
    "buffer_load_dwordx4 a[24:27], v255, s[68:71], s77 offen offset:2048 sc1\n\t"
    "v_mfma_f32_16x16x32_bf16 v[176:179], v[196:199], a[132:135], v[176:179]\n\t"
    "v_mul_f32 v180, v68, v26\n\t"
    "v_mul_f32 v181, v69, v26\n\t"
    "v_mfma_f32_16x16x32_bf16 v[176:179], v[200:203], a[136:139], v[176:179]\n\t"
    "v_mul_f32 v182, v70, v26\n\t"
    "v_mul_f32 v183, v71, v26\n\t"
    "v_mfma_f32_16x16x32_bf16 v[176:179], v[204:207], a[140:143], v[176:179]\n\t"
    "buffer_load_dwordx4 a[28:31], v255, s[68:71], s77 offen offset:3072 sc1\n\t"
    "v_mfma_f32_16x16x32_bf16 v[184:187], v[192:195], a[192:195], v[184:187]\n\t"
    "v_mul_f32 v188, v68, v30\n\t"
    "v_mul_f32 v189, v69, v30\n\t"
    "v_mfma_f32_16x16x32_bf16 v[184:187], v[196:199], a[196:199], v[184:187]\n\t"
    "v_mul_f32 v190, v70, v30\n\t"
    "v_mul_f32 v191, v71, v30\n\t"
    "v_mfma_f32_16x16x32_bf16 v[184:187], v[200:203], a[200:203], v[184:187]\n\t"
    "s_add_u32 s77, s81, 4096\n\t"
    "buffer_load_dwordx4 a[80:83], v255, s[68:71], s77 offen offset:0 sc1\n\t"
    "v_mfma_f32_16x16x32_bf16 v[184:187], v[204:207], a[204:207], v[184:187]\n\t"
    "s_waitcnt lgkmcnt(0)\n\t"
    "v_mfma_f32_16x16x32_bf16 v[164:167], v[208:211], a[0:3], v[164:167]\n\t"
    "s_barrier\n\t"
    "ds_read_b64_tr_b16 v[192:193], v236 offset:0\n\t"
    "v_mfma_f32_16x16x32_bf16 v[164:167], v[212:215], a[4:7], v[164:167]\n\t"
    "ds_read_b64_tr_b16 v[194:195], v236 offset:4096\n\t"
    "v_mfma_f32_16x16x32_bf16 v[164:167], v[216:219], a[8:11], v[164:167]\n\t"
    "ds_read_b64_tr_b16 v[196:197], v236 offset:8192\n\t"
    "v_mfma_f32_16x16x32_bf16 v[164:167], v[220:223], a[12:15], v[164:167]\n\t"
    "ds_read_b64_tr_b16 v[198:199], v236 offset:12288\n\t"
    "v_mfma_f32_16x16x32_bf16 v[172:175], v[208:211], a[64:67], v[172:175]\n\t"
    "ds_read_b64_tr_b16 v[200:201], v236 offset:16384\n\t"
    "v_mfma_f32_16x16x32_bf16 v[172:175], v[212:215], a[68:71], v[172:175]\n\t"
    "ds_read_b64_tr_b16 v[202:203], v236 offset:20480\n\t"
    "v_mfma_f32_16x16x32_bf16 v[172:175], v[216:219], a[72:75], v[172:175]\n\t"
    "ds_read_b64_tr_b16 v[204:205], v236 offset:24576\n\t"
    "v_mfma_f32_16x16x32_bf16 v[172:175], v[220:223], a[76:79], v[172:175]\n\t"
    "ds_read_b64_tr_b16 v[206:207], v236 offset:28672\n\t"
    "v_mfma_f32_16x16x32_bf16 v[180:183], v[208:211], a[128:131], v[180:183]\n\t"
    "buffer_load_dwordx4 a[84:87], v255, s[68:71], s77 offen offset:1024 sc1\n\t"
    "v_mfma_f32_16x16x32_bf16 v[180:183], v[212:215], a[132:135], v[180:183]\n\t"
    "v_mfma_f32_16x16x32_bf16 v[180:183], v[216:219], a[136:139], v[180:183]\n\t"
    "v_mfma_f32_16x16x32_bf16 v[180:183], v[220:223], a[140:143], v[180:183]\n\t"
    "buffer_load_dwordx4 a[88:91], v255, s[68:71], s77 offen offset:2048 sc1\n\t"
    "v_mfma_f32_16x16x32_bf16 v[188:191], v[208:211], a[192:195], v[188:191]\n\t"
    "v_mfma_f32_16x16x32_bf16 v[188:191], v[212:215], a[196:199], v[188:191]\n\t"
    "v_mfma_f32_16x16x32_bf16 v[188:191], v[216:219], a[200:203], v[188:191]\n\t"
    "buffer_load_dwordx4 a[92:95], v255, s[68:71], s77 offen offset:3072 sc1\n\t"
    "v_mfma_f32_16x16x32_bf16 v[188:191], v[220:223], a[204:207], v[188:191]\n\t"
    "s_waitcnt lgkmcnt(0)\n\t"
    "v_mfma_f32_16x16x32_bf16 v[0:3], v[192:195], a[0:3], 0\n\t"
    "ds_read_b64_tr_b16 v[208:209], v236 offset:8\n\t"
    "v_mfma_f32_16x16x32_bf16 v[0:3], v[196:199], a[4:7], v[0:3]\n\t"
    "ds_read_b64_tr_b16 v[210:211], v236 offset:4104\n\t"
    "v_mfma_f32_16x16x32_bf16 v[0:3], v[200:203], a[8:11], v[0:3]\n\t"
    "ds_read_b64_tr_b16 v[212:213], v236 offset:8200\n\t"
    "v_mfma_f32_16x16x32_bf16 v[0:3], v[204:207], a[12:15], v[0:3]\n\t"
    "ds_read_b64_tr_b16 v[214:215], v236 offset:12296\n\t"
    "v_mfma_f32_16x16x32_bf16 v[8:11], v[192:195], a[64:67], 0\n\t"
    "ds_read_b64_tr_b16 v[216:217], v236 offset:16392\n\t"
    "v_mfma_f32_16x16x32_bf16 v[8:11], v[196:199], a[68:71], v[8:11]\n\t"
    "ds_read_b64_tr_b16 v[218:219], v236 offset:20488\n\t"
    "v_mfma_f32_16x16x32_bf16 v[8:11], v[200:203], a[72:75], v[8:11]\n\t"
    "ds_read_b64_tr_b16 v[220:221], v236 offset:24584\n\t"
    "v_mfma_f32_16x16x32_bf16 v[8:11], v[204:207], a[76:79], v[8:11]\n\t"
    "ds_read_b64_tr_b16 v[222:223], v236 offset:28680\n\t"
    "v_mfma_f32_16x16x32_bf16 v[16:19], v[192:195], a[128:131], 0\n\t"
    "s_add_u32 s77, s82, 4096\n\t"
    "buffer_load_dwordx4 a[144:147], v255, s[68:71], s77 offen offset:0 sc1\n\t"
    "v_mfma_f32_16x16x32_bf16 v[16:19], v[196:199], a[132:135], v[16:19]\n\t"
    "v_mfma_f32_16x16x32_bf16 v[16:19], v[200:203], a[136:139], v[16:19]\n\t"
    "v_mfma_f32_16x16x32_bf16 v[16:19], v[204:207], a[140:143], v[16:19]\n\t"
    "buffer_load_dwordx4 a[148:151], v255, s[68:71], s77 offen offset:1024 sc1\n\t"
    "v_mfma_f32_16x16x32_bf16 v[24:27], v[192:195], a[192:195], 0\n\t"
    "v_mfma_f32_16x16x32_bf16 v[24:27], v[196:199], a[196:199], v[24:27]\n\t"
    "v_mfma_f32_16x16x32_bf16 v[24:27], v[200:203], a[200:203], v[24:27]\n\t"
    "buffer_load_dwordx4 a[152:155], v255, s[68:71], s77 offen offset:2048 sc1\n\t"
    "v_mfma_f32_16x16x32_bf16 v[24:27], v[204:207], a[204:207], v[24:27]\n\t"
    "s_waitcnt lgkmcnt(0)\n\t"
    "v_mfma_f32_16x16x32_bf16 v[4:7], v[208:211], a[0:3], 0\n\t"
    "ds_read_b64_tr_b16 v[192:193], v237 offset:0\n\t"
    "v_mfma_f32_16x16x32_bf16 v[4:7], v[212:215], a[4:7], v[4:7]\n\t"
    "ds_read_b64_tr_b16 v[194:195], v237 offset:4096\n\t"
    "v_mfma_f32_16x16x32_bf16 v[4:7], v[216:219], a[8:11], v[4:7]\n\t"
    "ds_read_b64_tr_b16 v[196:197], v237 offset:8192\n\t"
    "v_mfma_f32_16x16x32_bf16 v[4:7], v[220:223], a[12:15], v[4:7]\n\t"
    "ds_read_b64_tr_b16 v[198:199], v237 offset:12288\n\t"
    "v_mfma_f32_16x16x32_bf16 v[12:15], v[208:211], a[64:67], 0\n\t"
    "ds_read_b64_tr_b16 v[200:201], v237 offset:16384\n\t"
    "v_mfma_f32_16x16x32_bf16 v[12:15], v[212:215], a[68:71], v[12:15]\n\t"
    "ds_read_b64_tr_b16 v[202:203], v237 offset:20480\n\t"
    "v_mfma_f32_16x16x32_bf16 v[12:15], v[216:219], a[72:75], v[12:15]\n\t"
    "ds_read_b64_tr_b16 v[204:205], v237 offset:24576\n\t"
    "v_mfma_f32_16x16x32_bf16 v[12:15], v[220:223], a[76:79], v[12:15]\n\t"
    "ds_read_b64_tr_b16 v[206:207], v237 offset:28672\n\t"
    "v_mfma_f32_16x16x32_bf16 v[20:23], v[208:211], a[128:131], 0\n\t"
    "buffer_load_dwordx4 a[156:159], v255, s[68:71], s77 offen offset:3072 sc1\n\t"
    "v_mfma_f32_16x16x32_bf16 v[20:23], v[212:215], a[132:135], v[20:23]\n\t"
    "v_mfma_f32_16x16x32_bf16 v[20:23], v[216:219], a[136:139], v[20:23]\n\t"
    "v_mfma_f32_16x16x32_bf16 v[20:23], v[220:223], a[140:143], v[20:23]\n\t"
    "s_add_u32 s77, s83, 4096\n\t"
    "buffer_load_dwordx4 a[208:211], v255, s[68:71], s77 offen offset:0 sc1\n\t"
    "v_mfma_f32_16x16x32_bf16 v[28:31], v[208:211], a[192:195], 0\n\t"
    "v_mfma_f32_16x16x32_bf16 v[28:31], v[212:215], a[196:199], v[28:31]\n\t"
    "v_mfma_f32_16x16x32_bf16 v[28:31], v[216:219], a[200:203], v[28:31]\n\t"
    "buffer_load_dwordx4 a[212:215], v255, s[68:71], s77 offen offset:1024 sc1\n\t"
    "v_mfma_f32_16x16x32_bf16 v[28:31], v[220:223], a[204:207], v[28:31]\n\t"
    "s_waitcnt lgkmcnt(0)\n\t"
    "v_mfma_f32_16x16x32_bf16 v[32:35], v[192:195], a[0:3], 0\n\t"
    "ds_read_b64_tr_b16 v[208:209], v237 offset:8\n\t"
    "v_mfma_f32_16x16x32_bf16 v[32:35], v[196:199], a[4:7], v[32:35]\n\t"
    "ds_read_b64_tr_b16 v[210:211], v237 offset:4104\n\t"
    "v_mfma_f32_16x16x32_bf16 v[32:35], v[200:203], a[8:11], v[32:35]\n\t"
    "ds_read_b64_tr_b16 v[212:213], v237 offset:8200\n\t"
    "v_mfma_f32_16x16x32_bf16 v[32:35], v[204:207], a[12:15], v[32:35]\n\t"
    "ds_read_b64_tr_b16 v[214:215], v237 offset:12296\n\t"
    "v_mfma_f32_16x16x32_bf16 v[40:43], v[192:195], a[64:67], 0\n\t"
    "ds_read_b64_tr_b16 v[216:217], v237 offset:16392\n\t"
    "v_mfma_f32_16x16x32_bf16 v[40:43], v[196:199], a[68:71], v[40:43]\n\t"
    "ds_read_b64_tr_b16 v[218:219], v237 offset:20488\n\t"
    "v_mfma_f32_16x16x32_bf16 v[40:43], v[200:203], a[72:75], v[40:43]\n\t"
    "ds_read_b64_tr_b16 v[220:221], v237 offset:24584\n\t"
    "v_mfma_f32_16x16x32_bf16 v[40:43], v[204:207], a[76:79], v[40:43]\n\t"
    "ds_read_b64_tr_b16 v[222:223], v237 offset:28680\n\t"
    "v_mfma_f32_16x16x32_bf16 v[48:51], v[192:195], a[128:131], 0\n\t"
    "buffer_load_dwordx4 a[216:219], v255, s[68:71], s77 offen offset:2048 sc1\n\t"
    "v_mfma_f32_16x16x32_bf16 v[48:51], v[196:199], a[132:135], v[48:51]\n\t"
    "v_mfma_f32_16x16x32_bf16 v[48:51], v[200:203], a[136:139], v[48:51]\n\t"
    "v_mfma_f32_16x16x32_bf16 v[48:51], v[204:207], a[140:143], v[48:51]\n\t"
    "buffer_load_dwordx4 a[220:223], v255, s[68:71], s77 offen offset:3072 sc1\n\t"
    "v_mfma_f32_16x16x32_bf16 v[56:59], v[192:195], a[192:195], 0\n\t"
    "v_mfma_f32_16x16x32_bf16 v[56:59], v[196:199], a[196:199], v[56:59]\n\t"
    "v_mfma_f32_16x16x32_bf16 v[56:59], v[200:203], a[200:203], v[56:59]\n\t"
    "s_add_u32 m0, s95, 65536\n\t"
    "s_add_u32 s76, s87, 1048576\n\t"
    "buffer_load_dwordx4 v231, s[64:67], s76 offen lds\n\t"
    "v_mfma_f32_16x16x32_bf16 v[56:59], v[204:207], a[204:207], v[56:59]\n\t"
    "s_waitcnt lgkmcnt(0)\n\t"
    "v_mfma_f32_16x16x32_bf16 v[36:39], v[208:211], a[0:3], 0\n\t"
    "ds_read_b64_tr_b16 v[192:193], v238 offset:0\n\t"
    "v_mfma_f32_16x16x32_bf16 v[36:39], v[212:215], a[4:7], v[36:39]\n\t"
    "ds_read_b64_tr_b16 v[194:195], v238 offset:4096\n\t"
    "v_mfma_f32_16x16x32_bf16 v[36:39], v[216:219], a[8:11], v[36:39]\n\t"
    "ds_read_b64_tr_b16 v[196:197], v238 offset:8192\n\t"
    "v_mfma_f32_16x16x32_bf16 v[36:39], v[220:223], a[12:15], v[36:39]\n\t"
    "ds_read_b64_tr_b16 v[198:199], v238 offset:12288\n\t"
    "v_mfma_f32_16x16x32_bf16 v[44:47], v[208:211], a[64:67], 0\n\t"
    "ds_read_b64_tr_b16 v[200:201], v238 offset:16384\n\t"
    "v_mfma_f32_16x16x32_bf16 v[44:47], v[212:215], a[68:71], v[44:47]\n\t"
    "ds_read_b64_tr_b16 v[202:203], v238 offset:20480\n\t"
    "v_mfma_f32_16x16x32_bf16 v[44:47], v[216:219], a[72:75], v[44:47]\n\t"
    "ds_read_b64_tr_b16 v[204:205], v238 offset:24576\n\t"
    "v_mfma_f32_16x16x32_bf16 v[44:47], v[220:223], a[76:79], v[44:47]\n\t"
    "ds_read_b64_tr_b16 v[206:207], v238 offset:28672\n\t"
    "v_mfma_f32_16x16x32_bf16 v[52:55], v[208:211], a[128:131], 0\n\t"
    "s_add_u32 m0, s95, 66560\n\t"
    "s_add_u32 s76, s87, 1081344\n\t"
    "buffer_load_dwordx4 v231, s[64:67], s76 offen lds\n\t"
    "v_mfma_f32_16x16x32_bf16 v[52:55], v[212:215], a[132:135], v[52:55]\n\t"
    "v_mfma_f32_16x16x32_bf16 v[52:55], v[216:219], a[136:139], v[52:55]\n\t"
    "v_mfma_f32_16x16x32_bf16 v[52:55], v[220:223], a[140:143], v[52:55]\n\t"
    "s_add_u32 m0, s95, 67584\n\t"
    "s_add_u32 s76, s87, 1114112\n\t"
    "buffer_load_dwordx4 v231, s[64:67], s76 offen lds\n\t"
    "v_mfma_f32_16x16x32_bf16 v[60:63], v[208:211], a[192:195], 0\n\t"
    "v_mfma_f32_16x16x32_bf16 v[60:63], v[212:215], a[196:199], v[60:63]\n\t"
    "v_mfma_f32_16x16x32_bf16 v[60:63], v[216:219], a[200:203], v[60:63]\n\t"
    "s_add_u32 m0, s95, 68608\n\t"
    "s_add_u32 s76, s87, 1146880\n\t"
    "buffer_load_dwordx4 v231, s[64:67], s76 offen lds\n\t"
    "v_mfma_f32_16x16x32_bf16 v[60:63], v[220:223], a[204:207], v[60:63]\n\t"
    "s_waitcnt lgkmcnt(0)\n\t"
    "v_mfma_f32_16x16x32_bf16 v[64:67], v[192:195], a[0:3], 0\n\t"
    "ds_read_b64_tr_b16 v[208:209], v238 offset:8\n\t"
    "v_mfma_f32_16x16x32_bf16 v[64:67], v[196:199], a[4:7], v[64:67]\n\t"
    "ds_read_b64_tr_b16 v[210:211], v238 offset:4104\n\t"
    "v_mfma_f32_16x16x32_bf16 v[64:67], v[200:203], a[8:11], v[64:67]\n\t"
    "ds_read_b64_tr_b16 v[212:213], v238 offset:8200\n\t"
    "v_mfma_f32_16x16x32_bf16 v[64:67], v[204:207], a[12:15], v[64:67]\n\t"
    "ds_read_b64_tr_b16 v[214:215], v238 offset:12296\n\t"
    "v_mfma_f32_16x16x32_bf16 v[72:75], v[192:195], a[64:67], 0\n\t"
    "ds_read_b64_tr_b16 v[216:217], v238 offset:16392\n\t"
    "v_mfma_f32_16x16x32_bf16 v[72:75], v[196:199], a[68:71], v[72:75]\n\t"
    "ds_read_b64_tr_b16 v[218:219], v238 offset:20488\n\t"
    "v_mfma_f32_16x16x32_bf16 v[72:75], v[200:203], a[72:75], v[72:75]\n\t"
    "ds_read_b64_tr_b16 v[220:221], v238 offset:24584\n\t"
    "v_mfma_f32_16x16x32_bf16 v[72:75], v[204:207], a[76:79], v[72:75]\n\t"
    "ds_read_b64_tr_b16 v[222:223], v238 offset:28680\n\t"
    "v_mfma_f32_16x16x32_bf16 v[80:83], v[192:195], a[128:131], 0\n\t"
    "s_add_u32 m0, s95, 69632\n\t"
    "s_add_u32 s76, s87, 1179648\n\t"
    "buffer_load_dwordx4 v231, s[64:67], s76 offen lds\n\t"
    "v_mfma_f32_16x16x32_bf16 v[80:83], v[196:199], a[132:135], v[80:83]\n\t"
    "v_mfma_f32_16x16x32_bf16 v[80:83], v[200:203], a[136:139], v[80:83]\n\t"
    "v_mfma_f32_16x16x32_bf16 v[80:83], v[204:207], a[140:143], v[80:83]\n\t"
    "s_add_u32 m0, s95, 70656\n\t"
    "s_add_u32 s76, s87, 1212416\n\t"
    "buffer_load_dwordx4 v231, s[64:67], s76 offen lds\n\t"
    "v_mfma_f32_16x16x32_bf16 v[88:91], v[192:195], a[192:195], 0\n\t"
    "v_mfma_f32_16x16x32_bf16 v[88:91], v[196:199], a[196:199], v[88:91]\n\t"
    "v_mfma_f32_16x16x32_bf16 v[88:91], v[200:203], a[200:203], v[88:91]\n\t"
    "s_add_u32 m0, s95, 71680\n\t"
    "s_add_u32 s76, s87, 1245184\n\t"
    "buffer_load_dwordx4 v231, s[64:67], s76 offen lds\n\t"
    "v_mfma_f32_16x16x32_bf16 v[88:91], v[204:207], a[204:207], v[88:91]\n\t"
    "s_waitcnt lgkmcnt(0)\n\t"
    "v_mfma_f32_16x16x32_bf16 v[68:71], v[208:211], a[0:3], 0\n\t"
    "ds_read_b64_tr_b16 v[192:193], v239 offset:0\n\t"
    "v_mfma_f32_16x16x32_bf16 v[68:71], v[212:215], a[4:7], v[68:71]\n\t"
    "ds_read_b64_tr_b16 v[194:195], v239 offset:4096\n\t"
    "v_mfma_f32_16x16x32_bf16 v[68:71], v[216:219], a[8:11], v[68:71]\n\t"
    "ds_read_b64_tr_b16 v[196:197], v239 offset:8192\n\t"
    "v_mfma_f32_16x16x32_bf16 v[68:71], v[220:223], a[12:15], v[68:71]\n\t"
    "ds_read_b64_tr_b16 v[198:199], v239 offset:12288\n\t"
    "v_mfma_f32_16x16x32_bf16 v[76:79], v[208:211], a[64:67], 0\n\t"
    "ds_read_b64_tr_b16 v[200:201], v239 offset:16384\n\t"
    "v_mfma_f32_16x16x32_bf16 v[76:79], v[212:215], a[68:71], v[76:79]\n\t"
    "ds_read_b64_tr_b16 v[202:203], v239 offset:20480\n\t"
    "v_mfma_f32_16x16x32_bf16 v[76:79], v[216:219], a[72:75], v[76:79]\n\t"
    "ds_read_b64_tr_b16 v[204:205], v239 offset:24576\n\t"
    "v_mfma_f32_16x16x32_bf16 v[76:79], v[220:223], a[76:79], v[76:79]\n\t"
    "ds_read_b64_tr_b16 v[206:207], v239 offset:28672\n\t"
    "v_mfma_f32_16x16x32_bf16 v[84:87], v[208:211], a[128:131], 0\n\t"
    "s_add_u32 m0, s95, 72704\n\t"
    "s_add_u32 s76, s87, 1277952\n\t"
    "buffer_load_dwordx4 v231, s[64:67], s76 offen lds\n\t"
    "v_mfma_f32_16x16x32_bf16 v[84:87], v[212:215], a[132:135], v[84:87]\n\t"
    "v_mfma_f32_16x16x32_bf16 v[84:87], v[216:219], a[136:139], v[84:87]\n\t"
    "v_mfma_f32_16x16x32_bf16 v[84:87], v[220:223], a[140:143], v[84:87]\n\t"
    "s_add_u32 m0, s94, 131072\n\t"
    "s_add_u32 s76, s86, 917504\n\t"
    "buffer_load_dwordx4 v230, s[60:63], s76 offen lds\n\t"
    "v_mfma_f32_16x16x32_bf16 v[92:95], v[208:211], a[192:195], 0\n\t"
    "v_mfma_f32_16x16x32_bf16 v[92:95], v[212:215], a[196:199], v[92:95]\n\t"
    "v_mfma_f32_16x16x32_bf16 v[92:95], v[216:219], a[200:203], v[92:95]\n\t"
    "s_add_u32 m0, s94, 132096\n\t"
    "s_add_u32 s76, s86, 946176\n\t"
    "buffer_load_dwordx4 v230, s[60:63], s76 offen lds\n\t"
    "v_mfma_f32_16x16x32_bf16 v[92:95], v[220:223], a[204:207], v[92:95]\n\t"
    "s_waitcnt lgkmcnt(0)\n\t"
    "v_mfma_f32_16x16x32_bf16 v[96:99], v[192:195], a[0:3], 0\n\t"
    "ds_read_b64_tr_b16 v[208:209], v239 offset:8\n\t"
    "v_mfma_f32_16x16x32_bf16 v[96:99], v[196:199], a[4:7], v[96:99]\n\t"
    "ds_read_b64_tr_b16 v[210:211], v239 offset:4104\n\t"
    "v_mfma_f32_16x16x32_bf16 v[96:99], v[200:203], a[8:11], v[96:99]\n\t"
    "ds_read_b64_tr_b16 v[212:213], v239 offset:8200\n\t"
    "v_mfma_f32_16x16x32_bf16 v[96:99], v[204:207], a[12:15], v[96:99]\n\t"
    "ds_read_b64_tr_b16 v[214:215], v239 offset:12296\n\t"
    "v_mfma_f32_16x16x32_bf16 v[104:107], v[192:195], a[64:67], 0\n\t"
    "ds_read_b64_tr_b16 v[216:217], v239 offset:16392\n\t"
    "v_mfma_f32_16x16x32_bf16 v[104:107], v[196:199], a[68:71], v[104:107]\n\t"
    "ds_read_b64_tr_b16 v[218:219], v239 offset:20488\n\t"
    "v_mfma_f32_16x16x32_bf16 v[104:107], v[200:203], a[72:75], v[104:107]\n\t"
    "ds_read_b64_tr_b16 v[220:221], v239 offset:24584\n\t"
    "v_mfma_f32_16x16x32_bf16 v[104:107], v[204:207], a[76:79], v[104:107]\n\t"
    "ds_read_b64_tr_b16 v[222:223], v239 offset:28680\n\t"
    "v_mfma_f32_16x16x32_bf16 v[112:115], v[192:195], a[128:131], 0\n\t"
    "s_add_u32 m0, s94, 133120\n\t"
    "s_add_u32 s76, s86, 974848\n\t"
    "buffer_load_dwordx4 v230, s[60:63], s76 offen lds\n\t"
    "v_mfma_f32_16x16x32_bf16 v[112:115], v[196:199], a[132:135], v[112:115]\n\t"
    "v_mfma_f32_16x16x32_bf16 v[112:115], v[200:203], a[136:139], v[112:115]\n\t"
    "v_mfma_f32_16x16x32_bf16 v[112:115], v[204:207], a[140:143], v[112:115]\n\t"
    "s_add_u32 m0, s94, 134144\n\t"
    "s_add_u32 s76, s86, 1003520\n\t"
    "buffer_load_dwordx4 v230, s[60:63], s76 offen lds\n\t"
    "v_mfma_f32_16x16x32_bf16 v[120:123], v[192:195], a[192:195], 0\n\t"
    "v_mfma_f32_16x16x32_bf16 v[120:123], v[196:199], a[196:199], v[120:123]\n\t"
    "v_mfma_f32_16x16x32_bf16 v[120:123], v[200:203], a[200:203], v[120:123]\n\t"
    "s_add_u32 s77, s80, 8192\n\t"
    "buffer_load_dwordx4 a[32:35], v255, s[68:71], s77 offen offset:0 sc1\n\t"
    "v_mfma_f32_16x16x32_bf16 v[120:123], v[204:207], a[204:207], v[120:123]\n\t"
    "s_waitcnt vmcnt(13) lgkmcnt(0)\n\t"
    "v_mfma_f32_16x16x32_bf16 v[100:103], v[208:211], a[0:3], 0\n\t"
    "s_barrier\n\t"
    "ds_read_b64_tr_b16 v[192:193], v232 offset:16384\n\t"
    "v_mfma_f32_16x16x32_bf16 v[100:103], v[212:215], a[4:7], v[100:103]\n\t"
    "ds_read_b64_tr_b16 v[194:195], v232 offset:18432\n\t"
    "v_mfma_f32_16x16x32_bf16 v[100:103], v[216:219], a[8:11], v[100:103]\n\t"
    "ds_read_b64_tr_b16 v[196:197], v232 offset:20480\n\t"
    "v_mfma_f32_16x16x32_bf16 v[100:103], v[220:223], a[12:15], v[100:103]\n\t"
    "ds_read_b64_tr_b16 v[198:199], v232 offset:22528\n\t"
    "v_mfma_f32_16x16x32_bf16 v[108:111], v[208:211], a[64:67], 0\n\t"
    "ds_read_b64_tr_b16 v[200:201], v232 offset:24576\n\t"
    "v_mfma_f32_16x16x32_bf16 v[108:111], v[212:215], a[68:71], v[108:111]\n\t"
    "ds_read_b64_tr_b16 v[202:203], v232 offset:26624\n\t"
    "v_mfma_f32_16x16x32_bf16 v[108:111], v[216:219], a[72:75], v[108:111]\n\t"
    "ds_read_b64_tr_b16 v[204:205], v232 offset:28672\n\t"
    "v_mfma_f32_16x16x32_bf16 v[108:111], v[220:223], a[76:79], v[108:111]\n\t"
    "ds_read_b64_tr_b16 v[206:207], v232 offset:30720\n\t"
    "v_mfma_f32_16x16x32_bf16 v[116:119], v[208:211], a[128:131], 0\n\t"
    "buffer_load_dwordx4 a[36:39], v255, s[68:71], s77 offen offset:1024 sc1\n\t"
    "v_mfma_f32_16x16x32_bf16 v[116:119], v[212:215], a[132:135], v[116:119]\n\t"
    "v_mfma_f32_16x16x32_bf16 v[116:119], v[216:219], a[136:139], v[116:119]\n\t"
    "v_mfma_f32_16x16x32_bf16 v[116:119], v[220:223], a[140:143], v[116:119]\n\t"
    "buffer_load_dwordx4 a[40:43], v255, s[68:71], s77 offen offset:2048 sc1\n\t"
    "v_mfma_f32_16x16x32_bf16 v[124:127], v[208:211], a[192:195], 0\n\t"
    "v_mfma_f32_16x16x32_bf16 v[124:127], v[212:215], a[196:199], v[124:127]\n\t"
    "v_mfma_f32_16x16x32_bf16 v[124:127], v[216:219], a[200:203], v[124:127]\n\t"
    "buffer_load_dwordx4 a[44:47], v255, s[68:71], s77 offen offset:3072 sc1\n\t"
    "v_mfma_f32_16x16x32_bf16 v[124:127], v[220:223], a[204:207], v[124:127]\n\t"
    "s_waitcnt lgkmcnt(0)\n\t"
    "v_mfma_f32_16x16x32_bf16 v[128:131], v[192:195], a[16:19], v[128:131]\n\t"
    "ds_read_b64_tr_b16 v[208:209], v233 offset:16384\n\t"
    "v_mfma_f32_16x16x32_bf16 v[128:131], v[196:199], a[20:23], v[128:131]\n\t"
    "ds_read_b64_tr_b16 v[210:211], v233 offset:18432\n\t"
    "v_mfma_f32_16x16x32_bf16 v[128:131], v[200:203], a[24:27], v[128:131]\n\t"
    "ds_read_b64_tr_b16 v[212:213], v233 offset:20480\n\t"
    "v_mfma_f32_16x16x32_bf16 v[128:131], v[204:207], a[28:31], v[128:131]\n\t"
    "ds_read_b64_tr_b16 v[214:215], v233 offset:22528\n\t"
    "v_mfma_f32_16x16x32_bf16 v[136:139], v[192:195], a[80:83], v[136:139]\n\t"
    "ds_read_b64_tr_b16 v[216:217], v233 offset:24576\n\t"
    "v_mfma_f32_16x16x32_bf16 v[136:139], v[196:199], a[84:87], v[136:139]\n\t"
    "ds_read_b64_tr_b16 v[218:219], v233 offset:26624\n\t"
    "v_mfma_f32_16x16x32_bf16 v[136:139], v[200:203], a[88:91], v[136:139]\n\t"
    "ds_read_b64_tr_b16 v[220:221], v233 offset:28672\n\t"
    "v_mfma_f32_16x16x32_bf16 v[136:139], v[204:207], a[92:95], v[136:139]\n\t"
    "ds_read_b64_tr_b16 v[222:223], v233 offset:30720\n\t"
    "v_mfma_f32_16x16x32_bf16 v[144:147], v[192:195], a[144:147], v[144:147]\n\t"
    "s_add_u32 s77, s81, 8192\n\t"
    "buffer_load_dwordx4 a[96:99], v255, s[68:71], s77 offen offset:0 sc1\n\t"
    "v_mfma_f32_16x16x32_bf16 v[144:147], v[196:199], a[148:151], v[144:147]\n\t"
    "v_mfma_f32_16x16x32_bf16 v[144:147], v[200:203], a[152:155], v[144:147]\n\t"
    "v_mfma_f32_16x16x32_bf16 v[144:147], v[204:207], a[156:159], v[144:147]\n\t"
    "buffer_load_dwordx4 a[100:103], v255, s[68:71], s77 offen offset:1024 sc1\n\t"
    "v_mfma_f32_16x16x32_bf16 v[152:155], v[192:195], a[208:211], v[152:155]\n\t"
    "v_mfma_f32_16x16x32_bf16 v[152:155], v[196:199], a[212:215], v[152:155]\n\t"
    "v_mfma_f32_16x16x32_bf16 v[152:155], v[200:203], a[216:219], v[152:155]\n\t"
    "buffer_load_dwordx4 a[104:107], v255, s[68:71], s77 offen offset:2048 sc1\n\t"
    "v_mfma_f32_16x16x32_bf16 v[152:155], v[204:207], a[220:223], v[152:155]\n\t"
    "s_waitcnt lgkmcnt(0)\n\t"
    "v_mfma_f32_16x16x32_bf16 v[132:135], v[208:211], a[16:19], v[132:135]\n\t"
    "ds_read_b64_tr_b16 v[192:193], v234 offset:16384\n\t"
    "v_mfma_f32_16x16x32_bf16 v[132:135], v[212:215], a[20:23], v[132:135]\n\t"
    "ds_read_b64_tr_b16 v[194:195], v234 offset:18432\n\t"
    "v_mfma_f32_16x16x32_bf16 v[132:135], v[216:219], a[24:27], v[132:135]\n\t"
    "ds_read_b64_tr_b16 v[196:197], v234 offset:20480\n\t"
    "v_mfma_f32_16x16x32_bf16 v[132:135], v[220:223], a[28:31], v[132:135]\n\t"
    "ds_read_b64_tr_b16 v[198:199], v234 offset:22528\n\t"
    "v_mfma_f32_16x16x32_bf16 v[140:143], v[208:211], a[80:83], v[140:143]\n\t"
    "ds_read_b64_tr_b16 v[200:201], v234 offset:24576\n\t"
    "v_mfma_f32_16x16x32_bf16 v[140:143], v[212:215], a[84:87], v[140:143]\n\t"
    "ds_read_b64_tr_b16 v[202:203], v234 offset:26624\n\t"
    "v_mfma_f32_16x16x32_bf16 v[140:143], v[216:219], a[88:91], v[140:143]\n\t"
    "ds_read_b64_tr_b16 v[204:205], v234 offset:28672\n\t"
    "v_mfma_f32_16x16x32_bf16 v[140:143], v[220:223], a[92:95], v[140:143]\n\t"
    "ds_read_b64_tr_b16 v[206:207], v234 offset:30720\n\t"
    "v_mfma_f32_16x16x32_bf16 v[148:151], v[208:211], a[144:147], v[148:151]\n\t"
    "buffer_load_dwordx4 a[108:111], v255, s[68:71], s77 offen offset:3072 sc1\n\t"
    "v_mfma_f32_16x16x32_bf16 v[148:151], v[212:215], a[148:151], v[148:151]\n\t"
    "v_mfma_f32_16x16x32_bf16 v[148:151], v[216:219], a[152:155], v[148:151]\n\t"
    "v_mfma_f32_16x16x32_bf16 v[148:151], v[220:223], a[156:159], v[148:151]\n\t"
    "s_add_u32 s77, s82, 8192\n\t"
    "buffer_load_dwordx4 a[160:163], v255, s[68:71], s77 offen offset:0 sc1\n\t"
    "v_mfma_f32_16x16x32_bf16 v[156:159], v[208:211], a[208:211], v[156:159]\n\t"
    "v_mfma_f32_16x16x32_bf16 v[156:159], v[212:215], a[212:215], v[156:159]\n\t"
    "v_mfma_f32_16x16x32_bf16 v[156:159], v[216:219], a[216:219], v[156:159]\n\t"
    "buffer_load_dwordx4 a[164:167], v255, s[68:71], s77 offen offset:1024 sc1\n\t"
    "v_mfma_f32_16x16x32_bf16 v[156:159], v[220:223], a[220:223], v[156:159]\n\t"
    "s_waitcnt lgkmcnt(0)\n\t"
    "v_mfma_f32_16x16x32_bf16 v[160:163], v[192:195], a[16:19], v[160:163]\n\t"
    "ds_read_b64_tr_b16 v[208:209], v235 offset:16384\n\t"
    "v_mfma_f32_16x16x32_bf16 v[160:163], v[196:199], a[20:23], v[160:163]\n\t"
    "ds_read_b64_tr_b16 v[210:211], v235 offset:18432\n\t"
    "v_mfma_f32_16x16x32_bf16 v[160:163], v[200:203], a[24:27], v[160:163]\n\t"
    "ds_read_b64_tr_b16 v[212:213], v235 offset:20480\n\t"
    "v_mfma_f32_16x16x32_bf16 v[160:163], v[204:207], a[28:31], v[160:163]\n\t"
    "ds_read_b64_tr_b16 v[214:215], v235 offset:22528\n\t"
    "v_mfma_f32_16x16x32_bf16 v[168:171], v[192:195], a[80:83], v[168:171]\n\t"
    "ds_read_b64_tr_b16 v[216:217], v235 offset:24576\n\t"
    "v_mfma_f32_16x16x32_bf16 v[168:171], v[196:199], a[84:87], v[168:171]\n\t"
    "ds_read_b64_tr_b16 v[218:219], v235 offset:26624\n\t"
    "v_mfma_f32_16x16x32_bf16 v[168:171], v[200:203], a[88:91], v[168:171]\n\t"
    "ds_read_b64_tr_b16 v[220:221], v235 offset:28672\n\t"
    "v_mfma_f32_16x16x32_bf16 v[168:171], v[204:207], a[92:95], v[168:171]\n\t"
    "ds_read_b64_tr_b16 v[222:223], v235 offset:30720\n\t"
    "v_mfma_f32_16x16x32_bf16 v[176:179], v[192:195], a[144:147], v[176:179]\n\t"
    "buffer_load_dwordx4 a[168:171], v255, s[68:71], s77 offen offset:2048 sc1\n\t"
    "v_mfma_f32_16x16x32_bf16 v[176:179], v[196:199], a[148:151], v[176:179]\n\t"
    "v_mfma_f32_16x16x32_bf16 v[176:179], v[200:203], a[152:155], v[176:179]\n\t"
    "v_mfma_f32_16x16x32_bf16 v[176:179], v[204:207], a[156:159], v[176:179]\n\t"
    "buffer_load_dwordx4 a[172:175], v255, s[68:71], s77 offen offset:3072 sc1\n\t"
    "v_mfma_f32_16x16x32_bf16 v[184:187], v[192:195], a[208:211], v[184:187]\n\t"
    "v_mfma_f32_16x16x32_bf16 v[184:187], v[196:199], a[212:215], v[184:187]\n\t"
    "v_mfma_f32_16x16x32_bf16 v[184:187], v[200:203], a[216:219], v[184:187]\n\t"
    "s_add_u32 s77, s83, 8192\n\t"
    "buffer_load_dwordx4 a[224:227], v255, s[68:71], s77 offen offset:0 sc1\n\t"
    "v_mfma_f32_16x16x32_bf16 v[184:187], v[204:207], a[220:223], v[184:187]\n\t"
    "s_waitcnt vmcnt(17) lgkmcnt(0)\n\t"
    "v_mfma_f32_16x16x32_bf16 v[164:167], v[208:211], a[16:19], v[164:167]\n\t"
    "s_barrier\n\t"
    "ds_read_b64_tr_b16 v[192:193], v240 offset:0\n\t"
    "v_mfma_f32_16x16x32_bf16 v[164:167], v[212:215], a[20:23], v[164:167]\n\t"
    "ds_read_b64_tr_b16 v[194:195], v240 offset:4096\n\t"
    "v_mfma_f32_16x16x32_bf16 v[164:167], v[216:219], a[24:27], v[164:167]\n\t"
    "ds_read_b64_tr_b16 v[196:197], v240 offset:8192\n\t"
    "v_mfma_f32_16x16x32_bf16 v[164:167], v[220:223], a[28:31], v[164:167]\n\t"
    "ds_read_b64_tr_b16 v[198:199], v240 offset:12288\n\t"
    "v_mfma_f32_16x16x32_bf16 v[172:175], v[208:211], a[80:83], v[172:175]\n\t"
    "ds_read_b64_tr_b16 v[200:201], v240 offset:16384\n\t"
    "v_mfma_f32_16x16x32_bf16 v[172:175], v[212:215], a[84:87], v[172:175]\n\t"
    "ds_read_b64_tr_b16 v[202:203], v240 offset:20480\n\t"
    "v_mfma_f32_16x16x32_bf16 v[172:175], v[216:219], a[88:91], v[172:175]\n\t"
    "ds_read_b64_tr_b16 v[204:205], v240 offset:24576\n\t"
    "v_mfma_f32_16x16x32_bf16 v[172:175], v[220:223], a[92:95], v[172:175]\n\t"
    "ds_read_b64_tr_b16 v[206:207], v240 offset:28672\n\t"
    "v_mfma_f32_16x16x32_bf16 v[180:183], v[208:211], a[144:147], v[180:183]\n\t"
    "buffer_load_dwordx4 a[228:231], v255, s[68:71], s77 offen offset:1024 sc1\n\t"
    "v_mfma_f32_16x16x32_bf16 v[180:183], v[212:215], a[148:151], v[180:183]\n\t"
    "v_mfma_f32_16x16x32_bf16 v[180:183], v[216:219], a[152:155], v[180:183]\n\t"
    "v_mfma_f32_16x16x32_bf16 v[180:183], v[220:223], a[156:159], v[180:183]\n\t"
    "buffer_load_dwordx4 a[232:235], v255, s[68:71], s77 offen offset:2048 sc1\n\t"
    "v_mfma_f32_16x16x32_bf16 v[188:191], v[208:211], a[208:211], v[188:191]\n\t"
    "v_mfma_f32_16x16x32_bf16 v[188:191], v[212:215], a[212:215], v[188:191]\n\t"
    "v_mfma_f32_16x16x32_bf16 v[188:191], v[216:219], a[216:219], v[188:191]\n\t"
    "buffer_load_dwordx4 a[236:239], v255, s[68:71], s77 offen offset:3072 sc1\n\t"
    "v_mfma_f32_16x16x32_bf16 v[188:191], v[220:223], a[220:223], v[188:191]\n\t"
    "s_waitcnt lgkmcnt(0)\n\t"
    "v_mfma_f32_16x16x32_bf16 v[0:3], v[192:195], a[16:19], v[0:3]\n\t"
    "ds_read_b64_tr_b16 v[208:209], v240 offset:8\n\t"
    "v_mfma_f32_16x16x32_bf16 v[0:3], v[196:199], a[20:23], v[0:3]\n\t"
    "ds_read_b64_tr_b16 v[210:211], v240 offset:4104\n\t"
    "v_mfma_f32_16x16x32_bf16 v[0:3], v[200:203], a[24:27], v[0:3]\n\t"
    "ds_read_b64_tr_b16 v[212:213], v240 offset:8200\n\t"
    "v_mfma_f32_16x16x32_bf16 v[0:3], v[204:207], a[28:31], v[0:3]\n\t"
    "ds_read_b64_tr_b16 v[214:215], v240 offset:12296\n\t"
    "v_mfma_f32_16x16x32_bf16 v[8:11], v[192:195], a[80:83], v[8:11]\n\t"
    "ds_read_b64_tr_b16 v[216:217], v240 offset:16392\n\t"
    "v_mfma_f32_16x16x32_bf16 v[8:11], v[196:199], a[84:87], v[8:11]\n\t"
    "ds_read_b64_tr_b16 v[218:219], v240 offset:20488\n\t"
    "v_mfma_f32_16x16x32_bf16 v[8:11], v[200:203], a[88:91], v[8:11]\n\t"
    "ds_read_b64_tr_b16 v[220:221], v240 offset:24584\n\t"
    "v_mfma_f32_16x16x32_bf16 v[8:11], v[204:207], a[92:95], v[8:11]\n\t"
    "ds_read_b64_tr_b16 v[222:223], v240 offset:28680\n\t"
    "v_mfma_f32_16x16x32_bf16 v[16:19], v[192:195], a[144:147], v[16:19]\n\t"
    "s_add_u32 m0, s94, 147456\n\t"
    "s_add_u32 s76, s86, 1376256\n\t"
    "buffer_load_dwordx4 v230, s[60:63], s76 offen lds\n\t"
    "v_mfma_f32_16x16x32_bf16 v[16:19], v[196:199], a[148:151], v[16:19]\n\t"
    "v_mfma_f32_16x16x32_bf16 v[16:19], v[200:203], a[152:155], v[16:19]\n\t"
    "v_mfma_f32_16x16x32_bf16 v[16:19], v[204:207], a[156:159], v[16:19]\n\t"
    "s_add_u32 m0, s94, 148480\n\t"
    "s_add_u32 s76, s86, 1404928\n\t"
    "buffer_load_dwordx4 v230, s[60:63], s76 offen lds\n\t"
    "v_mfma_f32_16x16x32_bf16 v[24:27], v[192:195], a[208:211], v[24:27]\n\t"
    "v_mfma_f32_16x16x32_bf16 v[24:27], v[196:199], a[212:215], v[24:27]\n\t"
    "v_mfma_f32_16x16x32_bf16 v[24:27], v[200:203], a[216:219], v[24:27]\n\t"
    "s_add_u32 m0, s94, 149504\n\t"
    "s_add_u32 s76, s86, 1433600\n\t"
    "buffer_load_dwordx4 v230, s[60:63], s76 offen lds\n\t"
    "v_mfma_f32_16x16x32_bf16 v[24:27], v[204:207], a[220:223], v[24:27]\n\t"
    "s_waitcnt lgkmcnt(0)\n\t"
    "v_mfma_f32_16x16x32_bf16 v[4:7], v[208:211], a[16:19], v[4:7]\n\t"
    "ds_read_b64_tr_b16 v[192:193], v241 offset:0\n\t"
    "v_mfma_f32_16x16x32_bf16 v[4:7], v[212:215], a[20:23], v[4:7]\n\t"
    "ds_read_b64_tr_b16 v[194:195], v241 offset:4096\n\t"
    "v_mfma_f32_16x16x32_bf16 v[4:7], v[216:219], a[24:27], v[4:7]\n\t"
    "ds_read_b64_tr_b16 v[196:197], v241 offset:8192\n\t"
    "v_mfma_f32_16x16x32_bf16 v[4:7], v[220:223], a[28:31], v[4:7]\n\t"
    "ds_read_b64_tr_b16 v[198:199], v241 offset:12288\n\t"
    "v_mfma_f32_16x16x32_bf16 v[12:15], v[208:211], a[80:83], v[12:15]\n\t"
    "ds_read_b64_tr_b16 v[200:201], v241 offset:16384\n\t"
    "v_mfma_f32_16x16x32_bf16 v[12:15], v[212:215], a[84:87], v[12:15]\n\t"
    "ds_read_b64_tr_b16 v[202:203], v241 offset:20480\n\t"
    "v_mfma_f32_16x16x32_bf16 v[12:15], v[216:219], a[88:91], v[12:15]\n\t"
    "ds_read_b64_tr_b16 v[204:205], v241 offset:24576\n\t"
    "v_mfma_f32_16x16x32_bf16 v[12:15], v[220:223], a[92:95], v[12:15]\n\t"
    "ds_read_b64_tr_b16 v[206:207], v241 offset:28672\n\t"
    "v_mfma_f32_16x16x32_bf16 v[20:23], v[208:211], a[144:147], v[20:23]\n\t"
    "s_add_u32 m0, s94, 150528\n\t"
    "s_add_u32 s76, s86, 1462272\n\t"
    "buffer_load_dwordx4 v230, s[60:63], s76 offen lds\n\t"
    "v_mfma_f32_16x16x32_bf16 v[20:23], v[212:215], a[148:151], v[20:23]\n\t"
    "v_mfma_f32_16x16x32_bf16 v[20:23], v[216:219], a[152:155], v[20:23]\n\t"
    "v_mfma_f32_16x16x32_bf16 v[20:23], v[220:223], a[156:159], v[20:23]\n\t"
    "s_add_u32 s77, s80, 12288\n\t"
    "buffer_load_dwordx4 a[48:51], v255, s[68:71], s77 offen offset:0 sc1\n\t"
    "v_mfma_f32_16x16x32_bf16 v[28:31], v[208:211], a[208:211], v[28:31]\n\t"
    "v_mfma_f32_16x16x32_bf16 v[28:31], v[212:215], a[212:215], v[28:31]\n\t"
    "v_mfma_f32_16x16x32_bf16 v[28:31], v[216:219], a[216:219], v[28:31]\n\t"
    "buffer_load_dwordx4 a[52:55], v255, s[68:71], s77 offen offset:1024 sc1\n\t"
    "v_mfma_f32_16x16x32_bf16 v[28:31], v[220:223], a[220:223], v[28:31]\n\t"
    "s_waitcnt lgkmcnt(0)\n\t"
    "v_mfma_f32_16x16x32_bf16 v[32:35], v[192:195], a[16:19], v[32:35]\n\t"
    "ds_read_b64_tr_b16 v[208:209], v241 offset:8\n\t"
    "v_mfma_f32_16x16x32_bf16 v[32:35], v[196:199], a[20:23], v[32:35]\n\t"
    "ds_read_b64_tr_b16 v[210:211], v241 offset:4104\n\t"
    "v_mfma_f32_16x16x32_bf16 v[32:35], v[200:203], a[24:27], v[32:35]\n\t"
    "ds_read_b64_tr_b16 v[212:213], v241 offset:8200\n\t"
    "v_mfma_f32_16x16x32_bf16 v[32:35], v[204:207], a[28:31], v[32:35]\n\t"
    "ds_read_b64_tr_b16 v[214:215], v241 offset:12296\n\t"
    "v_mfma_f32_16x16x32_bf16 v[40:43], v[192:195], a[80:83], v[40:43]\n\t"
    "ds_read_b64_tr_b16 v[216:217], v241 offset:16392\n\t"
    "v_mfma_f32_16x16x32_bf16 v[40:43], v[196:199], a[84:87], v[40:43]\n\t"
    "ds_read_b64_tr_b16 v[218:219], v241 offset:20488\n\t"
    "v_mfma_f32_16x16x32_bf16 v[40:43], v[200:203], a[88:91], v[40:43]\n\t"
    "ds_read_b64_tr_b16 v[220:221], v241 offset:24584\n\t"
    "v_mfma_f32_16x16x32_bf16 v[40:43], v[204:207], a[92:95], v[40:43]\n\t"
    "ds_read_b64_tr_b16 v[222:223], v241 offset:28680\n\t"
    "v_mfma_f32_16x16x32_bf16 v[48:51], v[192:195], a[144:147], v[48:51]\n\t"
    "buffer_load_dwordx4 a[56:59], v255, s[68:71], s77 offen offset:2048 sc1\n\t"
    "v_mfma_f32_16x16x32_bf16 v[48:51], v[196:199], a[148:151], v[48:51]\n\t"
    "v_mfma_f32_16x16x32_bf16 v[48:51], v[200:203], a[152:155], v[48:51]\n\t"
    "v_mfma_f32_16x16x32_bf16 v[48:51], v[204:207], a[156:159], v[48:51]\n\t"
    "buffer_load_dwordx4 a[60:63], v255, s[68:71], s77 offen offset:3072 sc1\n\t"
    "v_mfma_f32_16x16x32_bf16 v[56:59], v[192:195], a[208:211], v[56:59]\n\t"
    "v_mfma_f32_16x16x32_bf16 v[56:59], v[196:199], a[212:215], v[56:59]\n\t"
    "v_mfma_f32_16x16x32_bf16 v[56:59], v[200:203], a[216:219], v[56:59]\n\t"
    "s_add_u32 s77, s81, 12288\n\t"
    "buffer_load_dwordx4 a[112:115], v255, s[68:71], s77 offen offset:0 sc1\n\t"
    "v_mfma_f32_16x16x32_bf16 v[56:59], v[204:207], a[220:223], v[56:59]\n\t"
    "s_waitcnt lgkmcnt(0)\n\t"
    "v_mfma_f32_16x16x32_bf16 v[36:39], v[208:211], a[16:19], v[36:39]\n\t"
    "ds_read_b64_tr_b16 v[192:193], v242 offset:0\n\t"
    "v_mfma_f32_16x16x32_bf16 v[36:39], v[212:215], a[20:23], v[36:39]\n\t"
    "ds_read_b64_tr_b16 v[194:195], v242 offset:4096\n\t"
    "v_mfma_f32_16x16x32_bf16 v[36:39], v[216:219], a[24:27], v[36:39]\n\t"
    "ds_read_b64_tr_b16 v[196:197], v242 offset:8192\n\t"
    "v_mfma_f32_16x16x32_bf16 v[36:39], v[220:223], a[28:31], v[36:39]\n\t"
    "ds_read_b64_tr_b16 v[198:199], v242 offset:12288\n\t"
    "v_mfma_f32_16x16x32_bf16 v[44:47], v[208:211], a[80:83], v[44:47]\n\t"
    "ds_read_b64_tr_b16 v[200:201], v242 offset:16384\n\t"
    "v_mfma_f32_16x16x32_bf16 v[44:47], v[212:215], a[84:87], v[44:47]\n\t"
    "ds_read_b64_tr_b16 v[202:203], v242 offset:20480\n\t"
    "v_mfma_f32_16x16x32_bf16 v[44:47], v[216:219], a[88:91], v[44:47]\n\t"
    "ds_read_b64_tr_b16 v[204:205], v242 offset:24576\n\t"
    "v_mfma_f32_16x16x32_bf16 v[44:47], v[220:223], a[92:95], v[44:47]\n\t"
    "ds_read_b64_tr_b16 v[206:207], v242 offset:28672\n\t"
    "v_mfma_f32_16x16x32_bf16 v[52:55], v[208:211], a[144:147], v[52:55]\n\t"
    "buffer_load_dwordx4 a[116:119], v255, s[68:71], s77 offen offset:1024 sc1\n\t"
    "v_mfma_f32_16x16x32_bf16 v[52:55], v[212:215], a[148:151], v[52:55]\n\t"
    "v_mfma_f32_16x16x32_bf16 v[52:55], v[216:219], a[152:155], v[52:55]\n\t"
    "v_mfma_f32_16x16x32_bf16 v[52:55], v[220:223], a[156:159], v[52:55]\n\t"
    "buffer_load_dwordx4 a[120:123], v255, s[68:71], s77 offen offset:2048 sc1\n\t"
    "v_mfma_f32_16x16x32_bf16 v[60:63], v[208:211], a[208:211], v[60:63]\n\t"
    "v_mfma_f32_16x16x32_bf16 v[60:63], v[212:215], a[212:215], v[60:63]\n\t"
    "v_mfma_f32_16x16x32_bf16 v[60:63], v[216:219], a[216:219], v[60:63]\n\t"
    "buffer_load_dwordx4 a[124:127], v255, s[68:71], s77 offen offset:3072 sc1\n\t"
    "v_mfma_f32_16x16x32_bf16 v[60:63], v[220:223], a[220:223], v[60:63]\n\t"
    "s_waitcnt lgkmcnt(0)\n\t"
    "v_mfma_f32_16x16x32_bf16 v[64:67], v[192:195], a[16:19], v[64:67]\n\t"
    "ds_read_b64_tr_b16 v[208:209], v242 offset:8\n\t"
    "v_mfma_f32_16x16x32_bf16 v[64:67], v[196:199], a[20:23], v[64:67]\n\t"
    "ds_read_b64_tr_b16 v[210:211], v242 offset:4104\n\t"
    "v_mfma_f32_16x16x32_bf16 v[64:67], v[200:203], a[24:27], v[64:67]\n\t"
    "ds_read_b64_tr_b16 v[212:213], v242 offset:8200\n\t"
    "v_mfma_f32_16x16x32_bf16 v[64:67], v[204:207], a[28:31], v[64:67]\n\t"
    "ds_read_b64_tr_b16 v[214:215], v242 offset:12296\n\t"
    "v_mfma_f32_16x16x32_bf16 v[72:75], v[192:195], a[80:83], v[72:75]\n\t"
    "ds_read_b64_tr_b16 v[216:217], v242 offset:16392\n\t"
    "v_mfma_f32_16x16x32_bf16 v[72:75], v[196:199], a[84:87], v[72:75]\n\t"
    "ds_read_b64_tr_b16 v[218:219], v242 offset:20488\n\t"
    "v_mfma_f32_16x16x32_bf16 v[72:75], v[200:203], a[88:91], v[72:75]\n\t"
    "ds_read_b64_tr_b16 v[220:221], v242 offset:24584\n\t"
    "v_mfma_f32_16x16x32_bf16 v[72:75], v[204:207], a[92:95], v[72:75]\n\t"
    "ds_read_b64_tr_b16 v[222:223], v242 offset:28680\n\t"
    "v_mfma_f32_16x16x32_bf16 v[80:83], v[192:195], a[144:147], v[80:83]\n\t"
    "s_add_u32 s77, s82, 12288\n\t"
    "buffer_load_dwordx4 a[176:179], v255, s[68:71], s77 offen offset:0 sc1\n\t"
    "v_mfma_f32_16x16x32_bf16 v[80:83], v[196:199], a[148:151], v[80:83]\n\t"
    "v_mfma_f32_16x16x32_bf16 v[80:83], v[200:203], a[152:155], v[80:83]\n\t"
    "v_mfma_f32_16x16x32_bf16 v[80:83], v[204:207], a[156:159], v[80:83]\n\t"
    "buffer_load_dwordx4 a[180:183], v255, s[68:71], s77 offen offset:1024 sc1\n\t"
    "v_mfma_f32_16x16x32_bf16 v[88:91], v[192:195], a[208:211], v[88:91]\n\t"
    "v_mfma_f32_16x16x32_bf16 v[88:91], v[196:199], a[212:215], v[88:91]\n\t"
    "v_mfma_f32_16x16x32_bf16 v[88:91], v[200:203], a[216:219], v[88:91]\n\t"
    "buffer_load_dwordx4 a[184:187], v255, s[68:71], s77 offen offset:2048 sc1\n\t"
    "v_mfma_f32_16x16x32_bf16 v[88:91], v[204:207], a[220:223], v[88:91]\n\t"
    "s_waitcnt lgkmcnt(0)\n\t"
    "v_mfma_f32_16x16x32_bf16 v[68:71], v[208:211], a[16:19], v[68:71]\n\t"
    "ds_read_b64_tr_b16 v[192:193], v243 offset:0\n\t"
    "v_mfma_f32_16x16x32_bf16 v[68:71], v[212:215], a[20:23], v[68:71]\n\t"
    "ds_read_b64_tr_b16 v[194:195], v243 offset:4096\n\t"
    "v_mfma_f32_16x16x32_bf16 v[68:71], v[216:219], a[24:27], v[68:71]\n\t"
    "ds_read_b64_tr_b16 v[196:197], v243 offset:8192\n\t"
    "v_mfma_f32_16x16x32_bf16 v[68:71], v[220:223], a[28:31], v[68:71]\n\t"
    "ds_read_b64_tr_b16 v[198:199], v243 offset:12288\n\t"
    "v_mfma_f32_16x16x32_bf16 v[76:79], v[208:211], a[80:83], v[76:79]\n\t"
    "ds_read_b64_tr_b16 v[200:201], v243 offset:16384\n\t"
    "v_mfma_f32_16x16x32_bf16 v[76:79], v[212:215], a[84:87], v[76:79]\n\t"
    "ds_read_b64_tr_b16 v[202:203], v243 offset:20480\n\t"
    "v_mfma_f32_16x16x32_bf16 v[76:79], v[216:219], a[88:91], v[76:79]\n\t"
    "ds_read_b64_tr_b16 v[204:205], v243 offset:24576\n\t"
    "v_mfma_f32_16x16x32_bf16 v[76:79], v[220:223], a[92:95], v[76:79]\n\t"
    "ds_read_b64_tr_b16 v[206:207], v243 offset:28672\n\t"
    "v_mfma_f32_16x16x32_bf16 v[84:87], v[208:211], a[144:147], v[84:87]\n\t"
    "buffer_load_dwordx4 a[188:191], v255, s[68:71], s77 offen offset:3072 sc1\n\t"
    "v_mfma_f32_16x16x32_bf16 v[84:87], v[212:215], a[148:151], v[84:87]\n\t"
    "v_mfma_f32_16x16x32_bf16 v[84:87], v[216:219], a[152:155], v[84:87]\n\t"
    "v_mfma_f32_16x16x32_bf16 v[84:87], v[220:223], a[156:159], v[84:87]\n\t"
    "s_add_u32 s77, s83, 12288\n\t"
    "buffer_load_dwordx4 a[240:243], v255, s[68:71], s77 offen offset:0 sc1\n\t"
    "v_mfma_f32_16x16x32_bf16 v[92:95], v[208:211], a[208:211], v[92:95]\n\t"
    "v_mfma_f32_16x16x32_bf16 v[92:95], v[212:215], a[212:215], v[92:95]\n\t"
    "v_mfma_f32_16x16x32_bf16 v[92:95], v[216:219], a[216:219], v[92:95]\n\t"
    "buffer_load_dwordx4 a[244:247], v255, s[68:71], s77 offen offset:1024 sc1\n\t"
    "v_mfma_f32_16x16x32_bf16 v[92:95], v[220:223], a[220:223], v[92:95]\n\t"
    "s_waitcnt lgkmcnt(0)\n\t"
    "v_mfma_f32_16x16x32_bf16 v[96:99], v[192:195], a[16:19], v[96:99]\n\t"
    "ds_read_b64_tr_b16 v[208:209], v243 offset:8\n\t"
    "v_mfma_f32_16x16x32_bf16 v[96:99], v[196:199], a[20:23], v[96:99]\n\t"
    "ds_read_b64_tr_b16 v[210:211], v243 offset:4104\n\t"
    "v_mfma_f32_16x16x32_bf16 v[96:99], v[200:203], a[24:27], v[96:99]\n\t"
    "ds_read_b64_tr_b16 v[212:213], v243 offset:8200\n\t"
    "v_mfma_f32_16x16x32_bf16 v[96:99], v[204:207], a[28:31], v[96:99]\n\t"
    "ds_read_b64_tr_b16 v[214:215], v243 offset:12296\n\t"
    "v_mfma_f32_16x16x32_bf16 v[104:107], v[192:195], a[80:83], v[104:107]\n\t"
    "ds_read_b64_tr_b16 v[216:217], v243 offset:16392\n\t"
    "v_mfma_f32_16x16x32_bf16 v[104:107], v[196:199], a[84:87], v[104:107]\n\t"
    "ds_read_b64_tr_b16 v[218:219], v243 offset:20488\n\t"
    "v_mfma_f32_16x16x32_bf16 v[104:107], v[200:203], a[88:91], v[104:107]\n\t"
    "ds_read_b64_tr_b16 v[220:221], v243 offset:24584\n\t"
    "v_mfma_f32_16x16x32_bf16 v[104:107], v[204:207], a[92:95], v[104:107]\n\t"
    "ds_read_b64_tr_b16 v[222:223], v243 offset:28680\n\t"
    "v_mfma_f32_16x16x32_bf16 v[112:115], v[192:195], a[144:147], v[112:115]\n\t"
    "buffer_load_dwordx4 a[248:251], v255, s[68:71], s77 offen offset:2048 sc1\n\t"
    "v_mfma_f32_16x16x32_bf16 v[112:115], v[196:199], a[148:151], v[112:115]\n\t"
    "v_mfma_f32_16x16x32_bf16 v[112:115], v[200:203], a[152:155], v[112:115]\n\t"
    "v_mfma_f32_16x16x32_bf16 v[112:115], v[204:207], a[156:159], v[112:115]\n\t"
    "buffer_load_dwordx4 a[252:255], v255, s[68:71], s77 offen offset:3072 sc1\n\t"
    "v_mfma_f32_16x16x32_bf16 v[120:123], v[192:195], a[208:211], v[120:123]\n\t"
    "v_mfma_f32_16x16x32_bf16 v[120:123], v[196:199], a[212:215], v[120:123]\n\t"
    "v_mfma_f32_16x16x32_bf16 v[120:123], v[200:203], a[216:219], v[120:123]\n\t"
    "s_add_u32 m0, s95, 32768\n\t"
    "s_add_u32 s76, s87, 2097152\n\t"
    "buffer_load_dwordx4 v231, s[64:67], s76 offen lds\n\t"
    "v_mfma_f32_16x16x32_bf16 v[120:123], v[204:207], a[220:223], v[120:123]\n\t"
    "s_waitcnt vmcnt(21) lgkmcnt(0)\n\t"
    "v_mfma_f32_16x16x32_bf16 v[100:103], v[208:211], a[16:19], v[100:103]\n\t"
    "s_barrier\n\t"
    "ds_read_b64_tr_b16 v[192:193], v232 offset:32768\n\t"
    "v_mfma_f32_16x16x32_bf16 v[100:103], v[212:215], a[20:23], v[100:103]\n\t"
    "ds_read_b64_tr_b16 v[194:195], v232 offset:34816\n\t"
    "v_mfma_f32_16x16x32_bf16 v[100:103], v[216:219], a[24:27], v[100:103]\n\t"
    "ds_read_b64_tr_b16 v[196:197], v232 offset:36864\n\t"
    "v_mfma_f32_16x16x32_bf16 v[100:103], v[220:223], a[28:31], v[100:103]\n\t"
    "ds_read_b64_tr_b16 v[198:199], v232 offset:38912\n\t"
    "v_mfma_f32_16x16x32_bf16 v[108:111], v[208:211], a[80:83], v[108:111]\n\t"
    "ds_read_b64_tr_b16 v[200:201], v232 offset:40960\n\t"
    "v_mfma_f32_16x16x32_bf16 v[108:111], v[212:215], a[84:87], v[108:111]\n\t"
    "ds_read_b64_tr_b16 v[202:203], v232 offset:43008\n\t"
    "v_mfma_f32_16x16x32_bf16 v[108:111], v[216:219], a[88:91], v[108:111]\n\t"
    "ds_read_b64_tr_b16 v[204:205], v232 offset:45056\n\t"
    "v_mfma_f32_16x16x32_bf16 v[108:111], v[220:223], a[92:95], v[108:111]\n\t"
    "ds_read_b64_tr_b16 v[206:207], v232 offset:47104\n\t"
    "v_mfma_f32_16x16x32_bf16 v[116:119], v[208:211], a[144:147], v[116:119]\n\t"
    "s_add_u32 m0, s95, 33792\n\t"
    "s_add_u32 s76, s87, 2129920\n\t"
    "buffer_load_dwordx4 v231, s[64:67], s76 offen lds\n\t"
    "v_mfma_f32_16x16x32_bf16 v[116:119], v[212:215], a[148:151], v[116:119]\n\t"
    "v_mfma_f32_16x16x32_bf16 v[116:119], v[216:219], a[152:155], v[116:119]\n\t"
    "v_mfma_f32_16x16x32_bf16 v[116:119], v[220:223], a[156:159], v[116:119]\n\t"
    "s_add_u32 m0, s95, 34816\n\t"
    "s_add_u32 s76, s87, 2162688\n\t"
    "buffer_load_dwordx4 v231, s[64:67], s76 offen lds\n\t"
    "v_mfma_f32_16x16x32_bf16 v[124:127], v[208:211], a[208:211], v[124:127]\n\t"
    "v_mfma_f32_16x16x32_bf16 v[124:127], v[212:215], a[212:215], v[124:127]\n\t"
    "v_mfma_f32_16x16x32_bf16 v[124:127], v[216:219], a[216:219], v[124:127]\n\t"
    "s_add_u32 m0, s95, 35840\n\t"
    "s_add_u32 s76, s87, 2195456\n\t"
    "buffer_load_dwordx4 v231, s[64:67], s76 offen lds\n\t"
    "v_mfma_f32_16x16x32_bf16 v[124:127], v[220:223], a[220:223], v[124:127]\n\t"
    "s_waitcnt lgkmcnt(0)\n\t"
    "v_mfma_f32_16x16x32_bf16 v[128:131], v[192:195], a[32:35], v[128:131]\n\t"
    "ds_read_b64_tr_b16 v[208:209], v233 offset:32768\n\t"
    "v_mfma_f32_16x16x32_bf16 v[128:131], v[196:199], a[36:39], v[128:131]\n\t"
    "ds_read_b64_tr_b16 v[210:211], v233 offset:34816\n\t"
    "v_mfma_f32_16x16x32_bf16 v[128:131], v[200:203], a[40:43], v[128:131]\n\t"
    "ds_read_b64_tr_b16 v[212:213], v233 offset:36864\n\t"
    "v_mfma_f32_16x16x32_bf16 v[128:131], v[204:207], a[44:47], v[128:131]\n\t"
    "ds_read_b64_tr_b16 v[214:215], v233 offset:38912\n\t"
    "v_mfma_f32_16x16x32_bf16 v[136:139], v[192:195], a[96:99], v[136:139]\n\t"
    "ds_read_b64_tr_b16 v[216:217], v233 offset:40960\n\t"
    "v_mfma_f32_16x16x32_bf16 v[136:139], v[196:199], a[100:103], v[136:139]\n\t"
    "ds_read_b64_tr_b16 v[218:219], v233 offset:43008\n\t"
    "v_mfma_f32_16x16x32_bf16 v[136:139], v[200:203], a[104:107], v[136:139]\n\t"
    "ds_read_b64_tr_b16 v[220:221], v233 offset:45056\n\t"
    "v_mfma_f32_16x16x32_bf16 v[136:139], v[204:207], a[108:111], v[136:139]\n\t"
    "ds_read_b64_tr_b16 v[222:223], v233 offset:47104\n\t"
    "v_mfma_f32_16x16x32_bf16 v[144:147], v[192:195], a[160:163], v[144:147]\n\t"
    "s_add_u32 m0, s95, 36864\n\t"
    "s_add_u32 s76, s87, 2228224\n\t"
    "buffer_load_dwordx4 v231, s[64:67], s76 offen lds\n\t"
    "v_mfma_f32_16x16x32_bf16 v[144:147], v[196:199], a[164:167], v[144:147]\n\t"
    "v_mfma_f32_16x16x32_bf16 v[144:147], v[200:203], a[168:171], v[144:147]\n\t"
    "v_mfma_f32_16x16x32_bf16 v[144:147], v[204:207], a[172:175], v[144:147]\n\t"
    "s_add_u32 m0, s95, 37888\n\t"
    "s_add_u32 s76, s87, 2260992\n\t"
    "buffer_load_dwordx4 v231, s[64:67], s76 offen lds\n\t"
    "v_mfma_f32_16x16x32_bf16 v[152:155], v[192:195], a[224:227], v[152:155]\n\t"
    "v_mfma_f32_16x16x32_bf16 v[152:155], v[196:199], a[228:231], v[152:155]\n\t"
    "v_mfma_f32_16x16x32_bf16 v[152:155], v[200:203], a[232:235], v[152:155]\n\t"
    "s_add_u32 m0, s95, 38912\n\t"
    "s_add_u32 s76, s87, 2293760\n\t"
    "buffer_load_dwordx4 v231, s[64:67], s76 offen lds\n\t"
    "v_mfma_f32_16x16x32_bf16 v[152:155], v[204:207], a[236:239], v[152:155]\n\t"
    "s_waitcnt lgkmcnt(0)\n\t"
    "v_mfma_f32_16x16x32_bf16 v[132:135], v[208:211], a[32:35], v[132:135]\n\t"
    "ds_read_b64_tr_b16 v[192:193], v234 offset:32768\n\t"
    "v_mfma_f32_16x16x32_bf16 v[132:135], v[212:215], a[36:39], v[132:135]\n\t"
    "ds_read_b64_tr_b16 v[194:195], v234 offset:34816\n\t"
    "v_mfma_f32_16x16x32_bf16 v[132:135], v[216:219], a[40:43], v[132:135]\n\t"
    "ds_read_b64_tr_b16 v[196:197], v234 offset:36864\n\t"
    "v_mfma_f32_16x16x32_bf16 v[132:135], v[220:223], a[44:47], v[132:135]\n\t"
    "ds_read_b64_tr_b16 v[198:199], v234 offset:38912\n\t"
    "v_mfma_f32_16x16x32_bf16 v[140:143], v[208:211], a[96:99], v[140:143]\n\t"
    "ds_read_b64_tr_b16 v[200:201], v234 offset:40960\n\t"
    "v_mfma_f32_16x16x32_bf16 v[140:143], v[212:215], a[100:103], v[140:143]\n\t"
    "ds_read_b64_tr_b16 v[202:203], v234 offset:43008\n\t"
    "v_mfma_f32_16x16x32_bf16 v[140:143], v[216:219], a[104:107], v[140:143]\n\t"
    "ds_read_b64_tr_b16 v[204:205], v234 offset:45056\n\t"
    "v_mfma_f32_16x16x32_bf16 v[140:143], v[220:223], a[108:111], v[140:143]\n\t"
    "ds_read_b64_tr_b16 v[206:207], v234 offset:47104\n\t"
    "v_mfma_f32_16x16x32_bf16 v[148:151], v[208:211], a[160:163], v[148:151]\n\t"
    "s_add_u32 m0, s95, 39936\n\t"
    "s_add_u32 s76, s87, 2326528\n\t"
    "buffer_load_dwordx4 v231, s[64:67], s76 offen lds\n\t"
    "v_mfma_f32_16x16x32_bf16 v[148:151], v[212:215], a[164:167], v[148:151]\n\t"
    "v_mfma_f32_16x16x32_bf16 v[148:151], v[216:219], a[168:171], v[148:151]\n\t"
    "v_mfma_f32_16x16x32_bf16 v[148:151], v[220:223], a[172:175], v[148:151]\n\t"
    "s_add_u32 m0, s95, 98304\n\t"
    "s_add_u32 s76, s87, 3145728\n\t"
    "buffer_load_dwordx4 v231, s[64:67], s76 offen lds\n\t"
    "v_mfma_f32_16x16x32_bf16 v[156:159], v[208:211], a[224:227], v[156:159]\n\t"
    "v_mfma_f32_16x16x32_bf16 v[156:159], v[212:215], a[228:231], v[156:159]\n\t"
    "v_mfma_f32_16x16x32_bf16 v[156:159], v[216:219], a[232:235], v[156:159]\n\t"
    "s_add_u32 m0, s95, 99328\n\t"
    "s_add_u32 s76, s87, 3178496\n\t"
    "buffer_load_dwordx4 v231, s[64:67], s76 offen lds\n\t"
    "v_mfma_f32_16x16x32_bf16 v[156:159], v[220:223], a[236:239], v[156:159]\n\t"
    "s_waitcnt lgkmcnt(0)\n\t"
    "v_mfma_f32_16x16x32_bf16 v[160:163], v[192:195], a[32:35], v[160:163]\n\t"
    "ds_read_b64_tr_b16 v[208:209], v235 offset:32768\n\t"
    "v_mfma_f32_16x16x32_bf16 v[160:163], v[196:199], a[36:39], v[160:163]\n\t"
    "ds_read_b64_tr_b16 v[210:211], v235 offset:34816\n\t"
    "v_mfma_f32_16x16x32_bf16 v[160:163], v[200:203], a[40:43], v[160:163]\n\t"
    "ds_read_b64_tr_b16 v[212:213], v235 offset:36864\n\t"
    "v_mfma_f32_16x16x32_bf16 v[160:163], v[204:207], a[44:47], v[160:163]\n\t"
    "ds_read_b64_tr_b16 v[214:215], v235 offset:38912\n\t"
    "v_mfma_f32_16x16x32_bf16 v[168:171], v[192:195], a[96:99], v[168:171]\n\t"
    "ds_read_b64_tr_b16 v[216:217], v235 offset:40960\n\t"
    "v_mfma_f32_16x16x32_bf16 v[168:171], v[196:199], a[100:103], v[168:171]\n\t"
    "ds_read_b64_tr_b16 v[218:219], v235 offset:43008\n\t"
    "v_mfma_f32_16x16x32_bf16 v[168:171], v[200:203], a[104:107], v[168:171]\n\t"
    "ds_read_b64_tr_b16 v[220:221], v235 offset:45056\n\t"
    "v_mfma_f32_16x16x32_bf16 v[168:171], v[204:207], a[108:111], v[168:171]\n\t"
    "ds_read_b64_tr_b16 v[222:223], v235 offset:47104\n\t"
    "v_mfma_f32_16x16x32_bf16 v[176:179], v[192:195], a[160:163], v[176:179]\n\t"
    "s_add_u32 m0, s95, 100352\n\t"
    "s_add_u32 s76, s87, 3211264\n\t"
    "buffer_load_dwordx4 v231, s[64:67], s76 offen lds\n\t"
    "v_mfma_f32_16x16x32_bf16 v[176:179], v[196:199], a[164:167], v[176:179]\n\t"
    "v_mfma_f32_16x16x32_bf16 v[176:179], v[200:203], a[168:171], v[176:179]\n\t"
    "v_mfma_f32_16x16x32_bf16 v[176:179], v[204:207], a[172:175], v[176:179]\n\t"
    "s_add_u32 m0, s95, 101376\n\t"
    "s_add_u32 s76, s87, 3244032\n\t"
    "buffer_load_dwordx4 v231, s[64:67], s76 offen lds\n\t"
    "v_mfma_f32_16x16x32_bf16 v[184:187], v[192:195], a[224:227], v[184:187]\n\t"
    "v_mfma_f32_16x16x32_bf16 v[184:187], v[196:199], a[228:231], v[184:187]\n\t"
    "v_mfma_f32_16x16x32_bf16 v[184:187], v[200:203], a[232:235], v[184:187]\n\t"
    "s_add_u32 m0, s95, 102400\n\t"
    "s_add_u32 s76, s87, 3276800\n\t"
    "buffer_load_dwordx4 v231, s[64:67], s76 offen lds\n\t"
    "v_mfma_f32_16x16x32_bf16 v[184:187], v[204:207], a[236:239], v[184:187]\n\t"
    "s_waitcnt vmcnt(13) lgkmcnt(0)\n\t"
    "v_mfma_f32_16x16x32_bf16 v[164:167], v[208:211], a[32:35], v[164:167]\n\t"
    "s_barrier\n\t"
    "ds_read_b64_tr_b16 v[192:193], v232 offset:49152\n\t"
    "v_mfma_f32_16x16x32_bf16 v[164:167], v[212:215], a[36:39], v[164:167]\n\t"
    "ds_read_b64_tr_b16 v[194:195], v232 offset:51200\n\t"
    "v_mfma_f32_16x16x32_bf16 v[164:167], v[216:219], a[40:43], v[164:167]\n\t"
    "ds_read_b64_tr_b16 v[196:197], v232 offset:53248\n\t"
    "v_mfma_f32_16x16x32_bf16 v[164:167], v[220:223], a[44:47], v[164:167]\n\t"
    "ds_read_b64_tr_b16 v[198:199], v232 offset:55296\n\t"
    "v_mfma_f32_16x16x32_bf16 v[172:175], v[208:211], a[96:99], v[172:175]\n\t"
    "ds_read_b64_tr_b16 v[200:201], v232 offset:57344\n\t"
    "v_mfma_f32_16x16x32_bf16 v[172:175], v[212:215], a[100:103], v[172:175]\n\t"
    "ds_read_b64_tr_b16 v[202:203], v232 offset:59392\n\t"
    "v_mfma_f32_16x16x32_bf16 v[172:175], v[216:219], a[104:107], v[172:175]\n\t"
    "ds_read_b64_tr_b16 v[204:205], v232 offset:61440\n\t"
    "v_mfma_f32_16x16x32_bf16 v[172:175], v[220:223], a[108:111], v[172:175]\n\t"
    "ds_read_b64_tr_b16 v[206:207], v232 offset:63488\n\t"
    "v_mfma_f32_16x16x32_bf16 v[180:183], v[208:211], a[160:163], v[180:183]\n\t"
    "s_add_u32 m0, s95, 103424\n\t"
    "s_add_u32 s76, s87, 3309568\n\t"
    "buffer_load_dwordx4 v231, s[64:67], s76 offen lds\n\t"
    "v_mfma_f32_16x16x32_bf16 v[180:183], v[212:215], a[164:167], v[180:183]\n\t"
    "v_mfma_f32_16x16x32_bf16 v[180:183], v[216:219], a[168:171], v[180:183]\n\t"
    "v_mfma_f32_16x16x32_bf16 v[180:183], v[220:223], a[172:175], v[180:183]\n\t"
    "s_add_u32 m0, s95, 104448\n\t"
    "s_add_u32 s76, s87, 3342336\n\t"
    "buffer_load_dwordx4 v231, s[64:67], s76 offen lds\n\t"
    "v_mfma_f32_16x16x32_bf16 v[188:191], v[208:211], a[224:227], v[188:191]\n\t"
    "v_mfma_f32_16x16x32_bf16 v[188:191], v[212:215], a[228:231], v[188:191]\n\t"
    "v_mfma_f32_16x16x32_bf16 v[188:191], v[216:219], a[232:235], v[188:191]\n\t"
    "s_add_u32 m0, s95, 105472\n\t"
    "s_add_u32 s76, s87, 3375104\n\t"
    "buffer_load_dwordx4 v231, s[64:67], s76 offen lds\n\t"
    "v_mfma_f32_16x16x32_bf16 v[188:191], v[220:223], a[236:239], v[188:191]\n\t"
    "s_waitcnt lgkmcnt(0)\n\t"
    "v_mfma_f32_16x16x32_bf16 v[128:131], v[192:195], a[48:51], v[128:131]\n\t"
    "ds_read_b64_tr_b16 v[208:209], v233 offset:49152\n\t"
    "v_mfma_f32_16x16x32_bf16 v[128:131], v[196:199], a[52:55], v[128:131]\n\t"
    "ds_read_b64_tr_b16 v[210:211], v233 offset:51200\n\t"
    "v_mfma_f32_16x16x32_bf16 v[128:131], v[200:203], a[56:59], v[128:131]\n\t"
    "ds_read_b64_tr_b16 v[212:213], v233 offset:53248\n\t"
    "v_mfma_f32_16x16x32_bf16 v[128:131], v[204:207], a[60:63], v[128:131]\n\t"
    "ds_read_b64_tr_b16 v[214:215], v233 offset:55296\n\t"
    "v_mfma_f32_16x16x32_bf16 v[136:139], v[192:195], a[112:115], v[136:139]\n\t"
    "ds_read_b64_tr_b16 v[216:217], v233 offset:57344\n\t"
    "v_mfma_f32_16x16x32_bf16 v[136:139], v[196:199], a[116:119], v[136:139]\n\t"
    "ds_read_b64_tr_b16 v[218:219], v233 offset:59392\n\t"
    "v_mfma_f32_16x16x32_bf16 v[136:139], v[200:203], a[120:123], v[136:139]\n\t"
    "ds_read_b64_tr_b16 v[220:221], v233 offset:61440\n\t"
    "v_mfma_f32_16x16x32_bf16 v[136:139], v[204:207], a[124:127], v[136:139]\n\t"
    "ds_read_b64_tr_b16 v[222:223], v233 offset:63488\n\t"
    "v_mfma_f32_16x16x32_bf16 v[144:147], v[192:195], a[176:179], v[144:147]\n\t"
    "s_mov_b32 m0, s52\n\t"
    "s_nop 0\n\t"
    "buffer_load_dwordx4 v224, s[40:43], s48 offen offset:0 lds\n\t"
    "v_mfma_f32_16x16x32_bf16 v[144:147], v[196:199], a[180:183], v[144:147]\n\t"
    "v_mfma_f32_16x16x32_bf16 v[144:147], v[200:203], a[184:187], v[144:147]\n\t"
    "v_mfma_f32_16x16x32_bf16 v[144:147], v[204:207], a[188:191], v[144:147]\n\t"
    "s_mov_b32 m0, s52\n\t"
    "s_nop 0\n\t"
    "buffer_load_dwordx4 v225, s[40:43], s48 offen offset:1024 lds\n\t"
    "v_mfma_f32_16x16x32_bf16 v[152:155], v[192:195], a[240:243], v[152:155]\n\t"
    "v_mfma_f32_16x16x32_bf16 v[152:155], v[196:199], a[244:247], v[152:155]\n\t"
    "v_mfma_f32_16x16x32_bf16 v[152:155], v[200:203], a[248:251], v[152:155]\n\t"
    "s_mov_b32 m0, s52\n\t"
    "s_nop 0\n\t"
    "buffer_load_dwordx4 v226, s[40:43], s48 offen offset:2048 lds\n\t"
    "v_mfma_f32_16x16x32_bf16 v[152:155], v[204:207], a[252:255], v[152:155]\n\t"
    "s_waitcnt lgkmcnt(0)\n\t"
    "v_mfma_f32_16x16x32_bf16 v[132:135], v[208:211], a[48:51], v[132:135]\n\t"
    "ds_read_b64_tr_b16 v[192:193], v234 offset:49152\n\t"
    "v_mfma_f32_16x16x32_bf16 v[132:135], v[212:215], a[52:55], v[132:135]\n\t"
    "ds_read_b64_tr_b16 v[194:195], v234 offset:51200\n\t"
    "v_mfma_f32_16x16x32_bf16 v[132:135], v[216:219], a[56:59], v[132:135]\n\t"
    "ds_read_b64_tr_b16 v[196:197], v234 offset:53248\n\t"
    "v_mfma_f32_16x16x32_bf16 v[132:135], v[220:223], a[60:63], v[132:135]\n\t"
    "ds_read_b64_tr_b16 v[198:199], v234 offset:55296\n\t"
    "v_mfma_f32_16x16x32_bf16 v[140:143], v[208:211], a[112:115], v[140:143]\n\t"
    "ds_read_b64_tr_b16 v[200:201], v234 offset:57344\n\t"
    "v_mfma_f32_16x16x32_bf16 v[140:143], v[212:215], a[116:119], v[140:143]\n\t"
    "ds_read_b64_tr_b16 v[202:203], v234 offset:59392\n\t"
    "v_mfma_f32_16x16x32_bf16 v[140:143], v[216:219], a[120:123], v[140:143]\n\t"
    "ds_read_b64_tr_b16 v[204:205], v234 offset:61440\n\t"
    "v_mfma_f32_16x16x32_bf16 v[140:143], v[220:223], a[124:127], v[140:143]\n\t"
    "ds_read_b64_tr_b16 v[206:207], v234 offset:63488\n\t"
    "v_mfma_f32_16x16x32_bf16 v[148:151], v[208:211], a[176:179], v[148:151]\n\t"
    "s_mov_b32 m0, s52\n\t"
    "s_nop 0\n\t"
    "buffer_load_dwordx4 v227, s[40:43], s48 offen offset:3072 lds\n\t"
    "v_mfma_f32_16x16x32_bf16 v[148:151], v[212:215], a[180:183], v[148:151]\n\t"
    "v_mfma_f32_16x16x32_bf16 v[148:151], v[216:219], a[184:187], v[148:151]\n\t"
    "v_mfma_f32_16x16x32_bf16 v[148:151], v[220:223], a[188:191], v[148:151]\n\t"
    "s_mov_b32 m0, s53\n\t"
    "s_add_u32 s48, s48, 16384\n\t"
    "buffer_load_dwordx4 v224, s[40:43], s48 offen offset:0 lds\n\t"
    "v_mfma_f32_16x16x32_bf16 v[156:159], v[208:211], a[240:243], v[156:159]\n\t"
    "v_mfma_f32_16x16x32_bf16 v[156:159], v[212:215], a[244:247], v[156:159]\n\t"
    "v_mfma_f32_16x16x32_bf16 v[156:159], v[216:219], a[248:251], v[156:159]\n\t"
    "s_mov_b32 m0, s53\n\t"
    "s_nop 0\n\t"
    "buffer_load_dwordx4 v225, s[40:43], s48 offen offset:1024 lds\n\t"
    "v_mfma_f32_16x16x32_bf16 v[156:159], v[220:223], a[252:255], v[156:159]\n\t"
    "s_waitcnt lgkmcnt(0)\n\t"
    "v_mfma_f32_16x16x32_bf16 v[160:163], v[192:195], a[48:51], v[160:163]\n\t"
    "ds_read_b64_tr_b16 v[208:209], v235 offset:49152\n\t"
    "v_mfma_f32_16x16x32_bf16 v[160:163], v[196:199], a[52:55], v[160:163]\n\t"
    "ds_read_b64_tr_b16 v[210:211], v235 offset:51200\n\t"
    "v_mfma_f32_16x16x32_bf16 v[160:163], v[200:203], a[56:59], v[160:163]\n\t"
    "ds_read_b64_tr_b16 v[212:213], v235 offset:53248\n\t"
    "v_mfma_f32_16x16x32_bf16 v[160:163], v[204:207], a[60:63], v[160:163]\n\t"
    "ds_read_b64_tr_b16 v[214:215], v235 offset:55296\n\t"
    "v_mfma_f32_16x16x32_bf16 v[168:171], v[192:195], a[112:115], v[168:171]\n\t"
    "ds_read_b64_tr_b16 v[216:217], v235 offset:57344\n\t"
    "v_mfma_f32_16x16x32_bf16 v[168:171], v[196:199], a[116:119], v[168:171]\n\t"
    "ds_read_b64_tr_b16 v[218:219], v235 offset:59392\n\t"
    "v_mfma_f32_16x16x32_bf16 v[168:171], v[200:203], a[120:123], v[168:171]\n\t"
    "ds_read_b64_tr_b16 v[220:221], v235 offset:61440\n\t"
    "v_mfma_f32_16x16x32_bf16 v[168:171], v[204:207], a[124:127], v[168:171]\n\t"
    "ds_read_b64_tr_b16 v[222:223], v235 offset:63488\n\t"
    "v_mfma_f32_16x16x32_bf16 v[176:179], v[192:195], a[176:179], v[176:179]\n\t"
    "s_mov_b32 m0, s53\n\t"
    "s_nop 0\n\t"
    "buffer_load_dwordx4 v226, s[40:43], s48 offen offset:2048 lds\n\t"
    "v_mfma_f32_16x16x32_bf16 v[176:179], v[196:199], a[180:183], v[176:179]\n\t"
    "v_mfma_f32_16x16x32_bf16 v[176:179], v[200:203], a[184:187], v[176:179]\n\t"
    "v_mfma_f32_16x16x32_bf16 v[176:179], v[204:207], a[188:191], v[176:179]\n\t"
    "s_mov_b32 m0, s53\n\t"
    "s_nop 0\n\t"
    "buffer_load_dwordx4 v227, s[40:43], s48 offen offset:3072 lds\n\t"
    "v_mfma_f32_16x16x32_bf16 v[184:187], v[192:195], a[240:243], v[184:187]\n\t"
    "v_mfma_f32_16x16x32_bf16 v[184:187], v[196:199], a[244:247], v[184:187]\n\t"
    "v_mfma_f32_16x16x32_bf16 v[184:187], v[200:203], a[248:251], v[184:187]\n\t"
    "s_mov_b32 m0, s56\n\t"
    "s_nop 0\n\t"
    "buffer_load_dwordx4 v228, s[44:47], s51 offen offset:0 lds\n\t"
    "v_mfma_f32_16x16x32_bf16 v[184:187], v[204:207], a[252:255], v[184:187]\n\t"
    "s_waitcnt vmcnt(17) lgkmcnt(0)\n\t"
    "v_mfma_f32_16x16x32_bf16 v[164:167], v[208:211], a[48:51], v[164:167]\n\t"
    "s_barrier\n\t"
    "ds_read_b64_tr_b16 v[192:193], v236 offset:32768\n\t"
    "v_mfma_f32_16x16x32_bf16 v[164:167], v[212:215], a[52:55], v[164:167]\n\t"
    "ds_read_b64_tr_b16 v[194:195], v236 offset:36864\n\t"
    "v_mfma_f32_16x16x32_bf16 v[164:167], v[216:219], a[56:59], v[164:167]\n\t"
    "ds_read_b64_tr_b16 v[196:197], v236 offset:40960\n\t"
    "v_mfma_f32_16x16x32_bf16 v[164:167], v[220:223], a[60:63], v[164:167]\n\t"
    "ds_read_b64_tr_b16 v[198:199], v236 offset:45056\n\t"
    "v_mfma_f32_16x16x32_bf16 v[172:175], v[208:211], a[112:115], v[172:175]\n\t"
    "ds_read_b64_tr_b16 v[200:201], v236 offset:49152\n\t"
    "v_mfma_f32_16x16x32_bf16 v[172:175], v[212:215], a[116:119], v[172:175]\n\t"
    "ds_read_b64_tr_b16 v[202:203], v236 offset:53248\n\t"
    "v_mfma_f32_16x16x32_bf16 v[172:175], v[216:219], a[120:123], v[172:175]\n\t"
    "ds_read_b64_tr_b16 v[204:205], v236 offset:57344\n\t"
    "v_mfma_f32_16x16x32_bf16 v[172:175], v[220:223], a[124:127], v[172:175]\n\t"
    "ds_read_b64_tr_b16 v[206:207], v236 offset:61440\n\t"
    "v_mfma_f32_16x16x32_bf16 v[180:183], v[208:211], a[176:179], v[180:183]\n\t"
    "s_mov_b32 m0, s56\n\t"
    "s_nop 0\n\t"
    "buffer_load_dwordx4 v229, s[44:47], s51 offen offset:1024 lds\n\t"
    "v_mfma_f32_16x16x32_bf16 v[180:183], v[212:215], a[180:183], v[180:183]\n\t"
    "v_mfma_f32_16x16x32_bf16 v[180:183], v[216:219], a[184:187], v[180:183]\n\t"
    "v_mfma_f32_16x16x32_bf16 v[180:183], v[220:223], a[188:191], v[180:183]\n\t"
    "s_mov_b32 m0, s56\n\t"
    "s_nop 0\n\t"
    "buffer_load_dwordx4 v228, s[44:47], s51 offen offset:2048 lds\n\t"
    "v_mfma_f32_16x16x32_bf16 v[188:191], v[208:211], a[240:243], v[188:191]\n\t"
    "v_mfma_f32_16x16x32_bf16 v[188:191], v[212:215], a[244:247], v[188:191]\n\t"
    "v_mfma_f32_16x16x32_bf16 v[188:191], v[216:219], a[248:251], v[188:191]\n\t"
    "s_mov_b32 m0, s56\n\t"
    "s_nop 0\n\t"
    "buffer_load_dwordx4 v229, s[44:47], s51 offen offset:3072 lds\n\t"
    "v_mfma_f32_16x16x32_bf16 v[188:191], v[220:223], a[252:255], v[188:191]\n\t"
    "s_waitcnt lgkmcnt(0)\n\t"
    "v_mfma_f32_16x16x32_bf16 v[0:3], v[192:195], a[32:35], v[0:3]\n\t"
    "ds_read_b64_tr_b16 v[208:209], v236 offset:32776\n\t"
    "v_mul_f32 v128, v128, v244\n\t"
    "v_mfma_f32_16x16x32_bf16 v[0:3], v[196:199], a[36:39], v[0:3]\n\t"
    "ds_read_b64_tr_b16 v[210:211], v236 offset:36872\n\t"
    "v_mul_f32 v129, v129, v244\n\t"
    "v_mfma_f32_16x16x32_bf16 v[0:3], v[200:203], a[40:43], v[0:3]\n\t"
    "ds_read_b64_tr_b16 v[212:213], v236 offset:40968\n\t"
    "v_mul_f32 v130, v130, v244\n\t"
    "v_mfma_f32_16x16x32_bf16 v[0:3], v[204:207], a[44:47], v[0:3]\n\t"
    "ds_read_b64_tr_b16 v[214:215], v236 offset:45064\n\t"
    "v_mul_f32 v131, v131, v244\n\t"
    "v_mfma_f32_16x16x32_bf16 v[8:11], v[192:195], a[96:99], v[8:11]\n\t"
    "ds_read_b64_tr_b16 v[216:217], v236 offset:49160\n\t"
    "v_mul_f32 v248, v128, v128\n\t"
    "v_mfma_f32_16x16x32_bf16 v[8:11], v[196:199], a[100:103], v[8:11]\n\t"
    "ds_read_b64_tr_b16 v[218:219], v236 offset:53256\n\t"
    "v_mul_f32 v249, v129, v129\n\t"
    "v_mfma_f32_16x16x32_bf16 v[8:11], v[200:203], a[104:107], v[8:11]\n\t"
    "ds_read_b64_tr_b16 v[220:221], v236 offset:57352\n\t"
    "v_mul_f32 v251, v130, v130\n\t"
    "v_mfma_f32_16x16x32_bf16 v[8:11], v[204:207], a[108:111], v[8:11]\n\t"
    "ds_read_b64_tr_b16 v[222:223], v236 offset:61448\n\t"
    "v_mul_f32 v252, v131, v131\n\t"
    "v_mfma_f32_16x16x32_bf16 v[16:19], v[192:195], a[160:163], v[16:19]\n\t"
    "s_mov_b32 m0, s57\n\t"
    "s_add_u32 s51, s51, 16384\n\t"
    "buffer_load_dwordx4 v228, s[44:47], s51 offen offset:0 lds\n\t"
    "v_mfma_f32_16x16x32_bf16 v[16:19], v[196:199], a[164:167], v[16:19]\n\t"
    "v_fmaak_f32 v248, v253, v248, 0xc0135761\n\t"
    "v_fmaak_f32 v249, v253, v249, 0xc0135761\n\t"
    "v_mfma_f32_16x16x32_bf16 v[16:19], v[200:203], a[168:171], v[16:19]\n\t"
    "v_fmaak_f32 v251, v253, v251, 0xc0135761\n\t"
    "v_fmaak_f32 v252, v253, v252, 0xc0135761\n\t"
    "v_mfma_f32_16x16x32_bf16 v[16:19], v[204:207], a[172:175], v[16:19]\n\t"
    "s_mov_b32 m0, s57\n\t"
    "s_nop 0\n\t"
    "buffer_load_dwordx4 v229, s[44:47], s51 offen offset:1024 lds\n\t"
    "v_mfma_f32_16x16x32_bf16 v[24:27], v[192:195], a[224:227], v[24:27]\n\t"
    "v_mul_f32 v248, v128, v248\n\t"
    "v_mul_f32 v249, v129, v249\n\t"
    "v_mfma_f32_16x16x32_bf16 v[24:27], v[196:199], a[228:231], v[24:27]\n\t"
    "v_mul_f32 v251, v130, v251\n\t"
    "v_mul_f32 v252, v131, v252\n\t"
    "v_mfma_f32_16x16x32_bf16 v[24:27], v[200:203], a[232:235], v[24:27]\n\t"
    "s_mov_b32 m0, s57\n\t"
    "s_nop 0\n\t"
    "buffer_load_dwordx4 v228, s[44:47], s51 offen offset:2048 lds\n\t"
    "v_mfma_f32_16x16x32_bf16 v[24:27], v[204:207], a[236:239], v[24:27]\n\t"
    "s_waitcnt lgkmcnt(0)\n\t"
    "v_exp_f32 v248, v248\n\t"
    "v_mfma_f32_16x16x32_bf16 v[4:7], v[208:211], a[32:35], v[4:7]\n\t"
    "ds_read_b64_tr_b16 v[192:193], v237 offset:32768\n\t"
    "v_mfma_f32_16x16x32_bf16 v[4:7], v[212:215], a[36:39], v[4:7]\n\t"
    "ds_read_b64_tr_b16 v[194:195], v237 offset:36864\n\t"
    "v_mfma_f32_16x16x32_bf16 v[4:7], v[216:219], a[40:43], v[4:7]\n\t"
    "ds_read_b64_tr_b16 v[196:197], v237 offset:40960\n\t"
    "v_mfma_f32_16x16x32_bf16 v[4:7], v[220:223], a[44:47], v[4:7]\n\t"
    "ds_read_b64_tr_b16 v[198:199], v237 offset:45056\n\t"
    "v_mfma_f32_16x16x32_bf16 v[12:15], v[208:211], a[96:99], v[12:15]\n\t"
    "ds_read_b64_tr_b16 v[200:201], v237 offset:49152\n\t"
    "v_mfma_f32_16x16x32_bf16 v[12:15], v[212:215], a[100:103], v[12:15]\n\t"
    "ds_read_b64_tr_b16 v[202:203], v237 offset:53248\n\t"
    "v_mfma_f32_16x16x32_bf16 v[12:15], v[216:219], a[104:107], v[12:15]\n\t"
    "ds_read_b64_tr_b16 v[204:205], v237 offset:57344\n\t"
    "v_mfma_f32_16x16x32_bf16 v[12:15], v[220:223], a[108:111], v[12:15]\n\t"
    "ds_read_b64_tr_b16 v[206:207], v237 offset:61440\n\t"
    "v_mfma_f32_16x16x32_bf16 v[20:23], v[208:211], a[160:163], v[20:23]\n\t"
    "s_mov_b32 m0, s57\n\t"
    "s_nop 0\n\t"
    "buffer_load_dwordx4 v229, s[44:47], s51 offen offset:3072 lds\n\t"
    "v_mfma_f32_16x16x32_bf16 v[20:23], v[212:215], a[164:167], v[20:23]\n\t"
    "v_exp_f32 v249, v249\n\t"
    "v_mfma_f32_16x16x32_bf16 v[20:23], v[216:219], a[168:171], v[20:23]\n\t"
    "v_exp_f32 v251, v251\n\t"
    "v_mfma_f32_16x16x32_bf16 v[20:23], v[220:223], a[172:175], v[20:23]\n\t"
    "v_exp_f32 v252, v252\n\t"
    "v_mfma_f32_16x16x32_bf16 v[28:31], v[208:211], a[224:227], v[28:31]\n\t"
    "v_add_f32 v248, 1.0, v248\n\t"
    "v_add_f32 v249, 1.0, v249\n\t"
    "v_mfma_f32_16x16x32_bf16 v[28:31], v[212:215], a[228:231], v[28:31]\n\t"
    "v_add_f32 v251, 1.0, v251\n\t"
    "v_add_f32 v252, 1.0, v252\n\t"
    "v_mfma_f32_16x16x32_bf16 v[28:31], v[216:219], a[232:235], v[28:31]\n\t"
    "v_rcp_f32 v248, v248\n\t"
    "v_mfma_f32_16x16x32_bf16 v[28:31], v[220:223], a[236:239], v[28:31]\n\t"
    "s_waitcnt lgkmcnt(0)\n\t"
    "v_rcp_f32 v249, v249\n\t"
    "v_mfma_f32_16x16x32_bf16 v[32:35], v[192:195], a[32:35], v[32:35]\n\t"
    "ds_read_b64_tr_b16 v[208:209], v237 offset:32776\n\t"
    "v_mfma_f32_16x16x32_bf16 v[32:35], v[196:199], a[36:39], v[32:35]\n\t"
    "ds_read_b64_tr_b16 v[210:211], v237 offset:36872\n\t"
    "v_mfma_f32_16x16x32_bf16 v[32:35], v[200:203], a[40:43], v[32:35]\n\t"
    "ds_read_b64_tr_b16 v[212:213], v237 offset:40968\n\t"
    "v_mfma_f32_16x16x32_bf16 v[32:35], v[204:207], a[44:47], v[32:35]\n\t"
    "ds_read_b64_tr_b16 v[214:215], v237 offset:45064\n\t"
    "v_mfma_f32_16x16x32_bf16 v[40:43], v[192:195], a[96:99], v[40:43]\n\t"
    "ds_read_b64_tr_b16 v[216:217], v237 offset:49160\n\t"
    "v_mfma_f32_16x16x32_bf16 v[40:43], v[196:199], a[100:103], v[40:43]\n\t"
    "ds_read_b64_tr_b16 v[218:219], v237 offset:53256\n\t"
    "v_mfma_f32_16x16x32_bf16 v[40:43], v[200:203], a[104:107], v[40:43]\n\t"
    "ds_read_b64_tr_b16 v[220:221], v237 offset:57352\n\t"
    "v_mfma_f32_16x16x32_bf16 v[40:43], v[204:207], a[108:111], v[40:43]\n\t"
    "ds_read_b64_tr_b16 v[222:223], v237 offset:61448\n\t"
    "v_mfma_f32_16x16x32_bf16 v[48:51], v[192:195], a[160:163], v[48:51]\n\t"
    "v_rcp_f32 v251, v251\n\t"
    "v_mfma_f32_16x16x32_bf16 v[48:51], v[196:199], a[164:167], v[48:51]\n\t"
    "v_rcp_f32 v252, v252\n\t"
    "v_mfma_f32_16x16x32_bf16 v[48:51], v[200:203], a[168:171], v[48:51]\n\t"
    "v_mul_f32 v128, v128, v248\n\t"
    "v_mul_f32 v129, v129, v249\n\t"
    "v_mfma_f32_16x16x32_bf16 v[48:51], v[204:207], a[172:175], v[48:51]\n\t"
    "v_mul_f32 v130, v130, v251\n\t"
    "v_mul_f32 v131, v131, v252\n\t"
    "v_mfma_f32_16x16x32_bf16 v[56:59], v[192:195], a[224:227], v[56:59]\n\t"
    "v_mul_f32 v132, v132, v244\n\t"
    "v_mul_f32 v133, v133, v244\n\t"
    "v_mfma_f32_16x16x32_bf16 v[56:59], v[196:199], a[228:231], v[56:59]\n\t"
    "v_mul_f32 v134, v134, v244\n\t"
    "v_mul_f32 v135, v135, v244\n\t"
    "v_mfma_f32_16x16x32_bf16 v[56:59], v[200:203], a[232:235], v[56:59]\n\t"
    "v_mul_f32 v248, v132, v132\n\t"
    "v_mul_f32 v249, v133, v133\n\t"
    "v_mfma_f32_16x16x32_bf16 v[56:59], v[204:207], a[236:239], v[56:59]\n\t"
    "s_waitcnt lgkmcnt(0)\n\t"
    "v_mul_f32 v251, v134, v134\n\t"
    "v_mul_f32 v252, v135, v135\n\t"
    "v_mfma_f32_16x16x32_bf16 v[36:39], v[208:211], a[32:35], v[36:39]\n\t"
    "ds_read_b64_tr_b16 v[192:193], v238 offset:32768\n\t"
    "v_fmaak_f32 v248, v253, v248, 0xc0135761\n\t"
    "v_mfma_f32_16x16x32_bf16 v[36:39], v[212:215], a[36:39], v[36:39]\n\t"
    "ds_read_b64_tr_b16 v[194:195], v238 offset:36864\n\t"
    "v_fmaak_f32 v249, v253, v249, 0xc0135761\n\t"
    "v_mfma_f32_16x16x32_bf16 v[36:39], v[216:219], a[40:43], v[36:39]\n\t"
    "ds_read_b64_tr_b16 v[196:197], v238 offset:40960\n\t"
    "v_fmaak_f32 v251, v253, v251, 0xc0135761\n\t"
    "v_mfma_f32_16x16x32_bf16 v[36:39], v[220:223], a[44:47], v[36:39]\n\t"
    "ds_read_b64_tr_b16 v[198:199], v238 offset:45056\n\t"
    "v_fmaak_f32 v252, v253, v252, 0xc0135761\n\t"
    "v_mfma_f32_16x16x32_bf16 v[44:47], v[208:211], a[96:99], v[44:47]\n\t"
    "ds_read_b64_tr_b16 v[200:201], v238 offset:49152\n\t"
    "v_mul_f32 v248, v132, v248\n\t"
    "v_mfma_f32_16x16x32_bf16 v[44:47], v[212:215], a[100:103], v[44:47]\n\t"
    "ds_read_b64_tr_b16 v[202:203], v238 offset:53248\n\t"
    "v_mul_f32 v249, v133, v249\n\t"
    "v_mfma_f32_16x16x32_bf16 v[44:47], v[216:219], a[104:107], v[44:47]\n\t"
    "ds_read_b64_tr_b16 v[204:205], v238 offset:57344\n\t"
    "v_mul_f32 v251, v134, v251\n\t"
    "v_mfma_f32_16x16x32_bf16 v[44:47], v[220:223], a[108:111], v[44:47]\n\t"
    "ds_read_b64_tr_b16 v[206:207], v238 offset:61440\n\t"
    "v_mul_f32 v252, v135, v252\n\t"
    "v_mfma_f32_16x16x32_bf16 v[52:55], v[208:211], a[160:163], v[52:55]\n\t"
    "v_exp_f32 v248, v248\n\t"
    "v_mfma_f32_16x16x32_bf16 v[52:55], v[212:215], a[164:167], v[52:55]\n\t"
    "v_exp_f32 v249, v249\n\t"
    "v_mfma_f32_16x16x32_bf16 v[52:55], v[216:219], a[168:171], v[52:55]\n\t"
    "v_exp_f32 v251, v251\n\t"
    "v_mfma_f32_16x16x32_bf16 v[52:55], v[220:223], a[172:175], v[52:55]\n\t"
    "v_exp_f32 v252, v252\n\t"
    "v_mfma_f32_16x16x32_bf16 v[60:63], v[208:211], a[224:227], v[60:63]\n\t"
    "v_add_f32 v248, 1.0, v248\n\t"
    "v_add_f32 v249, 1.0, v249\n\t"
    "v_mfma_f32_16x16x32_bf16 v[60:63], v[212:215], a[228:231], v[60:63]\n\t"
    "v_add_f32 v251, 1.0, v251\n\t"
    "v_add_f32 v252, 1.0, v252\n\t"
    "v_mfma_f32_16x16x32_bf16 v[60:63], v[216:219], a[232:235], v[60:63]\n\t"
    "v_rcp_f32 v248, v248\n\t"
    "v_mfma_f32_16x16x32_bf16 v[60:63], v[220:223], a[236:239], v[60:63]\n\t"
    "s_waitcnt lgkmcnt(0)\n\t"
    "v_rcp_f32 v249, v249\n\t"
    "v_mfma_f32_16x16x32_bf16 v[64:67], v[192:195], a[32:35], v[64:67]\n\t"
    "ds_read_b64_tr_b16 v[208:209], v238 offset:32776\n\t"
    "v_mfma_f32_16x16x32_bf16 v[64:67], v[196:199], a[36:39], v[64:67]\n\t"
    "ds_read_b64_tr_b16 v[210:211], v238 offset:36872\n\t"
    "v_mfma_f32_16x16x32_bf16 v[64:67], v[200:203], a[40:43], v[64:67]\n\t"
    "ds_read_b64_tr_b16 v[212:213], v238 offset:40968\n\t"
    "v_mfma_f32_16x16x32_bf16 v[64:67], v[204:207], a[44:47], v[64:67]\n\t"
    "ds_read_b64_tr_b16 v[214:215], v238 offset:45064\n\t"
    "v_mfma_f32_16x16x32_bf16 v[72:75], v[192:195], a[96:99], v[72:75]\n\t"
    "ds_read_b64_tr_b16 v[216:217], v238 offset:49160\n\t"
    "v_mfma_f32_16x16x32_bf16 v[72:75], v[196:199], a[100:103], v[72:75]\n\t"
    "ds_read_b64_tr_b16 v[218:219], v238 offset:53256\n\t"
    "v_mfma_f32_16x16x32_bf16 v[72:75], v[200:203], a[104:107], v[72:75]\n\t"
    "ds_read_b64_tr_b16 v[220:221], v238 offset:57352\n\t"
    "v_mfma_f32_16x16x32_bf16 v[72:75], v[204:207], a[108:111], v[72:75]\n\t"
    "ds_read_b64_tr_b16 v[222:223], v238 offset:61448\n\t"
    "v_mfma_f32_16x16x32_bf16 v[80:83], v[192:195], a[160:163], v[80:83]\n\t"
    "v_rcp_f32 v251, v251\n\t"
    "v_mfma_f32_16x16x32_bf16 v[80:83], v[196:199], a[164:167], v[80:83]\n\t"
    "v_rcp_f32 v252, v252\n\t"
    "v_mfma_f32_16x16x32_bf16 v[80:83], v[200:203], a[168:171], v[80:83]\n\t"
    "v_mul_f32 v132, v132, v248\n\t"
    "v_mul_f32 v133, v133, v249\n\t"
    "v_mfma_f32_16x16x32_bf16 v[80:83], v[204:207], a[172:175], v[80:83]\n\t"
    "v_mul_f32 v134, v134, v251\n\t"
    "v_mul_f32 v135, v135, v252\n\t"
    "v_mfma_f32_16x16x32_bf16 v[88:91], v[192:195], a[224:227], v[88:91]\n\t"
    "v_cvt_pk_bf16_f32 v128, v128, v129\n\t"
    "v_cvt_pk_bf16_f32 v129, v130, v131\n\t"
    "v_mfma_f32_16x16x32_bf16 v[88:91], v[196:199], a[228:231], v[88:91]\n\t"
    "v_cvt_pk_bf16_f32 v130, v132, v133\n\t"
    "v_cvt_pk_bf16_f32 v131, v134, v135\n\t"
    "s_add_u32 s77, s84, 0\n\t"
    "buffer_store_dwordx4 v[128:131], v255, s[72:75], s77 offen sc1\n\t"
    "v_mfma_f32_16x16x32_bf16 v[88:91], v[200:203], a[232:235], v[88:91]\n\t"
    "v_mul_f32 v136, v136, v245\n\t"
    "v_mul_f32 v137, v137, v245\n\t"
    "v_mfma_f32_16x16x32_bf16 v[88:91], v[204:207], a[236:239], v[88:91]\n\t"
    "s_waitcnt lgkmcnt(0)\n\t"
    "v_mul_f32 v138, v138, v245\n\t"
    "v_mul_f32 v139, v139, v245\n\t"
    "v_mfma_f32_16x16x32_bf16 v[68:71], v[208:211], a[32:35], v[68:71]\n\t"
    "ds_read_b64_tr_b16 v[192:193], v239 offset:32768\n\t"
    "v_mul_f32 v248, v136, v136\n\t"
    "v_mfma_f32_16x16x32_bf16 v[68:71], v[212:215], a[36:39], v[68:71]\n\t"
    "ds_read_b64_tr_b16 v[194:195], v239 offset:36864\n\t"
    "v_mul_f32 v249, v137, v137\n\t"
    "v_mfma_f32_16x16x32_bf16 v[68:71], v[216:219], a[40:43], v[68:71]\n\t"
    "ds_read_b64_tr_b16 v[196:197], v239 offset:40960\n\t"
    "v_mul_f32 v251, v138, v138\n\t"
    "v_mfma_f32_16x16x32_bf16 v[68:71], v[220:223], a[44:47], v[68:71]\n\t"
    "ds_read_b64_tr_b16 v[198:199], v239 offset:45056\n\t"
    "v_mul_f32 v252, v139, v139\n\t"
    "v_mfma_f32_16x16x32_bf16 v[76:79], v[208:211], a[96:99], v[76:79]\n\t"
    "ds_read_b64_tr_b16 v[200:201], v239 offset:49152\n\t"
    "v_fmaak_f32 v248, v253, v248, 0xc0135761\n\t"
    "v_mfma_f32_16x16x32_bf16 v[76:79], v[212:215], a[100:103], v[76:79]\n\t"
    "ds_read_b64_tr_b16 v[202:203], v239 offset:53248\n\t"
    "v_fmaak_f32 v249, v253, v249, 0xc0135761\n\t"
    "v_mfma_f32_16x16x32_bf16 v[76:79], v[216:219], a[104:107], v[76:79]\n\t"
    "ds_read_b64_tr_b16 v[204:205], v239 offset:57344\n\t"
    "v_fmaak_f32 v251, v253, v251, 0xc0135761\n\t"
    "v_mfma_f32_16x16x32_bf16 v[76:79], v[220:223], a[108:111], v[76:79]\n\t"
    "ds_read_b64_tr_b16 v[206:207], v239 offset:61440\n\t"
    "v_fmaak_f32 v252, v253, v252, 0xc0135761\n\t"
    "v_mfma_f32_16x16x32_bf16 v[84:87], v[208:211], a[160:163], v[84:87]\n\t"
    "v_mul_f32 v248, v136, v248\n\t"
    "v_mul_f32 v249, v137, v249\n\t"
    "v_mfma_f32_16x16x32_bf16 v[84:87], v[212:215], a[164:167], v[84:87]\n\t"
    "v_mul_f32 v251, v138, v251\n\t"
    "v_mul_f32 v252, v139, v252\n\t"
    "v_mfma_f32_16x16x32_bf16 v[84:87], v[216:219], a[168:171], v[84:87]\n\t"
    "v_exp_f32 v248, v248\n\t"
    "v_mfma_f32_16x16x32_bf16 v[84:87], v[220:223], a[172:175], v[84:87]\n\t"
    "v_exp_f32 v249, v249\n\t"
    "v_mfma_f32_16x16x32_bf16 v[92:95], v[208:211], a[224:227], v[92:95]\n\t"
    "v_exp_f32 v251, v251\n\t"
    "v_mfma_f32_16x16x32_bf16 v[92:95], v[212:215], a[228:231], v[92:95]\n\t"
    "v_exp_f32 v252, v252\n\t"
    "v_mfma_f32_16x16x32_bf16 v[92:95], v[216:219], a[232:235], v[92:95]\n\t"
    "v_add_f32 v248, 1.0, v248\n\t"
    "v_add_f32 v249, 1.0, v249\n\t"
    "v_mfma_f32_16x16x32_bf16 v[92:95], v[220:223], a[236:239], v[92:95]\n\t"
    "s_waitcnt lgkmcnt(0)\n\t"
    "v_add_f32 v251, 1.0, v251\n\t"
    "v_add_f32 v252, 1.0, v252\n\t"
    "v_mfma_f32_16x16x32_bf16 v[96:99], v[192:195], a[32:35], v[96:99]\n\t"
    "ds_read_b64_tr_b16 v[208:209], v239 offset:32776\n\t"
    "v_mfma_f32_16x16x32_bf16 v[96:99], v[196:199], a[36:39], v[96:99]\n\t"
    "ds_read_b64_tr_b16 v[210:211], v239 offset:36872\n\t"
    "v_mfma_f32_16x16x32_bf16 v[96:99], v[200:203], a[40:43], v[96:99]\n\t"
    "ds_read_b64_tr_b16 v[212:213], v239 offset:40968\n\t"
    "v_mfma_f32_16x16x32_bf16 v[96:99], v[204:207], a[44:47], v[96:99]\n\t"
    "ds_read_b64_tr_b16 v[214:215], v239 offset:45064\n\t"
    "v_mfma_f32_16x16x32_bf16 v[104:107], v[192:195], a[96:99], v[104:107]\n\t"
    "ds_read_b64_tr_b16 v[216:217], v239 offset:49160\n\t"
    "v_mfma_f32_16x16x32_bf16 v[104:107], v[196:199], a[100:103], v[104:107]\n\t"
    "ds_read_b64_tr_b16 v[218:219], v239 offset:53256\n\t"
    "v_mfma_f32_16x16x32_bf16 v[104:107], v[200:203], a[104:107], v[104:107]\n\t"
    "ds_read_b64_tr_b16 v[220:221], v239 offset:57352\n\t"
    "v_mfma_f32_16x16x32_bf16 v[104:107], v[204:207], a[108:111], v[104:107]\n\t"
    "ds_read_b64_tr_b16 v[222:223], v239 offset:61448\n\t"
    "v_mfma_f32_16x16x32_bf16 v[112:115], v[192:195], a[160:163], v[112:115]\n\t"
    "v_rcp_f32 v248, v248\n\t"
    "v_mfma_f32_16x16x32_bf16 v[112:115], v[196:199], a[164:167], v[112:115]\n\t"
    "v_rcp_f32 v249, v249\n\t"
    "v_mfma_f32_16x16x32_bf16 v[112:115], v[200:203], a[168:171], v[112:115]\n\t"
    "v_rcp_f32 v251, v251\n\t"
    "v_mfma_f32_16x16x32_bf16 v[112:115], v[204:207], a[172:175], v[112:115]\n\t"
    "v_rcp_f32 v252, v252\n\t"
    "v_mfma_f32_16x16x32_bf16 v[120:123], v[192:195], a[224:227], v[120:123]\n\t"
    "v_mul_f32 v136, v136, v248\n\t"
    "v_mul_f32 v137, v137, v249\n\t"
    "v_mfma_f32_16x16x32_bf16 v[120:123], v[196:199], a[228:231], v[120:123]\n\t"
    "v_mul_f32 v138, v138, v251\n\t"
    "v_mul_f32 v139, v139, v252\n\t"
    "v_mfma_f32_16x16x32_bf16 v[120:123], v[200:203], a[232:235], v[120:123]\n\t"
    "v_mul_f32 v140, v140, v245\n\t"
    "v_mul_f32 v141, v141, v245\n\t"
    "v_mfma_f32_16x16x32_bf16 v[120:123], v[204:207], a[236:239], v[120:123]\n\t"
    "s_waitcnt vmcnt(17) lgkmcnt(0)\n\t"
    "v_mul_f32 v142, v142, v245\n\t"
    "v_mul_f32 v143, v143, v245\n\t"
    "v_mfma_f32_16x16x32_bf16 v[100:103], v[208:211], a[32:35], v[100:103]\n\t"
    "s_barrier\n\t"
    "ds_read_b64_tr_b16 v[192:193], v240 offset:32768\n\t"
    "v_mul_f32 v248, v140, v140\n\t"
    "v_mfma_f32_16x16x32_bf16 v[100:103], v[212:215], a[36:39], v[100:103]\n\t"
    "ds_read_b64_tr_b16 v[194:195], v240 offset:36864\n\t"
    "v_mul_f32 v249, v141, v141\n\t"
    "v_mfma_f32_16x16x32_bf16 v[100:103], v[216:219], a[40:43], v[100:103]\n\t"
    "ds_read_b64_tr_b16 v[196:197], v240 offset:40960\n\t"
    "v_mul_f32 v251, v142, v142\n\t"
    "v_mfma_f32_16x16x32_bf16 v[100:103], v[220:223], a[44:47], v[100:103]\n\t"
    "ds_read_b64_tr_b16 v[198:199], v240 offset:45056\n\t"
    "v_mul_f32 v252, v143, v143\n\t"
    "v_mfma_f32_16x16x32_bf16 v[108:111], v[208:211], a[96:99], v[108:111]\n\t"
    "ds_read_b64_tr_b16 v[200:201], v240 offset:49152\n\t"
    "v_fmaak_f32 v248, v253, v248, 0xc0135761\n\t"
    "v_mfma_f32_16x16x32_bf16 v[108:111], v[212:215], a[100:103], v[108:111]\n\t"
    "ds_read_b64_tr_b16 v[202:203], v240 offset:53248\n\t"
    "v_fmaak_f32 v249, v253, v249, 0xc0135761\n\t"
    "v_mfma_f32_16x16x32_bf16 v[108:111], v[216:219], a[104:107], v[108:111]\n\t"
    "ds_read_b64_tr_b16 v[204:205], v240 offset:57344\n\t"
    "v_fmaak_f32 v251, v253, v251, 0xc0135761\n\t"
    "v_mfma_f32_16x16x32_bf16 v[108:111], v[220:223], a[108:111], v[108:111]\n\t"
    "ds_read_b64_tr_b16 v[206:207], v240 offset:61440\n\t"
    "v_fmaak_f32 v252, v253, v252, 0xc0135761\n\t"
    "v_mfma_f32_16x16x32_bf16 v[116:119], v[208:211], a[160:163], v[116:119]\n\t"
    "s_mov_b32 m0, s54\n\t"
    "s_add_u32 s48, s48, 16384\n\t"
    "buffer_load_dwordx4 v224, s[40:43], s48 offen offset:0 lds\n\t"
    "v_mfma_f32_16x16x32_bf16 v[116:119], v[212:215], a[164:167], v[116:119]\n\t"
    "v_mul_f32 v248, v140, v248\n\t"
    "v_mul_f32 v249, v141, v249\n\t"
    "v_mfma_f32_16x16x32_bf16 v[116:119], v[216:219], a[168:171], v[116:119]\n\t"
    "v_mul_f32 v251, v142, v251\n\t"
    "v_mul_f32 v252, v143, v252\n\t"
    "v_mfma_f32_16x16x32_bf16 v[116:119], v[220:223], a[172:175], v[116:119]\n\t"
    "s_mov_b32 m0, s54\n\t"
    "s_nop 0\n\t"
    "buffer_load_dwordx4 v225, s[40:43], s48 offen offset:1024 lds\n\t"
    "v_mfma_f32_16x16x32_bf16 v[124:127], v[208:211], a[224:227], v[124:127]\n\t"
    "v_exp_f32 v248, v248\n\t"
    "v_mfma_f32_16x16x32_bf16 v[124:127], v[212:215], a[228:231], v[124:127]\n\t"
    "v_exp_f32 v249, v249\n\t"
    "v_mfma_f32_16x16x32_bf16 v[124:127], v[216:219], a[232:235], v[124:127]\n\t"
    "s_mov_b32 m0, s54\n\t"
    "s_nop 0\n\t"
    "buffer_load_dwordx4 v226, s[40:43], s48 offen offset:2048 lds\n\t"
    "v_mfma_f32_16x16x32_bf16 v[124:127], v[220:223], a[236:239], v[124:127]\n\t"
    "s_waitcnt lgkmcnt(0)\n\t"
    "v_exp_f32 v251, v251\n\t"
    "v_mfma_f32_16x16x32_bf16 v[0:3], v[192:195], a[48:51], v[0:3]\n\t"
    "ds_read_b64_tr_b16 v[208:209], v240 offset:32776\n\t"
    "v_mfma_f32_16x16x32_bf16 v[0:3], v[196:199], a[52:55], v[0:3]\n\t"
    "ds_read_b64_tr_b16 v[210:211], v240 offset:36872\n\t"
    "v_mfma_f32_16x16x32_bf16 v[0:3], v[200:203], a[56:59], v[0:3]\n\t"
    "ds_read_b64_tr_b16 v[212:213], v240 offset:40968\n\t"
    "v_mfma_f32_16x16x32_bf16 v[0:3], v[204:207], a[60:63], v[0:3]\n\t"
    "ds_read_b64_tr_b16 v[214:215], v240 offset:45064\n\t"
    "v_mfma_f32_16x16x32_bf16 v[8:11], v[192:195], a[112:115], v[8:11]\n\t"
    "ds_read_b64_tr_b16 v[216:217], v240 offset:49160\n\t"
    "v_mfma_f32_16x16x32_bf16 v[8:11], v[196:199], a[116:119], v[8:11]\n\t"
    "ds_read_b64_tr_b16 v[218:219], v240 offset:53256\n\t"
    "v_mfma_f32_16x16x32_bf16 v[8:11], v[200:203], a[120:123], v[8:11]\n\t"
    "ds_read_b64_tr_b16 v[220:221], v240 offset:57352\n\t"
    "v_mfma_f32_16x16x32_bf16 v[8:11], v[204:207], a[124:127], v[8:11]\n\t"
    "ds_read_b64_tr_b16 v[222:223], v240 offset:61448\n\t"
    "v_mfma_f32_16x16x32_bf16 v[16:19], v[192:195], a[176:179], v[16:19]\n\t"
    "s_mov_b32 m0, s54\n\t"
    "s_nop 0\n\t"
    "buffer_load_dwordx4 v227, s[40:43], s48 offen offset:3072 lds\n\t"
    "v_mfma_f32_16x16x32_bf16 v[16:19], v[196:199], a[180:183], v[16:19]\n\t"
    "v_exp_f32 v252, v252\n\t"
    "v_mfma_f32_16x16x32_bf16 v[16:19], v[200:203], a[184:187], v[16:19]\n\t"
    "v_add_f32 v248, 1.0, v248\n\t"
    "v_add_f32 v249, 1.0, v249\n\t"
    "v_mfma_f32_16x16x32_bf16 v[16:19], v[204:207], a[188:191], v[16:19]\n\t"
    "v_add_f32 v251, 1.0, v251\n\t"
    "v_add_f32 v252, 1.0, v252\n\t"
    "v_mfma_f32_16x16x32_bf16 v[24:27], v[192:195], a[240:243], v[24:27]\n\t"
    "v_rcp_f32 v248, v248\n\t"
    "v_mfma_f32_16x16x32_bf16 v[24:27], v[196:199], a[244:247], v[24:27]\n\t"
    "v_rcp_f32 v249, v249\n\t"
    "v_mfma_f32_16x16x32_bf16 v[24:27], v[200:203], a[248:251], v[24:27]\n\t"
    "v_rcp_f32 v251, v251\n\t"
    "v_mfma_f32_16x16x32_bf16 v[24:27], v[204:207], a[252:255], v[24:27]\n\t"
    "s_waitcnt lgkmcnt(0)\n\t"
    "v_rcp_f32 v252, v252\n\t"
    "v_mfma_f32_16x16x32_bf16 v[4:7], v[208:211], a[48:51], v[4:7]\n\t"
    "ds_read_b64_tr_b16 v[192:193], v241 offset:32768\n\t"
    "v_mul_f32 v140, v140, v248\n\t"
    "v_mfma_f32_16x16x32_bf16 v[4:7], v[212:215], a[52:55], v[4:7]\n\t"
    "ds_read_b64_tr_b16 v[194:195], v241 offset:36864\n\t"
    "v_mul_f32 v141, v141, v249\n\t"
    "v_mfma_f32_16x16x32_bf16 v[4:7], v[216:219], a[56:59], v[4:7]\n\t"
    "ds_read_b64_tr_b16 v[196:197], v241 offset:40960\n\t"
    "v_mul_f32 v142, v142, v251\n\t"
    "v_mfma_f32_16x16x32_bf16 v[4:7], v[220:223], a[60:63], v[4:7]\n\t"
    "ds_read_b64_tr_b16 v[198:199], v241 offset:45056\n\t"
    "v_mul_f32 v143, v143, v252\n\t"
    "v_mfma_f32_16x16x32_bf16 v[12:15], v[208:211], a[112:115], v[12:15]\n\t"
    "ds_read_b64_tr_b16 v[200:201], v241 offset:49152\n\t"
    "v_cvt_pk_bf16_f32 v136, v136, v137\n\t"
    "v_mfma_f32_16x16x32_bf16 v[12:15], v[212:215], a[116:119], v[12:15]\n\t"
    "ds_read_b64_tr_b16 v[202:203], v241 offset:53248\n\t"
    "v_cvt_pk_bf16_f32 v137, v138, v139\n\t"
    "v_mfma_f32_16x16x32_bf16 v[12:15], v[216:219], a[120:123], v[12:15]\n\t"
    "ds_read_b64_tr_b16 v[204:205], v241 offset:57344\n\t"
    "v_cvt_pk_bf16_f32 v138, v140, v141\n\t"
    "v_mfma_f32_16x16x32_bf16 v[12:15], v[220:223], a[124:127], v[12:15]\n\t"
    "ds_read_b64_tr_b16 v[206:207], v241 offset:61440\n\t"
    "v_cvt_pk_bf16_f32 v139, v142, v143\n\t"
    "s_add_u32 s77, s84, 57344\n\t"
    "buffer_store_dwordx4 v[136:139], v255, s[72:75], s77 offen sc1\n\t"
    "v_mfma_f32_16x16x32_bf16 v[20:23], v[208:211], a[176:179], v[20:23]\n\t"
    "v_mul_f32 v144, v144, v246\n\t"
    "v_mul_f32 v145, v145, v246\n\t"
    "v_mfma_f32_16x16x32_bf16 v[20:23], v[212:215], a[180:183], v[20:23]\n\t"
    "v_mul_f32 v146, v146, v246\n\t"
    "v_mul_f32 v147, v147, v246\n\t"
    "v_mfma_f32_16x16x32_bf16 v[20:23], v[216:219], a[184:187], v[20:23]\n\t"
    "v_mul_f32 v248, v144, v144\n\t"
    "v_mul_f32 v249, v145, v145\n\t"
    "v_mfma_f32_16x16x32_bf16 v[20:23], v[220:223], a[188:191], v[20:23]\n\t"
    "v_mul_f32 v251, v146, v146\n\t"
    "v_mul_f32 v252, v147, v147\n\t"
    "v_mfma_f32_16x16x32_bf16 v[28:31], v[208:211], a[240:243], v[28:31]\n\t"
    "v_fmaak_f32 v248, v253, v248, 0xc0135761\n\t"
    "v_fmaak_f32 v249, v253, v249, 0xc0135761\n\t"
    "v_mfma_f32_16x16x32_bf16 v[28:31], v[212:215], a[244:247], v[28:31]\n\t"
    "v_fmaak_f32 v251, v253, v251, 0xc0135761\n\t"
    "v_fmaak_f32 v252, v253, v252, 0xc0135761\n\t"
    "v_mfma_f32_16x16x32_bf16 v[28:31], v[216:219], a[248:251], v[28:31]\n\t"
    "v_mul_f32 v248, v144, v248\n\t"
    "v_mul_f32 v249, v145, v249\n\t"
    "v_mfma_f32_16x16x32_bf16 v[28:31], v[220:223], a[252:255], v[28:31]\n\t"
    "s_waitcnt lgkmcnt(0)\n\t"
    "v_mul_f32 v251, v146, v251\n\t"
    "v_mul_f32 v252, v147, v252\n\t"
    "v_mfma_f32_16x16x32_bf16 v[32:35], v[192:195], a[48:51], v[32:35]\n\t"
    "ds_read_b64_tr_b16 v[208:209], v241 offset:32776\n\t"
    "v_mfma_f32_16x16x32_bf16 v[32:35], v[196:199], a[52:55], v[32:35]\n\t"
    "ds_read_b64_tr_b16 v[210:211], v241 offset:36872\n\t"
    "v_mfma_f32_16x16x32_bf16 v[32:35], v[200:203], a[56:59], v[32:35]\n\t"
    "ds_read_b64_tr_b16 v[212:213], v241 offset:40968\n\t"
    "v_mfma_f32_16x16x32_bf16 v[32:35], v[204:207], a[60:63], v[32:35]\n\t"
    "ds_read_b64_tr_b16 v[214:215], v241 offset:45064\n\t"
    "v_mfma_f32_16x16x32_bf16 v[40:43], v[192:195], a[112:115], v[40:43]\n\t"
    "ds_read_b64_tr_b16 v[216:217], v241 offset:49160\n\t"
    "v_mfma_f32_16x16x32_bf16 v[40:43], v[196:199], a[116:119], v[40:43]\n\t"
    "ds_read_b64_tr_b16 v[218:219], v241 offset:53256\n\t"
    "v_mfma_f32_16x16x32_bf16 v[40:43], v[200:203], a[120:123], v[40:43]\n\t"
    "ds_read_b64_tr_b16 v[220:221], v241 offset:57352\n\t"
    "v_mfma_f32_16x16x32_bf16 v[40:43], v[204:207], a[124:127], v[40:43]\n\t"
    "ds_read_b64_tr_b16 v[222:223], v241 offset:61448\n\t"
    "v_mfma_f32_16x16x32_bf16 v[48:51], v[192:195], a[176:179], v[48:51]\n\t"
    "v_exp_f32 v248, v248\n\t"
    "v_mfma_f32_16x16x32_bf16 v[48:51], v[196:199], a[180:183], v[48:51]\n\t"
    "v_exp_f32 v249, v249\n\t"
    "v_mfma_f32_16x16x32_bf16 v[48:51], v[200:203], a[184:187], v[48:51]\n\t"
    "v_exp_f32 v251, v251\n\t"
    "v_mfma_f32_16x16x32_bf16 v[48:51], v[204:207], a[188:191], v[48:51]\n\t"
    "v_exp_f32 v252, v252\n\t"
    "v_mfma_f32_16x16x32_bf16 v[56:59], v[192:195], a[240:243], v[56:59]\n\t"
    "v_add_f32 v248, 1.0, v248\n\t"
    "v_add_f32 v249, 1.0, v249\n\t"
    "v_mfma_f32_16x16x32_bf16 v[56:59], v[196:199], a[244:247], v[56:59]\n\t"
    "v_add_f32 v251, 1.0, v251\n\t"
    "v_add_f32 v252, 1.0, v252\n\t"
    "v_mfma_f32_16x16x32_bf16 v[56:59], v[200:203], a[248:251], v[56:59]\n\t"
    "v_rcp_f32 v248, v248\n\t"
    "v_mfma_f32_16x16x32_bf16 v[56:59], v[204:207], a[252:255], v[56:59]\n\t"
    "s_waitcnt lgkmcnt(0)\n\t"
    "v_rcp_f32 v249, v249\n\t"
    "v_mfma_f32_16x16x32_bf16 v[36:39], v[208:211], a[48:51], v[36:39]\n\t"
    "ds_read_b64_tr_b16 v[192:193], v242 offset:32768\n\t"
    "v_mfma_f32_16x16x32_bf16 v[36:39], v[212:215], a[52:55], v[36:39]\n\t"
    "ds_read_b64_tr_b16 v[194:195], v242 offset:36864\n\t"
    "v_mfma_f32_16x16x32_bf16 v[36:39], v[216:219], a[56:59], v[36:39]\n\t"
    "ds_read_b64_tr_b16 v[196:197], v242 offset:40960\n\t"
    "v_mfma_f32_16x16x32_bf16 v[36:39], v[220:223], a[60:63], v[36:39]\n\t"
    "ds_read_b64_tr_b16 v[198:199], v242 offset:45056\n\t"
    "v_mfma_f32_16x16x32_bf16 v[44:47], v[208:211], a[112:115], v[44:47]\n\t"
    "ds_read_b64_tr_b16 v[200:201], v242 offset:49152\n\t"
    "v_mfma_f32_16x16x32_bf16 v[44:47], v[212:215], a[116:119], v[44:47]\n\t"
    "ds_read_b64_tr_b16 v[202:203], v242 offset:53248\n\t"
    "v_mfma_f32_16x16x32_bf16 v[44:47], v[216:219], a[120:123], v[44:47]\n\t"
    "ds_read_b64_tr_b16 v[204:205], v242 offset:57344\n\t"
    "v_mfma_f32_16x16x32_bf16 v[44:47], v[220:223], a[124:127], v[44:47]\n\t"
    "ds_read_b64_tr_b16 v[206:207], v242 offset:61440\n\t"
    "v_mfma_f32_16x16x32_bf16 v[52:55], v[208:211], a[176:179], v[52:55]\n\t"
    "v_rcp_f32 v251, v251\n\t"
    "v_mfma_f32_16x16x32_bf16 v[52:55], v[212:215], a[180:183], v[52:55]\n\t"
    "v_rcp_f32 v252, v252\n\t"
    "v_mfma_f32_16x16x32_bf16 v[52:55], v[216:219], a[184:187], v[52:55]\n\t"
    "v_mul_f32 v144, v144, v248\n\t"
    "v_mul_f32 v145, v145, v249\n\t"
    "v_mfma_f32_16x16x32_bf16 v[52:55], v[220:223], a[188:191], v[52:55]\n\t"
    "v_mul_f32 v146, v146, v251\n\t"
    "v_mul_f32 v147, v147, v252\n\t"
    "v_mfma_f32_16x16x32_bf16 v[60:63], v[208:211], a[240:243], v[60:63]\n\t"
    "v_mul_f32 v148, v148, v246\n\t"
    "v_mul_f32 v149, v149, v246\n\t"
    "v_mfma_f32_16x16x32_bf16 v[60:63], v[212:215], a[244:247], v[60:63]\n\t"
    "v_mul_f32 v150, v150, v246\n\t"
    "v_mul_f32 v151, v151, v246\n\t"
    "v_mfma_f32_16x16x32_bf16 v[60:63], v[216:219], a[248:251], v[60:63]\n\t"
    "v_mul_f32 v248, v148, v148\n\t"
    "v_mul_f32 v249, v149, v149\n\t"
    "v_mfma_f32_16x16x32_bf16 v[60:63], v[220:223], a[252:255], v[60:63]\n\t"
    "s_waitcnt lgkmcnt(0)\n\t"
    "v_mul_f32 v251, v150, v150\n\t"
    "v_mul_f32 v252, v151, v151\n\t"
    "v_mfma_f32_16x16x32_bf16 v[64:67], v[192:195], a[48:51], v[64:67]\n\t"
    "ds_read_b64_tr_b16 v[208:209], v242 offset:32776\n\t"
    "v_fmaak_f32 v248, v253, v248, 0xc0135761\n\t"
    "v_mfma_f32_16x16x32_bf16 v[64:67], v[196:199], a[52:55], v[64:67]\n\t"
    "ds_read_b64_tr_b16 v[210:211], v242 offset:36872\n\t"
    "v_fmaak_f32 v249, v253, v249, 0xc0135761\n\t"
    "v_mfma_f32_16x16x32_bf16 v[64:67], v[200:203], a[56:59], v[64:67]\n\t"
    "ds_read_b64_tr_b16 v[212:213], v242 offset:40968\n\t"
    "v_fmaak_f32 v251, v253, v251, 0xc0135761\n\t"
    "v_mfma_f32_16x16x32_bf16 v[64:67], v[204:207], a[60:63], v[64:67]\n\t"
    "ds_read_b64_tr_b16 v[214:215], v242 offset:45064\n\t"
    "v_fmaak_f32 v252, v253, v252, 0xc0135761\n\t"
    "v_mfma_f32_16x16x32_bf16 v[72:75], v[192:195], a[112:115], v[72:75]\n\t"
    "ds_read_b64_tr_b16 v[216:217], v242 offset:49160\n\t"
    "v_mul_f32 v248, v148, v248\n\t"
    "v_mfma_f32_16x16x32_bf16 v[72:75], v[196:199], a[116:119], v[72:75]\n\t"
    "ds_read_b64_tr_b16 v[218:219], v242 offset:53256\n\t"
    "v_mul_f32 v249, v149, v249\n\t"
    "v_mfma_f32_16x16x32_bf16 v[72:75], v[200:203], a[120:123], v[72:75]\n\t"
    "ds_read_b64_tr_b16 v[220:221], v242 offset:57352\n\t"
    "v_mul_f32 v251, v150, v251\n\t"
    "v_mfma_f32_16x16x32_bf16 v[72:75], v[204:207], a[124:127], v[72:75]\n\t"
    "ds_read_b64_tr_b16 v[222:223], v242 offset:61448\n\t"
    "v_mul_f32 v252, v151, v252\n\t"
    "v_mfma_f32_16x16x32_bf16 v[80:83], v[192:195], a[176:179], v[80:83]\n\t"
    "v_exp_f32 v248, v248\n\t"
    "v_mfma_f32_16x16x32_bf16 v[80:83], v[196:199], a[180:183], v[80:83]\n\t"
    "v_exp_f32 v249, v249\n\t"
    "v_mfma_f32_16x16x32_bf16 v[80:83], v[200:203], a[184:187], v[80:83]\n\t"
    "v_exp_f32 v251, v251\n\t"
    "v_mfma_f32_16x16x32_bf16 v[80:83], v[204:207], a[188:191], v[80:83]\n\t"
    "v_exp_f32 v252, v252\n\t"
    "v_mfma_f32_16x16x32_bf16 v[88:91], v[192:195], a[240:243], v[88:91]\n\t"
    "v_add_f32 v248, 1.0, v248\n\t"
    "v_add_f32 v249, 1.0, v249\n\t"
    "v_mfma_f32_16x16x32_bf16 v[88:91], v[196:199], a[244:247], v[88:91]\n\t"
    "v_add_f32 v251, 1.0, v251\n\t"
    "v_add_f32 v252, 1.0, v252\n\t"
    "v_mfma_f32_16x16x32_bf16 v[88:91], v[200:203], a[248:251], v[88:91]\n\t"
    "v_rcp_f32 v248, v248\n\t"
    "v_mfma_f32_16x16x32_bf16 v[88:91], v[204:207], a[252:255], v[88:91]\n\t"
    "s_waitcnt lgkmcnt(0)\n\t"
    "v_rcp_f32 v249, v249\n\t"
    "v_mfma_f32_16x16x32_bf16 v[68:71], v[208:211], a[48:51], v[68:71]\n\t"
    "ds_read_b64_tr_b16 v[192:193], v243 offset:32768\n\t"
    "v_mfma_f32_16x16x32_bf16 v[68:71], v[212:215], a[52:55], v[68:71]\n\t"
    "ds_read_b64_tr_b16 v[194:195], v243 offset:36864\n\t"
    "v_mfma_f32_16x16x32_bf16 v[68:71], v[216:219], a[56:59], v[68:71]\n\t"
    "ds_read_b64_tr_b16 v[196:197], v243 offset:40960\n\t"
    "v_mfma_f32_16x16x32_bf16 v[68:71], v[220:223], a[60:63], v[68:71]\n\t"
    "ds_read_b64_tr_b16 v[198:199], v243 offset:45056\n\t"
    "v_mfma_f32_16x16x32_bf16 v[76:79], v[208:211], a[112:115], v[76:79]\n\t"
    "ds_read_b64_tr_b16 v[200:201], v243 offset:49152\n\t"
    "v_mfma_f32_16x16x32_bf16 v[76:79], v[212:215], a[116:119], v[76:79]\n\t"
    "ds_read_b64_tr_b16 v[202:203], v243 offset:53248\n\t"
    "v_mfma_f32_16x16x32_bf16 v[76:79], v[216:219], a[120:123], v[76:79]\n\t"
    "ds_read_b64_tr_b16 v[204:205], v243 offset:57344\n\t"
    "v_mfma_f32_16x16x32_bf16 v[76:79], v[220:223], a[124:127], v[76:79]\n\t"
    "ds_read_b64_tr_b16 v[206:207], v243 offset:61440\n\t"
    "v_mfma_f32_16x16x32_bf16 v[84:87], v[208:211], a[176:179], v[84:87]\n\t"
    "v_rcp_f32 v251, v251\n\t"
    "v_mfma_f32_16x16x32_bf16 v[84:87], v[212:215], a[180:183], v[84:87]\n\t"
    "v_rcp_f32 v252, v252\n\t"
    "v_mfma_f32_16x16x32_bf16 v[84:87], v[216:219], a[184:187], v[84:87]\n\t"
    "v_mul_f32 v148, v148, v248\n\t"
    "v_mul_f32 v149, v149, v249\n\t"
    "v_mfma_f32_16x16x32_bf16 v[84:87], v[220:223], a[188:191], v[84:87]\n\t"
    "v_mul_f32 v150, v150, v251\n\t"
    "v_mul_f32 v151, v151, v252\n\t"
    "v_mfma_f32_16x16x32_bf16 v[92:95], v[208:211], a[240:243], v[92:95]\n\t"
    "v_cvt_pk_bf16_f32 v144, v144, v145\n\t"
    "v_cvt_pk_bf16_f32 v145, v146, v147\n\t"
    "v_mfma_f32_16x16x32_bf16 v[92:95], v[212:215], a[244:247], v[92:95]\n\t"
    "v_cvt_pk_bf16_f32 v146, v148, v149\n\t"
    "v_cvt_pk_bf16_f32 v147, v150, v151\n\t"
    "s_add_u32 s77, s84, 114688\n\t"
    "buffer_store_dwordx4 v[144:147], v255, s[72:75], s77 offen sc1\n\t"
    "v_mfma_f32_16x16x32_bf16 v[92:95], v[216:219], a[248:251], v[92:95]\n\t"
    "v_mul_f32 v152, v152, v247\n\t"
    "v_mul_f32 v153, v153, v247\n\t"
    "v_mfma_f32_16x16x32_bf16 v[92:95], v[220:223], a[252:255], v[92:95]\n\t"
    "s_waitcnt lgkmcnt(0)\n\t"
    "v_mul_f32 v154, v154, v247\n\t"
    "v_mul_f32 v155, v155, v247\n\t"
    "v_mfma_f32_16x16x32_bf16 v[96:99], v[192:195], a[48:51], v[96:99]\n\t"
    "ds_read_b64_tr_b16 v[208:209], v243 offset:32776\n\t"
    "v_mul_f32 v248, v152, v152\n\t"
    "v_mfma_f32_16x16x32_bf16 v[96:99], v[196:199], a[52:55], v[96:99]\n\t"
    "ds_read_b64_tr_b16 v[210:211], v243 offset:36872\n\t"
    "v_mul_f32 v249, v153, v153\n\t"
    "v_mfma_f32_16x16x32_bf16 v[96:99], v[200:203], a[56:59], v[96:99]\n\t"
    "ds_read_b64_tr_b16 v[212:213], v243 offset:40968\n\t"
    "v_mul_f32 v251, v154, v154\n\t"
    "v_mfma_f32_16x16x32_bf16 v[96:99], v[204:207], a[60:63], v[96:99]\n\t"
    "ds_read_b64_tr_b16 v[214:215], v243 offset:45064\n\t"
    "v_mul_f32 v252, v155, v155\n\t"
    "v_mfma_f32_16x16x32_bf16 v[104:107], v[192:195], a[112:115], v[104:107]\n\t"
    "ds_read_b64_tr_b16 v[216:217], v243 offset:49160\n\t"
    "v_fmaak_f32 v248, v253, v248, 0xc0135761\n\t"
    "v_mfma_f32_16x16x32_bf16 v[104:107], v[196:199], a[116:119], v[104:107]\n\t"
    "ds_read_b64_tr_b16 v[218:219], v243 offset:53256\n\t"
    "v_fmaak_f32 v249, v253, v249, 0xc0135761\n\t"
    "v_mfma_f32_16x16x32_bf16 v[104:107], v[200:203], a[120:123], v[104:107]\n\t"
    "ds_read_b64_tr_b16 v[220:221], v243 offset:57352\n\t"
    "v_fmaak_f32 v251, v253, v251, 0xc0135761\n\t"
    "v_mfma_f32_16x16x32_bf16 v[104:107], v[204:207], a[124:127], v[104:107]\n\t"
    "ds_read_b64_tr_b16 v[222:223], v243 offset:61448\n\t"
    "v_fmaak_f32 v252, v253, v252, 0xc0135761\n\t"
    "v_mfma_f32_16x16x32_bf16 v[112:115], v[192:195], a[176:179], v[112:115]\n\t"
    "v_mul_f32 v248, v152, v248\n\t"
    "v_mul_f32 v249, v153, v249\n\t"
    "v_mfma_f32_16x16x32_bf16 v[112:115], v[196:199], a[180:183], v[112:115]\n\t"
    "v_mul_f32 v251, v154, v251\n\t"
    "v_mul_f32 v252, v155, v252\n\t"
    "v_mfma_f32_16x16x32_bf16 v[112:115], v[200:203], a[184:187], v[112:115]\n\t"
    "v_exp_f32 v248, v248\n\t"
    "v_mfma_f32_16x16x32_bf16 v[112:115], v[204:207], a[188:191], v[112:115]\n\t"
    "v_exp_f32 v249, v249\n\t"
    "v_mfma_f32_16x16x32_bf16 v[120:123], v[192:195], a[240:243], v[120:123]\n\t"
    "v_exp_f32 v251, v251\n\t"
    "v_mfma_f32_16x16x32_bf16 v[120:123], v[196:199], a[244:247], v[120:123]\n\t"
    "v_exp_f32 v252, v252\n\t"
    "v_mfma_f32_16x16x32_bf16 v[120:123], v[200:203], a[248:251], v[120:123]\n\t"
    "v_add_f32 v248, 1.0, v248\n\t"
    "v_add_f32 v249, 1.0, v249\n\t"
    "v_mfma_f32_16x16x32_bf16 v[120:123], v[204:207], a[252:255], v[120:123]\n\t"
    "s_waitcnt lgkmcnt(0)\n\t"
    "v_add_f32 v251, 1.0, v251\n\t"
    "v_add_f32 v252, 1.0, v252\n\t"
    "v_mfma_f32_16x16x32_bf16 v[100:103], v[208:211], a[48:51], v[100:103]\n\t"
    "v_rcp_f32 v248, v248\n\t"
    "v_mfma_f32_16x16x32_bf16 v[100:103], v[212:215], a[52:55], v[100:103]\n\t"
    "v_rcp_f32 v249, v249\n\t"
    "v_mfma_f32_16x16x32_bf16 v[100:103], v[216:219], a[56:59], v[100:103]\n\t"
    "v_rcp_f32 v251, v251\n\t"
    "v_mfma_f32_16x16x32_bf16 v[100:103], v[220:223], a[60:63], v[100:103]\n\t"
    "v_rcp_f32 v252, v252\n\t"
    "v_mfma_f32_16x16x32_bf16 v[108:111], v[208:211], a[112:115], v[108:111]\n\t"
    "v_mul_f32 v152, v152, v248\n\t"
    "v_mul_f32 v153, v153, v249\n\t"
    "v_mfma_f32_16x16x32_bf16 v[108:111], v[212:215], a[116:119], v[108:111]\n\t"
    "v_mul_f32 v154, v154, v251\n\t"
    "v_mul_f32 v155, v155, v252\n\t"
    "v_mfma_f32_16x16x32_bf16 v[108:111], v[216:219], a[120:123], v[108:111]\n\t"
    "v_mul_f32 v156, v156, v247\n\t"
    "v_mul_f32 v157, v157, v247\n\t"
    "v_mfma_f32_16x16x32_bf16 v[108:111], v[220:223], a[124:127], v[108:111]\n\t"
    "v_mul_f32 v158, v158, v247\n\t"
    "v_mul_f32 v159, v159, v247\n\t"
    "v_mfma_f32_16x16x32_bf16 v[116:119], v[208:211], a[176:179], v[116:119]\n\t"
    "v_mul_f32 v248, v156, v156\n\t"
    "v_mul_f32 v249, v157, v157\n\t"
    "v_mfma_f32_16x16x32_bf16 v[116:119], v[212:215], a[180:183], v[116:119]\n\t"
    "v_mul_f32 v251, v158, v158\n\t"
    "v_mul_f32 v252, v159, v159\n\t"
    "v_mfma_f32_16x16x32_bf16 v[116:119], v[216:219], a[184:187], v[116:119]\n\t"
    "v_fmaak_f32 v248, v253, v248, 0xc0135761\n\t"
    "v_fmaak_f32 v249, v253, v249, 0xc0135761\n\t"
    "v_mfma_f32_16x16x32_bf16 v[116:119], v[220:223], a[188:191], v[116:119]\n\t"
    "v_fmaak_f32 v251, v253, v251, 0xc0135761\n\t"
    "v_fmaak_f32 v252, v253, v252, 0xc0135761\n\t"
    "v_mfma_f32_16x16x32_bf16 v[124:127], v[208:211], a[240:243], v[124:127]\n\t"
    "v_mul_f32 v248, v156, v248\n\t"
    "v_mul_f32 v249, v157, v249\n\t"
    "v_mfma_f32_16x16x32_bf16 v[124:127], v[212:215], a[244:247], v[124:127]\n\t"
    "v_mul_f32 v251, v158, v251\n\t"
    "v_mul_f32 v252, v159, v252\n\t"
    "v_mfma_f32_16x16x32_bf16 v[124:127], v[216:219], a[248:251], v[124:127]\n\t"
    "v_exp_f32 v248, v248\n\t"
    "v_mfma_f32_16x16x32_bf16 v[124:127], v[220:223], a[252:255], v[124:127]\n\t"
    "s_waitcnt lgkmcnt(0)\n\t"
    "v_exp_f32 v249, v249\n\t"
    "s_nop 7\n\t"
    "v_exp_f32 v251, v251\n\t"
    "v_exp_f32 v252, v252\n\t"
    "v_add_f32 v248, 1.0, v248\n\t"
    "v_add_f32 v249, 1.0, v249\n\t"
    "v_add_f32 v251, 1.0, v251\n\t"
    "v_add_f32 v252, 1.0, v252\n\t"
    "v_rcp_f32 v248, v248\n\t"
    "v_rcp_f32 v249, v249\n\t"
    "v_rcp_f32 v251, v251\n\t"
    "v_rcp_f32 v252, v252\n\t"
    "v_mul_f32 v156, v156, v248\n\t"
    "v_mul_f32 v157, v157, v249\n\t"
    "v_mul_f32 v158, v158, v251\n\t"
    "v_mul_f32 v159, v159, v252\n\t"
    "v_cvt_pk_bf16_f32 v152, v152, v153\n\t"
    "v_cvt_pk_bf16_f32 v153, v154, v155\n\t"
    "v_cvt_pk_bf16_f32 v154, v156, v157\n\t"
    "v_cvt_pk_bf16_f32 v155, v158, v159\n\t"
    "s_add_u32 s77, s84, 172032\n\t"
    "buffer_store_dwordx4 v[152:155], v255, s[72:75], s77 offen sc1\n\t"
    "v_mul_f32 v160, v160, v244\n\t"
    "v_mul_f32 v161, v161, v244\n\t"
    "v_mul_f32 v162, v162, v244\n\t"
    "v_mul_f32 v163, v163, v244\n\t"
    "v_mul_f32 v248, v160, v160\n\t"
    "v_mul_f32 v249, v161, v161\n\t"
    "v_mul_f32 v251, v162, v162\n\t"
    "v_mul_f32 v252, v163, v163\n\t"
    "v_fmaak_f32 v248, v253, v248, 0xc0135761\n\t"
    "v_fmaak_f32 v249, v253, v249, 0xc0135761\n\t"
    "v_fmaak_f32 v251, v253, v251, 0xc0135761\n\t"
    "v_fmaak_f32 v252, v253, v252, 0xc0135761\n\t"
    "v_mul_f32 v248, v160, v248\n\t"
    "v_mul_f32 v249, v161, v249\n\t"
    "v_mul_f32 v251, v162, v251\n\t"
    "v_mul_f32 v252, v163, v252\n\t"
    "v_exp_f32 v248, v248\n\t"
    "v_exp_f32 v249, v249\n\t"
    "v_exp_f32 v251, v251\n\t"
    "v_exp_f32 v252, v252\n\t"
    "v_add_f32 v248, 1.0, v248\n\t"
    "v_add_f32 v249, 1.0, v249\n\t"
    "v_add_f32 v251, 1.0, v251\n\t"
    "v_add_f32 v252, 1.0, v252\n\t"
    "v_rcp_f32 v248, v248\n\t"
    "v_rcp_f32 v249, v249\n\t"
    "v_rcp_f32 v251, v251\n\t"
    "v_rcp_f32 v252, v252\n\t"
    "v_mul_f32 v160, v160, v248\n\t"
    "v_mul_f32 v161, v161, v249\n\t"
    "v_mul_f32 v162, v162, v251\n\t"
    "v_mul_f32 v163, v163, v252\n\t"
    "v_mul_f32 v164, v164, v244\n\t"
    "v_mul_f32 v165, v165, v244\n\t"
    "v_mul_f32 v166, v166, v244\n\t"
    "v_mul_f32 v167, v167, v244\n\t"
    "v_mul_f32 v248, v164, v164\n\t"
    "v_mul_f32 v249, v165, v165\n\t"
    "v_mul_f32 v251, v166, v166\n\t"
    "v_mul_f32 v252, v167, v167\n\t"
    "v_fmaak_f32 v248, v253, v248, 0xc0135761\n\t"
    "v_fmaak_f32 v249, v253, v249, 0xc0135761\n\t"
    "v_fmaak_f32 v251, v253, v251, 0xc0135761\n\t"
    "v_fmaak_f32 v252, v253, v252, 0xc0135761\n\t"
    "v_mul_f32 v248, v164, v248\n\t"
    "v_mul_f32 v249, v165, v249\n\t"
    "v_mul_f32 v251, v166, v251\n\t"
    "v_mul_f32 v252, v167, v252\n\t"
    "v_exp_f32 v248, v248\n\t"
    "v_exp_f32 v249, v249\n\t"
    "v_exp_f32 v251, v251\n\t"
    "v_exp_f32 v252, v252\n\t"
    "v_add_f32 v248, 1.0, v248\n\t"
    "v_add_f32 v249, 1.0, v249\n\t"
    "v_add_f32 v251, 1.0, v251\n\t"
    "v_add_f32 v252, 1.0, v252\n\t"
    "v_rcp_f32 v248, v248\n\t"
    "v_rcp_f32 v249, v249\n\t"
    "v_rcp_f32 v251, v251\n\t"
    "v_rcp_f32 v252, v252\n\t"
    "v_mul_f32 v164, v164, v248\n\t"
    "v_mul_f32 v165, v165, v249\n\t"
    "v_mul_f32 v166, v166, v251\n\t"
    "v_mul_f32 v167, v167, v252\n\t"
    "v_cvt_pk_bf16_f32 v160, v160, v161\n\t"
    "v_cvt_pk_bf16_f32 v161, v162, v163\n\t"
    "v_cvt_pk_bf16_f32 v162, v164, v165\n\t"
    "v_cvt_pk_bf16_f32 v163, v166, v167\n\t"
    "s_add_u32 s77, s84, 1024\n\t"
    "buffer_store_dwordx4 v[160:163], v255, s[72:75], s77 offen sc1\n\t"
    "v_mul_f32 v168, v168, v245\n\t"
    "v_mul_f32 v169, v169, v245\n\t"
    "v_mul_f32 v170, v170, v245\n\t"
    "v_mul_f32 v171, v171, v245\n\t"
    "v_mul_f32 v248, v168, v168\n\t"
    "v_mul_f32 v249, v169, v169\n\t"
    "v_mul_f32 v251, v170, v170\n\t"
    "v_mul_f32 v252, v171, v171\n\t"
    "v_fmaak_f32 v248, v253, v248, 0xc0135761\n\t"
    "v_fmaak_f32 v249, v253, v249, 0xc0135761\n\t"
    "v_fmaak_f32 v251, v253, v251, 0xc0135761\n\t"
    "v_fmaak_f32 v252, v253, v252, 0xc0135761\n\t"
    "v_mul_f32 v248, v168, v248\n\t"
    "v_mul_f32 v249, v169, v249\n\t"
    "v_mul_f32 v251, v170, v251\n\t"
    "v_mul_f32 v252, v171, v252\n\t"
    "v_exp_f32 v248, v248\n\t"
    "v_exp_f32 v249, v249\n\t"
    "v_exp_f32 v251, v251\n\t"
    "v_exp_f32 v252, v252\n\t"
    "v_add_f32 v248, 1.0, v248\n\t"
    "v_add_f32 v249, 1.0, v249\n\t"
    "v_add_f32 v251, 1.0, v251\n\t"
    "v_add_f32 v252, 1.0, v252\n\t"
    "v_rcp_f32 v248, v248\n\t"
    "v_rcp_f32 v249, v249\n\t"
    "v_rcp_f32 v251, v251\n\t"
    "v_rcp_f32 v252, v252\n\t"
    "v_mul_f32 v168, v168, v248\n\t"
    "v_mul_f32 v169, v169, v249\n\t"
    "v_mul_f32 v170, v170, v251\n\t"
    "v_mul_f32 v171, v171, v252\n\t"
    "v_mul_f32 v172, v172, v245\n\t"
    "v_mul_f32 v173, v173, v245\n\t"
    "v_mul_f32 v174, v174, v245\n\t"
    "v_mul_f32 v175, v175, v245\n\t"
    "v_mul_f32 v248, v172, v172\n\t"
    "v_mul_f32 v249, v173, v173\n\t"
    "v_mul_f32 v251, v174, v174\n\t"
    "v_mul_f32 v252, v175, v175\n\t"
    "v_fmaak_f32 v248, v253, v248, 0xc0135761\n\t"
    "v_fmaak_f32 v249, v253, v249, 0xc0135761\n\t"
    "v_fmaak_f32 v251, v253, v251, 0xc0135761\n\t"
    "v_fmaak_f32 v252, v253, v252, 0xc0135761\n\t"
    "v_mul_f32 v248, v172, v248\n\t"
    "v_mul_f32 v249, v173, v249\n\t"
    "v_mul_f32 v251, v174, v251\n\t"
    "v_mul_f32 v252, v175, v252\n\t"
    "v_exp_f32 v248, v248\n\t"
    "v_exp_f32 v249, v249\n\t"
    "v_exp_f32 v251, v251\n\t"
    "v_exp_f32 v252, v252\n\t"
    "v_add_f32 v248, 1.0, v248\n\t"
    "v_add_f32 v249, 1.0, v249\n\t"
    "v_add_f32 v251, 1.0, v251\n\t"
    "v_add_f32 v252, 1.0, v252\n\t"
    "v_rcp_f32 v248, v248\n\t"
    "v_rcp_f32 v249, v249\n\t"
    "v_rcp_f32 v251, v251\n\t"
    "v_rcp_f32 v252, v252\n\t"
    "v_mul_f32 v172, v172, v248\n\t"
    "v_mul_f32 v173, v173, v249\n\t"
    "v_mul_f32 v174, v174, v251\n\t"
    "v_mul_f32 v175, v175, v252\n\t"
    "v_cvt_pk_bf16_f32 v168, v168, v169\n\t"
    "v_cvt_pk_bf16_f32 v169, v170, v171\n\t"
    "v_cvt_pk_bf16_f32 v170, v172, v173\n\t"
    "v_cvt_pk_bf16_f32 v171, v174, v175\n\t"
    "s_add_u32 s77, s84, 58368\n\t"
    "buffer_store_dwordx4 v[168:171], v255, s[72:75], s77 offen sc1\n\t"
    "v_mul_f32 v176, v176, v246\n\t"
    "v_mul_f32 v177, v177, v246\n\t"
    "v_mul_f32 v178, v178, v246\n\t"
    "v_mul_f32 v179, v179, v246\n\t"
    "v_mul_f32 v248, v176, v176\n\t"
    "v_mul_f32 v249, v177, v177\n\t"
    "v_mul_f32 v251, v178, v178\n\t"
    "v_mul_f32 v252, v179, v179\n\t"
    "v_fmaak_f32 v248, v253, v248, 0xc0135761\n\t"
    "v_fmaak_f32 v249, v253, v249, 0xc0135761\n\t"
    "v_fmaak_f32 v251, v253, v251, 0xc0135761\n\t"
    "v_fmaak_f32 v252, v253, v252, 0xc0135761\n\t"
    "v_mul_f32 v248, v176, v248\n\t"
    "v_mul_f32 v249, v177, v249\n\t"
    "v_mul_f32 v251, v178, v251\n\t"
    "v_mul_f32 v252, v179, v252\n\t"
    "v_exp_f32 v248, v248\n\t"
    "v_exp_f32 v249, v249\n\t"
    "v_exp_f32 v251, v251\n\t"
    "v_exp_f32 v252, v252\n\t"
    "v_add_f32 v248, 1.0, v248\n\t"
    "v_add_f32 v249, 1.0, v249\n\t"
    "v_add_f32 v251, 1.0, v251\n\t"
    "v_add_f32 v252, 1.0, v252\n\t"
    "v_rcp_f32 v248, v248\n\t"
    "v_rcp_f32 v249, v249\n\t"
    "v_rcp_f32 v251, v251\n\t"
    "v_rcp_f32 v252, v252\n\t"
    "v_mul_f32 v176, v176, v248\n\t"
    "v_mul_f32 v177, v177, v249\n\t"
    "v_mul_f32 v178, v178, v251\n\t"
    "v_mul_f32 v179, v179, v252\n\t"
    "v_mul_f32 v180, v180, v246\n\t"
    "v_mul_f32 v181, v181, v246\n\t"
    "v_mul_f32 v182, v182, v246\n\t"
    "v_mul_f32 v183, v183, v246\n\t"
    "v_mul_f32 v248, v180, v180\n\t"
    "v_mul_f32 v249, v181, v181\n\t"
    "v_mul_f32 v251, v182, v182\n\t"
    "v_mul_f32 v252, v183, v183\n\t"
    "v_fmaak_f32 v248, v253, v248, 0xc0135761\n\t"
    "v_fmaak_f32 v249, v253, v249, 0xc0135761\n\t"
    "v_fmaak_f32 v251, v253, v251, 0xc0135761\n\t"
    "v_fmaak_f32 v252, v253, v252, 0xc0135761\n\t"
    "v_mul_f32 v248, v180, v248\n\t"
    "v_mul_f32 v249, v181, v249\n\t"
    "v_mul_f32 v251, v182, v251\n\t"
    "v_mul_f32 v252, v183, v252\n\t"
    "v_exp_f32 v248, v248\n\t"
    "v_exp_f32 v249, v249\n\t"
    "v_exp_f32 v251, v251\n\t"
    "v_exp_f32 v252, v252\n\t"
    "v_add_f32 v248, 1.0, v248\n\t"
    "v_add_f32 v249, 1.0, v249\n\t"
    "v_add_f32 v251, 1.0, v251\n\t"
    "v_add_f32 v252, 1.0, v252\n\t"
    "v_rcp_f32 v248, v248\n\t"
    "v_rcp_f32 v249, v249\n\t"
    "v_rcp_f32 v251, v251\n\t"
    "v_rcp_f32 v252, v252\n\t"
    "v_mul_f32 v180, v180, v248\n\t"
    "v_mul_f32 v181, v181, v249\n\t"
    "v_mul_f32 v182, v182, v251\n\t"
    "v_mul_f32 v183, v183, v252\n\t"
    "v_cvt_pk_bf16_f32 v176, v176, v177\n\t"
    "v_cvt_pk_bf16_f32 v177, v178, v179\n\t"
    "v_cvt_pk_bf16_f32 v178, v180, v181\n\t"
    "v_cvt_pk_bf16_f32 v179, v182, v183\n\t"
    "s_add_u32 s77, s84, 115712\n\t"
    "buffer_store_dwordx4 v[176:179], v255, s[72:75], s77 offen sc1\n\t"
    "v_mul_f32 v184, v184, v247\n\t"
    "v_mul_f32 v185, v185, v247\n\t"
    "v_mul_f32 v186, v186, v247\n\t"
    "v_mul_f32 v187, v187, v247\n\t"
    "v_mul_f32 v248, v184, v184\n\t"
    "v_mul_f32 v249, v185, v185\n\t"
    "v_mul_f32 v251, v186, v186\n\t"
    "v_mul_f32 v252, v187, v187\n\t"
    "v_fmaak_f32 v248, v253, v248, 0xc0135761\n\t"
    "v_fmaak_f32 v249, v253, v249, 0xc0135761\n\t"
    "v_fmaak_f32 v251, v253, v251, 0xc0135761\n\t"
    "v_fmaak_f32 v252, v253, v252, 0xc0135761\n\t"
    "v_mul_f32 v248, v184, v248\n\t"
    "v_mul_f32 v249, v185, v249\n\t"
    "v_mul_f32 v251, v186, v251\n\t"
    "v_mul_f32 v252, v187, v252\n\t"
    "v_exp_f32 v248, v248\n\t"
    "v_exp_f32 v249, v249\n\t"
    "v_exp_f32 v251, v251\n\t"
    "v_exp_f32 v252, v252\n\t"
    "v_add_f32 v248, 1.0, v248\n\t"
    "v_add_f32 v249, 1.0, v249\n\t"
    "v_add_f32 v251, 1.0, v251\n\t"
    "v_add_f32 v252, 1.0, v252\n\t"
    "v_rcp_f32 v248, v248\n\t"
    "v_rcp_f32 v249, v249\n\t"
    "v_rcp_f32 v251, v251\n\t"
    "v_rcp_f32 v252, v252\n\t"
    "v_mul_f32 v184, v184, v248\n\t"
    "v_mul_f32 v185, v185, v249\n\t"
    "v_mul_f32 v186, v186, v251\n\t"
    "v_mul_f32 v187, v187, v252\n\t"
    "v_mul_f32 v188, v188, v247\n\t"
    "v_mul_f32 v189, v189, v247\n\t"
    "v_mul_f32 v190, v190, v247\n\t"
    "v_mul_f32 v191, v191, v247\n\t"
    "v_mul_f32 v248, v188, v188\n\t"
    "v_mul_f32 v249, v189, v189\n\t"
    "v_mul_f32 v251, v190, v190\n\t"
    "v_mul_f32 v252, v191, v191\n\t"
    "v_fmaak_f32 v248, v253, v248, 0xc0135761\n\t"
    "v_fmaak_f32 v249, v253, v249, 0xc0135761\n\t"
    "v_fmaak_f32 v251, v253, v251, 0xc0135761\n\t"
    "v_fmaak_f32 v252, v253, v252, 0xc0135761\n\t"
    "v_mul_f32 v248, v188, v248\n\t"
    "v_mul_f32 v249, v189, v249\n\t"
    "v_mul_f32 v251, v190, v251\n\t"
    "v_mul_f32 v252, v191, v252\n\t"
    "v_exp_f32 v248, v248\n\t"
    "v_exp_f32 v249, v249\n\t"
    "v_exp_f32 v251, v251\n\t"
    "v_exp_f32 v252, v252\n\t"
    "v_add_f32 v248, 1.0, v248\n\t"
    "v_add_f32 v249, 1.0, v249\n\t"
    "v_add_f32 v251, 1.0, v251\n\t"
    "v_add_f32 v252, 1.0, v252\n\t"
    "v_rcp_f32 v248, v248\n\t"
    "v_rcp_f32 v249, v249\n\t"
    "v_rcp_f32 v251, v251\n\t"
    "v_rcp_f32 v252, v252\n\t"
    "v_mul_f32 v188, v188, v248\n\t"
    "v_mul_f32 v189, v189, v249\n\t"
    "v_mul_f32 v190, v190, v251\n\t"
    "v_mul_f32 v191, v191, v252\n\t"
    "v_cvt_pk_bf16_f32 v184, v184, v185\n\t"
    "v_cvt_pk_bf16_f32 v185, v186, v187\n\t"
    "v_cvt_pk_bf16_f32 v186, v188, v189\n\t"
    "v_cvt_pk_bf16_f32 v187, v190, v191\n\t"
    "s_add_u32 s77, s84, 173056\n\t"
    "buffer_store_dwordx4 v[184:187], v255, s[72:75], s77 offen sc1\n\t"
    "v_cvt_pk_bf16_f32 v160, v0, v1\n\t"
    "v_cvt_pk_bf16_f32 v161, v2, v3\n\t"
    "v_cvt_pk_bf16_f32 v162, v4, v5\n\t"
    "v_cvt_pk_bf16_f32 v163, v6, v7\n\t"
    "v_cvt_pk_bf16_f32 v176, v8, v9\n\t"
    "v_cvt_pk_bf16_f32 v177, v10, v11\n\t"
    "v_cvt_pk_bf16_f32 v178, v12, v13\n\t"
    "v_cvt_pk_bf16_f32 v179, v14, v15\n\t"
    "v_cvt_pk_bf16_f32 v192, v16, v17\n\t"
    "v_cvt_pk_bf16_f32 v193, v18, v19\n\t"
    "v_cvt_pk_bf16_f32 v194, v20, v21\n\t"
    "v_cvt_pk_bf16_f32 v195, v22, v23\n\t"
    "v_cvt_pk_bf16_f32 v208, v24, v25\n\t"
    "v_cvt_pk_bf16_f32 v209, v26, v27\n\t"
    "v_cvt_pk_bf16_f32 v210, v28, v29\n\t"
    "v_cvt_pk_bf16_f32 v211, v30, v31\n\t"
    "v_cvt_pk_bf16_f32 v164, v32, v33\n\t"
    "v_cvt_pk_bf16_f32 v165, v34, v35\n\t"
    "v_cvt_pk_bf16_f32 v166, v36, v37\n\t"
    "v_cvt_pk_bf16_f32 v167, v38, v39\n\t"
    "v_cvt_pk_bf16_f32 v180, v40, v41\n\t"
    "v_cvt_pk_bf16_f32 v181, v42, v43\n\t"
    "v_cvt_pk_bf16_f32 v182, v44, v45\n\t"
    "v_cvt_pk_bf16_f32 v183, v46, v47\n\t"
    "v_cvt_pk_bf16_f32 v196, v48, v49\n\t"
    "v_cvt_pk_bf16_f32 v197, v50, v51\n\t"
    "v_cvt_pk_bf16_f32 v198, v52, v53\n\t"
    "v_cvt_pk_bf16_f32 v199, v54, v55\n\t"
    "v_cvt_pk_bf16_f32 v212, v56, v57\n\t"
    "v_cvt_pk_bf16_f32 v213, v58, v59\n\t"
    "v_cvt_pk_bf16_f32 v214, v60, v61\n\t"
    "v_cvt_pk_bf16_f32 v215, v62, v63\n\t"
    "v_cvt_pk_bf16_f32 v168, v64, v65\n\t"
    "v_cvt_pk_bf16_f32 v169, v66, v67\n\t"
    "v_cvt_pk_bf16_f32 v170, v68, v69\n\t"
    "v_cvt_pk_bf16_f32 v171, v70, v71\n\t"
    "v_cvt_pk_bf16_f32 v184, v72, v73\n\t"
    "v_cvt_pk_bf16_f32 v185, v74, v75\n\t"
    "v_cvt_pk_bf16_f32 v186, v76, v77\n\t"
    "v_cvt_pk_bf16_f32 v187, v78, v79\n\t"
    "v_cvt_pk_bf16_f32 v200, v80, v81\n\t"
    "v_cvt_pk_bf16_f32 v201, v82, v83\n\t"
    "v_cvt_pk_bf16_f32 v202, v84, v85\n\t"
    "v_cvt_pk_bf16_f32 v203, v86, v87\n\t"
    "v_cvt_pk_bf16_f32 v216, v88, v89\n\t"
    "v_cvt_pk_bf16_f32 v217, v90, v91\n\t"
    "v_cvt_pk_bf16_f32 v218, v92, v93\n\t"
    "v_cvt_pk_bf16_f32 v219, v94, v95\n\t"
    "v_cvt_pk_bf16_f32 v172, v96, v97\n\t"
    "v_cvt_pk_bf16_f32 v173, v98, v99\n\t"
    "v_cvt_pk_bf16_f32 v174, v100, v101\n\t"
    "v_cvt_pk_bf16_f32 v175, v102, v103\n\t"
    "v_cvt_pk_bf16_f32 v188, v104, v105\n\t"
    "v_cvt_pk_bf16_f32 v189, v106, v107\n\t"
    "v_cvt_pk_bf16_f32 v190, v108, v109\n\t"
    "v_cvt_pk_bf16_f32 v191, v110, v111\n\t"
    "v_cvt_pk_bf16_f32 v204, v112, v113\n\t"
    "v_cvt_pk_bf16_f32 v205, v114, v115\n\t"
    "v_cvt_pk_bf16_f32 v206, v116, v117\n\t"
    "v_cvt_pk_bf16_f32 v207, v118, v119\n\t"
    "v_cvt_pk_bf16_f32 v220, v120, v121\n\t"
    "v_cvt_pk_bf16_f32 v221, v122, v123\n\t"
    "v_cvt_pk_bf16_f32 v222, v124, v125\n\t"
    "v_cvt_pk_bf16_f32 v223, v126, v127\n\t"
    "s_load_dwordx2 s[64:65], %[ka], 56\n\t"
    "s_load_dwordx2 s[60:61], %[ka], 64\n\t"
    "s_load_dwordx2 s[88:89], %[ka], 72\n\t"
    "s_mov_b32 s66, 4194304\n\t"
    "s_mov_b32 s67, 0x20000\n\t"
    "s_mov_b32 s62, 1835008\n\t"
    "s_mov_b32 s63, 0x20000\n\t"
    "s_lshr_b32 s78, %[h], 2\n\t"
    "s_and_b32 s79, %[h], 3\n\t"
    "s_lshl_b32 s77, %[qb], 4\n\t"
    "s_lshl_b32 s76, s50, 2\n\t"
    "s_add_u32 s77, s77, s76\n\t"
    "s_lshl_b32 s73, s77, 17\n\t"
    "s_lshl_b32 s76, s78, 14\n\t"
    "s_add_u32 s73, s73, s76\n\t"
    "s_add_u32 s73, s73, 9568256\n\t"
    "s_lshl_b32 s76, s79, 8\n\t"
    "s_add_u32 s76, s76, s94\n\t"
    "s_lshl_b32 s75, s78, 19\n\t"
    "s_add_u32 s75, s75, s76\n\t"
    "s_lshl_b32 s80, s50, 10\n\t"
    "s_lshl_b32 s76, %[qb], 2\n\t"
    "s_add_u32 s76, s76, s79\n\t"
    "s_lshl_b32 s81, s76, 6\n\t"
    "s_lshl_b32 s77, s50, 4\n\t"
    "s_add_u32 s81, s81, s77\n\t"
    "s_add_u32 s81, s81, s78\n\t"
    "s_lshl_b32 s81, s81, 13\n\t"
    "s_lshl_b32 s82, s76, 6\n\t"
    "s_lshl_b32 s77, s78, 3\n\t"
    "s_add_u32 s82, s82, s77\n\t"
    "s_lshl_b32 s82, s82, 13\n\t"
    "s_lshl_b32 s77, s50, 11\n\t"
    "s_add_u32 s82, s82, s77\n\t"
    "s_lshl_b32 s84, %[qb], 2\n\t"
    "s_add_u32 s84, s84, s50\n\t"
    "s_lshl_b32 s84, s84, 6\n\t"
    "s_lshl_b32 s86, %[qb], 3\n\t"
    "s_add_u32 s86, s86, s78\n\t"
    "s_lshl_b32 s86, s86, 2\n\t"
    "s_add_u32 s86, s86, s50\n\t"
    "s_lshl_b32 s86, s86, 6\n\t"
    "s_add_u32 s86, s86, 4096\n\t"
    "s_lshl_b32 s87, s76, 3\n\t"
    "s_lshl_b32 s77, s50, 1\n\t"
    "s_add_u32 s87, s87, s77\n\t"
    "s_lshl_b32 s87, s87, 6\n\t"
    "s_add_u32 s87, s87, 20480\n\t"
    "s_lshl_b32 s83, %[qb], 18\n\t"
    "s_lshl_b32 s77, s78, 15\n\t"
    "s_add_u32 s83, s83, s77\n\t"
    "s_lshl_b32 s76, s79, 8\n\t"
    "s_lshl_b32 s77, s50, 6\n\t"
    "s_add_u32 s76, s76, s77\n\t"
    "s_add_u32 s83, s83, s76\n\t"
    "s_lshr_b32 s76, s78, 1\n\t"
    "s_lshl_b32 s76, s76, 2\n\t"
    "s_lshl_b32 s77, %[qb], 4\n\t"
    "s_add_u32 s76, s76, s77\n\t"
    "s_mul_i32 s72, s76, 57344\n\t"
    "s_mul_i32 s77, s50, 14336\n\t"
    "s_add_u32 s72, s72, s77\n\t"
    "s_add_u32 s72, s72, 2228224\n\t"
    "s_and_b32 s76, s78, 1\n\t"
    "s_lshl_b32 s76, s76, 7\n\t"
    "s_lshl_b32 s77, s79, 8\n\t"
    "s_add_u32 s76, s76, s77\n\t"
    "s_mul_i32 s74, s50, 458752\n\t"
    "s_add_u32 s74, s74, s76\n\t"
    "s_waitcnt lgkmcnt(0)\n\t"
    "s_add_u32 s88, s88, s76\n\t"
    "s_addc_u32 s89, s89, 0\n\t"
    "s_lshl_b32 s76, %[qb], 2\n\t"
    "s_add_u32 s76, s76, s79\n\t"
    "s_lshl_b32 s76, s76, 2\n\t"
    "s_lshr_b32 s77, s78, 1\n\t"
    "s_add_u32 s76, s76, s77\n\t"
    "s_lshl_b32 s90, s76, 6\n\t"
    "s_add_u32 s92, s90, 20480\n\t"
    "s_lshl_b32 s76, s76, 14\n\t"
    "s_lshl_b32 s77, s50, 12\n\t"
    "s_add_u32 s100, s76, s77\n\t"
    "s_and_b32 s77, s78, 1\n\t"
    "s_lshl_b32 s77, s77, 11\n\t"
    "s_add_u32 s100, s100, s77\n\t"
    "s_and_b32 s77, s78, 1\n\t"
    "s_lshl_b32 s77, s77, 13\n\t"
    "s_add_u32 s101, s76, s77\n\t"
    "s_lshl_b32 s77, s50, 10\n\t"
    "s_add_u32 s101, s101, s77\n\t"
    "s_lshl_b32 s76, %[qb], 2\n\t"
    "s_add_u32 s76, s76, s79\n\t"
    "s_lshl_b32 s90, s76, 3\n\t"
    "s_add_u32 s90, s90, s78\n\t"
    "s_lshl_b32 s90, s90, 6\n\t"
    "s_add_u32 s90, s90, 20480\n\t"
    "v_lshrrev_b32 v96, 4, v255\n\t"
    "v_and_b32 v97, 15, v96\n\t"
    "v_lshrrev_b32 v98, 4, v96\n\t"
    "v_or_b32 v99, 0, v98\n\t"
    "v_xor_b32 v99, v99, v97\n\t"
    "v_lshlrev_b32 v99, 4, v99\n\t"
    "v_lshl_add_u32 v128, v97, 8, v99\n\t"
    "v_or_b32 v99, 4, v98\n\t"
    "v_xor_b32 v99, v99, v97\n\t"
    "v_lshlrev_b32 v99, 4, v99\n\t"
    "v_lshl_add_u32 v129, v97, 8, v99\n\t"
    "v_or_b32 v99, 8, v98\n\t"
    "v_xor_b32 v99, v99, v97\n\t"
    "v_lshlrev_b32 v99, 4, v99\n\t"
    "v_lshl_add_u32 v130, v97, 8, v99\n\t"
    "v_or_b32 v99, 12, v98\n\t"
    "v_xor_b32 v99, v99, v97\n\t"
    "v_lshlrev_b32 v99, 4, v99\n\t"
    "v_lshl_add_u32 v131, v97, 8, v99\n\t"
    "v_lshrrev_b32 v101, 2, v97\n\t"
    "v_and_b32 v102, 3, v96\n\t"
    "v_and_b32 v103, 1, v98\n\t"
    "v_lshl_add_u32 v103, v103, 2, v101\n\t"
    "v_lshl_add_u32 v100, v98, 2, v101\n\t"
    "v_lshlrev_b32 v100, 8, v100\n\t"
    "v_lshl_add_u32 v100, v102, 3, v100\n\t"
    "v_add_u32 v100, 65536, v100\n\t"
    "v_xor_b32 v99, 0, v103\n\t"
    "v_lshl_add_u32 v132, v99, 5, v100\n\t"
    "v_xor_b32 v99, 1, v103\n\t"
    "v_lshl_add_u32 v133, v99, 5, v100\n\t"
    "v_xor_b32 v99, 2, v103\n\t"
    "v_lshl_add_u32 v134, v99, 5, v100\n\t"
    "v_xor_b32 v99, 3, v103\n\t"
    "v_lshl_add_u32 v135, v99, 5, v100\n\t"
    "v_xor_b32 v99, 4, v103\n\t"
    "v_lshl_add_u32 v136, v99, 5, v100\n\t"
    "v_xor_b32 v99, 5, v103\n\t"
    "v_lshl_add_u32 v137, v99, 5, v100\n\t"
    "v_xor_b32 v99, 6, v103\n\t"
    "v_lshl_add_u32 v138, v99, 5, v100\n\t"
    "v_xor_b32 v99, 7, v103\n\t"
    "v_lshl_add_u32 v139, v99, 5, v100\n\t"
    "v_mov_b32 v99, 0x3f803f80\n\t"
    "v_cmp_eq_u32 vcc, 0, v97\n\t"
    "s_nop 1\n\t"
    "v_cndmask_b32 v140, 0, v99, vcc\n\t"
    "v_cndmask_b32 v141, 0, v99, vcc\n\t"
    "v_cndmask_b32 v142, 0, v99, vcc\n\t"
    "v_cndmask_b32 v143, 0, v99, vcc\n\t"
    "v_mov_b32 v144, 0\n\t"
    "v_mov_b32 v146, 0\n\t"
    "v_mov_b32 v156, 0xff800000\n\t"
    "v_mov_b32 v157, 0xff800000\n\t"
    "v_mov_b32 v158, 0xff800000\n\t"
    "v_mov_b32 v159, 0xff800000\n\t"
    "s_mov_b32 s91, 0\n\t"
    ".Latt%=:\n\t"
    "s_waitcnt vmcnt(24)\n\t"
    "s_barrier\n\t"
    "ds_read_b128 a[128:131], v128 offset:0\n\t"
    "ds_read_b128 a[132:135], v129 offset:0\n\t"
    "ds_read_b128 a[136:139], v130 offset:0\n\t"
    "ds_read_b128 a[140:143], v131 offset:0\n\t"
    "ds_read_b128 a[144:147], v128 offset:4096\n\t"
    "s_waitcnt lgkmcnt(1)\n\t"
    "v_mfma_f32_16x16x32_bf16 v[0:3], a[128:131], v[160:163], 0\n\t"
    "v_mfma_f32_16x16x32_bf16 v[0:3], a[132:135], v[164:167], v[0:3]\n\t"
    "v_mfma_f32_16x16x32_bf16 v[0:3], a[136:139], v[168:171], v[0:3]\n\t"
    "v_mfma_f32_16x16x32_bf16 v[0:3], a[140:143], v[172:175], v[0:3]\n\t"
    "ds_read_b128 a[148:151], v129 offset:4096\n\t"
    "v_mfma_f32_16x16x32_bf16 v[4:7], a[128:131], v[176:179], 0\n\t"
    "v_mfma_f32_16x16x32_bf16 v[4:7], a[132:135], v[180:183], v[4:7]\n\t"
    "v_mfma_f32_16x16x32_bf16 v[4:7], a[136:139], v[184:187], v[4:7]\n\t"
    "v_mfma_f32_16x16x32_bf16 v[4:7], a[140:143], v[188:191], v[4:7]\n\t"
    "ds_read_b128 a[152:155], v130 offset:4096\n\t"
    "v_mfma_f32_16x16x32_bf16 v[8:11], a[128:131], v[192:195], 0\n\t"
    "v_mfma_f32_16x16x32_bf16 v[8:11], a[132:135], v[196:199], v[8:11]\n\t"
    "v_mfma_f32_16x16x32_bf16 v[8:11], a[136:139], v[200:203], v[8:11]\n\t"
    "v_mfma_f32_16x16x32_bf16 v[8:11], a[140:143], v[204:207], v[8:11]\n\t"
    "ds_read_b128 a[156:159], v131 offset:4096\n\t"
    "v_mfma_f32_16x16x32_bf16 v[12:15], a[128:131], v[208:211], 0\n\t"
    "v_mfma_f32_16x16x32_bf16 v[12:15], a[132:135], v[212:215], v[12:15]\n\t"
    "v_mfma_f32_16x16x32_bf16 v[12:15], a[136:139], v[216:219], v[12:15]\n\t"
    "v_mfma_f32_16x16x32_bf16 v[12:15], a[140:143], v[220:223], v[12:15]\n\t"
    "ds_read_b128 a[128:131], v128 offset:8192\n\t"
    "s_nop 7\n\t"
    "v_max3_f32 v96, v0, v1, v2\n\t"
    "v_max_f32 v96, v96, v3\n\t"
    "v_max3_f32 v97, v4, v5, v6\n\t"
    "v_max_f32 v97, v97, v7\n\t"
    "v_max3_f32 v98, v8, v9, v10\n\t"
    "v_max_f32 v98, v98, v11\n\t"
    "v_max3_f32 v99, v12, v13, v14\n\t"
    "v_max_f32 v99, v99, v15\n\t"
    "v_mov_b32 v100, v96\n\t"
    "v_mov_b32 v101, v97\n\t"
    "v_mov_b32 v102, v98\n\t"
    "v_mov_b32 v103, v99\n\t"
    "s_nop 0\n\t"
    "v_permlane32_swap_b32 v96, v100\n\t"
    "v_permlane32_swap_b32 v97, v101\n\t"
    "v_permlane32_swap_b32 v98, v102\n\t"
    "v_permlane32_swap_b32 v99, v103\n\t"
    "s_nop 0\n\t"
    "v_max_f32 v96, v96, v100\n\t"
    "v_max_f32 v97, v97, v101\n\t"
    "v_max_f32 v98, v98, v102\n\t"
    "v_max_f32 v99, v99, v103\n\t"
    "v_mov_b32 v100, v96\n\t"
    "v_mov_b32 v101, v97\n\t"
    "v_mov_b32 v102, v98\n\t"
    "v_mov_b32 v103, v99\n\t"
    "s_nop 0\n\t"
    "v_permlane16_swap_b32 v96, v100\n\t"
    "v_permlane16_swap_b32 v97, v101\n\t"
    "v_permlane16_swap_b32 v98, v102\n\t"
    "v_permlane16_swap_b32 v99, v103\n\t"
    "s_nop 0\n\t"
    "v_max_f32 v96, v96, v100\n\t"
    "v_max_f32 v97, v97, v101\n\t"
    "v_max_f32 v98, v98, v102\n\t"
    "v_max_f32 v99, v99, v103\n\t"
    "v_max_f32 v96, v96, v156\n\t"
    "v_max_f32 v97, v97, v157\n\t"
    "v_max_f32 v98, v98, v158\n\t"
    "v_max_f32 v99, v99, v159\n\t"
    "v_sub_f32 v0, v0, v96\n\t"
    "v_sub_f32 v1, v1, v96\n\t"
    "v_sub_f32 v2, v2, v96\n\t"
    "v_sub_f32 v3, v3, v96\n\t"
    "v_sub_f32 v80, 0, v96\n\t"
    "v_sub_f32 v81, 0, v96\n\t"
    "v_sub_f32 v82, 0, v96\n\t"
    "v_sub_f32 v83, 0, v96\n\t"
    "v_sub_f32 v4, v4, v97\n\t"
    "v_sub_f32 v5, v5, v97\n\t"
    "v_sub_f32 v6, v6, v97\n\t"
    "v_sub_f32 v7, v7, v97\n\t"
    "v_sub_f32 v84, 0, v97\n\t"
    "v_sub_f32 v85, 0, v97\n\t"
    "v_sub_f32 v86, 0, v97\n\t"
    "v_sub_f32 v87, 0, v97\n\t"
    "v_sub_f32 v8, v8, v98\n\t"
    "v_sub_f32 v9, v9, v98\n\t"
    "v_sub_f32 v10, v10, v98\n\t"
    "v_sub_f32 v11, v11, v98\n\t"
    "v_sub_f32 v88, 0, v98\n\t"
    "v_sub_f32 v89, 0, v98\n\t"
    "v_sub_f32 v90, 0, v98\n\t"
    "v_sub_f32 v91, 0, v98\n\t"
    "v_sub_f32 v12, v12, v99\n\t"
    "v_sub_f32 v13, v13, v99\n\t"
    "v_sub_f32 v14, v14, v99\n\t"
    "v_sub_f32 v15, v15, v99\n\t"
    "v_sub_f32 v92, 0, v99\n\t"
    "v_sub_f32 v93, 0, v99\n\t"
    "v_sub_f32 v94, 0, v99\n\t"
    "v_sub_f32 v95, 0, v99\n\t"
    "v_exp_f32 v0, v0\n\t"
    "v_exp_f32 v1, v1\n\t"
    "v_exp_f32 v2, v2\n\t"
    "v_exp_f32 v3, v3\n\t"
    "s_nop 0\n\t"
    "v_add_f32 v144, v144, v0\n\t"
    "v_add_f32 v144, v144, v1\n\t"
    "v_add_f32 v144, v144, v2\n\t"
    "v_add_f32 v144, v144, v3\n\t"
    "v_cvt_pk_bf16_f32 v16, v0, v1\n\t"
    "v_cvt_pk_bf16_f32 v17, v2, v3\n\t"
    "v_exp_f32 v4, v4\n\t"
    "v_exp_f32 v5, v5\n\t"
    "v_exp_f32 v6, v6\n\t"
    "v_exp_f32 v7, v7\n\t"
    "s_nop 0\n\t"
    "v_cvt_pk_bf16_f32 v24, v4, v5\n\t"
    "v_cvt_pk_bf16_f32 v25, v6, v7\n\t"
    "v_exp_f32 v8, v8\n\t"
    "v_exp_f32 v9, v9\n\t"
    "v_exp_f32 v10, v10\n\t"
    "v_exp_f32 v11, v11\n\t"
    "s_nop 0\n\t"
    "v_add_f32 v146, v146, v8\n\t"
    "v_add_f32 v146, v146, v9\n\t"
    "v_add_f32 v146, v146, v10\n\t"
    "v_add_f32 v146, v146, v11\n\t"
    "v_cvt_pk_bf16_f32 v32, v8, v9\n\t"
    "v_cvt_pk_bf16_f32 v33, v10, v11\n\t"
    "s_waitcnt lgkmcnt(1)\n\t"
    "v_mfma_f32_16x16x32_bf16 v[0:3], a[144:147], v[160:163], v[80:83]\n\t"
    "v_exp_f32 v12, v12\n\t"
    "v_mfma_f32_16x16x32_bf16 v[0:3], a[148:151], v[164:167], v[0:3]\n\t"
    "v_mfma_f32_16x16x32_bf16 v[0:3], a[152:155], v[168:171], v[0:3]\n\t"
    "v_exp_f32 v13, v13\n\t"
    "v_mfma_f32_16x16x32_bf16 v[0:3], a[156:159], v[172:175], v[0:3]\n\t"
    "s_nop 0\n\t"
    "v_exp_f32 v14, v14\n\t"
    "s_nop 0\n\t"
    "ds_read_b128 a[132:135], v129 offset:8192\n\t"
    "v_cvt_pk_bf16_f32 v40, v12, v13\n\t"
    "s_nop 0\n\t"
    "v_exp_f32 v15, v15\n\t"
    "s_waitcnt lgkmcnt(1)\n\t"
    "v_mfma_f32_16x16x32_bf16 v[4:7], a[144:147], v[176:179], v[84:87]\n\t"
    "v_exp_f32 v0, v0\n\t"
    "v_mfma_f32_16x16x32_bf16 v[4:7], a[148:151], v[180:183], v[4:7]\n\t"
    "v_cvt_pk_bf16_f32 v41, v14, v15\n\t"
    "v_mfma_f32_16x16x32_bf16 v[4:7], a[152:155], v[184:187], v[4:7]\n\t"
    "v_exp_f32 v1, v1\n\t"
    "v_mfma_f32_16x16x32_bf16 v[4:7], a[156:159], v[188:191], v[4:7]\n\t"
    "v_add_f32 v144, v144, v0\n\t"
    "s_nop 0\n\t"
    "v_exp_f32 v2, v2\n\t"
    "s_nop 0\n\t"
    "ds_read_b128 a[136:139], v130 offset:8192\n\t"
    "v_cvt_pk_bf16_f32 v18, v0, v1\n\t"
    "s_nop 0\n\t"
    "v_exp_f32 v3, v3\n\t"
    "s_nop 0\n\t"
    "s_waitcnt lgkmcnt(1)\n\t"
    "v_add_f32 v144, v144, v1\n\t"
    "v_add_f32 v144, v144, v2\n\t"
    "v_mfma_f32_16x16x32_bf16 v[8:11], a[144:147], v[192:195], v[88:91]\n\t"
    "v_exp_f32 v4, v4\n\t"
    "v_mfma_f32_16x16x32_bf16 v[8:11], a[148:151], v[196:199], v[8:11]\n\t"
    "v_add_f32 v144, v144, v3\n\t"
    "v_mfma_f32_16x16x32_bf16 v[8:11], a[152:155], v[200:203], v[8:11]\n\t"
    "v_exp_f32 v5, v5\n\t"
    "v_mfma_f32_16x16x32_bf16 v[8:11], a[156:159], v[204:207], v[8:11]\n\t"
    "v_cvt_pk_bf16_f32 v19, v2, v3\n\t"
    "s_nop 0\n\t"
    "v_exp_f32 v6, v6\n\t"
    "s_nop 0\n\t"
    "ds_read_b128 a[140:143], v131 offset:8192\n\t"
    "v_cvt_pk_bf16_f32 v26, v4, v5\n\t"
    "s_nop 0\n\t"
    "v_exp_f32 v7, v7\n\t"
    "s_waitcnt lgkmcnt(1)\n\t"
    "v_mfma_f32_16x16x32_bf16 v[12:15], a[144:147], v[208:211], v[92:95]\n\t"
    "v_exp_f32 v8, v8\n\t"
    "v_mfma_f32_16x16x32_bf16 v[12:15], a[148:151], v[212:215], v[12:15]\n\t"
    "v_cvt_pk_bf16_f32 v27, v6, v7\n\t"
    "v_mfma_f32_16x16x32_bf16 v[12:15], a[152:155], v[216:219], v[12:15]\n\t"
    "v_exp_f32 v9, v9\n\t"
    "v_mfma_f32_16x16x32_bf16 v[12:15], a[156:159], v[220:223], v[12:15]\n\t"
    "v_add_f32 v146, v146, v8\n\t"
    "s_nop 0\n\t"
    "v_exp_f32 v10, v10\n\t"
    "s_nop 0\n\t"
    "ds_read_b128 a[144:147], v128 offset:12288\n\t"
    "v_cvt_pk_bf16_f32 v34, v8, v9\n\t"
    "s_nop 0\n\t"
    "v_exp_f32 v11, v11\n\t"
    "s_nop 0\n\t"
    "s_waitcnt lgkmcnt(1)\n\t"
    "v_add_f32 v146, v146, v9\n\t"
    "v_add_f32 v146, v146, v10\n\t"
    "v_mfma_f32_16x16x32_bf16 v[0:3], a[128:131], v[160:163], v[80:83]\n\t"
    "v_exp_f32 v12, v12\n\t"
    "v_mfma_f32_16x16x32_bf16 v[0:3], a[132:135], v[164:167], v[0:3]\n\t"
    "v_add_f32 v146, v146, v11\n\t"
    "v_mfma_f32_16x16x32_bf16 v[0:3], a[136:139], v[168:171], v[0:3]\n\t"
    "v_exp_f32 v13, v13\n\t"
    "v_mfma_f32_16x16x32_bf16 v[0:3], a[140:143], v[172:175], v[0:3]\n\t"
    "v_cvt_pk_bf16_f32 v35, v10, v11\n\t"
    "s_nop 0\n\t"
    "v_exp_f32 v14, v14\n\t"
    "s_nop 0\n\t"
    "ds_read_b128 a[148:151], v129 offset:12288\n\t"
    "v_cvt_pk_bf16_f32 v42, v12, v13\n\t"
    "s_nop 0\n\t"
    "v_exp_f32 v15, v15\n\t"
    "s_waitcnt lgkmcnt(1)\n\t"
    "v_mfma_f32_16x16x32_bf16 v[4:7], a[128:131], v[176:179], v[84:87]\n\t"
    "v_exp_f32 v0, v0\n\t"
    "v_mfma_f32_16x16x32_bf16 v[4:7], a[132:135], v[180:183], v[4:7]\n\t"
    "v_cvt_pk_bf16_f32 v43, v14, v15\n\t"
    "v_mfma_f32_16x16x32_bf16 v[4:7], a[136:139], v[184:187], v[4:7]\n\t"
    "v_exp_f32 v1, v1\n\t"
    "v_mfma_f32_16x16x32_bf16 v[4:7], a[140:143], v[188:191], v[4:7]\n\t"
    "v_add_f32 v144, v144, v0\n\t"
    "s_nop 0\n\t"
    "v_exp_f32 v2, v2\n\t"
    "s_nop 0\n\t"
    "ds_read_b128 a[152:155], v130 offset:12288\n\t"
    "v_cvt_pk_bf16_f32 v20, v0, v1\n\t"
    "s_nop 0\n\t"
    "v_exp_f32 v3, v3\n\t"
    "s_nop 0\n\t"
    "s_waitcnt lgkmcnt(1)\n\t"
    "v_add_f32 v144, v144, v1\n\t"
    "v_add_f32 v144, v144, v2\n\t"
    "v_mfma_f32_16x16x32_bf16 v[8:11], a[128:131], v[192:195], v[88:91]\n\t"
    "v_exp_f32 v4, v4\n\t"
    "v_mfma_f32_16x16x32_bf16 v[8:11], a[132:135], v[196:199], v[8:11]\n\t"
    "v_add_f32 v144, v144, v3\n\t"
    "v_mfma_f32_16x16x32_bf16 v[8:11], a[136:139], v[200:203], v[8:11]\n\t"
    "v_exp_f32 v5, v5\n\t"
    "v_mfma_f32_16x16x32_bf16 v[8:11], a[140:143], v[204:207], v[8:11]\n\t"
    "v_cvt_pk_bf16_f32 v21, v2, v3\n\t"
    "s_nop 0\n\t"
    "v_exp_f32 v6, v6\n\t"
    "s_nop 0\n\t"
    "ds_read_b128 a[156:159], v131 offset:12288\n\t"
    "v_cvt_pk_bf16_f32 v28, v4, v5\n\t"
    "s_nop 0\n\t"
    "v_exp_f32 v7, v7\n\t"
    "s_waitcnt lgkmcnt(1)\n\t"
    "v_mfma_f32_16x16x32_bf16 v[12:15], a[128:131], v[208:211], v[92:95]\n\t"
    "s_waitcnt vmcnt(16)\n\t"
    "v_exp_f32 v8, v8\n\t"
    "v_mfma_f32_16x16x32_bf16 v[12:15], a[132:135], v[212:215], v[12:15]\n\t"
    "v_cvt_pk_bf16_f32 v29, v6, v7\n\t"
    "v_mfma_f32_16x16x32_bf16 v[12:15], a[136:139], v[216:219], v[12:15]\n\t"
    "s_barrier\n\t"
    "v_exp_f32 v9, v9\n\t"
    "v_mfma_f32_16x16x32_bf16 v[12:15], a[140:143], v[220:223], v[12:15]\n\t"
    "v_add_f32 v146, v146, v8\n\t"
    "s_nop 0\n\t"
    "v_exp_f32 v10, v10\n\t"
    "s_nop 0\n\t"
    "ds_read_b128 a[128:131], v128 offset:16384\n\t"
    "v_cvt_pk_bf16_f32 v36, v8, v9\n\t"
    "s_nop 0\n\t"
    "v_exp_f32 v11, v11\n\t"
    "s_nop 0\n\t"
    "s_waitcnt lgkmcnt(1)\n\t"
    "v_add_f32 v146, v146, v9\n\t"
    "v_add_f32 v146, v146, v10\n\t"
    "v_mfma_f32_16x16x32_bf16 v[0:3], a[144:147], v[160:163], v[80:83]\n\t"
    "v_exp_f32 v12, v12\n\t"
    "v_mfma_f32_16x16x32_bf16 v[0:3], a[148:151], v[164:167], v[0:3]\n\t"
    "v_add_f32 v146, v146, v11\n\t"
    "v_mfma_f32_16x16x32_bf16 v[0:3], a[152:155], v[168:171], v[0:3]\n\t"
    "s_mov_b32 m0, s55\n\t"
    "v_exp_f32 v13, v13\n\t"
    "v_mfma_f32_16x16x32_bf16 v[0:3], a[156:159], v[172:175], v[0:3]\n\t"
    "v_cvt_pk_bf16_f32 v37, v10, v11\n\t"
    "s_nop 0\n\t"
    "s_add_u32 s48, s48, 16384\n\t"
    "v_exp_f32 v14, v14\n\t"
    "s_nop 0\n\t"
    "ds_read_b128 a[132:135], v129 offset:16384\n\t"
    "v_cvt_pk_bf16_f32 v44, v12, v13\n\t"
    "s_nop 0\n\t"
    "v_exp_f32 v15, v15\n\t"
    "s_waitcnt lgkmcnt(1)\n\t"
    "buffer_load_dwordx4 v224, s[40:43], s48 offen offset:0 lds\n\t"
    "v_mfma_f32_16x16x32_bf16 v[4:7], a[144:147], v[176:179], v[84:87]\n\t"
    "v_exp_f32 v0, v0\n\t"
    "v_mfma_f32_16x16x32_bf16 v[4:7], a[148:151], v[180:183], v[4:7]\n\t"
    "ds_read_b64_tr_b16 a[160:161], v132 offset:0\n\t"
    "v_cvt_pk_bf16_f32 v45, v14, v15\n\t"
    "v_mfma_f32_16x16x32_bf16 v[4:7], a[152:155], v[184:187], v[4:7]\n\t"
    "v_exp_f32 v1, v1\n\t"
    "v_mfma_f32_16x16x32_bf16 v[4:7], a[156:159], v[188:191], v[4:7]\n\t"
    "ds_read_b64_tr_b16 a[162:163], v132 offset:4096\n\t"
    "v_add_f32 v144, v144, v0\n\t"
    "s_nop 0\n\t"
    "v_exp_f32 v2, v2\n\t"
    "s_nop 0\n\t"
    "ds_read_b128 a[136:139], v130 offset:16384\n\t"
    "v_cvt_pk_bf16_f32 v22, v0, v1\n\t"
    "s_nop 0\n\t"
    "v_exp_f32 v3, v3\n\t"
    "s_nop 0\n\t"
    "s_waitcnt lgkmcnt(3)\n\t"
    "v_add_f32 v144, v144, v1\n\t"
    "v_add_f32 v144, v144, v2\n\t"
    "v_mfma_f32_16x16x32_bf16 v[8:11], a[144:147], v[192:195], v[88:91]\n\t"
    "v_exp_f32 v4, v4\n\t"
    "v_mfma_f32_16x16x32_bf16 v[8:11], a[148:151], v[196:199], v[8:11]\n\t"
    "ds_read_b64_tr_b16 a[164:165], v132 offset:8192\n\t"
    "v_add_f32 v144, v144, v3\n\t"
    "v_mfma_f32_16x16x32_bf16 v[8:11], a[152:155], v[200:203], v[8:11]\n\t"
    "s_mov_b32 m0, s55\n\t"
    "v_exp_f32 v5, v5\n\t"
    "v_mfma_f32_16x16x32_bf16 v[8:11], a[156:159], v[204:207], v[8:11]\n\t"
    "ds_read_b64_tr_b16 a[166:167], v132 offset:12288\n\t"
    "v_cvt_pk_bf16_f32 v23, v2, v3\n\t"
    "s_nop 0\n\t"
    "v_exp_f32 v6, v6\n\t"
    "s_nop 0\n\t"
    "ds_read_b128 a[140:143], v131 offset:16384\n\t"
    "v_cvt_pk_bf16_f32 v30, v4, v5\n\t"
    "s_nop 0\n\t"
    "v_exp_f32 v7, v7\n\t"
    "s_waitcnt lgkmcnt(3)\n\t"
    "buffer_load_dwordx4 v225, s[40:43], s48 offen offset:1024 lds\n\t"
    "v_mfma_f32_16x16x32_bf16 v[12:15], a[144:147], v[208:211], v[92:95]\n\t"
    "v_exp_f32 v8, v8\n\t"
    "v_mfma_f32_16x16x32_bf16 v[12:15], a[148:151], v[212:215], v[12:15]\n\t"
    "ds_read_b64_tr_b16 a[168:169], v133 offset:0\n\t"
    "v_cvt_pk_bf16_f32 v31, v6, v7\n\t"
    "v_mfma_f32_16x16x32_bf16 v[12:15], a[152:155], v[216:219], v[12:15]\n\t"
    "v_exp_f32 v9, v9\n\t"
    "v_mfma_f32_16x16x32_bf16 v[12:15], a[156:159], v[220:223], v[12:15]\n\t"
    "ds_read_b64_tr_b16 a[170:171], v133 offset:4096\n\t"
    "v_add_f32 v146, v146, v8\n\t"
    "s_nop 0\n\t"
    "v_exp_f32 v10, v10\n\t"
    "s_nop 0\n\t"
    "ds_read_b128 a[144:147], v128 offset:20480\n\t"
    "v_cvt_pk_bf16_f32 v38, v8, v9\n\t"
    "s_nop 0\n\t"
    "v_exp_f32 v11, v11\n\t"
    "s_nop 0\n\t"
    "s_waitcnt lgkmcnt(3)\n\t"
    "v_add_f32 v146, v146, v9\n\t"
    "v_add_f32 v146, v146, v10\n\t"
    "v_mfma_f32_16x16x32_bf16 v[0:3], a[128:131], v[160:163], v[80:83]\n\t"
    "v_exp_f32 v12, v12\n\t"
    "v_mfma_f32_16x16x32_bf16 v[0:3], a[132:135], v[164:167], v[0:3]\n\t"
    "ds_read_b64_tr_b16 a[172:173], v133 offset:8192\n\t"
    "v_add_f32 v146, v146, v11\n\t"
    "v_mfma_f32_16x16x32_bf16 v[0:3], a[136:139], v[168:171], v[0:3]\n\t"
    "s_mov_b32 m0, s55\n\t"
    "v_exp_f32 v13, v13\n\t"
    "v_mfma_f32_16x16x32_bf16 v[0:3], a[140:143], v[172:175], v[0:3]\n\t"
    "ds_read_b64_tr_b16 a[174:175], v133 offset:12288\n\t"
    "v_cvt_pk_bf16_f32 v39, v10, v11\n\t"
    "v_mfma_f32_16x16x32_bf16 a[0:3], a[160:163], v[16:19], 0\n\t"
    "v_exp_f32 v14, v14\n\t"
    "v_mfma_f32_16x16x32_bf16 a[0:3], a[164:167], v[20:23], a[0:3]\n\t"
    "ds_read_b128 a[148:151], v129 offset:20480\n\t"
    "v_cvt_pk_bf16_f32 v46, v12, v13\n\t"
    "v_mfma_f32_16x16x32_bf16 a[4:7], a[160:163], v[24:27], 0\n\t"
    "v_exp_f32 v15, v15\n\t"
    "v_mfma_f32_16x16x32_bf16 a[4:7], a[164:167], v[28:31], a[4:7]\n\t"
    "s_waitcnt lgkmcnt(3)\n\t"
    "buffer_load_dwordx4 v226, s[40:43], s48 offen offset:2048 lds\n\t"
    "v_mfma_f32_16x16x32_bf16 v[4:7], a[128:131], v[176:179], v[84:87]\n\t"
    "v_exp_f32 v0, v0\n\t"
    "v_mfma_f32_16x16x32_bf16 v[4:7], a[132:135], v[180:183], v[4:7]\n\t"
    "ds_read_b64_tr_b16 a[176:177], v134 offset:0\n\t"
    "v_cvt_pk_bf16_f32 v47, v14, v15\n\t"
    "v_mfma_f32_16x16x32_bf16 v[4:7], a[136:139], v[184:187], v[4:7]\n\t"
    "v_exp_f32 v1, v1\n\t"
    "v_mfma_f32_16x16x32_bf16 v[4:7], a[140:143], v[188:191], v[4:7]\n\t"
    "ds_read_b64_tr_b16 a[178:179], v134 offset:4096\n\t"
    "v_add_f32 v144, v144, v0\n\t"
    "v_mfma_f32_16x16x32_bf16 a[8:11], a[160:163], v[32:35], 0\n\t"
    "v_exp_f32 v2, v2\n\t"
    "v_mfma_f32_16x16x32_bf16 a[8:11], a[164:167], v[36:39], a[8:11]\n\t"
    "ds_read_b128 a[152:155], v130 offset:20480\n\t"
    "v_cvt_pk_bf16_f32 v48, v0, v1\n\t"
    "v_mfma_f32_16x16x32_bf16 a[12:15], a[160:163], v[40:43], 0\n\t"
    "v_exp_f32 v3, v3\n\t"
    "v_mfma_f32_16x16x32_bf16 a[12:15], a[164:167], v[44:47], a[12:15]\n\t"
    "s_waitcnt lgkmcnt(3)\n\t"
    "v_add_f32 v144, v144, v1\n\t"
    "v_add_f32 v144, v144, v2\n\t"
    "v_mfma_f32_16x16x32_bf16 v[148:151], v[140:143], v[24:27], 0\n\t"
    "v_mfma_f32_16x16x32_bf16 v[148:151], v[140:143], v[28:31], v[148:151]\n\t"
    "v_mfma_f32_16x16x32_bf16 v[8:11], a[128:131], v[192:195], v[88:91]\n\t"
    "v_exp_f32 v4, v4\n\t"
    "v_mfma_f32_16x16x32_bf16 v[8:11], a[132:135], v[196:199], v[8:11]\n\t"
    "ds_read_b64_tr_b16 a[180:181], v134 offset:8192\n\t"
    "v_add_f32 v144, v144, v3\n\t"
    "v_mfma_f32_16x16x32_bf16 v[8:11], a[136:139], v[200:203], v[8:11]\n\t"
    "s_mov_b32 m0, s55\n\t"
    "v_exp_f32 v5, v5\n\t"
    "v_mfma_f32_16x16x32_bf16 v[8:11], a[140:143], v[204:207], v[8:11]\n\t"
    "ds_read_b64_tr_b16 a[182:183], v134 offset:12288\n\t"
    "v_cvt_pk_bf16_f32 v49, v2, v3\n\t"
    "v_mfma_f32_16x16x32_bf16 a[16:19], a[168:171], v[16:19], 0\n\t"
    "v_exp_f32 v6, v6\n\t"
    "v_mfma_f32_16x16x32_bf16 a[16:19], a[172:175], v[20:23], a[16:19]\n\t"
    "ds_read_b128 a[156:159], v131 offset:20480\n\t"
    "v_cvt_pk_bf16_f32 v56, v4, v5\n\t"
    "v_mfma_f32_16x16x32_bf16 a[20:23], a[168:171], v[24:27], 0\n\t"
    "v_exp_f32 v7, v7\n\t"
    "v_mfma_f32_16x16x32_bf16 a[20:23], a[172:175], v[28:31], a[20:23]\n\t"
    "s_waitcnt lgkmcnt(3)\n\t"
    "buffer_load_dwordx4 v227, s[40:43], s48 offen offset:3072 lds\n\t"
    "v_mfma_f32_16x16x32_bf16 v[12:15], a[128:131], v[208:211], v[92:95]\n\t"
    "v_exp_f32 v8, v8\n\t"
    "v_mfma_f32_16x16x32_bf16 v[12:15], a[132:135], v[212:215], v[12:15]\n\t"
    "ds_read_b64_tr_b16 a[184:185], v135 offset:0\n\t"
    "v_cvt_pk_bf16_f32 v57, v6, v7\n\t"
    "v_mfma_f32_16x16x32_bf16 v[12:15], a[136:139], v[216:219], v[12:15]\n\t"
    "v_exp_f32 v9, v9\n\t"
    "v_mfma_f32_16x16x32_bf16 v[12:15], a[140:143], v[220:223], v[12:15]\n\t"
    "ds_read_b64_tr_b16 a[186:187], v135 offset:4096\n\t"
    "v_add_f32 v146, v146, v8\n\t"
    "v_mfma_f32_16x16x32_bf16 a[24:27], a[168:171], v[32:35], 0\n\t"
    "v_exp_f32 v10, v10\n\t"
    "v_mfma_f32_16x16x32_bf16 a[24:27], a[172:175], v[36:39], a[24:27]\n\t"
    "ds_read_b128 a[128:131], v128 offset:24576\n\t"
    "v_cvt_pk_bf16_f32 v64, v8, v9\n\t"
    "v_mfma_f32_16x16x32_bf16 a[28:31], a[168:171], v[40:43], 0\n\t"
    "v_exp_f32 v11, v11\n\t"
    "v_mfma_f32_16x16x32_bf16 a[28:31], a[172:175], v[44:47], a[28:31]\n\t"
    "s_waitcnt lgkmcnt(3)\n\t"
    "v_add_f32 v146, v146, v9\n\t"
    "v_add_f32 v146, v146, v10\n\t"
    "v_mfma_f32_16x16x32_bf16 v[152:155], v[140:143], v[40:43], 0\n\t"
    "v_mfma_f32_16x16x32_bf16 v[152:155], v[140:143], v[44:47], v[152:155]\n\t"
    "v_mfma_f32_16x16x32_bf16 v[0:3], a[144:147], v[160:163], v[80:83]\n\t"
    "v_exp_f32 v12, v12\n\t"
    "v_mfma_f32_16x16x32_bf16 v[0:3], a[148:151], v[164:167], v[0:3]\n\t"
    "ds_read_b64_tr_b16 a[188:189], v135 offset:8192\n\t"
    "v_add_f32 v146, v146, v11\n\t"
    "v_mfma_f32_16x16x32_bf16 v[0:3], a[152:155], v[168:171], v[0:3]\n\t"
    "s_mov_b32 m0, s58\n\t"
    "v_exp_f32 v13, v13\n\t"
    "v_mfma_f32_16x16x32_bf16 v[0:3], a[156:159], v[172:175], v[0:3]\n\t"
    "ds_read_b64_tr_b16 a[190:191], v135 offset:12288\n\t"
    "v_cvt_pk_bf16_f32 v65, v10, v11\n\t"
    "v_mfma_f32_16x16x32_bf16 a[32:35], a[176:179], v[16:19], 0\n\t"
    "s_add_u32 s51, s51, 16384\n\t"
    "v_exp_f32 v14, v14\n\t"
    "v_mfma_f32_16x16x32_bf16 a[32:35], a[180:183], v[20:23], a[32:35]\n\t"
    "ds_read_b128 a[132:135], v129 offset:24576\n\t"
    "v_cvt_pk_bf16_f32 v72, v12, v13\n\t"
    "v_mfma_f32_16x16x32_bf16 a[36:39], a[176:179], v[24:27], 0\n\t"
    "v_exp_f32 v15, v15\n\t"
    "v_mfma_f32_16x16x32_bf16 a[36:39], a[180:183], v[28:31], a[36:39]\n\t"
    "s_waitcnt lgkmcnt(3)\n\t"
    "buffer_load_dwordx4 v228, s[44:47], s51 offen offset:0 lds\n\t"
    "v_mfma_f32_16x16x32_bf16 v[4:7], a[144:147], v[176:179], v[84:87]\n\t"
    "v_exp_f32 v0, v0\n\t"
    "v_mfma_f32_16x16x32_bf16 v[4:7], a[148:151], v[180:183], v[4:7]\n\t"
    "ds_read_b64_tr_b16 a[160:161], v136 offset:0\n\t"
    "v_cvt_pk_bf16_f32 v73, v14, v15\n\t"
    "v_mfma_f32_16x16x32_bf16 v[4:7], a[152:155], v[184:187], v[4:7]\n\t"
    "v_exp_f32 v1, v1\n\t"
    "v_mfma_f32_16x16x32_bf16 v[4:7], a[156:159], v[188:191], v[4:7]\n\t"
    "ds_read_b64_tr_b16 a[162:163], v136 offset:4096\n\t"
    "v_add_f32 v144, v144, v0\n\t"
    "v_mfma_f32_16x16x32_bf16 a[40:43], a[176:179], v[32:35], 0\n\t"
    "v_exp_f32 v2, v2\n\t"
    "v_mfma_f32_16x16x32_bf16 a[40:43], a[180:183], v[36:39], a[40:43]\n\t"
    "ds_read_b128 a[136:139], v130 offset:24576\n\t"
    "v_cvt_pk_bf16_f32 v50, v0, v1\n\t"
    "v_mfma_f32_16x16x32_bf16 a[44:47], a[176:179], v[40:43], 0\n\t"
    "v_exp_f32 v3, v3\n\t"
    "v_mfma_f32_16x16x32_bf16 a[44:47], a[180:183], v[44:47], a[44:47]\n\t"
    "s_waitcnt lgkmcnt(3)\n\t"
    "v_add_f32 v144, v144, v1\n\t"
    "v_add_f32 v144, v144, v2\n\t"
    "v_mfma_f32_16x16x32_bf16 v[8:11], a[144:147], v[192:195], v[88:91]\n\t"
    "v_exp_f32 v4, v4\n\t"
    "v_mfma_f32_16x16x32_bf16 v[8:11], a[148:151], v[196:199], v[8:11]\n\t"
    "ds_read_b64_tr_b16 a[164:165], v136 offset:8192\n\t"
    "v_add_f32 v144, v144, v3\n\t"
    "v_mfma_f32_16x16x32_bf16 v[8:11], a[152:155], v[200:203], v[8:11]\n\t"
    "s_mov_b32 m0, s58\n\t"
    "v_exp_f32 v5, v5\n\t"
    "v_mfma_f32_16x16x32_bf16 v[8:11], a[156:159], v[204:207], v[8:11]\n\t"
    "ds_read_b64_tr_b16 a[166:167], v136 offset:12288\n\t"
    "v_cvt_pk_bf16_f32 v51, v2, v3\n\t"
    "v_mfma_f32_16x16x32_bf16 a[48:51], a[184:187], v[16:19], 0\n\t"
    "v_exp_f32 v6, v6\n\t"
    "v_mfma_f32_16x16x32_bf16 a[48:51], a[188:191], v[20:23], a[48:51]\n\t"
    "ds_read_b128 a[140:143], v131 offset:24576\n\t"
    "v_cvt_pk_bf16_f32 v58, v4, v5\n\t"
    "v_mfma_f32_16x16x32_bf16 a[52:55], a[184:187], v[24:27], 0\n\t"
    "v_exp_f32 v7, v7\n\t"
    "v_mfma_f32_16x16x32_bf16 a[52:55], a[188:191], v[28:31], a[52:55]\n\t"
    "s_waitcnt lgkmcnt(3)\n\t"
    "buffer_load_dwordx4 v229, s[44:47], s51 offen offset:1024 lds\n\t"
    "v_mfma_f32_16x16x32_bf16 v[12:15], a[144:147], v[208:211], v[92:95]\n\t"
    "v_exp_f32 v8, v8\n\t"
    "v_mfma_f32_16x16x32_bf16 v[12:15], a[148:151], v[212:215], v[12:15]\n\t"
    "ds_read_b64_tr_b16 a[168:169], v137 offset:0\n\t"
    "v_cvt_pk_bf16_f32 v59, v6, v7\n\t"
    "v_mfma_f32_16x16x32_bf16 v[12:15], a[152:155], v[216:219], v[12:15]\n\t"
    "v_exp_f32 v9, v9\n\t"
    "v_mfma_f32_16x16x32_bf16 v[12:15], a[156:159], v[220:223], v[12:15]\n\t"
    "ds_read_b64_tr_b16 a[170:171], v137 offset:4096\n\t"
    "v_add_f32 v146, v146, v8\n\t"
    "v_mfma_f32_16x16x32_bf16 a[56:59], a[184:187], v[32:35], 0\n\t"
    "v_exp_f32 v10, v10\n\t"
    "v_mfma_f32_16x16x32_bf16 a[56:59], a[188:191], v[36:39], a[56:59]\n\t"
    "ds_read_b128 a[144:147], v128 offset:28672\n\t"
    "v_cvt_pk_bf16_f32 v66, v8, v9\n\t"
    "v_mfma_f32_16x16x32_bf16 a[60:63], a[184:187], v[40:43], 0\n\t"
    "v_exp_f32 v11, v11\n\t"
    "v_mfma_f32_16x16x32_bf16 a[60:63], a[188:191], v[44:47], a[60:63]\n\t"
    "s_waitcnt lgkmcnt(3)\n\t"
    "v_add_f32 v146, v146, v9\n\t"
    "v_add_f32 v146, v146, v10\n\t"
    "v_mfma_f32_16x16x32_bf16 v[0:3], a[128:131], v[160:163], v[80:83]\n\t"
    "v_exp_f32 v12, v12\n\t"
    "v_mfma_f32_16x16x32_bf16 v[0:3], a[132:135], v[164:167], v[0:3]\n\t"
    "ds_read_b64_tr_b16 a[172:173], v137 offset:8192\n\t"
    "v_add_f32 v146, v146, v11\n\t"
    "v_mfma_f32_16x16x32_bf16 v[0:3], a[136:139], v[168:171], v[0:3]\n\t"
    "s_mov_b32 m0, s58\n\t"
    "v_exp_f32 v13, v13\n\t"
    "v_mfma_f32_16x16x32_bf16 v[0:3], a[140:143], v[172:175], v[0:3]\n\t"
    "ds_read_b64_tr_b16 a[174:175], v137 offset:12288\n\t"
    "v_cvt_pk_bf16_f32 v67, v10, v11\n\t"
    "v_mfma_f32_16x16x32_bf16 a[64:67], a[160:163], v[16:19], 0\n\t"
    "v_exp_f32 v14, v14\n\t"
    "v_mfma_f32_16x16x32_bf16 a[64:67], a[164:167], v[20:23], a[64:67]\n\t"
    "ds_read_b128 a[148:151], v129 offset:28672\n\t"
    "v_cvt_pk_bf16_f32 v74, v12, v13\n\t"
    "v_mfma_f32_16x16x32_bf16 a[68:71], a[160:163], v[24:27], 0\n\t"
    "v_exp_f32 v15, v15\n\t"
    "v_mfma_f32_16x16x32_bf16 a[68:71], a[164:167], v[28:31], a[68:71]\n\t"
    "s_waitcnt lgkmcnt(3)\n\t"
    "buffer_load_dwordx4 v228, s[44:47], s51 offen offset:2048 lds\n\t"
    "v_mfma_f32_16x16x32_bf16 v[4:7], a[128:131], v[176:179], v[84:87]\n\t"
    "v_exp_f32 v0, v0\n\t"
    "v_mfma_f32_16x16x32_bf16 v[4:7], a[132:135], v[180:183], v[4:7]\n\t"
    "ds_read_b64_tr_b16 a[176:177], v138 offset:0\n\t"
    "v_cvt_pk_bf16_f32 v75, v14, v15\n\t"
    "v_mfma_f32_16x16x32_bf16 v[4:7], a[136:139], v[184:187], v[4:7]\n\t"
    "v_exp_f32 v1, v1\n\t"
    "v_mfma_f32_16x16x32_bf16 v[4:7], a[140:143], v[188:191], v[4:7]\n\t"
    "ds_read_b64_tr_b16 a[178:179], v138 offset:4096\n\t"
    "v_add_f32 v144, v144, v0\n\t"
    "v_mfma_f32_16x16x32_bf16 a[72:75], a[160:163], v[32:35], 0\n\t"
    "v_exp_f32 v2, v2\n\t"
    "v_mfma_f32_16x16x32_bf16 a[72:75], a[164:167], v[36:39], a[72:75]\n\t"
    "ds_read_b128 a[152:155], v130 offset:28672\n\t"
    "v_cvt_pk_bf16_f32 v52, v0, v1\n\t"
    "v_mfma_f32_16x16x32_bf16 a[76:79], a[160:163], v[40:43], 0\n\t"
    "v_exp_f32 v3, v3\n\t"
    "v_mfma_f32_16x16x32_bf16 a[76:79], a[164:167], v[44:47], a[76:79]\n\t"
    "s_waitcnt lgkmcnt(3)\n\t"
    "v_add_f32 v144, v144, v1\n\t"
    "v_add_f32 v144, v144, v2\n\t"
    "v_mfma_f32_16x16x32_bf16 v[8:11], a[128:131], v[192:195], v[88:91]\n\t"
    "v_exp_f32 v4, v4\n\t"
    "v_mfma_f32_16x16x32_bf16 v[8:11], a[132:135], v[196:199], v[8:11]\n\t"
    "ds_read_b64_tr_b16 a[180:181], v138 offset:8192\n\t"
    "v_add_f32 v144, v144, v3\n\t"
    "v_mfma_f32_16x16x32_bf16 v[8:11], a[136:139], v[200:203], v[8:11]\n\t"
    "s_mov_b32 m0, s58\n\t"
    "v_exp_f32 v5, v5\n\t"
    "v_mfma_f32_16x16x32_bf16 v[8:11], a[140:143], v[204:207], v[8:11]\n\t"
    "ds_read_b64_tr_b16 a[182:183], v138 offset:12288\n\t"
    "v_cvt_pk_bf16_f32 v53, v2, v3\n\t"
    "v_mfma_f32_16x16x32_bf16 a[80:83], a[168:171], v[16:19], 0\n\t"
    "v_exp_f32 v6, v6\n\t"
    "v_mfma_f32_16x16x32_bf16 a[80:83], a[172:175], v[20:23], a[80:83]\n\t"
    "ds_read_b128 a[156:159], v131 offset:28672\n\t"
    "v_cvt_pk_bf16_f32 v60, v4, v5\n\t"
    "v_mfma_f32_16x16x32_bf16 a[84:87], a[168:171], v[24:27], 0\n\t"
    "v_exp_f32 v7, v7\n\t"
    "v_mfma_f32_16x16x32_bf16 a[84:87], a[172:175], v[28:31], a[84:87]\n\t"
    "s_waitcnt lgkmcnt(3)\n\t"
    "buffer_load_dwordx4 v229, s[44:47], s51 offen offset:3072 lds\n\t"
    "v_mfma_f32_16x16x32_bf16 v[12:15], a[128:131], v[208:211], v[92:95]\n\t"
    "s_waitcnt vmcnt(15)\n\t"
    "v_exp_f32 v8, v8\n\t"
    "v_mfma_f32_16x16x32_bf16 v[12:15], a[132:135], v[212:215], v[12:15]\n\t"
    "ds_read_b64_tr_b16 a[184:185], v139 offset:0\n\t"
    "v_cvt_pk_bf16_f32 v61, v6, v7\n\t"
    "v_mfma_f32_16x16x32_bf16 v[12:15], a[136:139], v[216:219], v[12:15]\n\t"
    "s_barrier\n\t"
    "v_exp_f32 v9, v9\n\t"
    "v_mfma_f32_16x16x32_bf16 v[12:15], a[140:143], v[220:223], v[12:15]\n\t"
    "ds_read_b64_tr_b16 a[186:187], v139 offset:4096\n\t"
    "v_add_f32 v146, v146, v8\n\t"
    "v_mfma_f32_16x16x32_bf16 a[88:91], a[168:171], v[32:35], 0\n\t"
    "v_exp_f32 v10, v10\n\t"
    "v_mfma_f32_16x16x32_bf16 a[88:91], a[172:175], v[36:39], a[88:91]\n\t"
    "ds_read_b128 a[128:131], v128 offset:32768\n\t"
    "v_cvt_pk_bf16_f32 v68, v8, v9\n\t"
    "v_mfma_f32_16x16x32_bf16 a[92:95], a[168:171], v[40:43], 0\n\t"
    "v_exp_f32 v11, v11\n\t"
    "v_mfma_f32_16x16x32_bf16 a[92:95], a[172:175], v[44:47], a[92:95]\n\t"
    "s_waitcnt lgkmcnt(3)\n\t"
    "v_add_f32 v146, v146, v9\n\t"
    "v_add_f32 v146, v146, v10\n\t"
    "v_mfma_f32_16x16x32_bf16 v[0:3], a[144:147], v[160:163], v[80:83]\n\t"
    "v_exp_f32 v12, v12\n\t"
    "v_mfma_f32_16x16x32_bf16 v[0:3], a[148:151], v[164:167], v[0:3]\n\t"
    "ds_read_b64_tr_b16 a[188:189], v139 offset:8192\n\t"
    "v_add_f32 v146, v146, v11\n\t"
    "v_mfma_f32_16x16x32_bf16 v[0:3], a[152:155], v[168:171], v[0:3]\n\t"
    "s_mov_b32 m0, s52\n\t"
    "v_exp_f32 v13, v13\n\t"
    "v_mfma_f32_16x16x32_bf16 v[0:3], a[156:159], v[172:175], v[0:3]\n\t"
    "ds_read_b64_tr_b16 a[190:191], v139 offset:12288\n\t"
    "v_cvt_pk_bf16_f32 v69, v10, v11\n\t"
    "v_mfma_f32_16x16x32_bf16 a[96:99], a[176:179], v[16:19], 0\n\t"
    "s_add_u32 s48, s48, 16384\n\t"
    "v_exp_f32 v14, v14\n\t"
    "v_mfma_f32_16x16x32_bf16 a[96:99], a[180:183], v[20:23], a[96:99]\n\t"
    "ds_read_b128 a[132:135], v129 offset:32768\n\t"
    "v_cvt_pk_bf16_f32 v76, v12, v13\n\t"
    "v_mfma_f32_16x16x32_bf16 a[100:103], a[176:179], v[24:27], 0\n\t"
    "v_exp_f32 v15, v15\n\t"
    "v_mfma_f32_16x16x32_bf16 a[100:103], a[180:183], v[28:31], a[100:103]\n\t"
    "s_waitcnt lgkmcnt(3)\n\t"
    "buffer_load_dwordx4 v224, s[40:43], s48 offen offset:0 lds\n\t"
    "s_cmp_lg_u32 s91, 0\n\t"
    "s_cbranch_scc1 .Lnohs%=\n\t"
    "v_mov_b32 v251, 1\n\t"
    "v_mov_b32 v253, 0\n\t"
    "s_waitcnt vmcnt(9)\n\t"
    "s_mov_b64 exec, 1\n\t"
    "buffer_atomic_add v251, v253, s[68:71], s84 offen\n\t"
    "s_mov_b64 exec, -1\n\t"
    ".Lnohs%=:\n\t"
    "v_mfma_f32_16x16x32_bf16 v[4:7], a[144:147], v[176:179], v[84:87]\n\t"
    "v_exp_f32 v0, v0\n\t"
    "v_mfma_f32_16x16x32_bf16 v[4:7], a[148:151], v[180:183], v[4:7]\n\t"
    "ds_read_b64_tr_b16 a[160:161], v132 offset:16384\n\t"
    "v_cvt_pk_bf16_f32 v77, v14, v15\n\t"
    "v_mfma_f32_16x16x32_bf16 v[4:7], a[152:155], v[184:187], v[4:7]\n\t"
    "v_exp_f32 v1, v1\n\t"
    "v_mfma_f32_16x16x32_bf16 v[4:7], a[156:159], v[188:191], v[4:7]\n\t"
    "ds_read_b64_tr_b16 a[162:163], v132 offset:20480\n\t"
    "v_add_f32 v144, v144, v0\n\t"
    "v_mfma_f32_16x16x32_bf16 a[104:107], a[176:179], v[32:35], 0\n\t"
    "v_exp_f32 v2, v2\n\t"
    "v_mfma_f32_16x16x32_bf16 a[104:107], a[180:183], v[36:39], a[104:107]\n\t"
    "ds_read_b128 a[136:139], v130 offset:32768\n\t"
    "v_cvt_pk_bf16_f32 v54, v0, v1\n\t"
    "v_mfma_f32_16x16x32_bf16 a[108:111], a[176:179], v[40:43], 0\n\t"
    "v_exp_f32 v3, v3\n\t"
    "v_mfma_f32_16x16x32_bf16 a[108:111], a[180:183], v[44:47], a[108:111]\n\t"
    "s_waitcnt lgkmcnt(3)\n\t"
    "v_add_f32 v144, v144, v1\n\t"
    "v_add_f32 v144, v144, v2\n\t"
    "v_mfma_f32_16x16x32_bf16 v[8:11], a[144:147], v[192:195], v[88:91]\n\t"
    "v_exp_f32 v4, v4\n\t"
    "v_mfma_f32_16x16x32_bf16 v[8:11], a[148:151], v[196:199], v[8:11]\n\t"
    "ds_read_b64_tr_b16 a[164:165], v132 offset:24576\n\t"
    "v_add_f32 v144, v144, v3\n\t"
    "v_mfma_f32_16x16x32_bf16 v[8:11], a[152:155], v[200:203], v[8:11]\n\t"
    "s_mov_b32 m0, s52\n\t"
    "v_exp_f32 v5, v5\n\t"
    "v_mfma_f32_16x16x32_bf16 v[8:11], a[156:159], v[204:207], v[8:11]\n\t"
    "ds_read_b64_tr_b16 a[166:167], v132 offset:28672\n\t"
    "v_cvt_pk_bf16_f32 v55, v2, v3\n\t"
    "v_mfma_f32_16x16x32_bf16 a[112:115], a[184:187], v[16:19], 0\n\t"
    "v_exp_f32 v6, v6\n\t"
    "v_mfma_f32_16x16x32_bf16 a[112:115], a[188:191], v[20:23], a[112:115]\n\t"
    "ds_read_b128 a[140:143], v131 offset:32768\n\t"
    "v_cvt_pk_bf16_f32 v62, v4, v5\n\t"
    "v_mfma_f32_16x16x32_bf16 a[116:119], a[184:187], v[24:27], 0\n\t"
    "v_exp_f32 v7, v7\n\t"
    "v_mfma_f32_16x16x32_bf16 a[116:119], a[188:191], v[28:31], a[116:119]\n\t"
    "s_waitcnt lgkmcnt(3)\n\t"
    "buffer_load_dwordx4 v225, s[40:43], s48 offen offset:1024 lds\n\t"
    "v_mfma_f32_16x16x32_bf16 v[12:15], a[144:147], v[208:211], v[92:95]\n\t"
    "v_exp_f32 v8, v8\n\t"
    "v_mfma_f32_16x16x32_bf16 v[12:15], a[148:151], v[212:215], v[12:15]\n\t"
    "ds_read_b64_tr_b16 a[168:169], v133 offset:16384\n\t"
    "v_cvt_pk_bf16_f32 v63, v6, v7\n\t"
    "v_mfma_f32_16x16x32_bf16 v[12:15], a[152:155], v[216:219], v[12:15]\n\t"
    "v_exp_f32 v9, v9\n\t"
    "v_mfma_f32_16x16x32_bf16 v[12:15], a[156:159], v[220:223], v[12:15]\n\t"
    "ds_read_b64_tr_b16 a[170:171], v133 offset:20480\n\t"
    "v_add_f32 v146, v146, v8\n\t"
    "v_mfma_f32_16x16x32_bf16 a[120:123], a[184:187], v[32:35], 0\n\t"
    "v_exp_f32 v10, v10\n\t"
    "v_mfma_f32_16x16x32_bf16 a[120:123], a[188:191], v[36:39], a[120:123]\n\t"
    "ds_read_b128 a[144:147], v128 offset:36864\n\t"
    "v_cvt_pk_bf16_f32 v70, v8, v9\n\t"
    "v_mfma_f32_16x16x32_bf16 a[124:127], a[184:187], v[40:43], 0\n\t"
    "v_exp_f32 v11, v11\n\t"
    "v_mfma_f32_16x16x32_bf16 a[124:127], a[188:191], v[44:47], a[124:127]\n\t"
    "s_waitcnt lgkmcnt(3)\n\t"
    "v_add_f32 v146, v146, v9\n\t"
    "v_add_f32 v146, v146, v10\n\t"
    "s_mov_b32 s49, 6\n\t"
    ".p2align 6\n\t"
    "1:\n\t"
    "v_mfma_f32_16x16x32_bf16 v[0:3], a[128:131], v[160:163], v[80:83]\n\t"
    "v_exp_f32 v12, v12\n\t"
    "v_mfma_f32_16x16x32_bf16 v[0:3], a[132:135], v[164:167], v[0:3]\n\t"
    "ds_read_b64_tr_b16 a[172:173], v133 offset:24576\n\t"
    "v_add_f32 v146, v146, v11\n\t"
    "v_mfma_f32_16x16x32_bf16 v[0:3], a[136:139], v[168:171], v[0:3]\n\t"
    "v_exp_f32 v13, v13\n\t"
    "v_mfma_f32_16x16x32_bf16 v[0:3], a[140:143], v[172:175], v[0:3]\n\t"
    "ds_read_b64_tr_b16 a[174:175], v133 offset:28672\n\t"
    "v_cvt_pk_bf16_f32 v71, v10, v11\n\t"
    "v_mfma_f32_16x16x32_bf16 a[0:3], a[160:163], v[48:51], a[0:3]\n\t"
    "v_exp_f32 v14, v14\n\t"
    "v_mfma_f32_16x16x32_bf16 a[0:3], a[164:167], v[52:55], a[0:3]\n\t"
    "ds_read_b128 a[148:151], v129 offset:36864\n\t"
    "v_cvt_pk_bf16_f32 v78, v12, v13\n\t"
    "v_mfma_f32_16x16x32_bf16 a[4:7], a[160:163], v[56:59], a[4:7]\n\t"
    "v_exp_f32 v15, v15\n\t"
    "v_mfma_f32_16x16x32_bf16 a[4:7], a[164:167], v[60:63], a[4:7]\n\t"
    "s_waitcnt lgkmcnt(3)\n\t"
    "buffer_load_dwordx4 v226, s[40:43], s48 offen offset:2048 lds\n\t"
    "v_mfma_f32_16x16x32_bf16 v[4:7], a[128:131], v[176:179], v[84:87]\n\t"
    "v_exp_f32 v0, v0\n\t"
    "v_mfma_f32_16x16x32_bf16 v[4:7], a[132:135], v[180:183], v[4:7]\n\t"
    "ds_read_b64_tr_b16 a[176:177], v134 offset:16384\n\t"
    "v_cvt_pk_bf16_f32 v79, v14, v15\n\t"
    "v_mfma_f32_16x16x32_bf16 v[4:7], a[136:139], v[184:187], v[4:7]\n\t"
    "v_exp_f32 v1, v1\n\t"
    "v_mfma_f32_16x16x32_bf16 v[4:7], a[140:143], v[188:191], v[4:7]\n\t"
    "ds_read_b64_tr_b16 a[178:179], v134 offset:20480\n\t"
    "v_add_f32 v144, v144, v0\n\t"
    "v_mfma_f32_16x16x32_bf16 a[8:11], a[160:163], v[64:67], a[8:11]\n\t"
    "v_exp_f32 v2, v2\n\t"
    "v_mfma_f32_16x16x32_bf16 a[8:11], a[164:167], v[68:71], a[8:11]\n\t"
    "ds_read_b128 a[152:155], v130 offset:36864\n\t"
    "v_cvt_pk_bf16_f32 v16, v0, v1\n\t"
    "v_mfma_f32_16x16x32_bf16 a[12:15], a[160:163], v[72:75], a[12:15]\n\t"
    "v_exp_f32 v3, v3\n\t"
    "v_mfma_f32_16x16x32_bf16 a[12:15], a[164:167], v[76:79], a[12:15]\n\t"
    "s_waitcnt lgkmcnt(3)\n\t"
    "v_add_f32 v144, v144, v1\n\t"
    "v_add_f32 v144, v144, v2\n\t"
    "v_mfma_f32_16x16x32_bf16 v[148:151], v[140:143], v[56:59], v[148:151]\n\t"
    "v_mfma_f32_16x16x32_bf16 v[148:151], v[140:143], v[60:63], v[148:151]\n\t"
    "v_mfma_f32_16x16x32_bf16 v[8:11], a[128:131], v[192:195], v[88:91]\n\t"
    "v_exp_f32 v4, v4\n\t"
    "v_mfma_f32_16x16x32_bf16 v[8:11], a[132:135], v[196:199], v[8:11]\n\t"
    "ds_read_b64_tr_b16 a[180:181], v134 offset:24576\n\t"
    "v_add_f32 v144, v144, v3\n\t"
    "v_mfma_f32_16x16x32_bf16 v[8:11], a[136:139], v[200:203], v[8:11]\n\t"
    "v_exp_f32 v5, v5\n\t"
    "v_mfma_f32_16x16x32_bf16 v[8:11], a[140:143], v[204:207], v[8:11]\n\t"
    "ds_read_b64_tr_b16 a[182:183], v134 offset:28672\n\t"
    "v_cvt_pk_bf16_f32 v17, v2, v3\n\t"
    "v_mfma_f32_16x16x32_bf16 a[16:19], a[168:171], v[48:51], a[16:19]\n\t"
    "v_exp_f32 v6, v6\n\t"
    "v_mfma_f32_16x16x32_bf16 a[16:19], a[172:175], v[52:55], a[16:19]\n\t"
    "ds_read_b128 a[156:159], v131 offset:36864\n\t"
    "v_cvt_pk_bf16_f32 v24, v4, v5\n\t"
    "v_mfma_f32_16x16x32_bf16 a[20:23], a[168:171], v[56:59], a[20:23]\n\t"
    "v_exp_f32 v7, v7\n\t"
    "v_mfma_f32_16x16x32_bf16 a[20:23], a[172:175], v[60:63], a[20:23]\n\t"
    "s_waitcnt lgkmcnt(3)\n\t"
    "buffer_load_dwordx4 v227, s[40:43], s48 offen offset:3072 lds\n\t"
    "v_mfma_f32_16x16x32_bf16 v[12:15], a[128:131], v[208:211], v[92:95]\n\t"
    "v_exp_f32 v8, v8\n\t"
    "v_mfma_f32_16x16x32_bf16 v[12:15], a[132:135], v[212:215], v[12:15]\n\t"
    "ds_read_b64_tr_b16 a[184:185], v135 offset:16384\n\t"
    "v_cvt_pk_bf16_f32 v25, v6, v7\n\t"
    "v_mfma_f32_16x16x32_bf16 v[12:15], a[136:139], v[216:219], v[12:15]\n\t"
    "v_exp_f32 v9, v9\n\t"
    "v_mfma_f32_16x16x32_bf16 v[12:15], a[140:143], v[220:223], v[12:15]\n\t"
    "ds_read_b64_tr_b16 a[186:187], v135 offset:20480\n\t"
    "v_add_f32 v146, v146, v8\n\t"
    "v_mfma_f32_16x16x32_bf16 a[24:27], a[168:171], v[64:67], a[24:27]\n\t"
    "v_exp_f32 v10, v10\n\t"
    "v_mfma_f32_16x16x32_bf16 a[24:27], a[172:175], v[68:71], a[24:27]\n\t"
    "ds_read_b128 a[128:131], v128 offset:40960\n\t"
    "v_cvt_pk_bf16_f32 v32, v8, v9\n\t"
    "v_mfma_f32_16x16x32_bf16 a[28:31], a[168:171], v[72:75], a[28:31]\n\t"
    "v_exp_f32 v11, v11\n\t"
    "v_mfma_f32_16x16x32_bf16 a[28:31], a[172:175], v[76:79], a[28:31]\n\t"
    "s_waitcnt lgkmcnt(3)\n\t"
    "v_add_f32 v146, v146, v9\n\t"
    "v_add_f32 v146, v146, v10\n\t"
    "v_mfma_f32_16x16x32_bf16 v[152:155], v[140:143], v[72:75], v[152:155]\n\t"
    "v_mfma_f32_16x16x32_bf16 v[152:155], v[140:143], v[76:79], v[152:155]\n\t"
    "v_mfma_f32_16x16x32_bf16 v[0:3], a[144:147], v[160:163], v[80:83]\n\t"
    "v_exp_f32 v12, v12\n\t"
    "v_mfma_f32_16x16x32_bf16 v[0:3], a[148:151], v[164:167], v[0:3]\n\t"
    "ds_read_b64_tr_b16 a[188:189], v135 offset:24576\n\t"
    "v_add_f32 v146, v146, v11\n\t"
    "v_mfma_f32_16x16x32_bf16 v[0:3], a[152:155], v[168:171], v[0:3]\n\t"
    "s_mov_b32 m0, s59\n\t"
    "v_exp_f32 v13, v13\n\t"
    "v_mfma_f32_16x16x32_bf16 v[0:3], a[156:159], v[172:175], v[0:3]\n\t"
    "ds_read_b64_tr_b16 a[190:191], v135 offset:28672\n\t"
    "v_cvt_pk_bf16_f32 v33, v10, v11\n\t"
    "v_mfma_f32_16x16x32_bf16 a[32:35], a[176:179], v[48:51], a[32:35]\n\t"
    "s_add_u32 s51, s51, 16384\n\t"
    "v_exp_f32 v14, v14\n\t"
    "v_mfma_f32_16x16x32_bf16 a[32:35], a[180:183], v[52:55], a[32:35]\n\t"
    "ds_read_b128 a[132:135], v129 offset:40960\n\t"
    "v_cvt_pk_bf16_f32 v40, v12, v13\n\t"
    "v_mfma_f32_16x16x32_bf16 a[36:39], a[176:179], v[56:59], a[36:39]\n\t"
    "v_exp_f32 v15, v15\n\t"
    "v_mfma_f32_16x16x32_bf16 a[36:39], a[180:183], v[60:63], a[36:39]\n\t"
    "s_waitcnt lgkmcnt(3)\n\t"
    "buffer_load_dwordx4 v228, s[44:47], s51 offen offset:0 lds\n\t"
    "v_mfma_f32_16x16x32_bf16 v[4:7], a[144:147], v[176:179], v[84:87]\n\t"
    "v_exp_f32 v0, v0\n\t"
    "v_mfma_f32_16x16x32_bf16 v[4:7], a[148:151], v[180:183], v[4:7]\n\t"
    "ds_read_b64_tr_b16 a[160:161], v136 offset:16384\n\t"
    "v_cvt_pk_bf16_f32 v41, v14, v15\n\t"
    "v_mfma_f32_16x16x32_bf16 v[4:7], a[152:155], v[184:187], v[4:7]\n\t"
    "v_exp_f32 v1, v1\n\t"
    "v_mfma_f32_16x16x32_bf16 v[4:7], a[156:159], v[188:191], v[4:7]\n\t"
    "ds_read_b64_tr_b16 a[162:163], v136 offset:20480\n\t"
    "v_add_f32 v144, v144, v0\n\t"
    "v_mfma_f32_16x16x32_bf16 a[40:43], a[176:179], v[64:67], a[40:43]\n\t"
    "v_exp_f32 v2, v2\n\t"
    "v_mfma_f32_16x16x32_bf16 a[40:43], a[180:183], v[68:71], a[40:43]\n\t"
    "ds_read_b128 a[136:139], v130 offset:40960\n\t"
    "v_cvt_pk_bf16_f32 v18, v0, v1\n\t"
    "v_mfma_f32_16x16x32_bf16 a[44:47], a[176:179], v[72:75], a[44:47]\n\t"
    "v_exp_f32 v3, v3\n\t"
    "v_mfma_f32_16x16x32_bf16 a[44:47], a[180:183], v[76:79], a[44:47]\n\t"
    "s_waitcnt lgkmcnt(3)\n\t"
    "v_add_f32 v144, v144, v1\n\t"
    "v_add_f32 v144, v144, v2\n\t"
    "v_mfma_f32_16x16x32_bf16 v[8:11], a[144:147], v[192:195], v[88:91]\n\t"
    "v_exp_f32 v4, v4\n\t"
    "v_mfma_f32_16x16x32_bf16 v[8:11], a[148:151], v[196:199], v[8:11]\n\t"
    "ds_read_b64_tr_b16 a[164:165], v136 offset:24576\n\t"
    "v_add_f32 v144, v144, v3\n\t"
    "v_mfma_f32_16x16x32_bf16 v[8:11], a[152:155], v[200:203], v[8:11]\n\t"
    "v_exp_f32 v5, v5\n\t"
    "v_mfma_f32_16x16x32_bf16 v[8:11], a[156:159], v[204:207], v[8:11]\n\t"
    "ds_read_b64_tr_b16 a[166:167], v136 offset:28672\n\t"
    "v_cvt_pk_bf16_f32 v19, v2, v3\n\t"
    "v_mfma_f32_16x16x32_bf16 a[48:51], a[184:187], v[48:51], a[48:51]\n\t"
    "v_exp_f32 v6, v6\n\t"
    "v_mfma_f32_16x16x32_bf16 a[48:51], a[188:191], v[52:55], a[48:51]\n\t"
    "ds_read_b128 a[140:143], v131 offset:40960\n\t"
    "v_cvt_pk_bf16_f32 v26, v4, v5\n\t"
    "v_mfma_f32_16x16x32_bf16 a[52:55], a[184:187], v[56:59], a[52:55]\n\t"
    "v_exp_f32 v7, v7\n\t"
    "v_mfma_f32_16x16x32_bf16 a[52:55], a[188:191], v[60:63], a[52:55]\n\t"
    "s_waitcnt lgkmcnt(3)\n\t"
    "buffer_load_dwordx4 v229, s[44:47], s51 offen offset:1024 lds\n\t"
    "v_mfma_f32_16x16x32_bf16 v[12:15], a[144:147], v[208:211], v[92:95]\n\t"
    "v_exp_f32 v8, v8\n\t"
    "v_mfma_f32_16x16x32_bf16 v[12:15], a[148:151], v[212:215], v[12:15]\n\t"
    "ds_read_b64_tr_b16 a[168:169], v137 offset:16384\n\t"
    "v_cvt_pk_bf16_f32 v27, v6, v7\n\t"
    "v_mfma_f32_16x16x32_bf16 v[12:15], a[152:155], v[216:219], v[12:15]\n\t"
    "v_exp_f32 v9, v9\n\t"
    "v_mfma_f32_16x16x32_bf16 v[12:15], a[156:159], v[220:223], v[12:15]\n\t"
    "ds_read_b64_tr_b16 a[170:171], v137 offset:20480\n\t"
    "v_add_f32 v146, v146, v8\n\t"
    "v_mfma_f32_16x16x32_bf16 a[56:59], a[184:187], v[64:67], a[56:59]\n\t"
    "v_exp_f32 v10, v10\n\t"
    "v_mfma_f32_16x16x32_bf16 a[56:59], a[188:191], v[68:71], a[56:59]\n\t"
    "ds_read_b128 a[144:147], v128 offset:45056\n\t"
    "v_cvt_pk_bf16_f32 v34, v8, v9\n\t"
    "v_mfma_f32_16x16x32_bf16 a[60:63], a[184:187], v[72:75], a[60:63]\n\t"
    "v_exp_f32 v11, v11\n\t"
    "v_mfma_f32_16x16x32_bf16 a[60:63], a[188:191], v[76:79], a[60:63]\n\t"
    "s_waitcnt lgkmcnt(3)\n\t"
    "v_add_f32 v146, v146, v9\n\t"
    "v_add_f32 v146, v146, v10\n\t"
    "v_mfma_f32_16x16x32_bf16 v[0:3], a[128:131], v[160:163], v[80:83]\n\t"
    "v_exp_f32 v12, v12\n\t"
    "v_mfma_f32_16x16x32_bf16 v[0:3], a[132:135], v[164:167], v[0:3]\n\t"
    "ds_read_b64_tr_b16 a[172:173], v137 offset:24576\n\t"
    "v_add_f32 v146, v146, v11\n\t"
    "v_mfma_f32_16x16x32_bf16 v[0:3], a[136:139], v[168:171], v[0:3]\n\t"
    "v_exp_f32 v13, v13\n\t"
    "v_mfma_f32_16x16x32_bf16 v[0:3], a[140:143], v[172:175], v[0:3]\n\t"
    "ds_read_b64_tr_b16 a[174:175], v137 offset:28672\n\t"
    "v_cvt_pk_bf16_f32 v35, v10, v11\n\t"
    "v_mfma_f32_16x16x32_bf16 a[64:67], a[160:163], v[48:51], a[64:67]\n\t"
    "v_exp_f32 v14, v14\n\t"
    "v_mfma_f32_16x16x32_bf16 a[64:67], a[164:167], v[52:55], a[64:67]\n\t"
    "ds_read_b128 a[148:151], v129 offset:45056\n\t"
    "v_cvt_pk_bf16_f32 v42, v12, v13\n\t"
    "v_mfma_f32_16x16x32_bf16 a[68:71], a[160:163], v[56:59], a[68:71]\n\t"
    "v_exp_f32 v15, v15\n\t"
    "v_mfma_f32_16x16x32_bf16 a[68:71], a[164:167], v[60:63], a[68:71]\n\t"
    "s_waitcnt lgkmcnt(3)\n\t"
    "buffer_load_dwordx4 v228, s[44:47], s51 offen offset:2048 lds\n\t"
    "v_mfma_f32_16x16x32_bf16 v[4:7], a[128:131], v[176:179], v[84:87]\n\t"
    "v_exp_f32 v0, v0\n\t"
    "v_mfma_f32_16x16x32_bf16 v[4:7], a[132:135], v[180:183], v[4:7]\n\t"
    "ds_read_b64_tr_b16 a[176:177], v138 offset:16384\n\t"
    "v_cvt_pk_bf16_f32 v43, v14, v15\n\t"
    "v_mfma_f32_16x16x32_bf16 v[4:7], a[136:139], v[184:187], v[4:7]\n\t"
    "v_exp_f32 v1, v1\n\t"
    "v_mfma_f32_16x16x32_bf16 v[4:7], a[140:143], v[188:191], v[4:7]\n\t"
    "ds_read_b64_tr_b16 a[178:179], v138 offset:20480\n\t"
    "v_add_f32 v144, v144, v0\n\t"
    "v_mfma_f32_16x16x32_bf16 a[72:75], a[160:163], v[64:67], a[72:75]\n\t"
    "v_exp_f32 v2, v2\n\t"
    "v_mfma_f32_16x16x32_bf16 a[72:75], a[164:167], v[68:71], a[72:75]\n\t"
    "ds_read_b128 a[152:155], v130 offset:45056\n\t"
    "v_cvt_pk_bf16_f32 v20, v0, v1\n\t"
    "v_mfma_f32_16x16x32_bf16 a[76:79], a[160:163], v[72:75], a[76:79]\n\t"
    "v_exp_f32 v3, v3\n\t"
    "v_mfma_f32_16x16x32_bf16 a[76:79], a[164:167], v[76:79], a[76:79]\n\t"
    "s_waitcnt lgkmcnt(3)\n\t"
    "v_add_f32 v144, v144, v1\n\t"
    "v_add_f32 v144, v144, v2\n\t"
    "v_mfma_f32_16x16x32_bf16 v[8:11], a[128:131], v[192:195], v[88:91]\n\t"
    "v_exp_f32 v4, v4\n\t"
    "v_mfma_f32_16x16x32_bf16 v[8:11], a[132:135], v[196:199], v[8:11]\n\t"
    "ds_read_b64_tr_b16 a[180:181], v138 offset:24576\n\t"
    "v_add_f32 v144, v144, v3\n\t"
    "v_mfma_f32_16x16x32_bf16 v[8:11], a[136:139], v[200:203], v[8:11]\n\t"
    "v_exp_f32 v5, v5\n\t"
    "v_mfma_f32_16x16x32_bf16 v[8:11], a[140:143], v[204:207], v[8:11]\n\t"
    "ds_read_b64_tr_b16 a[182:183], v138 offset:28672\n\t"
    "v_cvt_pk_bf16_f32 v21, v2, v3\n\t"
    "v_mfma_f32_16x16x32_bf16 a[80:83], a[168:171], v[48:51], a[80:83]\n\t"
    "v_exp_f32 v6, v6\n\t"
    "v_mfma_f32_16x16x32_bf16 a[80:83], a[172:175], v[52:55], a[80:83]\n\t"
    "ds_read_b128 a[156:159], v131 offset:45056\n\t"
    "v_cvt_pk_bf16_f32 v28, v4, v5\n\t"
    "v_mfma_f32_16x16x32_bf16 a[84:87], a[168:171], v[56:59], a[84:87]\n\t"
    "v_exp_f32 v7, v7\n\t"
    "v_mfma_f32_16x16x32_bf16 a[84:87], a[172:175], v[60:63], a[84:87]\n\t"
    "s_waitcnt lgkmcnt(3)\n\t"
    "buffer_load_dwordx4 v229, s[44:47], s51 offen offset:3072 lds\n\t"
    "v_mfma_f32_16x16x32_bf16 v[12:15], a[128:131], v[208:211], v[92:95]\n\t"
    "s_waitcnt vmcnt(8)\n\t"
    "v_exp_f32 v8, v8\n\t"
    "v_mfma_f32_16x16x32_bf16 v[12:15], a[132:135], v[212:215], v[12:15]\n\t"
    "ds_read_b64_tr_b16 a[184:185], v139 offset:16384\n\t"
    "v_cvt_pk_bf16_f32 v29, v6, v7\n\t"
    "v_mfma_f32_16x16x32_bf16 v[12:15], a[136:139], v[216:219], v[12:15]\n\t"
    "s_barrier\n\t"
    "v_exp_f32 v9, v9\n\t"
    "v_mfma_f32_16x16x32_bf16 v[12:15], a[140:143], v[220:223], v[12:15]\n\t"
    "ds_read_b64_tr_b16 a[186:187], v139 offset:20480\n\t"
    "v_add_f32 v146, v146, v8\n\t"
    "v_mfma_f32_16x16x32_bf16 a[88:91], a[168:171], v[64:67], a[88:91]\n\t"
    "v_exp_f32 v10, v10\n\t"
    "v_mfma_f32_16x16x32_bf16 a[88:91], a[172:175], v[68:71], a[88:91]\n\t"
    "ds_read_b128 a[128:131], v128 offset:49152\n\t"
    "v_cvt_pk_bf16_f32 v36, v8, v9\n\t"
    "v_mfma_f32_16x16x32_bf16 a[92:95], a[168:171], v[72:75], a[92:95]\n\t"
    "v_exp_f32 v11, v11\n\t"
    "v_mfma_f32_16x16x32_bf16 a[92:95], a[172:175], v[76:79], a[92:95]\n\t"
    "s_waitcnt lgkmcnt(3)\n\t"
    "v_add_f32 v146, v146, v9\n\t"
    "v_add_f32 v146, v146, v10\n\t"
    "v_mfma_f32_16x16x32_bf16 v[0:3], a[144:147], v[160:163], v[80:83]\n\t"
    "v_exp_f32 v12, v12\n\t"
    "v_mfma_f32_16x16x32_bf16 v[0:3], a[148:151], v[164:167], v[0:3]\n\t"
    "ds_read_b64_tr_b16 a[188:189], v139 offset:24576\n\t"
    "v_add_f32 v146, v146, v11\n\t"
    "v_mfma_f32_16x16x32_bf16 v[0:3], a[152:155], v[168:171], v[0:3]\n\t"
    "s_mov_b32 m0, s53\n\t"
    "v_exp_f32 v13, v13\n\t"
    "v_mfma_f32_16x16x32_bf16 v[0:3], a[156:159], v[172:175], v[0:3]\n\t"
    "ds_read_b64_tr_b16 a[190:191], v139 offset:28672\n\t"
    "v_cvt_pk_bf16_f32 v37, v10, v11\n\t"
    "v_mfma_f32_16x16x32_bf16 a[96:99], a[176:179], v[48:51], a[96:99]\n\t"
    "s_add_u32 s48, s48, 16384\n\t"
    "v_exp_f32 v14, v14\n\t"
    "v_mfma_f32_16x16x32_bf16 a[96:99], a[180:183], v[52:55], a[96:99]\n\t"
    "ds_read_b128 a[132:135], v129 offset:49152\n\t"
    "v_cvt_pk_bf16_f32 v44, v12, v13\n\t"
    "v_mfma_f32_16x16x32_bf16 a[100:103], a[176:179], v[56:59], a[100:103]\n\t"
    "v_exp_f32 v15, v15\n\t"
    "v_mfma_f32_16x16x32_bf16 a[100:103], a[180:183], v[60:63], a[100:103]\n\t"
    "s_waitcnt lgkmcnt(3)\n\t"
    "buffer_load_dwordx4 v224, s[40:43], s48 offen offset:0 lds\n\t"
    "v_mfma_f32_16x16x32_bf16 v[4:7], a[144:147], v[176:179], v[84:87]\n\t"
    "v_exp_f32 v0, v0\n\t"
    "v_mfma_f32_16x16x32_bf16 v[4:7], a[148:151], v[180:183], v[4:7]\n\t"
    "ds_read_b64_tr_b16 a[160:161], v132 offset:32768\n\t"
    "v_cvt_pk_bf16_f32 v45, v14, v15\n\t"
    "v_mfma_f32_16x16x32_bf16 v[4:7], a[152:155], v[184:187], v[4:7]\n\t"
    "v_exp_f32 v1, v1\n\t"
    "v_mfma_f32_16x16x32_bf16 v[4:7], a[156:159], v[188:191], v[4:7]\n\t"
    "ds_read_b64_tr_b16 a[162:163], v132 offset:36864\n\t"
    "v_add_f32 v144, v144, v0\n\t"
    "v_mfma_f32_16x16x32_bf16 a[104:107], a[176:179], v[64:67], a[104:107]\n\t"
    "v_exp_f32 v2, v2\n\t"
    "v_mfma_f32_16x16x32_bf16 a[104:107], a[180:183], v[68:71], a[104:107]\n\t"
    "ds_read_b128 a[136:139], v130 offset:49152\n\t"
    "v_cvt_pk_bf16_f32 v22, v0, v1\n\t"
    "v_mfma_f32_16x16x32_bf16 a[108:111], a[176:179], v[72:75], a[108:111]\n\t"
    "v_exp_f32 v3, v3\n\t"
    "v_mfma_f32_16x16x32_bf16 a[108:111], a[180:183], v[76:79], a[108:111]\n\t"
    "s_waitcnt lgkmcnt(3)\n\t"
    "v_add_f32 v144, v144, v1\n\t"
    "v_add_f32 v144, v144, v2\n\t"
    "v_mfma_f32_16x16x32_bf16 v[8:11], a[144:147], v[192:195], v[88:91]\n\t"
    "v_exp_f32 v4, v4\n\t"
    "v_mfma_f32_16x16x32_bf16 v[8:11], a[148:151], v[196:199], v[8:11]\n\t"
    "ds_read_b64_tr_b16 a[164:165], v132 offset:40960\n\t"
    "v_add_f32 v144, v144, v3\n\t"
    "v_mfma_f32_16x16x32_bf16 v[8:11], a[152:155], v[200:203], v[8:11]\n\t"
    "v_exp_f32 v5, v5\n\t"
    "v_mfma_f32_16x16x32_bf16 v[8:11], a[156:159], v[204:207], v[8:11]\n\t"
    "ds_read_b64_tr_b16 a[166:167], v132 offset:45056\n\t"
    "v_cvt_pk_bf16_f32 v23, v2, v3\n\t"
    "v_mfma_f32_16x16x32_bf16 a[112:115], a[184:187], v[48:51], a[112:115]\n\t"
    "v_exp_f32 v6, v6\n\t"
    "v_mfma_f32_16x16x32_bf16 a[112:115], a[188:191], v[52:55], a[112:115]\n\t"
    "ds_read_b128 a[140:143], v131 offset:49152\n\t"
    "v_cvt_pk_bf16_f32 v30, v4, v5\n\t"
    "v_mfma_f32_16x16x32_bf16 a[116:119], a[184:187], v[56:59], a[116:119]\n\t"
    "v_exp_f32 v7, v7\n\t"
    "v_mfma_f32_16x16x32_bf16 a[116:119], a[188:191], v[60:63], a[116:119]\n\t"
    "s_waitcnt lgkmcnt(3)\n\t"
    "buffer_load_dwordx4 v225, s[40:43], s48 offen offset:1024 lds\n\t"
    "v_mfma_f32_16x16x32_bf16 v[12:15], a[144:147], v[208:211], v[92:95]\n\t"
    "v_exp_f32 v8, v8\n\t"
    "v_mfma_f32_16x16x32_bf16 v[12:15], a[148:151], v[212:215], v[12:15]\n\t"
    "ds_read_b64_tr_b16 a[168:169], v133 offset:32768\n\t"
    "v_cvt_pk_bf16_f32 v31, v6, v7\n\t"
    "v_mfma_f32_16x16x32_bf16 v[12:15], a[152:155], v[216:219], v[12:15]\n\t"
    "v_exp_f32 v9, v9\n\t"
    "v_mfma_f32_16x16x32_bf16 v[12:15], a[156:159], v[220:223], v[12:15]\n\t"
    "ds_read_b64_tr_b16 a[170:171], v133 offset:36864\n\t"
    "v_add_f32 v146, v146, v8\n\t"
    "v_mfma_f32_16x16x32_bf16 a[120:123], a[184:187], v[64:67], a[120:123]\n\t"
    "v_exp_f32 v10, v10\n\t"
    "v_mfma_f32_16x16x32_bf16 a[120:123], a[188:191], v[68:71], a[120:123]\n\t"
    "ds_read_b128 a[144:147], v128 offset:53248\n\t"
    "v_cvt_pk_bf16_f32 v38, v8, v9\n\t"
    "v_mfma_f32_16x16x32_bf16 a[124:127], a[184:187], v[72:75], a[124:127]\n\t"
    "v_exp_f32 v11, v11\n\t"
    "v_mfma_f32_16x16x32_bf16 a[124:127], a[188:191], v[76:79], a[124:127]\n\t"
    "s_waitcnt lgkmcnt(3)\n\t"
    "v_add_f32 v146, v146, v9\n\t"
    "v_add_f32 v146, v146, v10\n\t"
    "v_mfma_f32_16x16x32_bf16 v[0:3], a[128:131], v[160:163], v[80:83]\n\t"
    "v_exp_f32 v12, v12\n\t"
    "v_mfma_f32_16x16x32_bf16 v[0:3], a[132:135], v[164:167], v[0:3]\n\t"
    "ds_read_b64_tr_b16 a[172:173], v133 offset:40960\n\t"
    "v_add_f32 v146, v146, v11\n\t"
    "v_mfma_f32_16x16x32_bf16 v[0:3], a[136:139], v[168:171], v[0:3]\n\t"
    "v_exp_f32 v13, v13\n\t"
    "v_mfma_f32_16x16x32_bf16 v[0:3], a[140:143], v[172:175], v[0:3]\n\t"
    "ds_read_b64_tr_b16 a[174:175], v133 offset:45056\n\t"
    "v_cvt_pk_bf16_f32 v39, v10, v11\n\t"
    "v_mfma_f32_16x16x32_bf16 a[0:3], a[160:163], v[16:19], a[0:3]\n\t"
    "v_exp_f32 v14, v14\n\t"
    "v_mfma_f32_16x16x32_bf16 a[0:3], a[164:167], v[20:23], a[0:3]\n\t"
    "ds_read_b128 a[148:151], v129 offset:53248\n\t"
    "v_cvt_pk_bf16_f32 v46, v12, v13\n\t"
    "v_mfma_f32_16x16x32_bf16 a[4:7], a[160:163], v[24:27], a[4:7]\n\t"
    "v_exp_f32 v15, v15\n\t"
    "v_mfma_f32_16x16x32_bf16 a[4:7], a[164:167], v[28:31], a[4:7]\n\t"
    "s_waitcnt lgkmcnt(3)\n\t"
    "buffer_load_dwordx4 v226, s[40:43], s48 offen offset:2048 lds\n\t"
    "v_mfma_f32_16x16x32_bf16 v[4:7], a[128:131], v[176:179], v[84:87]\n\t"
    "v_exp_f32 v0, v0\n\t"
    "v_mfma_f32_16x16x32_bf16 v[4:7], a[132:135], v[180:183], v[4:7]\n\t"
    "ds_read_b64_tr_b16 a[176:177], v134 offset:32768\n\t"
    "v_cvt_pk_bf16_f32 v47, v14, v15\n\t"
    "v_mfma_f32_16x16x32_bf16 v[4:7], a[136:139], v[184:187], v[4:7]\n\t"
    "v_exp_f32 v1, v1\n\t"
    "v_mfma_f32_16x16x32_bf16 v[4:7], a[140:143], v[188:191], v[4:7]\n\t"
    "ds_read_b64_tr_b16 a[178:179], v134 offset:36864\n\t"
    "v_add_f32 v144, v144, v0\n\t"
    "v_mfma_f32_16x16x32_bf16 a[8:11], a[160:163], v[32:35], a[8:11]\n\t"
    "v_exp_f32 v2, v2\n\t"
    "v_mfma_f32_16x16x32_bf16 a[8:11], a[164:167], v[36:39], a[8:11]\n\t"
    "ds_read_b128 a[152:155], v130 offset:53248\n\t"
    "v_cvt_pk_bf16_f32 v48, v0, v1\n\t"
    "v_mfma_f32_16x16x32_bf16 a[12:15], a[160:163], v[40:43], a[12:15]\n\t"
    "v_exp_f32 v3, v3\n\t"
    "v_mfma_f32_16x16x32_bf16 a[12:15], a[164:167], v[44:47], a[12:15]\n\t"
    "s_waitcnt lgkmcnt(3)\n\t"
    "v_add_f32 v144, v144, v1\n\t"
    "v_add_f32 v144, v144, v2\n\t"
    "v_mfma_f32_16x16x32_bf16 v[148:151], v[140:143], v[24:27], v[148:151]\n\t"
    "v_mfma_f32_16x16x32_bf16 v[148:151], v[140:143], v[28:31], v[148:151]\n\t"
    "v_mfma_f32_16x16x32_bf16 v[8:11], a[128:131], v[192:195], v[88:91]\n\t"
    "v_exp_f32 v4, v4\n\t"
    "v_mfma_f32_16x16x32_bf16 v[8:11], a[132:135], v[196:199], v[8:11]\n\t"
    "ds_read_b64_tr_b16 a[180:181], v134 offset:40960\n\t"
    "v_add_f32 v144, v144, v3\n\t"
    "v_mfma_f32_16x16x32_bf16 v[8:11], a[136:139], v[200:203], v[8:11]\n\t"
    "v_exp_f32 v5, v5\n\t"
    "v_mfma_f32_16x16x32_bf16 v[8:11], a[140:143], v[204:207], v[8:11]\n\t"
    "ds_read_b64_tr_b16 a[182:183], v134 offset:45056\n\t"
    "v_cvt_pk_bf16_f32 v49, v2, v3\n\t"
    "v_mfma_f32_16x16x32_bf16 a[16:19], a[168:171], v[16:19], a[16:19]\n\t"
    "v_exp_f32 v6, v6\n\t"
    "v_mfma_f32_16x16x32_bf16 a[16:19], a[172:175], v[20:23], a[16:19]\n\t"
    "ds_read_b128 a[156:159], v131 offset:53248\n\t"
    "v_cvt_pk_bf16_f32 v56, v4, v5\n\t"
    "v_mfma_f32_16x16x32_bf16 a[20:23], a[168:171], v[24:27], a[20:23]\n\t"
    "v_exp_f32 v7, v7\n\t"
    "v_mfma_f32_16x16x32_bf16 a[20:23], a[172:175], v[28:31], a[20:23]\n\t"
    "s_waitcnt lgkmcnt(3)\n\t"
    "buffer_load_dwordx4 v227, s[40:43], s48 offen offset:3072 lds\n\t"
    "v_mfma_f32_16x16x32_bf16 v[12:15], a[128:131], v[208:211], v[92:95]\n\t"
    "v_exp_f32 v8, v8\n\t"
    "v_mfma_f32_16x16x32_bf16 v[12:15], a[132:135], v[212:215], v[12:15]\n\t"
    "ds_read_b64_tr_b16 a[184:185], v135 offset:32768\n\t"
    "v_cvt_pk_bf16_f32 v57, v6, v7\n\t"
    "v_mfma_f32_16x16x32_bf16 v[12:15], a[136:139], v[216:219], v[12:15]\n\t"
    "v_exp_f32 v9, v9\n\t"
    "v_mfma_f32_16x16x32_bf16 v[12:15], a[140:143], v[220:223], v[12:15]\n\t"
    "ds_read_b64_tr_b16 a[186:187], v135 offset:36864\n\t"
    "v_add_f32 v146, v146, v8\n\t"
    "v_mfma_f32_16x16x32_bf16 a[24:27], a[168:171], v[32:35], a[24:27]\n\t"
    "v_exp_f32 v10, v10\n\t"
    "v_mfma_f32_16x16x32_bf16 a[24:27], a[172:175], v[36:39], a[24:27]\n\t"
    "ds_read_b128 a[128:131], v128 offset:57344\n\t"
    "v_cvt_pk_bf16_f32 v64, v8, v9\n\t"
    "v_mfma_f32_16x16x32_bf16 a[28:31], a[168:171], v[40:43], a[28:31]\n\t"
    "v_exp_f32 v11, v11\n\t"
    "v_mfma_f32_16x16x32_bf16 a[28:31], a[172:175], v[44:47], a[28:31]\n\t"
    "s_waitcnt lgkmcnt(3)\n\t"
    "v_add_f32 v146, v146, v9\n\t"
    "v_add_f32 v146, v146, v10\n\t"
    "v_mfma_f32_16x16x32_bf16 v[152:155], v[140:143], v[40:43], v[152:155]\n\t"
    "v_mfma_f32_16x16x32_bf16 v[152:155], v[140:143], v[44:47], v[152:155]\n\t"
    "v_mfma_f32_16x16x32_bf16 v[0:3], a[144:147], v[160:163], v[80:83]\n\t"
    "v_exp_f32 v12, v12\n\t"
    "v_mfma_f32_16x16x32_bf16 v[0:3], a[148:151], v[164:167], v[0:3]\n\t"
    "ds_read_b64_tr_b16 a[188:189], v135 offset:40960\n\t"
    "v_add_f32 v146, v146, v11\n\t"
    "v_mfma_f32_16x16x32_bf16 v[0:3], a[152:155], v[168:171], v[0:3]\n\t"
    "s_mov_b32 m0, s56\n\t"
    "v_exp_f32 v13, v13\n\t"
    "v_mfma_f32_16x16x32_bf16 v[0:3], a[156:159], v[172:175], v[0:3]\n\t"
    "ds_read_b64_tr_b16 a[190:191], v135 offset:45056\n\t"
    "v_cvt_pk_bf16_f32 v65, v10, v11\n\t"
    "v_mfma_f32_16x16x32_bf16 a[32:35], a[176:179], v[16:19], a[32:35]\n\t"
    "s_add_u32 s51, s51, 16384\n\t"
    "v_exp_f32 v14, v14\n\t"
    "v_mfma_f32_16x16x32_bf16 a[32:35], a[180:183], v[20:23], a[32:35]\n\t"
    "ds_read_b128 a[132:135], v129 offset:57344\n\t"
    "v_cvt_pk_bf16_f32 v72, v12, v13\n\t"
    "v_mfma_f32_16x16x32_bf16 a[36:39], a[176:179], v[24:27], a[36:39]\n\t"
    "v_exp_f32 v15, v15\n\t"
    "v_mfma_f32_16x16x32_bf16 a[36:39], a[180:183], v[28:31], a[36:39]\n\t"
    "s_waitcnt lgkmcnt(3)\n\t"
    "buffer_load_dwordx4 v228, s[44:47], s51 offen offset:0 lds\n\t"
    "v_mfma_f32_16x16x32_bf16 v[4:7], a[144:147], v[176:179], v[84:87]\n\t"
    "v_exp_f32 v0, v0\n\t"
    "v_mfma_f32_16x16x32_bf16 v[4:7], a[148:151], v[180:183], v[4:7]\n\t"
    "ds_read_b64_tr_b16 a[160:161], v136 offset:32768\n\t"
    "v_cvt_pk_bf16_f32 v73, v14, v15\n\t"
    "v_mfma_f32_16x16x32_bf16 v[4:7], a[152:155], v[184:187], v[4:7]\n\t"
    "v_exp_f32 v1, v1\n\t"
    "v_mfma_f32_16x16x32_bf16 v[4:7], a[156:159], v[188:191], v[4:7]\n\t"
    "ds_read_b64_tr_b16 a[162:163], v136 offset:36864\n\t"
    "v_add_f32 v144, v144, v0\n\t"
    "v_mfma_f32_16x16x32_bf16 a[40:43], a[176:179], v[32:35], a[40:43]\n\t"
    "v_exp_f32 v2, v2\n\t"
    "v_mfma_f32_16x16x32_bf16 a[40:43], a[180:183], v[36:39], a[40:43]\n\t"
    "ds_read_b128 a[136:139], v130 offset:57344\n\t"
    "v_cvt_pk_bf16_f32 v50, v0, v1\n\t"
    "v_mfma_f32_16x16x32_bf16 a[44:47], a[176:179], v[40:43], a[44:47]\n\t"
    "v_exp_f32 v3, v3\n\t"
    "v_mfma_f32_16x16x32_bf16 a[44:47], a[180:183], v[44:47], a[44:47]\n\t"
    "s_waitcnt lgkmcnt(3)\n\t"
    "v_add_f32 v144, v144, v1\n\t"
    "v_add_f32 v144, v144, v2\n\t"
    "v_mfma_f32_16x16x32_bf16 v[8:11], a[144:147], v[192:195], v[88:91]\n\t"
    "v_exp_f32 v4, v4\n\t"
    "v_mfma_f32_16x16x32_bf16 v[8:11], a[148:151], v[196:199], v[8:11]\n\t"
    "ds_read_b64_tr_b16 a[164:165], v136 offset:40960\n\t"
    "v_add_f32 v144, v144, v3\n\t"
    "v_mfma_f32_16x16x32_bf16 v[8:11], a[152:155], v[200:203], v[8:11]\n\t"
    "v_exp_f32 v5, v5\n\t"
    "v_mfma_f32_16x16x32_bf16 v[8:11], a[156:159], v[204:207], v[8:11]\n\t"
    "ds_read_b64_tr_b16 a[166:167], v136 offset:45056\n\t"
    "v_cvt_pk_bf16_f32 v51, v2, v3\n\t"
    "v_mfma_f32_16x16x32_bf16 a[48:51], a[184:187], v[16:19], a[48:51]\n\t"
    "v_exp_f32 v6, v6\n\t"
    "v_mfma_f32_16x16x32_bf16 a[48:51], a[188:191], v[20:23], a[48:51]\n\t"
    "ds_read_b128 a[140:143], v131 offset:57344\n\t"
    "v_cvt_pk_bf16_f32 v58, v4, v5\n\t"
    "v_mfma_f32_16x16x32_bf16 a[52:55], a[184:187], v[24:27], a[52:55]\n\t"
    "v_exp_f32 v7, v7\n\t"
    "v_mfma_f32_16x16x32_bf16 a[52:55], a[188:191], v[28:31], a[52:55]\n\t"
    "s_waitcnt lgkmcnt(3)\n\t"
    "buffer_load_dwordx4 v229, s[44:47], s51 offen offset:1024 lds\n\t"
    "v_mfma_f32_16x16x32_bf16 v[12:15], a[144:147], v[208:211], v[92:95]\n\t"
    "v_exp_f32 v8, v8\n\t"
    "v_mfma_f32_16x16x32_bf16 v[12:15], a[148:151], v[212:215], v[12:15]\n\t"
    "ds_read_b64_tr_b16 a[168:169], v137 offset:32768\n\t"
    "v_cvt_pk_bf16_f32 v59, v6, v7\n\t"
    "v_mfma_f32_16x16x32_bf16 v[12:15], a[152:155], v[216:219], v[12:15]\n\t"
    "v_exp_f32 v9, v9\n\t"
    "v_mfma_f32_16x16x32_bf16 v[12:15], a[156:159], v[220:223], v[12:15]\n\t"
    "ds_read_b64_tr_b16 a[170:171], v137 offset:36864\n\t"
    "v_add_f32 v146, v146, v8\n\t"
    "v_mfma_f32_16x16x32_bf16 a[56:59], a[184:187], v[32:35], a[56:59]\n\t"
    "v_exp_f32 v10, v10\n\t"
    "v_mfma_f32_16x16x32_bf16 a[56:59], a[188:191], v[36:39], a[56:59]\n\t"
    "ds_read_b128 a[144:147], v128 offset:61440\n\t"
    "v_cvt_pk_bf16_f32 v66, v8, v9\n\t"
    "v_mfma_f32_16x16x32_bf16 a[60:63], a[184:187], v[40:43], a[60:63]\n\t"
    "v_exp_f32 v11, v11\n\t"
    "v_mfma_f32_16x16x32_bf16 a[60:63], a[188:191], v[44:47], a[60:63]\n\t"
    "s_waitcnt lgkmcnt(3)\n\t"
    "v_add_f32 v146, v146, v9\n\t"
    "v_add_f32 v146, v146, v10\n\t"
    "v_mfma_f32_16x16x32_bf16 v[0:3], a[128:131], v[160:163], v[80:83]\n\t"
    "v_exp_f32 v12, v12\n\t"
    "v_mfma_f32_16x16x32_bf16 v[0:3], a[132:135], v[164:167], v[0:3]\n\t"
    "ds_read_b64_tr_b16 a[172:173], v137 offset:40960\n\t"
    "v_add_f32 v146, v146, v11\n\t"
    "v_mfma_f32_16x16x32_bf16 v[0:3], a[136:139], v[168:171], v[0:3]\n\t"
    "v_exp_f32 v13, v13\n\t"
    "v_mfma_f32_16x16x32_bf16 v[0:3], a[140:143], v[172:175], v[0:3]\n\t"
    "ds_read_b64_tr_b16 a[174:175], v137 offset:45056\n\t"
    "v_cvt_pk_bf16_f32 v67, v10, v11\n\t"
    "v_mfma_f32_16x16x32_bf16 a[64:67], a[160:163], v[16:19], a[64:67]\n\t"
    "v_exp_f32 v14, v14\n\t"
    "v_mfma_f32_16x16x32_bf16 a[64:67], a[164:167], v[20:23], a[64:67]\n\t"
    "ds_read_b128 a[148:151], v129 offset:61440\n\t"
    "v_cvt_pk_bf16_f32 v74, v12, v13\n\t"
    "v_mfma_f32_16x16x32_bf16 a[68:71], a[160:163], v[24:27], a[68:71]\n\t"
    "v_exp_f32 v15, v15\n\t"
    "v_mfma_f32_16x16x32_bf16 a[68:71], a[164:167], v[28:31], a[68:71]\n\t"
    "s_waitcnt lgkmcnt(3)\n\t"
    "buffer_load_dwordx4 v228, s[44:47], s51 offen offset:2048 lds\n\t"
    "v_mfma_f32_16x16x32_bf16 v[4:7], a[128:131], v[176:179], v[84:87]\n\t"
    "v_exp_f32 v0, v0\n\t"
    "v_mfma_f32_16x16x32_bf16 v[4:7], a[132:135], v[180:183], v[4:7]\n\t"
    "ds_read_b64_tr_b16 a[176:177], v138 offset:32768\n\t"
    "v_cvt_pk_bf16_f32 v75, v14, v15\n\t"
    "v_mfma_f32_16x16x32_bf16 v[4:7], a[136:139], v[184:187], v[4:7]\n\t"
    "v_exp_f32 v1, v1\n\t"
    "v_mfma_f32_16x16x32_bf16 v[4:7], a[140:143], v[188:191], v[4:7]\n\t"
    "ds_read_b64_tr_b16 a[178:179], v138 offset:36864\n\t"
    "v_add_f32 v144, v144, v0\n\t"
    "v_mfma_f32_16x16x32_bf16 a[72:75], a[160:163], v[32:35], a[72:75]\n\t"
    "v_exp_f32 v2, v2\n\t"
    "v_mfma_f32_16x16x32_bf16 a[72:75], a[164:167], v[36:39], a[72:75]\n\t"
    "ds_read_b128 a[152:155], v130 offset:61440\n\t"
    "v_cvt_pk_bf16_f32 v52, v0, v1\n\t"
    "v_mfma_f32_16x16x32_bf16 a[76:79], a[160:163], v[40:43], a[76:79]\n\t"
    "v_exp_f32 v3, v3\n\t"
    "v_mfma_f32_16x16x32_bf16 a[76:79], a[164:167], v[44:47], a[76:79]\n\t"
    "s_waitcnt lgkmcnt(3)\n\t"
    "v_add_f32 v144, v144, v1\n\t"
    "v_add_f32 v144, v144, v2\n\t"
    "v_mfma_f32_16x16x32_bf16 v[8:11], a[128:131], v[192:195], v[88:91]\n\t"
    "v_exp_f32 v4, v4\n\t"
    "v_mfma_f32_16x16x32_bf16 v[8:11], a[132:135], v[196:199], v[8:11]\n\t"
    "ds_read_b64_tr_b16 a[180:181], v138 offset:40960\n\t"
    "v_add_f32 v144, v144, v3\n\t"
    "v_mfma_f32_16x16x32_bf16 v[8:11], a[136:139], v[200:203], v[8:11]\n\t"
    "v_exp_f32 v5, v5\n\t"
    "v_mfma_f32_16x16x32_bf16 v[8:11], a[140:143], v[204:207], v[8:11]\n\t"
    "ds_read_b64_tr_b16 a[182:183], v138 offset:45056\n\t"
    "v_cvt_pk_bf16_f32 v53, v2, v3\n\t"
    "v_mfma_f32_16x16x32_bf16 a[80:83], a[168:171], v[16:19], a[80:83]\n\t"
    "v_exp_f32 v6, v6\n\t"
    "v_mfma_f32_16x16x32_bf16 a[80:83], a[172:175], v[20:23], a[80:83]\n\t"
    "ds_read_b128 a[156:159], v131 offset:61440\n\t"
    "v_cvt_pk_bf16_f32 v60, v4, v5\n\t"
    "v_mfma_f32_16x16x32_bf16 a[84:87], a[168:171], v[24:27], a[84:87]\n\t"
    "v_exp_f32 v7, v7\n\t"
    "v_mfma_f32_16x16x32_bf16 a[84:87], a[172:175], v[28:31], a[84:87]\n\t"
    "s_waitcnt lgkmcnt(3)\n\t"
    "buffer_load_dwordx4 v229, s[44:47], s51 offen offset:3072 lds\n\t"
    "v_mfma_f32_16x16x32_bf16 v[12:15], a[128:131], v[208:211], v[92:95]\n\t"
    "s_waitcnt vmcnt(8)\n\t"
    "v_exp_f32 v8, v8\n\t"
    "v_mfma_f32_16x16x32_bf16 v[12:15], a[132:135], v[212:215], v[12:15]\n\t"
    "ds_read_b64_tr_b16 a[184:185], v139 offset:32768\n\t"
    "v_cvt_pk_bf16_f32 v61, v6, v7\n\t"
    "v_mfma_f32_16x16x32_bf16 v[12:15], a[136:139], v[216:219], v[12:15]\n\t"
    "s_barrier\n\t"
    "v_exp_f32 v9, v9\n\t"
    "v_mfma_f32_16x16x32_bf16 v[12:15], a[140:143], v[220:223], v[12:15]\n\t"
    "ds_read_b64_tr_b16 a[186:187], v139 offset:36864\n\t"
    "v_add_f32 v146, v146, v8\n\t"
    "v_mfma_f32_16x16x32_bf16 a[88:91], a[168:171], v[32:35], a[88:91]\n\t"
    "v_exp_f32 v10, v10\n\t"
    "v_mfma_f32_16x16x32_bf16 a[88:91], a[172:175], v[36:39], a[88:91]\n\t"
    "ds_read_b128 a[128:131], v128 offset:0\n\t"
    "v_cvt_pk_bf16_f32 v68, v8, v9\n\t"
    "v_mfma_f32_16x16x32_bf16 a[92:95], a[168:171], v[40:43], a[92:95]\n\t"
    "v_exp_f32 v11, v11\n\t"
    "v_mfma_f32_16x16x32_bf16 a[92:95], a[172:175], v[44:47], a[92:95]\n\t"
    "s_waitcnt lgkmcnt(3)\n\t"
    "v_add_f32 v146, v146, v9\n\t"
    "v_add_f32 v146, v146, v10\n\t"
    "v_mfma_f32_16x16x32_bf16 v[0:3], a[144:147], v[160:163], v[80:83]\n\t"
    "v_exp_f32 v12, v12\n\t"
    "v_mfma_f32_16x16x32_bf16 v[0:3], a[148:151], v[164:167], v[0:3]\n\t"
    "ds_read_b64_tr_b16 a[188:189], v139 offset:40960\n\t"
    "v_add_f32 v146, v146, v11\n\t"
    "v_mfma_f32_16x16x32_bf16 v[0:3], a[152:155], v[168:171], v[0:3]\n\t"
    "s_mov_b32 m0, s54\n\t"
    "v_exp_f32 v13, v13\n\t"
    "v_mfma_f32_16x16x32_bf16 v[0:3], a[156:159], v[172:175], v[0:3]\n\t"
    "ds_read_b64_tr_b16 a[190:191], v139 offset:45056\n\t"
    "v_cvt_pk_bf16_f32 v69, v10, v11\n\t"
    "v_mfma_f32_16x16x32_bf16 a[96:99], a[176:179], v[16:19], a[96:99]\n\t"
    "s_add_u32 s48, s48, 16384\n\t"
    "v_exp_f32 v14, v14\n\t"
    "v_mfma_f32_16x16x32_bf16 a[96:99], a[180:183], v[20:23], a[96:99]\n\t"
    "ds_read_b128 a[132:135], v129 offset:0\n\t"
    "v_cvt_pk_bf16_f32 v76, v12, v13\n\t"
    "v_mfma_f32_16x16x32_bf16 a[100:103], a[176:179], v[24:27], a[100:103]\n\t"
    "v_exp_f32 v15, v15\n\t"
    "v_mfma_f32_16x16x32_bf16 a[100:103], a[180:183], v[28:31], a[100:103]\n\t"
    "s_waitcnt lgkmcnt(3)\n\t"
    "buffer_load_dwordx4 v224, s[40:43], s48 offen offset:0 lds\n\t"
    "v_mfma_f32_16x16x32_bf16 v[4:7], a[144:147], v[176:179], v[84:87]\n\t"
    "v_exp_f32 v0, v0\n\t"
    "v_mfma_f32_16x16x32_bf16 v[4:7], a[148:151], v[180:183], v[4:7]\n\t"
    "ds_read_b64_tr_b16 a[160:161], v132 offset:49152\n\t"
    "v_cvt_pk_bf16_f32 v77, v14, v15\n\t"
    "v_mfma_f32_16x16x32_bf16 v[4:7], a[152:155], v[184:187], v[4:7]\n\t"
    "v_exp_f32 v1, v1\n\t"
    "v_mfma_f32_16x16x32_bf16 v[4:7], a[156:159], v[188:191], v[4:7]\n\t"
    "ds_read_b64_tr_b16 a[162:163], v132 offset:53248\n\t"
    "v_add_f32 v144, v144, v0\n\t"
    "v_mfma_f32_16x16x32_bf16 a[104:107], a[176:179], v[32:35], a[104:107]\n\t"
    "v_exp_f32 v2, v2\n\t"
    "v_mfma_f32_16x16x32_bf16 a[104:107], a[180:183], v[36:39], a[104:107]\n\t"
    "ds_read_b128 a[136:139], v130 offset:0\n\t"
    "v_cvt_pk_bf16_f32 v54, v0, v1\n\t"
    "v_mfma_f32_16x16x32_bf16 a[108:111], a[176:179], v[40:43], a[108:111]\n\t"
    "v_exp_f32 v3, v3\n\t"
    "v_mfma_f32_16x16x32_bf16 a[108:111], a[180:183], v[44:47], a[108:111]\n\t"
    "s_waitcnt lgkmcnt(3)\n\t"
    "v_add_f32 v144, v144, v1\n\t"
    "v_add_f32 v144, v144, v2\n\t"
    "v_mfma_f32_16x16x32_bf16 v[8:11], a[144:147], v[192:195], v[88:91]\n\t"
    "v_exp_f32 v4, v4\n\t"
    "v_mfma_f32_16x16x32_bf16 v[8:11], a[148:151], v[196:199], v[8:11]\n\t"
    "ds_read_b64_tr_b16 a[164:165], v132 offset:57344\n\t"
    "v_add_f32 v144, v144, v3\n\t"
    "v_mfma_f32_16x16x32_bf16 v[8:11], a[152:155], v[200:203], v[8:11]\n\t"
    "v_exp_f32 v5, v5\n\t"
    "v_mfma_f32_16x16x32_bf16 v[8:11], a[156:159], v[204:207], v[8:11]\n\t"
    "ds_read_b64_tr_b16 a[166:167], v132 offset:61440\n\t"
    "v_cvt_pk_bf16_f32 v55, v2, v3\n\t"
    "v_mfma_f32_16x16x32_bf16 a[112:115], a[184:187], v[16:19], a[112:115]\n\t"
    "v_exp_f32 v6, v6\n\t"
    "v_mfma_f32_16x16x32_bf16 a[112:115], a[188:191], v[20:23], a[112:115]\n\t"
    "ds_read_b128 a[140:143], v131 offset:0\n\t"
    "v_cvt_pk_bf16_f32 v62, v4, v5\n\t"
    "v_mfma_f32_16x16x32_bf16 a[116:119], a[184:187], v[24:27], a[116:119]\n\t"
    "v_exp_f32 v7, v7\n\t"
    "v_mfma_f32_16x16x32_bf16 a[116:119], a[188:191], v[28:31], a[116:119]\n\t"
    "s_waitcnt lgkmcnt(3)\n\t"
    "buffer_load_dwordx4 v225, s[40:43], s48 offen offset:1024 lds\n\t"
    "v_mfma_f32_16x16x32_bf16 v[12:15], a[144:147], v[208:211], v[92:95]\n\t"
    "v_exp_f32 v8, v8\n\t"
    "v_mfma_f32_16x16x32_bf16 v[12:15], a[148:151], v[212:215], v[12:15]\n\t"
    "ds_read_b64_tr_b16 a[168:169], v133 offset:49152\n\t"
    "v_cvt_pk_bf16_f32 v63, v6, v7\n\t"
    "v_mfma_f32_16x16x32_bf16 v[12:15], a[152:155], v[216:219], v[12:15]\n\t"
    "v_exp_f32 v9, v9\n\t"
    "v_mfma_f32_16x16x32_bf16 v[12:15], a[156:159], v[220:223], v[12:15]\n\t"
    "ds_read_b64_tr_b16 a[170:171], v133 offset:53248\n\t"
    "v_add_f32 v146, v146, v8\n\t"
    "v_mfma_f32_16x16x32_bf16 a[120:123], a[184:187], v[32:35], a[120:123]\n\t"
    "v_exp_f32 v10, v10\n\t"
    "v_mfma_f32_16x16x32_bf16 a[120:123], a[188:191], v[36:39], a[120:123]\n\t"
    "ds_read_b128 a[144:147], v128 offset:4096\n\t"
    "v_cvt_pk_bf16_f32 v70, v8, v9\n\t"
    "v_mfma_f32_16x16x32_bf16 a[124:127], a[184:187], v[40:43], a[124:127]\n\t"
    "v_exp_f32 v11, v11\n\t"
    "v_mfma_f32_16x16x32_bf16 a[124:127], a[188:191], v[44:47], a[124:127]\n\t"
    "s_waitcnt lgkmcnt(3)\n\t"
    "v_add_f32 v146, v146, v9\n\t"
    "v_add_f32 v146, v146, v10\n\t"
    "v_mfma_f32_16x16x32_bf16 v[0:3], a[128:131], v[160:163], v[80:83]\n\t"
    "v_exp_f32 v12, v12\n\t"
    "v_mfma_f32_16x16x32_bf16 v[0:3], a[132:135], v[164:167], v[0:3]\n\t"
    "ds_read_b64_tr_b16 a[172:173], v133 offset:57344\n\t"
    "v_add_f32 v146, v146, v11\n\t"
    "v_mfma_f32_16x16x32_bf16 v[0:3], a[136:139], v[168:171], v[0:3]\n\t"
    "v_exp_f32 v13, v13\n\t"
    "v_mfma_f32_16x16x32_bf16 v[0:3], a[140:143], v[172:175], v[0:3]\n\t"
    "ds_read_b64_tr_b16 a[174:175], v133 offset:61440\n\t"
    "v_cvt_pk_bf16_f32 v71, v10, v11\n\t"
    "v_mfma_f32_16x16x32_bf16 a[0:3], a[160:163], v[48:51], a[0:3]\n\t"
    "v_exp_f32 v14, v14\n\t"
    "v_mfma_f32_16x16x32_bf16 a[0:3], a[164:167], v[52:55], a[0:3]\n\t"
    "ds_read_b128 a[148:151], v129 offset:4096\n\t"
    "v_cvt_pk_bf16_f32 v78, v12, v13\n\t"
    "v_mfma_f32_16x16x32_bf16 a[4:7], a[160:163], v[56:59], a[4:7]\n\t"
    "v_exp_f32 v15, v15\n\t"
    "v_mfma_f32_16x16x32_bf16 a[4:7], a[164:167], v[60:63], a[4:7]\n\t"
    "s_waitcnt lgkmcnt(3)\n\t"
    "buffer_load_dwordx4 v226, s[40:43], s48 offen offset:2048 lds\n\t"
    "v_mfma_f32_16x16x32_bf16 v[4:7], a[128:131], v[176:179], v[84:87]\n\t"
    "v_exp_f32 v0, v0\n\t"
    "v_mfma_f32_16x16x32_bf16 v[4:7], a[132:135], v[180:183], v[4:7]\n\t"
    "ds_read_b64_tr_b16 a[176:177], v134 offset:49152\n\t"
    "v_cvt_pk_bf16_f32 v79, v14, v15\n\t"
    "v_mfma_f32_16x16x32_bf16 v[4:7], a[136:139], v[184:187], v[4:7]\n\t"
    "v_exp_f32 v1, v1\n\t"
    "v_mfma_f32_16x16x32_bf16 v[4:7], a[140:143], v[188:191], v[4:7]\n\t"
    "ds_read_b64_tr_b16 a[178:179], v134 offset:53248\n\t"
    "v_add_f32 v144, v144, v0\n\t"
    "v_mfma_f32_16x16x32_bf16 a[8:11], a[160:163], v[64:67], a[8:11]\n\t"
    "v_exp_f32 v2, v2\n\t"
    "v_mfma_f32_16x16x32_bf16 a[8:11], a[164:167], v[68:71], a[8:11]\n\t"
    "ds_read_b128 a[152:155], v130 offset:4096\n\t"
    "v_cvt_pk_bf16_f32 v16, v0, v1\n\t"
    "v_mfma_f32_16x16x32_bf16 a[12:15], a[160:163], v[72:75], a[12:15]\n\t"
    "v_exp_f32 v3, v3\n\t"
    "v_mfma_f32_16x16x32_bf16 a[12:15], a[164:167], v[76:79], a[12:15]\n\t"
    "s_waitcnt lgkmcnt(3)\n\t"
    "v_add_f32 v144, v144, v1\n\t"
    "v_add_f32 v144, v144, v2\n\t"
    "v_mfma_f32_16x16x32_bf16 v[148:151], v[140:143], v[56:59], v[148:151]\n\t"
    "v_mfma_f32_16x16x32_bf16 v[148:151], v[140:143], v[60:63], v[148:151]\n\t"
    "v_mfma_f32_16x16x32_bf16 v[8:11], a[128:131], v[192:195], v[88:91]\n\t"
    "v_exp_f32 v4, v4\n\t"
    "v_mfma_f32_16x16x32_bf16 v[8:11], a[132:135], v[196:199], v[8:11]\n\t"
    "ds_read_b64_tr_b16 a[180:181], v134 offset:57344\n\t"
    "v_add_f32 v144, v144, v3\n\t"
    "v_mfma_f32_16x16x32_bf16 v[8:11], a[136:139], v[200:203], v[8:11]\n\t"
    "v_exp_f32 v5, v5\n\t"
    "v_mfma_f32_16x16x32_bf16 v[8:11], a[140:143], v[204:207], v[8:11]\n\t"
    "ds_read_b64_tr_b16 a[182:183], v134 offset:61440\n\t"
    "v_cvt_pk_bf16_f32 v17, v2, v3\n\t"
    "v_mfma_f32_16x16x32_bf16 a[16:19], a[168:171], v[48:51], a[16:19]\n\t"
    "v_exp_f32 v6, v6\n\t"
    "v_mfma_f32_16x16x32_bf16 a[16:19], a[172:175], v[52:55], a[16:19]\n\t"
    "ds_read_b128 a[156:159], v131 offset:4096\n\t"
    "v_cvt_pk_bf16_f32 v24, v4, v5\n\t"
    "v_mfma_f32_16x16x32_bf16 a[20:23], a[168:171], v[56:59], a[20:23]\n\t"
    "v_exp_f32 v7, v7\n\t"
    "v_mfma_f32_16x16x32_bf16 a[20:23], a[172:175], v[60:63], a[20:23]\n\t"
    "s_waitcnt lgkmcnt(3)\n\t"
    "buffer_load_dwordx4 v227, s[40:43], s48 offen offset:3072 lds\n\t"
    "v_mfma_f32_16x16x32_bf16 v[12:15], a[128:131], v[208:211], v[92:95]\n\t"
    "v_exp_f32 v8, v8\n\t"
    "v_mfma_f32_16x16x32_bf16 v[12:15], a[132:135], v[212:215], v[12:15]\n\t"
    "ds_read_b64_tr_b16 a[184:185], v135 offset:49152\n\t"
    "v_cvt_pk_bf16_f32 v25, v6, v7\n\t"
    "v_mfma_f32_16x16x32_bf16 v[12:15], a[136:139], v[216:219], v[12:15]\n\t"
    "v_exp_f32 v9, v9\n\t"
    "v_mfma_f32_16x16x32_bf16 v[12:15], a[140:143], v[220:223], v[12:15]\n\t"
    "ds_read_b64_tr_b16 a[186:187], v135 offset:53248\n\t"
    "v_add_f32 v146, v146, v8\n\t"
    "v_mfma_f32_16x16x32_bf16 a[24:27], a[168:171], v[64:67], a[24:27]\n\t"
    "v_exp_f32 v10, v10\n\t"
    "v_mfma_f32_16x16x32_bf16 a[24:27], a[172:175], v[68:71], a[24:27]\n\t"
    "ds_read_b128 a[128:131], v128 offset:8192\n\t"
    "v_cvt_pk_bf16_f32 v32, v8, v9\n\t"
    "v_mfma_f32_16x16x32_bf16 a[28:31], a[168:171], v[72:75], a[28:31]\n\t"
    "v_exp_f32 v11, v11\n\t"
    "v_mfma_f32_16x16x32_bf16 a[28:31], a[172:175], v[76:79], a[28:31]\n\t"
    "s_waitcnt lgkmcnt(3)\n\t"
    "v_add_f32 v146, v146, v9\n\t"
    "v_add_f32 v146, v146, v10\n\t"
    "v_mfma_f32_16x16x32_bf16 v[152:155], v[140:143], v[72:75], v[152:155]\n\t"
    "v_mfma_f32_16x16x32_bf16 v[152:155], v[140:143], v[76:79], v[152:155]\n\t"
    "v_mfma_f32_16x16x32_bf16 v[0:3], a[144:147], v[160:163], v[80:83]\n\t"
    "v_exp_f32 v12, v12\n\t"
    "v_mfma_f32_16x16x32_bf16 v[0:3], a[148:151], v[164:167], v[0:3]\n\t"
    "ds_read_b64_tr_b16 a[188:189], v135 offset:57344\n\t"
    "v_add_f32 v146, v146, v11\n\t"
    "v_mfma_f32_16x16x32_bf16 v[0:3], a[152:155], v[168:171], v[0:3]\n\t"
    "s_mov_b32 m0, s57\n\t"
    "v_exp_f32 v13, v13\n\t"
    "v_mfma_f32_16x16x32_bf16 v[0:3], a[156:159], v[172:175], v[0:3]\n\t"
    "ds_read_b64_tr_b16 a[190:191], v135 offset:61440\n\t"
    "v_cvt_pk_bf16_f32 v33, v10, v11\n\t"
    "v_mfma_f32_16x16x32_bf16 a[32:35], a[176:179], v[48:51], a[32:35]\n\t"
    "s_add_u32 s51, s51, 16384\n\t"
    "v_exp_f32 v14, v14\n\t"
    "v_mfma_f32_16x16x32_bf16 a[32:35], a[180:183], v[52:55], a[32:35]\n\t"
    "ds_read_b128 a[132:135], v129 offset:8192\n\t"
    "v_cvt_pk_bf16_f32 v40, v12, v13\n\t"
    "v_mfma_f32_16x16x32_bf16 a[36:39], a[176:179], v[56:59], a[36:39]\n\t"
    "v_exp_f32 v15, v15\n\t"
    "v_mfma_f32_16x16x32_bf16 a[36:39], a[180:183], v[60:63], a[36:39]\n\t"
    "s_waitcnt lgkmcnt(3)\n\t"
    "buffer_load_dwordx4 v228, s[44:47], s51 offen offset:0 lds\n\t"
    "v_mfma_f32_16x16x32_bf16 v[4:7], a[144:147], v[176:179], v[84:87]\n\t"
    "v_exp_f32 v0, v0\n\t"
    "v_mfma_f32_16x16x32_bf16 v[4:7], a[148:151], v[180:183], v[4:7]\n\t"
    "ds_read_b64_tr_b16 a[160:161], v136 offset:49152\n\t"
    "v_cvt_pk_bf16_f32 v41, v14, v15\n\t"
    "v_mfma_f32_16x16x32_bf16 v[4:7], a[152:155], v[184:187], v[4:7]\n\t"
    "v_exp_f32 v1, v1\n\t"
    "v_mfma_f32_16x16x32_bf16 v[4:7], a[156:159], v[188:191], v[4:7]\n\t"
    "ds_read_b64_tr_b16 a[162:163], v136 offset:53248\n\t"
    "v_add_f32 v144, v144, v0\n\t"
    "v_mfma_f32_16x16x32_bf16 a[40:43], a[176:179], v[64:67], a[40:43]\n\t"
    "v_exp_f32 v2, v2\n\t"
    "v_mfma_f32_16x16x32_bf16 a[40:43], a[180:183], v[68:71], a[40:43]\n\t"
    "ds_read_b128 a[136:139], v130 offset:8192\n\t"
    "v_cvt_pk_bf16_f32 v18, v0, v1\n\t"
    "v_mfma_f32_16x16x32_bf16 a[44:47], a[176:179], v[72:75], a[44:47]\n\t"
    "v_exp_f32 v3, v3\n\t"
    "v_mfma_f32_16x16x32_bf16 a[44:47], a[180:183], v[76:79], a[44:47]\n\t"
    "s_waitcnt lgkmcnt(3)\n\t"
    "v_add_f32 v144, v144, v1\n\t"
    "v_add_f32 v144, v144, v2\n\t"
    "v_mfma_f32_16x16x32_bf16 v[8:11], a[144:147], v[192:195], v[88:91]\n\t"
    "v_exp_f32 v4, v4\n\t"
    "v_mfma_f32_16x16x32_bf16 v[8:11], a[148:151], v[196:199], v[8:11]\n\t"
    "ds_read_b64_tr_b16 a[164:165], v136 offset:57344\n\t"
    "v_add_f32 v144, v144, v3\n\t"
    "v_mfma_f32_16x16x32_bf16 v[8:11], a[152:155], v[200:203], v[8:11]\n\t"
    "v_exp_f32 v5, v5\n\t"
    "v_mfma_f32_16x16x32_bf16 v[8:11], a[156:159], v[204:207], v[8:11]\n\t"
    "ds_read_b64_tr_b16 a[166:167], v136 offset:61440\n\t"
    "v_cvt_pk_bf16_f32 v19, v2, v3\n\t"
    "v_mfma_f32_16x16x32_bf16 a[48:51], a[184:187], v[48:51], a[48:51]\n\t"
    "v_exp_f32 v6, v6\n\t"
    "v_mfma_f32_16x16x32_bf16 a[48:51], a[188:191], v[52:55], a[48:51]\n\t"
    "ds_read_b128 a[140:143], v131 offset:8192\n\t"
    "v_cvt_pk_bf16_f32 v26, v4, v5\n\t"
    "v_mfma_f32_16x16x32_bf16 a[52:55], a[184:187], v[56:59], a[52:55]\n\t"
    "v_exp_f32 v7, v7\n\t"
    "v_mfma_f32_16x16x32_bf16 a[52:55], a[188:191], v[60:63], a[52:55]\n\t"
    "s_waitcnt lgkmcnt(3)\n\t"
    "buffer_load_dwordx4 v229, s[44:47], s51 offen offset:1024 lds\n\t"
    "v_mfma_f32_16x16x32_bf16 v[12:15], a[144:147], v[208:211], v[92:95]\n\t"
    "v_exp_f32 v8, v8\n\t"
    "v_mfma_f32_16x16x32_bf16 v[12:15], a[148:151], v[212:215], v[12:15]\n\t"
    "ds_read_b64_tr_b16 a[168:169], v137 offset:49152\n\t"
    "v_cvt_pk_bf16_f32 v27, v6, v7\n\t"
    "v_mfma_f32_16x16x32_bf16 v[12:15], a[152:155], v[216:219], v[12:15]\n\t"
    "v_exp_f32 v9, v9\n\t"
    "v_mfma_f32_16x16x32_bf16 v[12:15], a[156:159], v[220:223], v[12:15]\n\t"
    "ds_read_b64_tr_b16 a[170:171], v137 offset:53248\n\t"
    "v_add_f32 v146, v146, v8\n\t"
    "v_mfma_f32_16x16x32_bf16 a[56:59], a[184:187], v[64:67], a[56:59]\n\t"
    "v_exp_f32 v10, v10\n\t"
    "v_mfma_f32_16x16x32_bf16 a[56:59], a[188:191], v[68:71], a[56:59]\n\t"
    "ds_read_b128 a[144:147], v128 offset:12288\n\t"
    "v_cvt_pk_bf16_f32 v34, v8, v9\n\t"
    "v_mfma_f32_16x16x32_bf16 a[60:63], a[184:187], v[72:75], a[60:63]\n\t"
    "v_exp_f32 v11, v11\n\t"
    "v_mfma_f32_16x16x32_bf16 a[60:63], a[188:191], v[76:79], a[60:63]\n\t"
    "s_waitcnt lgkmcnt(3)\n\t"
    "v_add_f32 v146, v146, v9\n\t"
    "v_add_f32 v146, v146, v10\n\t"
    "v_mfma_f32_16x16x32_bf16 v[0:3], a[128:131], v[160:163], v[80:83]\n\t"
    "v_exp_f32 v12, v12\n\t"
    "v_mfma_f32_16x16x32_bf16 v[0:3], a[132:135], v[164:167], v[0:3]\n\t"
    "ds_read_b64_tr_b16 a[172:173], v137 offset:57344\n\t"
    "v_add_f32 v146, v146, v11\n\t"
    "v_mfma_f32_16x16x32_bf16 v[0:3], a[136:139], v[168:171], v[0:3]\n\t"
    "v_exp_f32 v13, v13\n\t"
    "v_mfma_f32_16x16x32_bf16 v[0:3], a[140:143], v[172:175], v[0:3]\n\t"
    "ds_read_b64_tr_b16 a[174:175], v137 offset:61440\n\t"
    "v_cvt_pk_bf16_f32 v35, v10, v11\n\t"
    "v_mfma_f32_16x16x32_bf16 a[64:67], a[160:163], v[48:51], a[64:67]\n\t"
    "v_exp_f32 v14, v14\n\t"
    "v_mfma_f32_16x16x32_bf16 a[64:67], a[164:167], v[52:55], a[64:67]\n\t"
    "ds_read_b128 a[148:151], v129 offset:12288\n\t"
    "v_cvt_pk_bf16_f32 v42, v12, v13\n\t"
    "v_mfma_f32_16x16x32_bf16 a[68:71], a[160:163], v[56:59], a[68:71]\n\t"
    "v_exp_f32 v15, v15\n\t"
    "v_mfma_f32_16x16x32_bf16 a[68:71], a[164:167], v[60:63], a[68:71]\n\t"
    "s_waitcnt lgkmcnt(3)\n\t"
    "buffer_load_dwordx4 v228, s[44:47], s51 offen offset:2048 lds\n\t"
    "v_mfma_f32_16x16x32_bf16 v[4:7], a[128:131], v[176:179], v[84:87]\n\t"
    "v_exp_f32 v0, v0\n\t"
    "v_mfma_f32_16x16x32_bf16 v[4:7], a[132:135], v[180:183], v[4:7]\n\t"
    "ds_read_b64_tr_b16 a[176:177], v138 offset:49152\n\t"
    "v_cvt_pk_bf16_f32 v43, v14, v15\n\t"
    "v_mfma_f32_16x16x32_bf16 v[4:7], a[136:139], v[184:187], v[4:7]\n\t"
    "v_exp_f32 v1, v1\n\t"
    "v_mfma_f32_16x16x32_bf16 v[4:7], a[140:143], v[188:191], v[4:7]\n\t"
    "ds_read_b64_tr_b16 a[178:179], v138 offset:53248\n\t"
    "v_add_f32 v144, v144, v0\n\t"
    "v_mfma_f32_16x16x32_bf16 a[72:75], a[160:163], v[64:67], a[72:75]\n\t"
    "v_exp_f32 v2, v2\n\t"
    "v_mfma_f32_16x16x32_bf16 a[72:75], a[164:167], v[68:71], a[72:75]\n\t"
    "ds_read_b128 a[152:155], v130 offset:12288\n\t"
    "v_cvt_pk_bf16_f32 v20, v0, v1\n\t"
    "v_mfma_f32_16x16x32_bf16 a[76:79], a[160:163], v[72:75], a[76:79]\n\t"
    "v_exp_f32 v3, v3\n\t"
    "v_mfma_f32_16x16x32_bf16 a[76:79], a[164:167], v[76:79], a[76:79]\n\t"
    "s_waitcnt lgkmcnt(3)\n\t"
    "v_add_f32 v144, v144, v1\n\t"
    "v_add_f32 v144, v144, v2\n\t"
    "v_mfma_f32_16x16x32_bf16 v[8:11], a[128:131], v[192:195], v[88:91]\n\t"
    "v_exp_f32 v4, v4\n\t"
    "v_mfma_f32_16x16x32_bf16 v[8:11], a[132:135], v[196:199], v[8:11]\n\t"
    "ds_read_b64_tr_b16 a[180:181], v138 offset:57344\n\t"
    "v_add_f32 v144, v144, v3\n\t"
    "v_mfma_f32_16x16x32_bf16 v[8:11], a[136:139], v[200:203], v[8:11]\n\t"
    "v_exp_f32 v5, v5\n\t"
    "v_mfma_f32_16x16x32_bf16 v[8:11], a[140:143], v[204:207], v[8:11]\n\t"
    "ds_read_b64_tr_b16 a[182:183], v138 offset:61440\n\t"
    "v_cvt_pk_bf16_f32 v21, v2, v3\n\t"
    "v_mfma_f32_16x16x32_bf16 a[80:83], a[168:171], v[48:51], a[80:83]\n\t"
    "v_exp_f32 v6, v6\n\t"
    "v_mfma_f32_16x16x32_bf16 a[80:83], a[172:175], v[52:55], a[80:83]\n\t"
    "ds_read_b128 a[156:159], v131 offset:12288\n\t"
    "v_cvt_pk_bf16_f32 v28, v4, v5\n\t"
    "v_mfma_f32_16x16x32_bf16 a[84:87], a[168:171], v[56:59], a[84:87]\n\t"
    "v_exp_f32 v7, v7\n\t"
    "v_mfma_f32_16x16x32_bf16 a[84:87], a[172:175], v[60:63], a[84:87]\n\t"
    "s_waitcnt lgkmcnt(3)\n\t"
    "buffer_load_dwordx4 v229, s[44:47], s51 offen offset:3072 lds\n\t"
    "v_mfma_f32_16x16x32_bf16 v[12:15], a[128:131], v[208:211], v[92:95]\n\t"
    "s_waitcnt vmcnt(8)\n\t"
    "v_exp_f32 v8, v8\n\t"
    "v_mfma_f32_16x16x32_bf16 v[12:15], a[132:135], v[212:215], v[12:15]\n\t"
    "ds_read_b64_tr_b16 a[184:185], v139 offset:49152\n\t"
    "v_cvt_pk_bf16_f32 v29, v6, v7\n\t"
    "v_mfma_f32_16x16x32_bf16 v[12:15], a[136:139], v[216:219], v[12:15]\n\t"
    "s_barrier\n\t"
    "v_exp_f32 v9, v9\n\t"
    "v_mfma_f32_16x16x32_bf16 v[12:15], a[140:143], v[220:223], v[12:15]\n\t"
    "ds_read_b64_tr_b16 a[186:187], v139 offset:53248\n\t"
    "v_add_f32 v146, v146, v8\n\t"
    "v_mfma_f32_16x16x32_bf16 a[88:91], a[168:171], v[64:67], a[88:91]\n\t"
    "v_exp_f32 v10, v10\n\t"
    "v_mfma_f32_16x16x32_bf16 a[88:91], a[172:175], v[68:71], a[88:91]\n\t"
    "ds_read_b128 a[128:131], v128 offset:16384\n\t"
    "v_cvt_pk_bf16_f32 v36, v8, v9\n\t"
    "v_mfma_f32_16x16x32_bf16 a[92:95], a[168:171], v[72:75], a[92:95]\n\t"
    "v_exp_f32 v11, v11\n\t"
    "v_mfma_f32_16x16x32_bf16 a[92:95], a[172:175], v[76:79], a[92:95]\n\t"
    "s_waitcnt lgkmcnt(3)\n\t"
    "v_add_f32 v146, v146, v9\n\t"
    "v_add_f32 v146, v146, v10\n\t"
    "v_mfma_f32_16x16x32_bf16 v[0:3], a[144:147], v[160:163], v[80:83]\n\t"
    "v_exp_f32 v12, v12\n\t"
    "v_mfma_f32_16x16x32_bf16 v[0:3], a[148:151], v[164:167], v[0:3]\n\t"
    "ds_read_b64_tr_b16 a[188:189], v139 offset:57344\n\t"
    "v_add_f32 v146, v146, v11\n\t"
    "v_mfma_f32_16x16x32_bf16 v[0:3], a[152:155], v[168:171], v[0:3]\n\t"
    "s_mov_b32 m0, s55\n\t"
    "v_exp_f32 v13, v13\n\t"
    "v_mfma_f32_16x16x32_bf16 v[0:3], a[156:159], v[172:175], v[0:3]\n\t"
    "ds_read_b64_tr_b16 a[190:191], v139 offset:61440\n\t"
    "v_cvt_pk_bf16_f32 v37, v10, v11\n\t"
    "v_mfma_f32_16x16x32_bf16 a[96:99], a[176:179], v[48:51], a[96:99]\n\t"
    "s_add_u32 s48, s48, 16384\n\t"
    "v_exp_f32 v14, v14\n\t"
    "v_mfma_f32_16x16x32_bf16 a[96:99], a[180:183], v[52:55], a[96:99]\n\t"
    "ds_read_b128 a[132:135], v129 offset:16384\n\t"
    "v_cvt_pk_bf16_f32 v44, v12, v13\n\t"
    "v_mfma_f32_16x16x32_bf16 a[100:103], a[176:179], v[56:59], a[100:103]\n\t"
    "v_exp_f32 v15, v15\n\t"
    "v_mfma_f32_16x16x32_bf16 a[100:103], a[180:183], v[60:63], a[100:103]\n\t"
    "s_waitcnt lgkmcnt(3)\n\t"
    "buffer_load_dwordx4 v224, s[40:43], s48 offen offset:0 lds\n\t"
    "v_mfma_f32_16x16x32_bf16 v[4:7], a[144:147], v[176:179], v[84:87]\n\t"
    "v_exp_f32 v0, v0\n\t"
    "v_mfma_f32_16x16x32_bf16 v[4:7], a[148:151], v[180:183], v[4:7]\n\t"
    "ds_read_b64_tr_b16 a[160:161], v132 offset:0\n\t"
    "v_cvt_pk_bf16_f32 v45, v14, v15\n\t"
    "v_mfma_f32_16x16x32_bf16 v[4:7], a[152:155], v[184:187], v[4:7]\n\t"
    "v_exp_f32 v1, v1\n\t"
    "v_mfma_f32_16x16x32_bf16 v[4:7], a[156:159], v[188:191], v[4:7]\n\t"
    "ds_read_b64_tr_b16 a[162:163], v132 offset:4096\n\t"
    "v_add_f32 v144, v144, v0\n\t"
    "v_mfma_f32_16x16x32_bf16 a[104:107], a[176:179], v[64:67], a[104:107]\n\t"
    "v_exp_f32 v2, v2\n\t"
    "v_mfma_f32_16x16x32_bf16 a[104:107], a[180:183], v[68:71], a[104:107]\n\t"
    "ds_read_b128 a[136:139], v130 offset:16384\n\t"
    "v_cvt_pk_bf16_f32 v22, v0, v1\n\t"
    "v_mfma_f32_16x16x32_bf16 a[108:111], a[176:179], v[72:75], a[108:111]\n\t"
    "v_exp_f32 v3, v3\n\t"
    "v_mfma_f32_16x16x32_bf16 a[108:111], a[180:183], v[76:79], a[108:111]\n\t"
    "s_waitcnt lgkmcnt(3)\n\t"
    "v_add_f32 v144, v144, v1\n\t"
    "v_add_f32 v144, v144, v2\n\t"
    "v_mfma_f32_16x16x32_bf16 v[8:11], a[144:147], v[192:195], v[88:91]\n\t"
    "v_exp_f32 v4, v4\n\t"
    "v_mfma_f32_16x16x32_bf16 v[8:11], a[148:151], v[196:199], v[8:11]\n\t"
    "ds_read_b64_tr_b16 a[164:165], v132 offset:8192\n\t"
    "v_add_f32 v144, v144, v3\n\t"
    "v_mfma_f32_16x16x32_bf16 v[8:11], a[152:155], v[200:203], v[8:11]\n\t"
    "v_exp_f32 v5, v5\n\t"
    "v_mfma_f32_16x16x32_bf16 v[8:11], a[156:159], v[204:207], v[8:11]\n\t"
    "ds_read_b64_tr_b16 a[166:167], v132 offset:12288\n\t"
    "v_cvt_pk_bf16_f32 v23, v2, v3\n\t"
    "v_mfma_f32_16x16x32_bf16 a[112:115], a[184:187], v[48:51], a[112:115]\n\t"
    "v_exp_f32 v6, v6\n\t"
    "v_mfma_f32_16x16x32_bf16 a[112:115], a[188:191], v[52:55], a[112:115]\n\t"
    "ds_read_b128 a[140:143], v131 offset:16384\n\t"
    "v_cvt_pk_bf16_f32 v30, v4, v5\n\t"
    "v_mfma_f32_16x16x32_bf16 a[116:119], a[184:187], v[56:59], a[116:119]\n\t"
    "v_exp_f32 v7, v7\n\t"
    "v_mfma_f32_16x16x32_bf16 a[116:119], a[188:191], v[60:63], a[116:119]\n\t"
    "s_waitcnt lgkmcnt(3)\n\t"
    "buffer_load_dwordx4 v225, s[40:43], s48 offen offset:1024 lds\n\t"
    "v_mfma_f32_16x16x32_bf16 v[12:15], a[144:147], v[208:211], v[92:95]\n\t"
    "v_exp_f32 v8, v8\n\t"
    "v_mfma_f32_16x16x32_bf16 v[12:15], a[148:151], v[212:215], v[12:15]\n\t"
    "ds_read_b64_tr_b16 a[168:169], v133 offset:0\n\t"
    "v_cvt_pk_bf16_f32 v31, v6, v7\n\t"
    "v_mfma_f32_16x16x32_bf16 v[12:15], a[152:155], v[216:219], v[12:15]\n\t"
    "v_exp_f32 v9, v9\n\t"
    "v_mfma_f32_16x16x32_bf16 v[12:15], a[156:159], v[220:223], v[12:15]\n\t"
    "ds_read_b64_tr_b16 a[170:171], v133 offset:4096\n\t"
    "v_add_f32 v146, v146, v8\n\t"
    "v_mfma_f32_16x16x32_bf16 a[120:123], a[184:187], v[64:67], a[120:123]\n\t"
    "v_exp_f32 v10, v10\n\t"
    "v_mfma_f32_16x16x32_bf16 a[120:123], a[188:191], v[68:71], a[120:123]\n\t"
    "ds_read_b128 a[144:147], v128 offset:20480\n\t"
    "v_cvt_pk_bf16_f32 v38, v8, v9\n\t"
    "v_mfma_f32_16x16x32_bf16 a[124:127], a[184:187], v[72:75], a[124:127]\n\t"
    "v_exp_f32 v11, v11\n\t"
    "v_mfma_f32_16x16x32_bf16 a[124:127], a[188:191], v[76:79], a[124:127]\n\t"
    "s_waitcnt lgkmcnt(3)\n\t"
    "v_add_f32 v146, v146, v9\n\t"
    "v_add_f32 v146, v146, v10\n\t"
    "v_mfma_f32_16x16x32_bf16 v[0:3], a[128:131], v[160:163], v[80:83]\n\t"
    "v_exp_f32 v12, v12\n\t"
    "v_mfma_f32_16x16x32_bf16 v[0:3], a[132:135], v[164:167], v[0:3]\n\t"
    "ds_read_b64_tr_b16 a[172:173], v133 offset:8192\n\t"
    "v_add_f32 v146, v146, v11\n\t"
    "v_mfma_f32_16x16x32_bf16 v[0:3], a[136:139], v[168:171], v[0:3]\n\t"
    "v_exp_f32 v13, v13\n\t"
    "v_mfma_f32_16x16x32_bf16 v[0:3], a[140:143], v[172:175], v[0:3]\n\t"
    "ds_read_b64_tr_b16 a[174:175], v133 offset:12288\n\t"
    "v_cvt_pk_bf16_f32 v39, v10, v11\n\t"
    "v_mfma_f32_16x16x32_bf16 a[0:3], a[160:163], v[16:19], a[0:3]\n\t"
    "v_exp_f32 v14, v14\n\t"
    "v_mfma_f32_16x16x32_bf16 a[0:3], a[164:167], v[20:23], a[0:3]\n\t"
    "ds_read_b128 a[148:151], v129 offset:20480\n\t"
    "v_cvt_pk_bf16_f32 v46, v12, v13\n\t"
    "v_mfma_f32_16x16x32_bf16 a[4:7], a[160:163], v[24:27], a[4:7]\n\t"
    "v_exp_f32 v15, v15\n\t"
    "v_mfma_f32_16x16x32_bf16 a[4:7], a[164:167], v[28:31], a[4:7]\n\t"
    "s_waitcnt lgkmcnt(3)\n\t"
    "buffer_load_dwordx4 v226, s[40:43], s48 offen offset:2048 lds\n\t"
    "v_mfma_f32_16x16x32_bf16 v[4:7], a[128:131], v[176:179], v[84:87]\n\t"
    "v_exp_f32 v0, v0\n\t"
    "v_mfma_f32_16x16x32_bf16 v[4:7], a[132:135], v[180:183], v[4:7]\n\t"
    "ds_read_b64_tr_b16 a[176:177], v134 offset:0\n\t"
    "v_cvt_pk_bf16_f32 v47, v14, v15\n\t"
    "v_mfma_f32_16x16x32_bf16 v[4:7], a[136:139], v[184:187], v[4:7]\n\t"
    "v_exp_f32 v1, v1\n\t"
    "v_mfma_f32_16x16x32_bf16 v[4:7], a[140:143], v[188:191], v[4:7]\n\t"
    "ds_read_b64_tr_b16 a[178:179], v134 offset:4096\n\t"
    "v_add_f32 v144, v144, v0\n\t"
    "v_mfma_f32_16x16x32_bf16 a[8:11], a[160:163], v[32:35], a[8:11]\n\t"
    "v_exp_f32 v2, v2\n\t"
    "v_mfma_f32_16x16x32_bf16 a[8:11], a[164:167], v[36:39], a[8:11]\n\t"
    "ds_read_b128 a[152:155], v130 offset:20480\n\t"
    "v_cvt_pk_bf16_f32 v48, v0, v1\n\t"
    "v_mfma_f32_16x16x32_bf16 a[12:15], a[160:163], v[40:43], a[12:15]\n\t"
    "v_exp_f32 v3, v3\n\t"
    "v_mfma_f32_16x16x32_bf16 a[12:15], a[164:167], v[44:47], a[12:15]\n\t"
    "s_waitcnt lgkmcnt(3)\n\t"
    "v_add_f32 v144, v144, v1\n\t"
    "v_add_f32 v144, v144, v2\n\t"
    "v_mfma_f32_16x16x32_bf16 v[148:151], v[140:143], v[24:27], v[148:151]\n\t"
    "v_mfma_f32_16x16x32_bf16 v[148:151], v[140:143], v[28:31], v[148:151]\n\t"
    "v_mfma_f32_16x16x32_bf16 v[8:11], a[128:131], v[192:195], v[88:91]\n\t"
    "v_exp_f32 v4, v4\n\t"
    "v_mfma_f32_16x16x32_bf16 v[8:11], a[132:135], v[196:199], v[8:11]\n\t"
    "ds_read_b64_tr_b16 a[180:181], v134 offset:8192\n\t"
    "v_add_f32 v144, v144, v3\n\t"
    "v_mfma_f32_16x16x32_bf16 v[8:11], a[136:139], v[200:203], v[8:11]\n\t"
    "v_exp_f32 v5, v5\n\t"
    "v_mfma_f32_16x16x32_bf16 v[8:11], a[140:143], v[204:207], v[8:11]\n\t"
    "ds_read_b64_tr_b16 a[182:183], v134 offset:12288\n\t"
    "v_cvt_pk_bf16_f32 v49, v2, v3\n\t"
    "v_mfma_f32_16x16x32_bf16 a[16:19], a[168:171], v[16:19], a[16:19]\n\t"
    "v_exp_f32 v6, v6\n\t"
    "v_mfma_f32_16x16x32_bf16 a[16:19], a[172:175], v[20:23], a[16:19]\n\t"
    "ds_read_b128 a[156:159], v131 offset:20480\n\t"
    "v_cvt_pk_bf16_f32 v56, v4, v5\n\t"
    "v_mfma_f32_16x16x32_bf16 a[20:23], a[168:171], v[24:27], a[20:23]\n\t"
    "v_exp_f32 v7, v7\n\t"
    "v_mfma_f32_16x16x32_bf16 a[20:23], a[172:175], v[28:31], a[20:23]\n\t"
    "s_waitcnt lgkmcnt(3)\n\t"
    "buffer_load_dwordx4 v227, s[40:43], s48 offen offset:3072 lds\n\t"
    "v_mfma_f32_16x16x32_bf16 v[12:15], a[128:131], v[208:211], v[92:95]\n\t"
    "v_exp_f32 v8, v8\n\t"
    "v_mfma_f32_16x16x32_bf16 v[12:15], a[132:135], v[212:215], v[12:15]\n\t"
    "ds_read_b64_tr_b16 a[184:185], v135 offset:0\n\t"
    "v_cvt_pk_bf16_f32 v57, v6, v7\n\t"
    "v_mfma_f32_16x16x32_bf16 v[12:15], a[136:139], v[216:219], v[12:15]\n\t"
    "v_exp_f32 v9, v9\n\t"
    "v_mfma_f32_16x16x32_bf16 v[12:15], a[140:143], v[220:223], v[12:15]\n\t"
    "ds_read_b64_tr_b16 a[186:187], v135 offset:4096\n\t"
    "v_add_f32 v146, v146, v8\n\t"
    "v_mfma_f32_16x16x32_bf16 a[24:27], a[168:171], v[32:35], a[24:27]\n\t"
    "v_exp_f32 v10, v10\n\t"
    "v_mfma_f32_16x16x32_bf16 a[24:27], a[172:175], v[36:39], a[24:27]\n\t"
    "ds_read_b128 a[128:131], v128 offset:24576\n\t"
    "v_cvt_pk_bf16_f32 v64, v8, v9\n\t"
    "v_mfma_f32_16x16x32_bf16 a[28:31], a[168:171], v[40:43], a[28:31]\n\t"
    "v_exp_f32 v11, v11\n\t"
    "v_mfma_f32_16x16x32_bf16 a[28:31], a[172:175], v[44:47], a[28:31]\n\t"
    "s_waitcnt lgkmcnt(3)\n\t"
    "v_add_f32 v146, v146, v9\n\t"
    "v_add_f32 v146, v146, v10\n\t"
    "v_mfma_f32_16x16x32_bf16 v[152:155], v[140:143], v[40:43], v[152:155]\n\t"
    "v_mfma_f32_16x16x32_bf16 v[152:155], v[140:143], v[44:47], v[152:155]\n\t"
    "v_mfma_f32_16x16x32_bf16 v[0:3], a[144:147], v[160:163], v[80:83]\n\t"
    "v_exp_f32 v12, v12\n\t"
    "v_mfma_f32_16x16x32_bf16 v[0:3], a[148:151], v[164:167], v[0:3]\n\t"
    "ds_read_b64_tr_b16 a[188:189], v135 offset:8192\n\t"
    "v_add_f32 v146, v146, v11\n\t"
    "v_mfma_f32_16x16x32_bf16 v[0:3], a[152:155], v[168:171], v[0:3]\n\t"
    "s_mov_b32 m0, s58\n\t"
    "v_exp_f32 v13, v13\n\t"
    "v_mfma_f32_16x16x32_bf16 v[0:3], a[156:159], v[172:175], v[0:3]\n\t"
    "ds_read_b64_tr_b16 a[190:191], v135 offset:12288\n\t"
    "v_cvt_pk_bf16_f32 v65, v10, v11\n\t"
    "v_mfma_f32_16x16x32_bf16 a[32:35], a[176:179], v[16:19], a[32:35]\n\t"
    "s_add_u32 s51, s51, 16384\n\t"
    "v_exp_f32 v14, v14\n\t"
    "v_mfma_f32_16x16x32_bf16 a[32:35], a[180:183], v[20:23], a[32:35]\n\t"
    "ds_read_b128 a[132:135], v129 offset:24576\n\t"
    "v_cvt_pk_bf16_f32 v72, v12, v13\n\t"
    "v_mfma_f32_16x16x32_bf16 a[36:39], a[176:179], v[24:27], a[36:39]\n\t"
    "v_exp_f32 v15, v15\n\t"
    "v_mfma_f32_16x16x32_bf16 a[36:39], a[180:183], v[28:31], a[36:39]\n\t"
    "s_waitcnt lgkmcnt(3)\n\t"
    "buffer_load_dwordx4 v228, s[44:47], s51 offen offset:0 lds\n\t"
    "v_mfma_f32_16x16x32_bf16 v[4:7], a[144:147], v[176:179], v[84:87]\n\t"
    "v_exp_f32 v0, v0\n\t"
    "v_mfma_f32_16x16x32_bf16 v[4:7], a[148:151], v[180:183], v[4:7]\n\t"
    "ds_read_b64_tr_b16 a[160:161], v136 offset:0\n\t"
    "v_cvt_pk_bf16_f32 v73, v14, v15\n\t"
    "v_mfma_f32_16x16x32_bf16 v[4:7], a[152:155], v[184:187], v[4:7]\n\t"
    "v_exp_f32 v1, v1\n\t"
    "v_mfma_f32_16x16x32_bf16 v[4:7], a[156:159], v[188:191], v[4:7]\n\t"
    "ds_read_b64_tr_b16 a[162:163], v136 offset:4096\n\t"
    "v_add_f32 v144, v144, v0\n\t"
    "v_mfma_f32_16x16x32_bf16 a[40:43], a[176:179], v[32:35], a[40:43]\n\t"
    "v_exp_f32 v2, v2\n\t"
    "v_mfma_f32_16x16x32_bf16 a[40:43], a[180:183], v[36:39], a[40:43]\n\t"
    "ds_read_b128 a[136:139], v130 offset:24576\n\t"
    "v_cvt_pk_bf16_f32 v50, v0, v1\n\t"
    "v_mfma_f32_16x16x32_bf16 a[44:47], a[176:179], v[40:43], a[44:47]\n\t"
    "v_exp_f32 v3, v3\n\t"
    "v_mfma_f32_16x16x32_bf16 a[44:47], a[180:183], v[44:47], a[44:47]\n\t"
    "s_waitcnt lgkmcnt(3)\n\t"
    "v_add_f32 v144, v144, v1\n\t"
    "v_add_f32 v144, v144, v2\n\t"
    "v_mfma_f32_16x16x32_bf16 v[8:11], a[144:147], v[192:195], v[88:91]\n\t"
    "v_exp_f32 v4, v4\n\t"
    "v_mfma_f32_16x16x32_bf16 v[8:11], a[148:151], v[196:199], v[8:11]\n\t"
    "ds_read_b64_tr_b16 a[164:165], v136 offset:8192\n\t"
    "v_add_f32 v144, v144, v3\n\t"
    "v_mfma_f32_16x16x32_bf16 v[8:11], a[152:155], v[200:203], v[8:11]\n\t"
    "v_exp_f32 v5, v5\n\t"
    "v_mfma_f32_16x16x32_bf16 v[8:11], a[156:159], v[204:207], v[8:11]\n\t"
    "ds_read_b64_tr_b16 a[166:167], v136 offset:12288\n\t"
    "v_cvt_pk_bf16_f32 v51, v2, v3\n\t"
    "v_mfma_f32_16x16x32_bf16 a[48:51], a[184:187], v[16:19], a[48:51]\n\t"
    "v_exp_f32 v6, v6\n\t"
    "v_mfma_f32_16x16x32_bf16 a[48:51], a[188:191], v[20:23], a[48:51]\n\t"
    "ds_read_b128 a[140:143], v131 offset:24576\n\t"
    "v_cvt_pk_bf16_f32 v58, v4, v5\n\t"
    "v_mfma_f32_16x16x32_bf16 a[52:55], a[184:187], v[24:27], a[52:55]\n\t"
    "v_exp_f32 v7, v7\n\t"
    "v_mfma_f32_16x16x32_bf16 a[52:55], a[188:191], v[28:31], a[52:55]\n\t"
    "s_waitcnt lgkmcnt(3)\n\t"
    "buffer_load_dwordx4 v229, s[44:47], s51 offen offset:1024 lds\n\t"
    "v_mfma_f32_16x16x32_bf16 v[12:15], a[144:147], v[208:211], v[92:95]\n\t"
    "v_exp_f32 v8, v8\n\t"
    "v_mfma_f32_16x16x32_bf16 v[12:15], a[148:151], v[212:215], v[12:15]\n\t"
    "ds_read_b64_tr_b16 a[168:169], v137 offset:0\n\t"
    "v_cvt_pk_bf16_f32 v59, v6, v7\n\t"
    "v_mfma_f32_16x16x32_bf16 v[12:15], a[152:155], v[216:219], v[12:15]\n\t"
    "v_exp_f32 v9, v9\n\t"
    "v_mfma_f32_16x16x32_bf16 v[12:15], a[156:159], v[220:223], v[12:15]\n\t"
    "ds_read_b64_tr_b16 a[170:171], v137 offset:4096\n\t"
    "v_add_f32 v146, v146, v8\n\t"
    "v_mfma_f32_16x16x32_bf16 a[56:59], a[184:187], v[32:35], a[56:59]\n\t"
    "v_exp_f32 v10, v10\n\t"
    "v_mfma_f32_16x16x32_bf16 a[56:59], a[188:191], v[36:39], a[56:59]\n\t"
    "ds_read_b128 a[144:147], v128 offset:28672\n\t"
    "v_cvt_pk_bf16_f32 v66, v8, v9\n\t"
    "v_mfma_f32_16x16x32_bf16 a[60:63], a[184:187], v[40:43], a[60:63]\n\t"
    "v_exp_f32 v11, v11\n\t"
    "v_mfma_f32_16x16x32_bf16 a[60:63], a[188:191], v[44:47], a[60:63]\n\t"
    "s_waitcnt lgkmcnt(3)\n\t"
    "v_add_f32 v146, v146, v9\n\t"
    "v_add_f32 v146, v146, v10\n\t"
    "v_mfma_f32_16x16x32_bf16 v[0:3], a[128:131], v[160:163], v[80:83]\n\t"
    "v_exp_f32 v12, v12\n\t"
    "v_mfma_f32_16x16x32_bf16 v[0:3], a[132:135], v[164:167], v[0:3]\n\t"
    "ds_read_b64_tr_b16 a[172:173], v137 offset:8192\n\t"
    "v_add_f32 v146, v146, v11\n\t"
    "v_mfma_f32_16x16x32_bf16 v[0:3], a[136:139], v[168:171], v[0:3]\n\t"
    "v_exp_f32 v13, v13\n\t"
    "v_mfma_f32_16x16x32_bf16 v[0:3], a[140:143], v[172:175], v[0:3]\n\t"
    "ds_read_b64_tr_b16 a[174:175], v137 offset:12288\n\t"
    "v_cvt_pk_bf16_f32 v67, v10, v11\n\t"
    "v_mfma_f32_16x16x32_bf16 a[64:67], a[160:163], v[16:19], a[64:67]\n\t"
    "v_exp_f32 v14, v14\n\t"
    "v_mfma_f32_16x16x32_bf16 a[64:67], a[164:167], v[20:23], a[64:67]\n\t"
    "ds_read_b128 a[148:151], v129 offset:28672\n\t"
    "v_cvt_pk_bf16_f32 v74, v12, v13\n\t"
    "v_mfma_f32_16x16x32_bf16 a[68:71], a[160:163], v[24:27], a[68:71]\n\t"
    "v_exp_f32 v15, v15\n\t"
    "v_mfma_f32_16x16x32_bf16 a[68:71], a[164:167], v[28:31], a[68:71]\n\t"
    "s_waitcnt lgkmcnt(3)\n\t"
    "buffer_load_dwordx4 v228, s[44:47], s51 offen offset:2048 lds\n\t"
    "v_mfma_f32_16x16x32_bf16 v[4:7], a[128:131], v[176:179], v[84:87]\n\t"
    "v_exp_f32 v0, v0\n\t"
    "v_mfma_f32_16x16x32_bf16 v[4:7], a[132:135], v[180:183], v[4:7]\n\t"
    "ds_read_b64_tr_b16 a[176:177], v138 offset:0\n\t"
    "v_cvt_pk_bf16_f32 v75, v14, v15\n\t"
    "v_mfma_f32_16x16x32_bf16 v[4:7], a[136:139], v[184:187], v[4:7]\n\t"
    "v_exp_f32 v1, v1\n\t"
    "v_mfma_f32_16x16x32_bf16 v[4:7], a[140:143], v[188:191], v[4:7]\n\t"
    "ds_read_b64_tr_b16 a[178:179], v138 offset:4096\n\t"
    "v_add_f32 v144, v144, v0\n\t"
    "v_mfma_f32_16x16x32_bf16 a[72:75], a[160:163], v[32:35], a[72:75]\n\t"
    "v_exp_f32 v2, v2\n\t"
    "v_mfma_f32_16x16x32_bf16 a[72:75], a[164:167], v[36:39], a[72:75]\n\t"
    "ds_read_b128 a[152:155], v130 offset:28672\n\t"
    "v_cvt_pk_bf16_f32 v52, v0, v1\n\t"
    "v_mfma_f32_16x16x32_bf16 a[76:79], a[160:163], v[40:43], a[76:79]\n\t"
    "v_exp_f32 v3, v3\n\t"
    "v_mfma_f32_16x16x32_bf16 a[76:79], a[164:167], v[44:47], a[76:79]\n\t"
    "s_waitcnt lgkmcnt(3)\n\t"
    "v_add_f32 v144, v144, v1\n\t"
    "v_add_f32 v144, v144, v2\n\t"
    "v_mfma_f32_16x16x32_bf16 v[8:11], a[128:131], v[192:195], v[88:91]\n\t"
    "v_exp_f32 v4, v4\n\t"
    "v_mfma_f32_16x16x32_bf16 v[8:11], a[132:135], v[196:199], v[8:11]\n\t"
    "ds_read_b64_tr_b16 a[180:181], v138 offset:8192\n\t"
    "v_add_f32 v144, v144, v3\n\t"
    "v_mfma_f32_16x16x32_bf16 v[8:11], a[136:139], v[200:203], v[8:11]\n\t"
    "v_exp_f32 v5, v5\n\t"
    "v_mfma_f32_16x16x32_bf16 v[8:11], a[140:143], v[204:207], v[8:11]\n\t"
    "ds_read_b64_tr_b16 a[182:183], v138 offset:12288\n\t"
    "v_cvt_pk_bf16_f32 v53, v2, v3\n\t"
    "v_mfma_f32_16x16x32_bf16 a[80:83], a[168:171], v[16:19], a[80:83]\n\t"
    "v_exp_f32 v6, v6\n\t"
    "v_mfma_f32_16x16x32_bf16 a[80:83], a[172:175], v[20:23], a[80:83]\n\t"
    "ds_read_b128 a[156:159], v131 offset:28672\n\t"
    "v_cvt_pk_bf16_f32 v60, v4, v5\n\t"
    "v_mfma_f32_16x16x32_bf16 a[84:87], a[168:171], v[24:27], a[84:87]\n\t"
    "v_exp_f32 v7, v7\n\t"
    "v_mfma_f32_16x16x32_bf16 a[84:87], a[172:175], v[28:31], a[84:87]\n\t"
    "s_waitcnt lgkmcnt(3)\n\t"
    "buffer_load_dwordx4 v229, s[44:47], s51 offen offset:3072 lds\n\t"
    "v_mfma_f32_16x16x32_bf16 v[12:15], a[128:131], v[208:211], v[92:95]\n\t"
    "s_waitcnt vmcnt(8)\n\t"
    "v_exp_f32 v8, v8\n\t"
    "v_mfma_f32_16x16x32_bf16 v[12:15], a[132:135], v[212:215], v[12:15]\n\t"
    "ds_read_b64_tr_b16 a[184:185], v139 offset:0\n\t"
    "v_cvt_pk_bf16_f32 v61, v6, v7\n\t"
    "v_mfma_f32_16x16x32_bf16 v[12:15], a[136:139], v[216:219], v[12:15]\n\t"
    "s_barrier\n\t"
    "v_exp_f32 v9, v9\n\t"
    "v_mfma_f32_16x16x32_bf16 v[12:15], a[140:143], v[220:223], v[12:15]\n\t"
    "ds_read_b64_tr_b16 a[186:187], v139 offset:4096\n\t"
    "v_add_f32 v146, v146, v8\n\t"
    "v_mfma_f32_16x16x32_bf16 a[88:91], a[168:171], v[32:35], a[88:91]\n\t"
    "v_exp_f32 v10, v10\n\t"
    "v_mfma_f32_16x16x32_bf16 a[88:91], a[172:175], v[36:39], a[88:91]\n\t"
    "ds_read_b128 a[128:131], v128 offset:32768\n\t"
    "v_cvt_pk_bf16_f32 v68, v8, v9\n\t"
    "v_mfma_f32_16x16x32_bf16 a[92:95], a[168:171], v[40:43], a[92:95]\n\t"
    "v_exp_f32 v11, v11\n\t"
    "v_mfma_f32_16x16x32_bf16 a[92:95], a[172:175], v[44:47], a[92:95]\n\t"
    "s_waitcnt lgkmcnt(3)\n\t"
    "v_add_f32 v146, v146, v9\n\t"
    "v_add_f32 v146, v146, v10\n\t"
    "v_mfma_f32_16x16x32_bf16 v[0:3], a[144:147], v[160:163], v[80:83]\n\t"
    "v_exp_f32 v12, v12\n\t"
    "v_mfma_f32_16x16x32_bf16 v[0:3], a[148:151], v[164:167], v[0:3]\n\t"
    "ds_read_b64_tr_b16 a[188:189], v139 offset:8192\n\t"
    "v_add_f32 v146, v146, v11\n\t"
    "v_mfma_f32_16x16x32_bf16 v[0:3], a[152:155], v[168:171], v[0:3]\n\t"
    "s_mov_b32 m0, s52\n\t"
    "v_exp_f32 v13, v13\n\t"
    "v_mfma_f32_16x16x32_bf16 v[0:3], a[156:159], v[172:175], v[0:3]\n\t"
    "ds_read_b64_tr_b16 a[190:191], v139 offset:12288\n\t"
    "v_cvt_pk_bf16_f32 v69, v10, v11\n\t"
    "v_mfma_f32_16x16x32_bf16 a[96:99], a[176:179], v[16:19], a[96:99]\n\t"
    "s_add_u32 s48, s48, 16384\n\t"
    "v_exp_f32 v14, v14\n\t"
    "v_mfma_f32_16x16x32_bf16 a[96:99], a[180:183], v[20:23], a[96:99]\n\t"
    "ds_read_b128 a[132:135], v129 offset:32768\n\t"
    "v_cvt_pk_bf16_f32 v76, v12, v13\n\t"
    "v_mfma_f32_16x16x32_bf16 a[100:103], a[176:179], v[24:27], a[100:103]\n\t"
    "v_exp_f32 v15, v15\n\t"
    "v_mfma_f32_16x16x32_bf16 a[100:103], a[180:183], v[28:31], a[100:103]\n\t"
    "s_waitcnt lgkmcnt(3)\n\t"
    "buffer_load_dwordx4 v224, s[40:43], s48 offen offset:0 lds\n\t"
    "v_mfma_f32_16x16x32_bf16 v[4:7], a[144:147], v[176:179], v[84:87]\n\t"
    "v_exp_f32 v0, v0\n\t"
    "v_mfma_f32_16x16x32_bf16 v[4:7], a[148:151], v[180:183], v[4:7]\n\t"
    "ds_read_b64_tr_b16 a[160:161], v132 offset:16384\n\t"
    "v_cvt_pk_bf16_f32 v77, v14, v15\n\t"
    "v_mfma_f32_16x16x32_bf16 v[4:7], a[152:155], v[184:187], v[4:7]\n\t"
    "v_exp_f32 v1, v1\n\t"
    "v_mfma_f32_16x16x32_bf16 v[4:7], a[156:159], v[188:191], v[4:7]\n\t"
    "ds_read_b64_tr_b16 a[162:163], v132 offset:20480\n\t"
    "v_add_f32 v144, v144, v0\n\t"
    "v_mfma_f32_16x16x32_bf16 a[104:107], a[176:179], v[32:35], a[104:107]\n\t"
    "v_exp_f32 v2, v2\n\t"
    "v_mfma_f32_16x16x32_bf16 a[104:107], a[180:183], v[36:39], a[104:107]\n\t"
    "ds_read_b128 a[136:139], v130 offset:32768\n\t"
    "v_cvt_pk_bf16_f32 v54, v0, v1\n\t"
    "v_mfma_f32_16x16x32_bf16 a[108:111], a[176:179], v[40:43], a[108:111]\n\t"
    "v_exp_f32 v3, v3\n\t"
    "v_mfma_f32_16x16x32_bf16 a[108:111], a[180:183], v[44:47], a[108:111]\n\t"
    "s_waitcnt lgkmcnt(3)\n\t"
    "v_add_f32 v144, v144, v1\n\t"
    "v_add_f32 v144, v144, v2\n\t"
    "v_mfma_f32_16x16x32_bf16 v[8:11], a[144:147], v[192:195], v[88:91]\n\t"
    "v_exp_f32 v4, v4\n\t"
    "v_mfma_f32_16x16x32_bf16 v[8:11], a[148:151], v[196:199], v[8:11]\n\t"
    "ds_read_b64_tr_b16 a[164:165], v132 offset:24576\n\t"
    "v_add_f32 v144, v144, v3\n\t"
    "v_mfma_f32_16x16x32_bf16 v[8:11], a[152:155], v[200:203], v[8:11]\n\t"
    "v_exp_f32 v5, v5\n\t"
    "v_mfma_f32_16x16x32_bf16 v[8:11], a[156:159], v[204:207], v[8:11]\n\t"
    "ds_read_b64_tr_b16 a[166:167], v132 offset:28672\n\t"
    "v_cvt_pk_bf16_f32 v55, v2, v3\n\t"
    "v_mfma_f32_16x16x32_bf16 a[112:115], a[184:187], v[16:19], a[112:115]\n\t"
    "v_exp_f32 v6, v6\n\t"
    "v_mfma_f32_16x16x32_bf16 a[112:115], a[188:191], v[20:23], a[112:115]\n\t"
    "ds_read_b128 a[140:143], v131 offset:32768\n\t"
    "v_cvt_pk_bf16_f32 v62, v4, v5\n\t"
    "v_mfma_f32_16x16x32_bf16 a[116:119], a[184:187], v[24:27], a[116:119]\n\t"
    "v_exp_f32 v7, v7\n\t"
    "v_mfma_f32_16x16x32_bf16 a[116:119], a[188:191], v[28:31], a[116:119]\n\t"
    "s_waitcnt lgkmcnt(3)\n\t"
    "buffer_load_dwordx4 v225, s[40:43], s48 offen offset:1024 lds\n\t"
    "v_mfma_f32_16x16x32_bf16 v[12:15], a[144:147], v[208:211], v[92:95]\n\t"
    "v_exp_f32 v8, v8\n\t"
    "v_mfma_f32_16x16x32_bf16 v[12:15], a[148:151], v[212:215], v[12:15]\n\t"
    "ds_read_b64_tr_b16 a[168:169], v133 offset:16384\n\t"
    "v_cvt_pk_bf16_f32 v63, v6, v7\n\t"
    "v_mfma_f32_16x16x32_bf16 v[12:15], a[152:155], v[216:219], v[12:15]\n\t"
    "v_exp_f32 v9, v9\n\t"
    "v_mfma_f32_16x16x32_bf16 v[12:15], a[156:159], v[220:223], v[12:15]\n\t"
    "ds_read_b64_tr_b16 a[170:171], v133 offset:20480\n\t"
    "v_add_f32 v146, v146, v8\n\t"
    "v_mfma_f32_16x16x32_bf16 a[120:123], a[184:187], v[32:35], a[120:123]\n\t"
    "v_exp_f32 v10, v10\n\t"
    "v_mfma_f32_16x16x32_bf16 a[120:123], a[188:191], v[36:39], a[120:123]\n\t"
    "ds_read_b128 a[144:147], v128 offset:36864\n\t"
    "v_cvt_pk_bf16_f32 v70, v8, v9\n\t"
    "v_mfma_f32_16x16x32_bf16 a[124:127], a[184:187], v[40:43], a[124:127]\n\t"
    "v_exp_f32 v11, v11\n\t"
    "v_mfma_f32_16x16x32_bf16 a[124:127], a[188:191], v[44:47], a[124:127]\n\t"
    "s_waitcnt lgkmcnt(3)\n\t"
    "v_add_f32 v146, v146, v9\n\t"
    "v_add_f32 v146, v146, v10\n\t"
    "s_sub_u32 s49, s49, 1\n\t"
    "s_cmp_lg_u32 s49, 0\n\t"
    "s_cbranch_scc1 1b\n\t"
    "v_mfma_f32_16x16x32_bf16 v[0:3], a[128:131], v[160:163], v[80:83]\n\t"
    "v_exp_f32 v12, v12\n\t"
    "v_mfma_f32_16x16x32_bf16 v[0:3], a[132:135], v[164:167], v[0:3]\n\t"
    "ds_read_b64_tr_b16 a[172:173], v133 offset:24576\n\t"
    "v_add_f32 v146, v146, v11\n\t"
    "v_mfma_f32_16x16x32_bf16 v[0:3], a[136:139], v[168:171], v[0:3]\n\t"
    "s_mov_b32 m0, s52\n\t"
    "v_exp_f32 v13, v13\n\t"
    "v_mfma_f32_16x16x32_bf16 v[0:3], a[140:143], v[172:175], v[0:3]\n\t"
    "ds_read_b64_tr_b16 a[174:175], v133 offset:28672\n\t"
    "v_cvt_pk_bf16_f32 v71, v10, v11\n\t"
    "v_mfma_f32_16x16x32_bf16 a[0:3], a[160:163], v[48:51], a[0:3]\n\t"
    "v_exp_f32 v14, v14\n\t"
    "v_mfma_f32_16x16x32_bf16 a[0:3], a[164:167], v[52:55], a[0:3]\n\t"
    "ds_read_b128 a[148:151], v129 offset:36864\n\t"
    "v_cvt_pk_bf16_f32 v78, v12, v13\n\t"
    "v_mfma_f32_16x16x32_bf16 a[4:7], a[160:163], v[56:59], a[4:7]\n\t"
    "v_exp_f32 v15, v15\n\t"
    "v_mfma_f32_16x16x32_bf16 a[4:7], a[164:167], v[60:63], a[4:7]\n\t"
    "s_waitcnt lgkmcnt(3)\n\t"
    "buffer_load_dwordx4 v226, s[40:43], s48 offen offset:2048 lds\n\t"
    "v_mov_b32 v251, 1\n\t"
    "v_mov_b32 v253, 0\n\t"
    "v_lshrrev_b32 v96, 8, v255\n\t"
    "v_bfe_u32 v97, v255, 4, 4\n\t"
    "s_and_b32 s76, s50, 1\n\t"
    "s_lshl_b32 s76, s76, 2\n\t"
    "v_or_b32 v98, s76, v96\n\t"
    "v_lshrrev_b32 v99, 1, v97\n\t"
    "v_xor_b32 v98, v98, v99\n\t"
    "v_lshlrev_b32 v98, 5, v98\n\t"
    "v_and_b32 v99, 1, v97\n\t"
    "v_lshl_add_u32 v98, v99, 4, v98\n\t"
    "v_lshl_add_u32 v254, v96, 10, v98\n\t"
    "v_lshrrev_b32 v98, 7, v255\n\t"
    "v_bfe_u32 v99, v255, 5, 2\n\t"
    "v_bfe_u32 v159, v98, 1, 2\n\t"
    "v_xor_b32 v99, v99, v159\n\t"
    "v_lshlrev_b32 v99, 5, v99\n\t"
    "v_lshl_add_u32 v99, v98, 10, v99\n\t"
    "v_and_b32 v98, 16, v255\n\t"
    "v_add_u32 v159, v99, v98\n\t"
    "v_lshrrev_b32 v98, 2, v97\n\t"
    "v_lshl_add_u32 v98, v96, 2, v98\n\t"
    "v_and_b32 v99, 3, v97\n\t"
    "v_lshlrev_b32 v99, 3, v99\n\t"
    "v_lshl_add_u32 v99, v98, 7, v99\n\t"
    "s_add_u32 s76, s95, 131072\n\t"
    "v_add_u32 v99, s76, v99\n\t"
    "v_bfe_u32 v98, v98, 1, 2\n\t"
    "v_xor_b32 v97, 0, v98\n\t"
    "v_lshl_add_u32 v230, v97, 5, v99\n\t"
    "v_xor_b32 v97, 1, v98\n\t"
    "v_lshl_add_u32 v231, v97, 5, v99\n\t"
    "v_xor_b32 v97, 2, v98\n\t"
    "v_lshl_add_u32 v248, v97, 5, v99\n\t"
    "v_xor_b32 v97, 3, v98\n\t"
    "v_lshl_add_u32 v249, v97, 5, v99\n\t"
    "s_lshr_b32 s76, %[h], 3\n\t"
    "s_lshl_b32 s84, %[qb], 2\n\t"
    "s_add_u32 s84, s84, s76\n\t"
    "s_lshl_b32 s84, s84, 6\n\t"
    "buffer_load_dword v252, v253, s[68:71], s84 offen sc1\n\t"
    "v_lshrrev_b32 v96, 5, v255\n\t"
    "v_bfe_u32 v97, v255, 4, 1\n\t"
    "v_lshlrev_b32 v97, 7, v97\n\t"
    "v_lshl_add_u32 v96, v96, 10, v97\n\t"
    "s_lshl_b32 s76, %[qb], 2\n\t"
    "s_add_u32 s76, s76, s50\n\t"
    "s_lshl_b32 s77, s76, 15\n\t"
    "s_sub_u32 s78, s75, s94\n\t"
    "s_add_u32 s77, s77, s78\n\t"
    "s_cmp_lt_u32 s76, 16\n\t"
    "s_cselect_b32 s77, s77, 0x40000000\n\t"
    "buffer_load_dword v100, v96, s[64:67], s77 offen\n\t"
    "v_lshlrev_b32 v97, 6, v255\n\t"
    "s_lshl_b32 s76, s76, 16\n\t"
    "s_mul_i32 s77, s50, 458752\n\t"
    "s_sub_u32 s77, s74, s77\n\t"
    "s_add_u32 s77, s77, s76\n\t"
    "buffer_load_dword v101, v97, s[60:63], s77 offen\n\t"
    "v_mfma_f32_16x16x32_bf16 v[4:7], a[128:131], v[176:179], v[84:87]\n\t"
    "v_exp_f32 v0, v0\n\t"
    "v_mfma_f32_16x16x32_bf16 v[4:7], a[132:135], v[180:183], v[4:7]\n\t"
    "ds_read_b64_tr_b16 a[176:177], v134 offset:16384\n\t"
    "v_cvt_pk_bf16_f32 v79, v14, v15\n\t"
    "v_mfma_f32_16x16x32_bf16 v[4:7], a[136:139], v[184:187], v[4:7]\n\t"
    "v_exp_f32 v1, v1\n\t"
    "v_mfma_f32_16x16x32_bf16 v[4:7], a[140:143], v[188:191], v[4:7]\n\t"
    "ds_read_b64_tr_b16 a[178:179], v134 offset:20480\n\t"
    "v_add_f32 v144, v144, v0\n\t"
    "v_mfma_f32_16x16x32_bf16 a[8:11], a[160:163], v[64:67], a[8:11]\n\t"
    "v_exp_f32 v2, v2\n\t"
    "v_mfma_f32_16x16x32_bf16 a[8:11], a[164:167], v[68:71], a[8:11]\n\t"
    "ds_read_b128 a[152:155], v130 offset:36864\n\t"
    "v_cvt_pk_bf16_f32 v16, v0, v1\n\t"
    "v_mfma_f32_16x16x32_bf16 a[12:15], a[160:163], v[72:75], a[12:15]\n\t"
    "v_exp_f32 v3, v3\n\t"
    "v_mfma_f32_16x16x32_bf16 a[12:15], a[164:167], v[76:79], a[12:15]\n\t"
    "s_waitcnt lgkmcnt(3)\n\t"
    "v_add_f32 v144, v144, v1\n\t"
    "v_add_f32 v144, v144, v2\n\t"
    "v_mfma_f32_16x16x32_bf16 v[148:151], v[140:143], v[56:59], v[148:151]\n\t"
    "v_mfma_f32_16x16x32_bf16 v[148:151], v[140:143], v[60:63], v[148:151]\n\t"
    "v_mfma_f32_16x16x32_bf16 v[8:11], a[128:131], v[192:195], v[88:91]\n\t"
    "v_exp_f32 v4, v4\n\t"
    "v_mfma_f32_16x16x32_bf16 v[8:11], a[132:135], v[196:199], v[8:11]\n\t"
    "ds_read_b64_tr_b16 a[180:181], v134 offset:24576\n\t"
    "v_add_f32 v144, v144, v3\n\t"
    "v_mfma_f32_16x16x32_bf16 v[8:11], a[136:139], v[200:203], v[8:11]\n\t"
    "s_mov_b32 m0, s52\n\t"
    "v_exp_f32 v5, v5\n\t"
    "v_mfma_f32_16x16x32_bf16 v[8:11], a[140:143], v[204:207], v[8:11]\n\t"
    "ds_read_b64_tr_b16 a[182:183], v134 offset:28672\n\t"
    "v_cvt_pk_bf16_f32 v17, v2, v3\n\t"
    "v_mfma_f32_16x16x32_bf16 a[16:19], a[168:171], v[48:51], a[16:19]\n\t"
    "v_exp_f32 v6, v6\n\t"
    "v_mfma_f32_16x16x32_bf16 a[16:19], a[172:175], v[52:55], a[16:19]\n\t"
    "ds_read_b128 a[156:159], v131 offset:36864\n\t"
    "v_cvt_pk_bf16_f32 v24, v4, v5\n\t"
    "v_mfma_f32_16x16x32_bf16 a[20:23], a[168:171], v[56:59], a[20:23]\n\t"
    "v_exp_f32 v7, v7\n\t"
    "v_mfma_f32_16x16x32_bf16 a[20:23], a[172:175], v[60:63], a[20:23]\n\t"
    "s_waitcnt lgkmcnt(3)\n\t"
    "buffer_load_dwordx4 v227, s[40:43], s48 offen offset:3072 lds\n\t"
    "v_mfma_f32_16x16x32_bf16 v[12:15], a[128:131], v[208:211], v[92:95]\n\t"
    "v_exp_f32 v8, v8\n\t"
    "v_mfma_f32_16x16x32_bf16 v[12:15], a[132:135], v[212:215], v[12:15]\n\t"
    "ds_read_b64_tr_b16 a[184:185], v135 offset:16384\n\t"
    "v_cvt_pk_bf16_f32 v25, v6, v7\n\t"
    "v_mfma_f32_16x16x32_bf16 v[12:15], a[136:139], v[216:219], v[12:15]\n\t"
    "v_exp_f32 v9, v9\n\t"
    "v_mfma_f32_16x16x32_bf16 v[12:15], a[140:143], v[220:223], v[12:15]\n\t"
    "ds_read_b64_tr_b16 a[186:187], v135 offset:20480\n\t"
    "v_add_f32 v146, v146, v8\n\t"
    "v_mfma_f32_16x16x32_bf16 a[24:27], a[168:171], v[64:67], a[24:27]\n\t"
    "v_exp_f32 v10, v10\n\t"
    "v_mfma_f32_16x16x32_bf16 a[24:27], a[172:175], v[68:71], a[24:27]\n\t"
    "ds_read_b128 a[128:131], v128 offset:40960\n\t"
    "v_cvt_pk_bf16_f32 v32, v8, v9\n\t"
    "v_mfma_f32_16x16x32_bf16 a[28:31], a[168:171], v[72:75], a[28:31]\n\t"
    "v_exp_f32 v11, v11\n\t"
    "v_mfma_f32_16x16x32_bf16 a[28:31], a[172:175], v[76:79], a[28:31]\n\t"
    "s_waitcnt lgkmcnt(3)\n\t"
    "v_add_f32 v146, v146, v9\n\t"
    "v_add_f32 v146, v146, v10\n\t"
    "v_mfma_f32_16x16x32_bf16 v[152:155], v[140:143], v[72:75], v[152:155]\n\t"
    "v_mfma_f32_16x16x32_bf16 v[152:155], v[140:143], v[76:79], v[152:155]\n\t"
    "v_mfma_f32_16x16x32_bf16 v[0:3], a[144:147], v[160:163], v[80:83]\n\t"
    "v_exp_f32 v12, v12\n\t"
    "v_mfma_f32_16x16x32_bf16 v[0:3], a[148:151], v[164:167], v[0:3]\n\t"
    "ds_read_b64_tr_b16 a[188:189], v135 offset:24576\n\t"
    "v_add_f32 v146, v146, v11\n\t"
    "v_mfma_f32_16x16x32_bf16 v[0:3], a[152:155], v[168:171], v[0:3]\n\t"
    "s_mov_b32 m0, s59\n\t"
    "v_exp_f32 v13, v13\n\t"
    "v_mfma_f32_16x16x32_bf16 v[0:3], a[156:159], v[172:175], v[0:3]\n\t"
    "ds_read_b64_tr_b16 a[190:191], v135 offset:28672\n\t"
    "v_cvt_pk_bf16_f32 v33, v10, v11\n\t"
    "v_mfma_f32_16x16x32_bf16 a[32:35], a[176:179], v[48:51], a[32:35]\n\t"
    "s_add_u32 s51, s51, 16384\n\t"
    "v_exp_f32 v14, v14\n\t"
    "v_mfma_f32_16x16x32_bf16 a[32:35], a[180:183], v[52:55], a[32:35]\n\t"
    "ds_read_b128 a[132:135], v129 offset:40960\n\t"
    "v_cvt_pk_bf16_f32 v40, v12, v13\n\t"
    "v_mfma_f32_16x16x32_bf16 a[36:39], a[176:179], v[56:59], a[36:39]\n\t"
    "v_exp_f32 v15, v15\n\t"
    "v_mfma_f32_16x16x32_bf16 a[36:39], a[180:183], v[60:63], a[36:39]\n\t"
    "s_waitcnt lgkmcnt(3)\n\t"
    "buffer_load_dwordx4 v228, s[44:47], s51 offen offset:0 lds\n\t"
    "v_mfma_f32_16x16x32_bf16 v[4:7], a[144:147], v[176:179], v[84:87]\n\t"
    "v_exp_f32 v0, v0\n\t"
    "v_mfma_f32_16x16x32_bf16 v[4:7], a[148:151], v[180:183], v[4:7]\n\t"
    "ds_read_b64_tr_b16 a[160:161], v136 offset:16384\n\t"
    "v_cvt_pk_bf16_f32 v41, v14, v15\n\t"
    "v_mfma_f32_16x16x32_bf16 v[4:7], a[152:155], v[184:187], v[4:7]\n\t"
    "v_exp_f32 v1, v1\n\t"
    "v_mfma_f32_16x16x32_bf16 v[4:7], a[156:159], v[188:191], v[4:7]\n\t"
    "ds_read_b64_tr_b16 a[162:163], v136 offset:20480\n\t"
    "v_add_f32 v144, v144, v0\n\t"
    "v_mfma_f32_16x16x32_bf16 a[40:43], a[176:179], v[64:67], a[40:43]\n\t"
    "v_exp_f32 v2, v2\n\t"
    "v_mfma_f32_16x16x32_bf16 a[40:43], a[180:183], v[68:71], a[40:43]\n\t"
    "ds_read_b128 a[136:139], v130 offset:40960\n\t"
    "v_cvt_pk_bf16_f32 v18, v0, v1\n\t"
    "v_mfma_f32_16x16x32_bf16 a[44:47], a[176:179], v[72:75], a[44:47]\n\t"
    "v_exp_f32 v3, v3\n\t"
    "v_mfma_f32_16x16x32_bf16 a[44:47], a[180:183], v[76:79], a[44:47]\n\t"
    "s_waitcnt lgkmcnt(3)\n\t"
    "v_add_f32 v144, v144, v1\n\t"
    "v_add_f32 v144, v144, v2\n\t"
    "s_waitcnt vmcnt(4)\n\t"
    "v_readfirstlane_b32 s78, v252\n\t"
    "s_nop 3\n\t"
    "s_cmp_ge_u32 s78, 32\n\t"
    "s_cbranch_scc1 .Lx%=_1\n\t"
    "s_mov_b32 s79, 0x8000\n\t"
    ".Lx%=_2:\n\t"
    "buffer_load_dword v252, v253, s[68:71], s84 offen sc1\n\t"
    "s_waitcnt vmcnt(0)\n\t"
    "v_readfirstlane_b32 s78, v252\n\t"
    "s_nop 3\n\t"
    "s_cmp_ge_u32 s78, 32\n\t"
    "s_cbranch_scc1 .Lx%=_3\n\t"
    "s_sleep 1\n\t"
    "s_sub_u32 s79, s79, 1\n\t"
    "s_cmp_lg_u32 s79, 0\n\t"
    "s_cbranch_scc1 .Lx%=_2\n\t"
    ".Lx%=_3:\n\t"
    ".Lx%=_1:\n\t"
    "s_add_u32 s93, %[h], 0\n\t"
    "s_and_b32 s93, s93, 3\n\t"
    "s_lshl_b32 s93, s93, 17\n\t"
    "s_add_u32 s93, s93, s75\n\t"
    "v_mfma_f32_16x16x32_bf16 v[8:11], a[144:147], v[192:195], v[88:91]\n\t"
    "v_exp_f32 v4, v4\n\t"
    "v_mfma_f32_16x16x32_bf16 v[8:11], a[148:151], v[196:199], v[8:11]\n\t"
    "ds_read_b64_tr_b16 a[164:165], v136 offset:24576\n\t"
    "v_add_f32 v144, v144, v3\n\t"
    "v_mfma_f32_16x16x32_bf16 v[8:11], a[152:155], v[200:203], v[8:11]\n\t"
    "s_mov_b32 m0, s59\n\t"
    "v_exp_f32 v5, v5\n\t"
    "v_mfma_f32_16x16x32_bf16 v[8:11], a[156:159], v[204:207], v[8:11]\n\t"
    "ds_read_b64_tr_b16 a[166:167], v136 offset:28672\n\t"
    "v_cvt_pk_bf16_f32 v19, v2, v3\n\t"
    "v_mfma_f32_16x16x32_bf16 a[48:51], a[184:187], v[48:51], a[48:51]\n\t"
    "v_exp_f32 v6, v6\n\t"
    "v_mfma_f32_16x16x32_bf16 a[48:51], a[188:191], v[52:55], a[48:51]\n\t"
    "ds_read_b128 a[140:143], v131 offset:40960\n\t"
    "v_cvt_pk_bf16_f32 v26, v4, v5\n\t"
    "v_mfma_f32_16x16x32_bf16 a[52:55], a[184:187], v[56:59], a[52:55]\n\t"
    "v_exp_f32 v7, v7\n\t"
    "v_mfma_f32_16x16x32_bf16 a[52:55], a[188:191], v[60:63], a[52:55]\n\t"
    "s_waitcnt lgkmcnt(3)\n\t"
    "buffer_load_dwordx4 v229, s[44:47], s51 offen offset:1024 lds\n\t"
    "v_mfma_f32_16x16x32_bf16 v[12:15], a[144:147], v[208:211], v[92:95]\n\t"
    "v_exp_f32 v8, v8\n\t"
    "v_mfma_f32_16x16x32_bf16 v[12:15], a[148:151], v[212:215], v[12:15]\n\t"
    "ds_read_b64_tr_b16 a[168:169], v137 offset:16384\n\t"
    "v_cvt_pk_bf16_f32 v27, v6, v7\n\t"
    "v_mfma_f32_16x16x32_bf16 v[12:15], a[152:155], v[216:219], v[12:15]\n\t"
    "v_exp_f32 v9, v9\n\t"
    "v_mfma_f32_16x16x32_bf16 v[12:15], a[156:159], v[220:223], v[12:15]\n\t"
    "ds_read_b64_tr_b16 a[170:171], v137 offset:20480\n\t"
    "v_add_f32 v146, v146, v8\n\t"
    "v_mfma_f32_16x16x32_bf16 a[56:59], a[184:187], v[64:67], a[56:59]\n\t"
    "v_exp_f32 v10, v10\n\t"
    "v_mfma_f32_16x16x32_bf16 a[56:59], a[188:191], v[68:71], a[56:59]\n\t"
    "ds_read_b128 a[144:147], v128 offset:45056\n\t"
    "v_cvt_pk_bf16_f32 v34, v8, v9\n\t"
    "v_mfma_f32_16x16x32_bf16 a[60:63], a[184:187], v[72:75], a[60:63]\n\t"
    "v_exp_f32 v11, v11\n\t"
    "v_mfma_f32_16x16x32_bf16 a[60:63], a[188:191], v[76:79], a[60:63]\n\t"
    "s_waitcnt lgkmcnt(3)\n\t"
    "v_add_f32 v146, v146, v9\n\t"
    "v_add_f32 v146, v146, v10\n\t"
    "s_add_u32 m0, s95, 131072\n\t"
    "s_add_u32 s76, s74, 0\n\t"
    "buffer_load_dwordx4 v159, s[60:63], s76 offen lds\n\t"
    "s_add_u32 m0, s95, 132096\n\t"
    "s_add_u32 s76, s74, 8192\n\t"
    "buffer_load_dwordx4 v159, s[60:63], s76 offen lds\n\t"
    "s_add_u32 m0, s95, 133120\n\t"
    "s_add_u32 s76, s74, 16384\n\t"
    "buffer_load_dwordx4 v159, s[60:63], s76 offen lds\n\t"
    "v_mfma_f32_16x16x32_bf16 v[0:3], a[128:131], v[160:163], v[80:83]\n\t"
    "v_exp_f32 v12, v12\n\t"
    "v_mfma_f32_16x16x32_bf16 v[0:3], a[132:135], v[164:167], v[0:3]\n\t"
    "ds_read_b64_tr_b16 a[172:173], v137 offset:24576\n\t"
    "v_add_f32 v146, v146, v11\n\t"
    "v_mfma_f32_16x16x32_bf16 v[0:3], a[136:139], v[168:171], v[0:3]\n\t"
    "s_mov_b32 m0, s59\n\t"
    "v_exp_f32 v13, v13\n\t"
    "v_mfma_f32_16x16x32_bf16 v[0:3], a[140:143], v[172:175], v[0:3]\n\t"
    "ds_read_b64_tr_b16 a[174:175], v137 offset:28672\n\t"
    "v_cvt_pk_bf16_f32 v35, v10, v11\n\t"
    "v_mfma_f32_16x16x32_bf16 a[64:67], a[160:163], v[48:51], a[64:67]\n\t"
    "v_exp_f32 v14, v14\n\t"
    "v_mfma_f32_16x16x32_bf16 a[64:67], a[164:167], v[52:55], a[64:67]\n\t"
    "ds_read_b128 a[148:151], v129 offset:45056\n\t"
    "v_cvt_pk_bf16_f32 v42, v12, v13\n\t"
    "v_mfma_f32_16x16x32_bf16 a[68:71], a[160:163], v[56:59], a[68:71]\n\t"
    "v_exp_f32 v15, v15\n\t"
    "v_mfma_f32_16x16x32_bf16 a[68:71], a[164:167], v[60:63], a[68:71]\n\t"
    "s_waitcnt lgkmcnt(3)\n\t"
    "buffer_load_dwordx4 v228, s[44:47], s51 offen offset:2048 lds\n\t"
    "v_mfma_f32_16x16x32_bf16 v[4:7], a[128:131], v[176:179], v[84:87]\n\t"
    "v_exp_f32 v0, v0\n\t"
    "v_mfma_f32_16x16x32_bf16 v[4:7], a[132:135], v[180:183], v[4:7]\n\t"
    "ds_read_b64_tr_b16 a[176:177], v138 offset:16384\n\t"
    "v_cvt_pk_bf16_f32 v43, v14, v15\n\t"
    "v_mfma_f32_16x16x32_bf16 v[4:7], a[136:139], v[184:187], v[4:7]\n\t"
    "v_exp_f32 v1, v1\n\t"
    "v_mfma_f32_16x16x32_bf16 v[4:7], a[140:143], v[188:191], v[4:7]\n\t"
    "ds_read_b64_tr_b16 a[178:179], v138 offset:20480\n\t"
    "v_add_f32 v144, v144, v0\n\t"
    "v_mfma_f32_16x16x32_bf16 a[72:75], a[160:163], v[64:67], a[72:75]\n\t"
    "v_exp_f32 v2, v2\n\t"
    "v_mfma_f32_16x16x32_bf16 a[72:75], a[164:167], v[68:71], a[72:75]\n\t"
    "ds_read_b128 a[152:155], v130 offset:45056\n\t"
    "v_cvt_pk_bf16_f32 v20, v0, v1\n\t"
    "v_mfma_f32_16x16x32_bf16 a[76:79], a[160:163], v[72:75], a[76:79]\n\t"
    "v_exp_f32 v3, v3\n\t"
    "v_mfma_f32_16x16x32_bf16 a[76:79], a[164:167], v[76:79], a[76:79]\n\t"
    "s_waitcnt lgkmcnt(3)\n\t"
    "v_add_f32 v144, v144, v1\n\t"
    "v_add_f32 v144, v144, v2\n\t"
    "s_add_u32 m0, s95, 134144\n\t"
    "s_add_u32 s76, s74, 24576\n\t"
    "buffer_load_dwordx4 v159, s[60:63], s76 offen lds\n\t"
    "s_add_u32 m0, s95, 135168\n\t"
    "s_add_u32 s76, s74, 32768\n\t"
    "buffer_load_dwordx4 v159, s[60:63], s76 offen lds\n\t"
    "s_add_u32 m0, s95, 136192\n\t"
    "s_add_u32 s76, s74, 40960\n\t"
    "buffer_load_dwordx4 v159, s[60:63], s76 offen lds\n\t"
    "v_mfma_f32_16x16x32_bf16 v[8:11], a[128:131], v[192:195], v[88:91]\n\t"
    "v_exp_f32 v4, v4\n\t"
    "v_mfma_f32_16x16x32_bf16 v[8:11], a[132:135], v[196:199], v[8:11]\n\t"
    "ds_read_b64_tr_b16 a[180:181], v138 offset:24576\n\t"
    "v_add_f32 v144, v144, v3\n\t"
    "v_mfma_f32_16x16x32_bf16 v[8:11], a[136:139], v[200:203], v[8:11]\n\t"
    "s_mov_b32 m0, s59\n\t"
    "v_exp_f32 v5, v5\n\t"
    "v_mfma_f32_16x16x32_bf16 v[8:11], a[140:143], v[204:207], v[8:11]\n\t"
    "ds_read_b64_tr_b16 a[182:183], v138 offset:28672\n\t"
    "v_cvt_pk_bf16_f32 v21, v2, v3\n\t"
    "v_mfma_f32_16x16x32_bf16 a[80:83], a[168:171], v[48:51], a[80:83]\n\t"
    "v_exp_f32 v6, v6\n\t"
    "v_mfma_f32_16x16x32_bf16 a[80:83], a[172:175], v[52:55], a[80:83]\n\t"
    "ds_read_b128 a[156:159], v131 offset:45056\n\t"
    "v_cvt_pk_bf16_f32 v28, v4, v5\n\t"
    "v_mfma_f32_16x16x32_bf16 a[84:87], a[168:171], v[56:59], a[84:87]\n\t"
    "v_exp_f32 v7, v7\n\t"
    "v_mfma_f32_16x16x32_bf16 a[84:87], a[172:175], v[60:63], a[84:87]\n\t"
    "s_waitcnt lgkmcnt(3)\n\t"
    "buffer_load_dwordx4 v229, s[44:47], s51 offen offset:3072 lds\n\t"
    "v_mfma_f32_16x16x32_bf16 v[12:15], a[128:131], v[208:211], v[92:95]\n\t"
    "s_waitcnt vmcnt(18)\n\t"
    "v_exp_f32 v8, v8\n\t"
    "v_mfma_f32_16x16x32_bf16 v[12:15], a[132:135], v[212:215], v[12:15]\n\t"
    "ds_read_b64_tr_b16 a[184:185], v139 offset:16384\n\t"
    "v_cvt_pk_bf16_f32 v29, v6, v7\n\t"
    "v_mfma_f32_16x16x32_bf16 v[12:15], a[136:139], v[216:219], v[12:15]\n\t"
    "s_barrier\n\t"
    "v_exp_f32 v9, v9\n\t"
    "v_mfma_f32_16x16x32_bf16 v[12:15], a[140:143], v[220:223], v[12:15]\n\t"
    "ds_read_b64_tr_b16 a[186:187], v139 offset:20480\n\t"
    "v_add_f32 v146, v146, v8\n\t"
    "v_mfma_f32_16x16x32_bf16 a[88:91], a[168:171], v[64:67], a[88:91]\n\t"
    "v_exp_f32 v10, v10\n\t"
    "v_mfma_f32_16x16x32_bf16 a[88:91], a[172:175], v[68:71], a[88:91]\n\t"
    "ds_read_b128 a[128:131], v128 offset:49152\n\t"
    "v_cvt_pk_bf16_f32 v36, v8, v9\n\t"
    "v_mfma_f32_16x16x32_bf16 a[92:95], a[168:171], v[72:75], a[92:95]\n\t"
    "v_exp_f32 v11, v11\n\t"
    "v_mfma_f32_16x16x32_bf16 a[92:95], a[172:175], v[76:79], a[92:95]\n\t"
    "s_waitcnt lgkmcnt(3)\n\t"
    "v_add_f32 v146, v146, v9\n\t"
    "v_add_f32 v146, v146, v10\n\t"
    "s_add_u32 m0, s95, 137216\n\t"
    "s_add_u32 s76, s74, 49152\n\t"
    "buffer_load_dwordx4 v159, s[60:63], s76 offen lds\n\t"
    "s_add_u32 m0, s95, 138240\n\t"
    "s_add_u32 s76, s74, 57344\n\t"
    "buffer_load_dwordx4 v159, s[60:63], s76 offen lds\n\t"
    "s_add_u32 s77, s72, 0\n\t"
    "buffer_load_dwordx4 v[96:99], v255, s[68:71], s77 offen offset:0 sc1\n\t"
    "v_mfma_f32_16x16x32_bf16 v[0:3], a[144:147], v[160:163], v[80:83]\n\t"
    "v_exp_f32 v12, v12\n\t"
    "v_mfma_f32_16x16x32_bf16 v[0:3], a[148:151], v[164:167], v[0:3]\n\t"
    "ds_read_b64_tr_b16 a[188:189], v139 offset:24576\n\t"
    "v_add_f32 v146, v146, v11\n\t"
    "v_mfma_f32_16x16x32_bf16 v[0:3], a[152:155], v[168:171], v[0:3]\n\t"
    "s_mov_b32 m0, s53\n\t"
    "v_exp_f32 v13, v13\n\t"
    "v_mfma_f32_16x16x32_bf16 v[0:3], a[156:159], v[172:175], v[0:3]\n\t"
    "ds_read_b64_tr_b16 a[190:191], v139 offset:28672\n\t"
    "v_cvt_pk_bf16_f32 v37, v10, v11\n\t"
    "v_mfma_f32_16x16x32_bf16 a[96:99], a[176:179], v[48:51], a[96:99]\n\t"
    "s_add_u32 s48, s48, 16384\n\t"
    "v_exp_f32 v14, v14\n\t"
    "v_mfma_f32_16x16x32_bf16 a[96:99], a[180:183], v[52:55], a[96:99]\n\t"
    "ds_read_b128 a[132:135], v129 offset:49152\n\t"
    "v_cvt_pk_bf16_f32 v44, v12, v13\n\t"
    "v_mfma_f32_16x16x32_bf16 a[100:103], a[176:179], v[56:59], a[100:103]\n\t"
    "v_exp_f32 v15, v15\n\t"
    "v_mfma_f32_16x16x32_bf16 a[100:103], a[180:183], v[60:63], a[100:103]\n\t"
    "s_waitcnt lgkmcnt(3)\n\t"
    "buffer_load_dwordx4 v224, s[40:43], s48 offen offset:0 lds\n\t"
    "v_mfma_f32_16x16x32_bf16 v[4:7], a[144:147], v[176:179], v[84:87]\n\t"
    "v_exp_f32 v0, v0\n\t"
    "v_mfma_f32_16x16x32_bf16 v[4:7], a[148:151], v[180:183], v[4:7]\n\t"
    "ds_read_b64_tr_b16 a[160:161], v132 offset:32768\n\t"
    "v_cvt_pk_bf16_f32 v45, v14, v15\n\t"
    "v_mfma_f32_16x16x32_bf16 v[4:7], a[152:155], v[184:187], v[4:7]\n\t"
    "v_exp_f32 v1, v1\n\t"
    "v_mfma_f32_16x16x32_bf16 v[4:7], a[156:159], v[188:191], v[4:7]\n\t"
    "ds_read_b64_tr_b16 a[162:163], v132 offset:36864\n\t"
    "v_add_f32 v144, v144, v0\n\t"
    "v_mfma_f32_16x16x32_bf16 a[104:107], a[176:179], v[64:67], a[104:107]\n\t"
    "v_exp_f32 v2, v2\n\t"
    "v_mfma_f32_16x16x32_bf16 a[104:107], a[180:183], v[68:71], a[104:107]\n\t"
    "ds_read_b128 a[136:139], v130 offset:49152\n\t"
    "v_cvt_pk_bf16_f32 v22, v0, v1\n\t"
    "v_mfma_f32_16x16x32_bf16 a[108:111], a[176:179], v[72:75], a[108:111]\n\t"
    "v_exp_f32 v3, v3\n\t"
    "v_mfma_f32_16x16x32_bf16 a[108:111], a[180:183], v[76:79], a[108:111]\n\t"
    "s_waitcnt lgkmcnt(3)\n\t"
    "v_add_f32 v144, v144, v1\n\t"
    "v_add_f32 v144, v144, v2\n\t"
    "buffer_load_dwordx4 v[100:103], v255, s[68:71], s77 offen offset:1024 sc1\n\t"
    "s_add_u32 s77, s72, 57344\n\t"
    "buffer_load_dwordx4 v[104:107], v255, s[68:71], s77 offen offset:0 sc1\n\t"
    "buffer_load_dwordx4 v[108:111], v255, s[68:71], s77 offen offset:1024 sc1\n\t"
    "v_mfma_f32_16x16x32_bf16 v[8:11], a[144:147], v[192:195], v[88:91]\n\t"
    "v_exp_f32 v4, v4\n\t"
    "v_mfma_f32_16x16x32_bf16 v[8:11], a[148:151], v[196:199], v[8:11]\n\t"
    "ds_read_b64_tr_b16 a[164:165], v132 offset:40960\n\t"
    "v_add_f32 v144, v144, v3\n\t"
    "v_mfma_f32_16x16x32_bf16 v[8:11], a[152:155], v[200:203], v[8:11]\n\t"
    "s_mov_b32 m0, s53\n\t"
    "v_exp_f32 v5, v5\n\t"
    "v_mfma_f32_16x16x32_bf16 v[8:11], a[156:159], v[204:207], v[8:11]\n\t"
    "ds_read_b64_tr_b16 a[166:167], v132 offset:45056\n\t"
    "v_cvt_pk_bf16_f32 v23, v2, v3\n\t"
    "v_mfma_f32_16x16x32_bf16 a[112:115], a[184:187], v[48:51], a[112:115]\n\t"
    "v_exp_f32 v6, v6\n\t"
    "v_mfma_f32_16x16x32_bf16 a[112:115], a[188:191], v[52:55], a[112:115]\n\t"
    "ds_read_b128 a[140:143], v131 offset:49152\n\t"
    "v_cvt_pk_bf16_f32 v30, v4, v5\n\t"
    "v_mfma_f32_16x16x32_bf16 a[116:119], a[184:187], v[56:59], a[116:119]\n\t"
    "v_exp_f32 v7, v7\n\t"
    "v_mfma_f32_16x16x32_bf16 a[116:119], a[188:191], v[60:63], a[116:119]\n\t"
    "s_waitcnt lgkmcnt(3)\n\t"
    "buffer_load_dwordx4 v225, s[40:43], s48 offen offset:1024 lds\n\t"
    "v_mfma_f32_16x16x32_bf16 v[12:15], a[144:147], v[208:211], v[92:95]\n\t"
    "v_exp_f32 v8, v8\n\t"
    "v_mfma_f32_16x16x32_bf16 v[12:15], a[148:151], v[212:215], v[12:15]\n\t"
    "ds_read_b64_tr_b16 a[168:169], v133 offset:32768\n\t"
    "v_cvt_pk_bf16_f32 v31, v6, v7\n\t"
    "v_mfma_f32_16x16x32_bf16 v[12:15], a[152:155], v[216:219], v[12:15]\n\t"
    "v_exp_f32 v9, v9\n\t"
    "v_mfma_f32_16x16x32_bf16 v[12:15], a[156:159], v[220:223], v[12:15]\n\t"
    "ds_read_b64_tr_b16 a[170:171], v133 offset:36864\n\t"
    "v_add_f32 v146, v146, v8\n\t"
    "v_mfma_f32_16x16x32_bf16 a[120:123], a[184:187], v[64:67], a[120:123]\n\t"
    "v_exp_f32 v10, v10\n\t"
    "v_mfma_f32_16x16x32_bf16 a[120:123], a[188:191], v[68:71], a[120:123]\n\t"
    "ds_read_b128 a[144:147], v128 offset:53248\n\t"
    "v_cvt_pk_bf16_f32 v38, v8, v9\n\t"
    "v_mfma_f32_16x16x32_bf16 a[124:127], a[184:187], v[72:75], a[124:127]\n\t"
    "v_exp_f32 v11, v11\n\t"
    "v_mfma_f32_16x16x32_bf16 a[124:127], a[188:191], v[76:79], a[124:127]\n\t"
    "s_waitcnt lgkmcnt(3)\n\t"
    "v_add_f32 v146, v146, v9\n\t"
    "v_add_f32 v146, v146, v10\n\t"
    "s_add_u32 s77, s72, 114688\n\t"
    "buffer_load_dwordx4 v[112:115], v255, s[68:71], s77 offen offset:0 sc1\n\t"
    "buffer_load_dwordx4 v[116:119], v255, s[68:71], s77 offen offset:1024 sc1\n\t"
    "s_add_u32 s77, s72, 172032\n\t"
    "buffer_load_dwordx4 v[120:123], v255, s[68:71], s77 offen offset:0 sc1\n\t"
    "v_mfma_f32_16x16x32_bf16 v[0:3], a[128:131], v[160:163], v[80:83]\n\t"
    "v_exp_f32 v12, v12\n\t"
    "v_mfma_f32_16x16x32_bf16 v[0:3], a[132:135], v[164:167], v[0:3]\n\t"
    "ds_read_b64_tr_b16 a[172:173], v133 offset:40960\n\t"
    "v_add_f32 v146, v146, v11\n\t"
    "v_mfma_f32_16x16x32_bf16 v[0:3], a[136:139], v[168:171], v[0:3]\n\t"
    "s_mov_b32 m0, s53\n\t"
    "v_exp_f32 v13, v13\n\t"
    "v_mfma_f32_16x16x32_bf16 v[0:3], a[140:143], v[172:175], v[0:3]\n\t"
    "ds_read_b64_tr_b16 a[174:175], v133 offset:45056\n\t"
    "v_cvt_pk_bf16_f32 v39, v10, v11\n\t"
    "v_mfma_f32_16x16x32_bf16 a[0:3], a[160:163], v[16:19], a[0:3]\n\t"
    "v_exp_f32 v14, v14\n\t"
    "v_mfma_f32_16x16x32_bf16 a[0:3], a[164:167], v[20:23], a[0:3]\n\t"
    "ds_read_b128 a[148:151], v129 offset:53248\n\t"
    "v_cvt_pk_bf16_f32 v46, v12, v13\n\t"
    "v_mfma_f32_16x16x32_bf16 a[4:7], a[160:163], v[24:27], a[4:7]\n\t"
    "v_exp_f32 v15, v15\n\t"
    "v_mfma_f32_16x16x32_bf16 a[4:7], a[164:167], v[28:31], a[4:7]\n\t"
    "s_waitcnt lgkmcnt(3)\n\t"
    "buffer_load_dwordx4 v226, s[40:43], s48 offen offset:2048 lds\n\t"
    "v_mfma_f32_16x16x32_bf16 v[4:7], a[128:131], v[176:179], v[84:87]\n\t"
    "v_exp_f32 v0, v0\n\t"
    "v_mfma_f32_16x16x32_bf16 v[4:7], a[132:135], v[180:183], v[4:7]\n\t"
    "ds_read_b64_tr_b16 a[176:177], v134 offset:32768\n\t"
    "v_cvt_pk_bf16_f32 v47, v14, v15\n\t"
    "v_mfma_f32_16x16x32_bf16 v[4:7], a[136:139], v[184:187], v[4:7]\n\t"
    "v_exp_f32 v1, v1\n\t"
    "v_mfma_f32_16x16x32_bf16 v[4:7], a[140:143], v[188:191], v[4:7]\n\t"
    "ds_read_b64_tr_b16 a[178:179], v134 offset:36864\n\t"
    "v_add_f32 v144, v144, v0\n\t"
    "v_mfma_f32_16x16x32_bf16 a[8:11], a[160:163], v[32:35], a[8:11]\n\t"
    "v_exp_f32 v2, v2\n\t"
    "v_mfma_f32_16x16x32_bf16 a[8:11], a[164:167], v[36:39], a[8:11]\n\t"
    "ds_read_b128 a[152:155], v130 offset:53248\n\t"
    "v_cvt_pk_bf16_f32 v48, v0, v1\n\t"
    "v_mfma_f32_16x16x32_bf16 a[12:15], a[160:163], v[40:43], a[12:15]\n\t"
    "v_exp_f32 v3, v3\n\t"
    "v_mfma_f32_16x16x32_bf16 a[12:15], a[164:167], v[44:47], a[12:15]\n\t"
    "s_waitcnt lgkmcnt(3)\n\t"
    "v_add_f32 v144, v144, v1\n\t"
    "v_add_f32 v144, v144, v2\n\t"
    "buffer_load_dwordx4 v[124:127], v255, s[68:71], s77 offen offset:1024 sc1\n\t"
    "v_mfma_f32_16x16x32_bf16 v[148:151], v[140:143], v[24:27], v[148:151]\n\t"
    "v_mfma_f32_16x16x32_bf16 v[148:151], v[140:143], v[28:31], v[148:151]\n\t"
    "v_mfma_f32_16x16x32_bf16 v[8:11], a[128:131], v[192:195], v[88:91]\n\t"
    "v_exp_f32 v4, v4\n\t"
    "v_mfma_f32_16x16x32_bf16 v[8:11], a[132:135], v[196:199], v[8:11]\n\t"
    "ds_read_b64_tr_b16 a[180:181], v134 offset:40960\n\t"
    "v_add_f32 v144, v144, v3\n\t"
    "v_mfma_f32_16x16x32_bf16 v[8:11], a[136:139], v[200:203], v[8:11]\n\t"
    "s_mov_b32 m0, s53\n\t"
    "v_exp_f32 v5, v5\n\t"
    "v_mfma_f32_16x16x32_bf16 v[8:11], a[140:143], v[204:207], v[8:11]\n\t"
    "ds_read_b64_tr_b16 a[182:183], v134 offset:45056\n\t"
    "v_cvt_pk_bf16_f32 v49, v2, v3\n\t"
    "v_mfma_f32_16x16x32_bf16 a[16:19], a[168:171], v[16:19], a[16:19]\n\t"
    "v_exp_f32 v6, v6\n\t"
    "v_mfma_f32_16x16x32_bf16 a[16:19], a[172:175], v[20:23], a[16:19]\n\t"
    "ds_read_b128 a[156:159], v131 offset:53248\n\t"
    "v_cvt_pk_bf16_f32 v56, v4, v5\n\t"
    "v_mfma_f32_16x16x32_bf16 a[20:23], a[168:171], v[24:27], a[20:23]\n\t"
    "v_exp_f32 v7, v7\n\t"
    "v_mfma_f32_16x16x32_bf16 a[20:23], a[172:175], v[28:31], a[20:23]\n\t"
    "s_waitcnt lgkmcnt(3)\n\t"
    "buffer_load_dwordx4 v227, s[40:43], s48 offen offset:3072 lds\n\t"
    "v_mfma_f32_16x16x32_bf16 v[12:15], a[128:131], v[208:211], v[92:95]\n\t"
    "v_exp_f32 v8, v8\n\t"
    "v_mfma_f32_16x16x32_bf16 v[12:15], a[132:135], v[212:215], v[12:15]\n\t"
    "ds_read_b64_tr_b16 a[184:185], v135 offset:32768\n\t"
    "v_cvt_pk_bf16_f32 v57, v6, v7\n\t"
    "v_mfma_f32_16x16x32_bf16 v[12:15], a[136:139], v[216:219], v[12:15]\n\t"
    "v_exp_f32 v9, v9\n\t"
    "v_mfma_f32_16x16x32_bf16 v[12:15], a[140:143], v[220:223], v[12:15]\n\t"
    "ds_read_b64_tr_b16 a[186:187], v135 offset:36864\n\t"
    "v_add_f32 v146, v146, v8\n\t"
    "v_mfma_f32_16x16x32_bf16 a[24:27], a[168:171], v[32:35], a[24:27]\n\t"
    "v_exp_f32 v10, v10\n\t"
    "v_mfma_f32_16x16x32_bf16 a[24:27], a[172:175], v[36:39], a[24:27]\n\t"
    "ds_read_b128 a[128:131], v128 offset:57344\n\t"
    "v_cvt_pk_bf16_f32 v64, v8, v9\n\t"
    "v_mfma_f32_16x16x32_bf16 a[28:31], a[168:171], v[40:43], a[28:31]\n\t"
    "v_exp_f32 v11, v11\n\t"
    "v_mfma_f32_16x16x32_bf16 a[28:31], a[172:175], v[44:47], a[28:31]\n\t"
    "s_waitcnt lgkmcnt(3)\n\t"
    "v_add_f32 v146, v146, v9\n\t"
    "v_add_f32 v146, v146, v10\n\t"
    "v_mfma_f32_16x16x32_bf16 v[152:155], v[140:143], v[40:43], v[152:155]\n\t"
    "v_mfma_f32_16x16x32_bf16 v[152:155], v[140:143], v[44:47], v[152:155]\n\t"
    "v_mfma_f32_16x16x32_bf16 v[0:3], a[144:147], v[160:163], v[80:83]\n\t"
    "v_exp_f32 v12, v12\n\t"
    "v_mfma_f32_16x16x32_bf16 v[0:3], a[148:151], v[164:167], v[0:3]\n\t"
    "ds_read_b64_tr_b16 a[188:189], v135 offset:40960\n\t"
    "v_add_f32 v146, v146, v11\n\t"
    "v_mfma_f32_16x16x32_bf16 v[0:3], a[152:155], v[168:171], v[0:3]\n\t"
    "s_mov_b32 m0, s56\n\t"
    "v_exp_f32 v13, v13\n\t"
    "v_mfma_f32_16x16x32_bf16 v[0:3], a[156:159], v[172:175], v[0:3]\n\t"
    "ds_read_b64_tr_b16 a[190:191], v135 offset:45056\n\t"
    "v_cvt_pk_bf16_f32 v65, v10, v11\n\t"
    "v_mfma_f32_16x16x32_bf16 a[32:35], a[176:179], v[16:19], a[32:35]\n\t"
    "s_add_u32 s51, s51, 16384\n\t"
    "v_exp_f32 v14, v14\n\t"
    "v_mfma_f32_16x16x32_bf16 a[32:35], a[180:183], v[20:23], a[32:35]\n\t"
    "ds_read_b128 a[132:135], v129 offset:57344\n\t"
    "v_cvt_pk_bf16_f32 v72, v12, v13\n\t"
    "v_mfma_f32_16x16x32_bf16 a[36:39], a[176:179], v[24:27], a[36:39]\n\t"
    "v_exp_f32 v15, v15\n\t"
    "v_mfma_f32_16x16x32_bf16 a[36:39], a[180:183], v[28:31], a[36:39]\n\t"
    "s_waitcnt lgkmcnt(3)\n\t"
    "buffer_load_dwordx4 v228, s[44:47], s51 offen offset:0 lds\n\t"
    "v_mfma_f32_16x16x32_bf16 v[4:7], a[144:147], v[176:179], v[84:87]\n\t"
    "v_exp_f32 v0, v0\n\t"
    "v_mfma_f32_16x16x32_bf16 v[4:7], a[148:151], v[180:183], v[4:7]\n\t"
    "ds_read_b64_tr_b16 a[160:161], v136 offset:32768\n\t"
    "v_cvt_pk_bf16_f32 v73, v14, v15\n\t"
    "v_mfma_f32_16x16x32_bf16 v[4:7], a[152:155], v[184:187], v[4:7]\n\t"
    "v_exp_f32 v1, v1\n\t"
    "v_mfma_f32_16x16x32_bf16 v[4:7], a[156:159], v[188:191], v[4:7]\n\t"
    "ds_read_b64_tr_b16 a[162:163], v136 offset:36864\n\t"
    "v_add_f32 v144, v144, v0\n\t"
    "v_mfma_f32_16x16x32_bf16 a[40:43], a[176:179], v[32:35], a[40:43]\n\t"
    "v_exp_f32 v2, v2\n\t"
    "v_mfma_f32_16x16x32_bf16 a[40:43], a[180:183], v[36:39], a[40:43]\n\t"
    "ds_read_b128 a[136:139], v130 offset:57344\n\t"
    "v_cvt_pk_bf16_f32 v50, v0, v1\n\t"
    "v_mfma_f32_16x16x32_bf16 a[44:47], a[176:179], v[40:43], a[44:47]\n\t"
    "v_exp_f32 v3, v3\n\t"
    "v_mfma_f32_16x16x32_bf16 a[44:47], a[180:183], v[44:47], a[44:47]\n\t"
    "s_waitcnt lgkmcnt(3)\n\t"
    "v_add_f32 v144, v144, v1\n\t"
    "v_add_f32 v144, v144, v2\n\t"
    "s_waitcnt vmcnt(13)\n\t"
    "ds_read_b64_tr_b16 v[232:233], v230 offset:0\n\t"
    "ds_read_b64_tr_b16 v[234:235], v230 offset:2048\n\t"
    "ds_read_b64_tr_b16 v[236:237], v230 offset:4096\n\t"
    "ds_read_b64_tr_b16 v[238:239], v230 offset:6144\n\t"
    "v_mfma_f32_16x16x32_bf16 v[8:11], a[144:147], v[192:195], v[88:91]\n\t"
    "v_exp_f32 v4, v4\n\t"
    "v_mfma_f32_16x16x32_bf16 v[8:11], a[148:151], v[196:199], v[8:11]\n\t"
    "ds_read_b64_tr_b16 a[164:165], v136 offset:40960\n\t"
    "v_add_f32 v144, v144, v3\n\t"
    "v_mfma_f32_16x16x32_bf16 v[8:11], a[152:155], v[200:203], v[8:11]\n\t"
    "s_mov_b32 m0, s56\n\t"
    "v_exp_f32 v5, v5\n\t"
    "v_mfma_f32_16x16x32_bf16 v[8:11], a[156:159], v[204:207], v[8:11]\n\t"
    "ds_read_b64_tr_b16 a[166:167], v136 offset:45056\n\t"
    "v_cvt_pk_bf16_f32 v51, v2, v3\n\t"
    "v_mfma_f32_16x16x32_bf16 a[48:51], a[184:187], v[16:19], a[48:51]\n\t"
    "v_exp_f32 v6, v6\n\t"
    "v_mfma_f32_16x16x32_bf16 a[48:51], a[188:191], v[20:23], a[48:51]\n\t"
    "ds_read_b128 a[140:143], v131 offset:57344\n\t"
    "v_cvt_pk_bf16_f32 v58, v4, v5\n\t"
    "v_mfma_f32_16x16x32_bf16 a[52:55], a[184:187], v[24:27], a[52:55]\n\t"
    "v_exp_f32 v7, v7\n\t"
    "v_mfma_f32_16x16x32_bf16 a[52:55], a[188:191], v[28:31], a[52:55]\n\t"
    "s_waitcnt lgkmcnt(3)\n\t"
    "buffer_load_dwordx4 v229, s[44:47], s51 offen offset:1024 lds\n\t"
    "v_mfma_f32_16x16x32_bf16 v[12:15], a[144:147], v[208:211], v[92:95]\n\t"
    "v_exp_f32 v8, v8\n\t"
    "v_mfma_f32_16x16x32_bf16 v[12:15], a[148:151], v[212:215], v[12:15]\n\t"
    "ds_read_b64_tr_b16 a[168:169], v137 offset:32768\n\t"
    "v_cvt_pk_bf16_f32 v59, v6, v7\n\t"
    "v_mfma_f32_16x16x32_bf16 v[12:15], a[152:155], v[216:219], v[12:15]\n\t"
    "v_exp_f32 v9, v9\n\t"
    "v_mfma_f32_16x16x32_bf16 v[12:15], a[156:159], v[220:223], v[12:15]\n\t"
    "ds_read_b64_tr_b16 a[170:171], v137 offset:36864\n\t"
    "v_add_f32 v146, v146, v8\n\t"
    "v_mfma_f32_16x16x32_bf16 a[56:59], a[184:187], v[32:35], a[56:59]\n\t"
    "v_exp_f32 v10, v10\n\t"
    "v_mfma_f32_16x16x32_bf16 a[56:59], a[188:191], v[36:39], a[56:59]\n\t"
    "ds_read_b128 a[144:147], v128 offset:61440\n\t"
    "v_cvt_pk_bf16_f32 v66, v8, v9\n\t"
    "v_mfma_f32_16x16x32_bf16 a[60:63], a[184:187], v[40:43], a[60:63]\n\t"
    "v_exp_f32 v11, v11\n\t"
    "v_mfma_f32_16x16x32_bf16 a[60:63], a[188:191], v[44:47], a[60:63]\n\t"
    "s_waitcnt lgkmcnt(3)\n\t"
    "v_add_f32 v146, v146, v9\n\t"
    "v_add_f32 v146, v146, v10\n\t"
    "s_waitcnt vmcnt(3)\n\t"
    "v_mfma_f32_16x16x32_bf16 a[192:195], v[232:235], v[96:99], 0\n\t"
    "ds_read_b64_tr_b16 v[240:241], v231 offset:0\n\t"
    "v_mfma_f32_16x16x32_bf16 a[192:195], v[236:239], v[100:103], a[192:195]\n\t"
    "ds_read_b64_tr_b16 v[242:243], v231 offset:2048\n\t"
    "v_mfma_f32_16x16x32_bf16 a[200:203], v[232:235], v[104:107], 0\n\t"
    "ds_read_b64_tr_b16 v[244:245], v231 offset:4096\n\t"
    "v_mfma_f32_16x16x32_bf16 a[200:203], v[236:239], v[108:111], a[200:203]\n\t"
    "ds_read_b64_tr_b16 v[246:247], v231 offset:6144\n\t"
    "v_mfma_f32_16x16x32_bf16 a[208:211], v[232:235], v[112:115], 0\n\t"
    "v_mfma_f32_16x16x32_bf16 a[208:211], v[236:239], v[116:119], a[208:211]\n\t"
    "v_mfma_f32_16x16x32_bf16 a[216:219], v[232:235], v[120:123], 0\n\t"
    "v_mfma_f32_16x16x32_bf16 a[216:219], v[236:239], v[124:127], a[216:219]\n\t"
    "s_waitcnt lgkmcnt(0)\n\t"
    "v_mfma_f32_16x16x32_bf16 a[196:199], v[240:243], v[96:99], 0\n\t"
    "ds_read_b64_tr_b16 v[232:233], v248 offset:0\n\t"
    "v_mfma_f32_16x16x32_bf16 a[196:199], v[244:247], v[100:103], a[196:199]\n\t"
    "ds_read_b64_tr_b16 v[234:235], v248 offset:2048\n\t"
    "v_mfma_f32_16x16x32_bf16 a[204:207], v[240:243], v[104:107], 0\n\t"
    "ds_read_b64_tr_b16 v[236:237], v248 offset:4096\n\t"
    "v_mfma_f32_16x16x32_bf16 a[204:207], v[244:247], v[108:111], a[204:207]\n\t"
    "ds_read_b64_tr_b16 v[238:239], v248 offset:6144\n\t"
    "v_mfma_f32_16x16x32_bf16 a[212:215], v[240:243], v[112:115], 0\n\t"
    "v_mfma_f32_16x16x32_bf16 a[212:215], v[244:247], v[116:119], a[212:215]\n\t"
    "v_mfma_f32_16x16x32_bf16 a[220:223], v[240:243], v[120:123], 0\n\t"
    "v_mfma_f32_16x16x32_bf16 a[220:223], v[244:247], v[124:127], a[220:223]\n\t"
    "s_waitcnt lgkmcnt(0)\n\t"
    "v_mfma_f32_16x16x32_bf16 a[224:227], v[232:235], v[96:99], 0\n\t"
    "ds_read_b64_tr_b16 v[240:241], v249 offset:0\n\t"
    "v_mfma_f32_16x16x32_bf16 a[224:227], v[236:239], v[100:103], a[224:227]\n\t"
    "ds_read_b64_tr_b16 v[242:243], v249 offset:2048\n\t"
    "v_mfma_f32_16x16x32_bf16 a[232:235], v[232:235], v[104:107], 0\n\t"
    "ds_read_b64_tr_b16 v[244:245], v249 offset:4096\n\t"
    "v_mfma_f32_16x16x32_bf16 a[232:235], v[236:239], v[108:111], a[232:235]\n\t"
    "ds_read_b64_tr_b16 v[246:247], v249 offset:6144\n\t"
    "v_mfma_f32_16x16x32_bf16 a[240:243], v[232:235], v[112:115], 0\n\t"
    "v_mfma_f32_16x16x32_bf16 a[240:243], v[236:239], v[116:119], a[240:243]\n\t"
    "v_mfma_f32_16x16x32_bf16 a[248:251], v[232:235], v[120:123], 0\n\t"
    "v_mfma_f32_16x16x32_bf16 a[248:251], v[236:239], v[124:127], a[248:251]\n\t"
    "s_waitcnt lgkmcnt(0)\n\t"
    "v_mfma_f32_16x16x32_bf16 a[228:231], v[240:243], v[96:99], 0\n\t"
    "s_add_u32 m0, s95, 131072\n\t"
    "s_add_u32 s76, s74, 65536\n\t"
    "buffer_load_dwordx4 v159, s[60:63], s76 offen lds\n\t"
    "v_mfma_f32_16x16x32_bf16 a[228:231], v[244:247], v[100:103], a[228:231]\n\t"
    "s_add_u32 m0, s95, 132096\n\t"
    "s_add_u32 s76, s74, 73728\n\t"
    "buffer_load_dwordx4 v159, s[60:63], s76 offen lds\n\t"
    "v_mfma_f32_16x16x32_bf16 a[236:239], v[240:243], v[104:107], 0\n\t"
    "s_add_u32 m0, s95, 133120\n\t"
    "s_add_u32 s76, s74, 81920\n\t"
    "buffer_load_dwordx4 v159, s[60:63], s76 offen lds\n\t"
    "v_mfma_f32_16x16x32_bf16 a[236:239], v[244:247], v[108:111], a[236:239]\n\t"
    "s_add_u32 m0, s95, 134144\n\t"
    "s_add_u32 s76, s74, 90112\n\t"
    "buffer_load_dwordx4 v159, s[60:63], s76 offen lds\n\t"
    "v_mfma_f32_16x16x32_bf16 a[244:247], v[240:243], v[112:115], 0\n\t"
    "s_add_u32 m0, s95, 135168\n\t"
    "s_add_u32 s76, s74, 98304\n\t"
    "buffer_load_dwordx4 v159, s[60:63], s76 offen lds\n\t"
    "v_mfma_f32_16x16x32_bf16 a[244:247], v[244:247], v[116:119], a[244:247]\n\t"
    "s_add_u32 m0, s95, 136192\n\t"
    "s_add_u32 s76, s74, 106496\n\t"
    "buffer_load_dwordx4 v159, s[60:63], s76 offen lds\n\t"
    "v_mfma_f32_16x16x32_bf16 a[252:255], v[240:243], v[120:123], 0\n\t"
    "s_add_u32 m0, s95, 137216\n\t"
    "s_add_u32 s76, s74, 114688\n\t"
    "buffer_load_dwordx4 v159, s[60:63], s76 offen lds\n\t"
    "v_mfma_f32_16x16x32_bf16 a[252:255], v[244:247], v[124:127], a[252:255]\n\t"
    "s_add_u32 m0, s95, 138240\n\t"
    "s_add_u32 s76, s74, 122880\n\t"
    "buffer_load_dwordx4 v159, s[60:63], s76 offen lds\n\t"
    "v_mfma_f32_16x16x32_bf16 v[0:3], a[128:131], v[160:163], v[80:83]\n\t"
    "v_exp_f32 v12, v12\n\t"
    "v_mfma_f32_16x16x32_bf16 v[0:3], a[132:135], v[164:167], v[0:3]\n\t"
    "ds_read_b64_tr_b16 a[172:173], v137 offset:40960\n\t"
    "v_add_f32 v146, v146, v11\n\t"
    "v_mfma_f32_16x16x32_bf16 v[0:3], a[136:139], v[168:171], v[0:3]\n\t"
    "s_mov_b32 m0, s56\n\t"
    "v_exp_f32 v13, v13\n\t"
    "v_mfma_f32_16x16x32_bf16 v[0:3], a[140:143], v[172:175], v[0:3]\n\t"
    "ds_read_b64_tr_b16 a[174:175], v137 offset:45056\n\t"
    "v_cvt_pk_bf16_f32 v67, v10, v11\n\t"
    "v_mfma_f32_16x16x32_bf16 a[64:67], a[160:163], v[16:19], a[64:67]\n\t"
    "v_exp_f32 v14, v14\n\t"
    "v_mfma_f32_16x16x32_bf16 a[64:67], a[164:167], v[20:23], a[64:67]\n\t"
    "ds_read_b128 a[148:151], v129 offset:61440\n\t"
    "v_cvt_pk_bf16_f32 v74, v12, v13\n\t"
    "v_mfma_f32_16x16x32_bf16 a[68:71], a[160:163], v[24:27], a[68:71]\n\t"
    "v_exp_f32 v15, v15\n\t"
    "v_mfma_f32_16x16x32_bf16 a[68:71], a[164:167], v[28:31], a[68:71]\n\t"
    "s_waitcnt lgkmcnt(3)\n\t"
    "buffer_load_dwordx4 v228, s[44:47], s51 offen offset:2048 lds\n\t"
    "v_mfma_f32_16x16x32_bf16 v[4:7], a[128:131], v[176:179], v[84:87]\n\t"
    "v_exp_f32 v0, v0\n\t"
    "v_mfma_f32_16x16x32_bf16 v[4:7], a[132:135], v[180:183], v[4:7]\n\t"
    "ds_read_b64_tr_b16 a[176:177], v138 offset:32768\n\t"
    "v_cvt_pk_bf16_f32 v75, v14, v15\n\t"
    "v_mfma_f32_16x16x32_bf16 v[4:7], a[136:139], v[184:187], v[4:7]\n\t"
    "v_exp_f32 v1, v1\n\t"
    "v_mfma_f32_16x16x32_bf16 v[4:7], a[140:143], v[188:191], v[4:7]\n\t"
    "ds_read_b64_tr_b16 a[178:179], v138 offset:36864\n\t"
    "v_add_f32 v144, v144, v0\n\t"
    "v_mfma_f32_16x16x32_bf16 a[72:75], a[160:163], v[32:35], a[72:75]\n\t"
    "v_exp_f32 v2, v2\n\t"
    "v_mfma_f32_16x16x32_bf16 a[72:75], a[164:167], v[36:39], a[72:75]\n\t"
    "ds_read_b128 a[152:155], v130 offset:61440\n\t"
    "v_cvt_pk_bf16_f32 v52, v0, v1\n\t"
    "v_mfma_f32_16x16x32_bf16 a[76:79], a[160:163], v[40:43], a[76:79]\n\t"
    "v_exp_f32 v3, v3\n\t"
    "v_mfma_f32_16x16x32_bf16 a[76:79], a[164:167], v[44:47], a[76:79]\n\t"
    "s_waitcnt lgkmcnt(3)\n\t"
    "v_add_f32 v144, v144, v1\n\t"
    "v_add_f32 v144, v144, v2\n\t"
    "s_add_u32 s77, s72, 2048\n\t"
    "buffer_load_dwordx4 v[96:99], v255, s[68:71], s77 offen offset:0 sc1\n\t"
    "buffer_load_dwordx4 v[100:103], v255, s[68:71], s77 offen offset:1024 sc1\n\t"
    "s_add_u32 s77, s72, 59392\n\t"
    "buffer_load_dwordx4 v[104:107], v255, s[68:71], s77 offen offset:0 sc1\n\t"
    "v_mfma_f32_16x16x32_bf16 v[8:11], a[128:131], v[192:195], v[88:91]\n\t"
    "v_exp_f32 v4, v4\n\t"
    "v_mfma_f32_16x16x32_bf16 v[8:11], a[132:135], v[196:199], v[8:11]\n\t"
    "ds_read_b64_tr_b16 a[180:181], v138 offset:40960\n\t"
    "v_add_f32 v144, v144, v3\n\t"
    "v_mfma_f32_16x16x32_bf16 v[8:11], a[136:139], v[200:203], v[8:11]\n\t"
    "s_mov_b32 m0, s56\n\t"
    "v_exp_f32 v5, v5\n\t"
    "v_mfma_f32_16x16x32_bf16 v[8:11], a[140:143], v[204:207], v[8:11]\n\t"
    "ds_read_b64_tr_b16 a[182:183], v138 offset:45056\n\t"
    "v_cvt_pk_bf16_f32 v53, v2, v3\n\t"
    "v_mfma_f32_16x16x32_bf16 a[80:83], a[168:171], v[16:19], a[80:83]\n\t"
    "v_exp_f32 v6, v6\n\t"
    "v_mfma_f32_16x16x32_bf16 a[80:83], a[172:175], v[20:23], a[80:83]\n\t"
    "ds_read_b128 a[156:159], v131 offset:61440\n\t"
    "v_cvt_pk_bf16_f32 v60, v4, v5\n\t"
    "v_mfma_f32_16x16x32_bf16 a[84:87], a[168:171], v[24:27], a[84:87]\n\t"
    "v_exp_f32 v7, v7\n\t"
    "v_mfma_f32_16x16x32_bf16 a[84:87], a[172:175], v[28:31], a[84:87]\n\t"
    "s_waitcnt lgkmcnt(3)\n\t"
    "buffer_load_dwordx4 v229, s[44:47], s51 offen offset:3072 lds\n\t"
    "v_mfma_f32_16x16x32_bf16 v[12:15], a[128:131], v[208:211], v[92:95]\n\t"
    "s_waitcnt vmcnt(29)\n\t"
    "v_exp_f32 v8, v8\n\t"
    "v_mfma_f32_16x16x32_bf16 v[12:15], a[132:135], v[212:215], v[12:15]\n\t"
    "ds_read_b64_tr_b16 a[184:185], v139 offset:32768\n\t"
    "v_cvt_pk_bf16_f32 v61, v6, v7\n\t"
    "v_mfma_f32_16x16x32_bf16 v[12:15], a[136:139], v[216:219], v[12:15]\n\t"
    "s_barrier\n\t"
    "v_exp_f32 v9, v9\n\t"
    "v_mfma_f32_16x16x32_bf16 v[12:15], a[140:143], v[220:223], v[12:15]\n\t"
    "ds_read_b64_tr_b16 a[186:187], v139 offset:36864\n\t"
    "v_add_f32 v146, v146, v8\n\t"
    "v_mfma_f32_16x16x32_bf16 a[88:91], a[168:171], v[32:35], a[88:91]\n\t"
    "v_exp_f32 v10, v10\n\t"
    "v_mfma_f32_16x16x32_bf16 a[88:91], a[172:175], v[36:39], a[88:91]\n\t"
    "ds_read_b128 a[128:131], v128 offset:0\n\t"
    "v_cvt_pk_bf16_f32 v68, v8, v9\n\t"
    "v_mfma_f32_16x16x32_bf16 a[92:95], a[168:171], v[40:43], a[92:95]\n\t"
    "v_exp_f32 v11, v11\n\t"
    "v_mfma_f32_16x16x32_bf16 a[92:95], a[172:175], v[44:47], a[92:95]\n\t"
    "s_waitcnt lgkmcnt(3)\n\t"
    "v_add_f32 v146, v146, v9\n\t"
    "v_add_f32 v146, v146, v10\n\t"
    "buffer_load_dwordx4 v[108:111], v255, s[68:71], s77 offen offset:1024 sc1\n\t"
    "s_add_u32 s77, s72, 116736\n\t"
    "buffer_load_dwordx4 v[112:115], v255, s[68:71], s77 offen offset:0 sc1\n\t"
    "buffer_load_dwordx4 v[116:119], v255, s[68:71], s77 offen offset:1024 sc1\n\t"
    "v_mfma_f32_16x16x32_bf16 v[0:3], a[144:147], v[160:163], v[80:83]\n\t"
    "v_exp_f32 v12, v12\n\t"
    "v_mfma_f32_16x16x32_bf16 v[0:3], a[148:151], v[164:167], v[0:3]\n\t"
    "ds_read_b64_tr_b16 a[188:189], v139 offset:40960\n\t"
    "v_add_f32 v146, v146, v11\n\t"
    "v_mfma_f32_16x16x32_bf16 v[0:3], a[152:155], v[168:171], v[0:3]\n\t"
    "s_mov_b32 m0, s54\n\t"
    "v_exp_f32 v13, v13\n\t"
    "v_mfma_f32_16x16x32_bf16 v[0:3], a[156:159], v[172:175], v[0:3]\n\t"
    "ds_read_b64_tr_b16 a[190:191], v139 offset:45056\n\t"
    "v_cvt_pk_bf16_f32 v69, v10, v11\n\t"
    "v_mfma_f32_16x16x32_bf16 a[96:99], a[176:179], v[16:19], a[96:99]\n\t"
    "s_add_u32 s48, s48, 16384\n\t"
    "v_exp_f32 v14, v14\n\t"
    "v_mfma_f32_16x16x32_bf16 a[96:99], a[180:183], v[20:23], a[96:99]\n\t"
    "ds_read_b128 a[132:135], v129 offset:0\n\t"
    "v_cvt_pk_bf16_f32 v76, v12, v13\n\t"
    "v_mfma_f32_16x16x32_bf16 a[100:103], a[176:179], v[24:27], a[100:103]\n\t"
    "v_exp_f32 v15, v15\n\t"
    "v_mfma_f32_16x16x32_bf16 a[100:103], a[180:183], v[28:31], a[100:103]\n\t"
    "s_waitcnt lgkmcnt(3)\n\t"
    "buffer_load_dwordx4 v224, s[40:43], s48 offen offset:0 lds\n\t"
    "v_mfma_f32_16x16x32_bf16 v[4:7], a[144:147], v[176:179], v[84:87]\n\t"
    "v_exp_f32 v0, v0\n\t"
    "v_mfma_f32_16x16x32_bf16 v[4:7], a[148:151], v[180:183], v[4:7]\n\t"
    "ds_read_b64_tr_b16 a[160:161], v132 offset:49152\n\t"
    "v_cvt_pk_bf16_f32 v77, v14, v15\n\t"
    "v_mfma_f32_16x16x32_bf16 v[4:7], a[152:155], v[184:187], v[4:7]\n\t"
    "v_exp_f32 v1, v1\n\t"
    "v_mfma_f32_16x16x32_bf16 v[4:7], a[156:159], v[188:191], v[4:7]\n\t"
    "ds_read_b64_tr_b16 a[162:163], v132 offset:53248\n\t"
    "v_add_f32 v144, v144, v0\n\t"
    "v_mfma_f32_16x16x32_bf16 a[104:107], a[176:179], v[32:35], a[104:107]\n\t"
    "v_exp_f32 v2, v2\n\t"
    "v_mfma_f32_16x16x32_bf16 a[104:107], a[180:183], v[36:39], a[104:107]\n\t"
    "ds_read_b128 a[136:139], v130 offset:0\n\t"
    "v_cvt_pk_bf16_f32 v54, v0, v1\n\t"
    "v_mfma_f32_16x16x32_bf16 a[108:111], a[176:179], v[40:43], a[108:111]\n\t"
    "v_exp_f32 v3, v3\n\t"
    "v_mfma_f32_16x16x32_bf16 a[108:111], a[180:183], v[44:47], a[108:111]\n\t"
    "s_waitcnt lgkmcnt(3)\n\t"
    "v_add_f32 v144, v144, v1\n\t"
    "v_add_f32 v144, v144, v2\n\t"
    "s_add_u32 s77, s72, 174080\n\t"
    "buffer_load_dwordx4 v[120:123], v255, s[68:71], s77 offen offset:0 sc1\n\t"
    "buffer_load_dwordx4 v[124:127], v255, s[68:71], s77 offen offset:1024 sc1\n\t"
    "v_mfma_f32_16x16x32_bf16 v[8:11], a[144:147], v[192:195], v[88:91]\n\t"
    "v_exp_f32 v4, v4\n\t"
    "v_mfma_f32_16x16x32_bf16 v[8:11], a[148:151], v[196:199], v[8:11]\n\t"
    "ds_read_b64_tr_b16 a[164:165], v132 offset:57344\n\t"
    "v_add_f32 v144, v144, v3\n\t"
    "v_mfma_f32_16x16x32_bf16 v[8:11], a[152:155], v[200:203], v[8:11]\n\t"
    "s_mov_b32 m0, s54\n\t"
    "v_exp_f32 v5, v5\n\t"
    "v_mfma_f32_16x16x32_bf16 v[8:11], a[156:159], v[204:207], v[8:11]\n\t"
    "ds_read_b64_tr_b16 a[166:167], v132 offset:61440\n\t"
    "v_cvt_pk_bf16_f32 v55, v2, v3\n\t"
    "v_mfma_f32_16x16x32_bf16 a[112:115], a[184:187], v[16:19], a[112:115]\n\t"
    "v_exp_f32 v6, v6\n\t"
    "v_mfma_f32_16x16x32_bf16 a[112:115], a[188:191], v[20:23], a[112:115]\n\t"
    "ds_read_b128 a[140:143], v131 offset:0\n\t"
    "v_cvt_pk_bf16_f32 v62, v4, v5\n\t"
    "v_mfma_f32_16x16x32_bf16 a[116:119], a[184:187], v[24:27], a[116:119]\n\t"
    "v_exp_f32 v7, v7\n\t"
    "v_mfma_f32_16x16x32_bf16 a[116:119], a[188:191], v[28:31], a[116:119]\n\t"
    "s_waitcnt lgkmcnt(3)\n\t"
    "buffer_load_dwordx4 v225, s[40:43], s48 offen offset:1024 lds\n\t"
    "v_mfma_f32_16x16x32_bf16 v[12:15], a[144:147], v[208:211], v[92:95]\n\t"
    "v_exp_f32 v8, v8\n\t"
    "v_mfma_f32_16x16x32_bf16 v[12:15], a[148:151], v[212:215], v[12:15]\n\t"
    "ds_read_b64_tr_b16 a[168:169], v133 offset:49152\n\t"
    "v_cvt_pk_bf16_f32 v63, v6, v7\n\t"
    "v_mfma_f32_16x16x32_bf16 v[12:15], a[152:155], v[216:219], v[12:15]\n\t"
    "v_exp_f32 v9, v9\n\t"
    "v_mfma_f32_16x16x32_bf16 v[12:15], a[156:159], v[220:223], v[12:15]\n\t"
    "ds_read_b64_tr_b16 a[170:171], v133 offset:53248\n\t"
    "v_add_f32 v146, v146, v8\n\t"
    "v_mfma_f32_16x16x32_bf16 a[120:123], a[184:187], v[32:35], a[120:123]\n\t"
    "v_exp_f32 v10, v10\n\t"
    "v_mfma_f32_16x16x32_bf16 a[120:123], a[188:191], v[36:39], a[120:123]\n\t"
    "ds_read_b128 a[144:147], v128 offset:4096\n\t"
    "v_cvt_pk_bf16_f32 v70, v8, v9\n\t"
    "v_mfma_f32_16x16x32_bf16 a[124:127], a[184:187], v[40:43], a[124:127]\n\t"
    "v_exp_f32 v11, v11\n\t"
    "v_mfma_f32_16x16x32_bf16 a[124:127], a[188:191], v[44:47], a[124:127]\n\t"
    "s_waitcnt lgkmcnt(3)\n\t"
    "v_add_f32 v146, v146, v9\n\t"
    "v_add_f32 v146, v146, v10\n\t"
    "v_mfma_f32_16x16x32_bf16 v[0:3], a[128:131], v[160:163], v[80:83]\n\t"
    "v_exp_f32 v12, v12\n\t"
    "v_mfma_f32_16x16x32_bf16 v[0:3], a[132:135], v[164:167], v[0:3]\n\t"
    "ds_read_b64_tr_b16 a[172:173], v133 offset:57344\n\t"
    "v_add_f32 v146, v146, v11\n\t"
    "v_mfma_f32_16x16x32_bf16 v[0:3], a[136:139], v[168:171], v[0:3]\n\t"
    "s_mov_b32 m0, s54\n\t"
    "v_exp_f32 v13, v13\n\t"
    "v_mfma_f32_16x16x32_bf16 v[0:3], a[140:143], v[172:175], v[0:3]\n\t"
    "ds_read_b64_tr_b16 a[174:175], v133 offset:61440\n\t"
    "v_cvt_pk_bf16_f32 v71, v10, v11\n\t"
    "v_mfma_f32_16x16x32_bf16 a[0:3], a[160:163], v[48:51], a[0:3]\n\t"
    "v_exp_f32 v14, v14\n\t"
    "v_mfma_f32_16x16x32_bf16 a[0:3], a[164:167], v[52:55], a[0:3]\n\t"
    "ds_read_b128 a[148:151], v129 offset:4096\n\t"
    "v_cvt_pk_bf16_f32 v78, v12, v13\n\t"
    "v_mfma_f32_16x16x32_bf16 a[4:7], a[160:163], v[56:59], a[4:7]\n\t"
    "v_exp_f32 v15, v15\n\t"
    "v_mfma_f32_16x16x32_bf16 a[4:7], a[164:167], v[60:63], a[4:7]\n\t"
    "s_waitcnt lgkmcnt(3)\n\t"
    "buffer_load_dwordx4 v226, s[40:43], s48 offen offset:2048 lds\n\t"
    "v_mfma_f32_16x16x32_bf16 v[4:7], a[128:131], v[176:179], v[84:87]\n\t"
    "v_exp_f32 v0, v0\n\t"
    "v_mfma_f32_16x16x32_bf16 v[4:7], a[132:135], v[180:183], v[4:7]\n\t"
    "ds_read_b64_tr_b16 a[176:177], v134 offset:49152\n\t"
    "v_cvt_pk_bf16_f32 v79, v14, v15\n\t"
    "v_mfma_f32_16x16x32_bf16 v[4:7], a[136:139], v[184:187], v[4:7]\n\t"
    "v_exp_f32 v1, v1\n\t"
    "v_mfma_f32_16x16x32_bf16 v[4:7], a[140:143], v[188:191], v[4:7]\n\t"
    "ds_read_b64_tr_b16 a[178:179], v134 offset:53248\n\t"
    "v_add_f32 v144, v144, v0\n\t"
    "v_mfma_f32_16x16x32_bf16 a[8:11], a[160:163], v[64:67], a[8:11]\n\t"
    "v_exp_f32 v2, v2\n\t"
    "v_mfma_f32_16x16x32_bf16 a[8:11], a[164:167], v[68:71], a[8:11]\n\t"
    "ds_read_b128 a[152:155], v130 offset:4096\n\t"
    "v_cvt_pk_bf16_f32 v16, v0, v1\n\t"
    "v_mfma_f32_16x16x32_bf16 a[12:15], a[160:163], v[72:75], a[12:15]\n\t"
    "v_exp_f32 v3, v3\n\t"
    "v_mfma_f32_16x16x32_bf16 a[12:15], a[164:167], v[76:79], a[12:15]\n\t"
    "s_waitcnt lgkmcnt(3)\n\t"
    "v_add_f32 v144, v144, v1\n\t"
    "v_add_f32 v144, v144, v2\n\t"
    "v_mfma_f32_16x16x32_bf16 v[148:151], v[140:143], v[56:59], v[148:151]\n\t"
    "v_mfma_f32_16x16x32_bf16 v[148:151], v[140:143], v[60:63], v[148:151]\n\t"
    "v_mfma_f32_16x16x32_bf16 v[8:11], a[128:131], v[192:195], v[88:91]\n\t"
    "v_exp_f32 v4, v4\n\t"
    "v_mfma_f32_16x16x32_bf16 v[8:11], a[132:135], v[196:199], v[8:11]\n\t"
    "ds_read_b64_tr_b16 a[180:181], v134 offset:57344\n\t"
    "v_add_f32 v144, v144, v3\n\t"
    "v_mfma_f32_16x16x32_bf16 v[8:11], a[136:139], v[200:203], v[8:11]\n\t"
    "s_mov_b32 m0, s54\n\t"
    "v_exp_f32 v5, v5\n\t"
    "v_mfma_f32_16x16x32_bf16 v[8:11], a[140:143], v[204:207], v[8:11]\n\t"
    "ds_read_b64_tr_b16 a[182:183], v134 offset:61440\n\t"
    "v_cvt_pk_bf16_f32 v17, v2, v3\n\t"
    "v_mfma_f32_16x16x32_bf16 a[16:19], a[168:171], v[48:51], a[16:19]\n\t"
    "v_exp_f32 v6, v6\n\t"
    "v_mfma_f32_16x16x32_bf16 a[16:19], a[172:175], v[52:55], a[16:19]\n\t"
    "ds_read_b128 a[156:159], v131 offset:4096\n\t"
    "v_cvt_pk_bf16_f32 v24, v4, v5\n\t"
    "v_mfma_f32_16x16x32_bf16 a[20:23], a[168:171], v[56:59], a[20:23]\n\t"
    "v_exp_f32 v7, v7\n\t"
    "v_mfma_f32_16x16x32_bf16 a[20:23], a[172:175], v[60:63], a[20:23]\n\t"
    "s_waitcnt lgkmcnt(3)\n\t"
    "buffer_load_dwordx4 v227, s[40:43], s48 offen offset:3072 lds\n\t"
    "v_mfma_f32_16x16x32_bf16 v[12:15], a[128:131], v[208:211], v[92:95]\n\t"
    "v_exp_f32 v8, v8\n\t"
    "v_mfma_f32_16x16x32_bf16 v[12:15], a[132:135], v[212:215], v[12:15]\n\t"
    "ds_read_b64_tr_b16 a[184:185], v135 offset:49152\n\t"
    "v_cvt_pk_bf16_f32 v25, v6, v7\n\t"
    "v_mfma_f32_16x16x32_bf16 v[12:15], a[136:139], v[216:219], v[12:15]\n\t"
    "v_exp_f32 v9, v9\n\t"
    "v_mfma_f32_16x16x32_bf16 v[12:15], a[140:143], v[220:223], v[12:15]\n\t"
    "ds_read_b64_tr_b16 a[186:187], v135 offset:53248\n\t"
    "v_add_f32 v146, v146, v8\n\t"
    "v_mfma_f32_16x16x32_bf16 a[24:27], a[168:171], v[64:67], a[24:27]\n\t"
    "v_exp_f32 v10, v10\n\t"
    "v_mfma_f32_16x16x32_bf16 a[24:27], a[172:175], v[68:71], a[24:27]\n\t"
    "ds_read_b128 a[128:131], v128 offset:8192\n\t"
    "v_cvt_pk_bf16_f32 v32, v8, v9\n\t"
    "v_mfma_f32_16x16x32_bf16 a[28:31], a[168:171], v[72:75], a[28:31]\n\t"
    "v_exp_f32 v11, v11\n\t"
    "v_mfma_f32_16x16x32_bf16 a[28:31], a[172:175], v[76:79], a[28:31]\n\t"
    "s_waitcnt lgkmcnt(3)\n\t"
    "v_add_f32 v146, v146, v9\n\t"
    "v_add_f32 v146, v146, v10\n\t"
    "v_mfma_f32_16x16x32_bf16 v[152:155], v[140:143], v[72:75], v[152:155]\n\t"
    "v_mfma_f32_16x16x32_bf16 v[152:155], v[140:143], v[76:79], v[152:155]\n\t"
    "s_waitcnt vmcnt(14)\n\t"
    "ds_read_b64_tr_b16 v[232:233], v230 offset:0\n\t"
    "ds_read_b64_tr_b16 v[234:235], v230 offset:2048\n\t"
    "ds_read_b64_tr_b16 v[236:237], v230 offset:4096\n\t"
    "ds_read_b64_tr_b16 v[238:239], v230 offset:6144\n\t"
    "v_mfma_f32_16x16x32_bf16 v[0:3], a[144:147], v[160:163], v[80:83]\n\t"
    "v_exp_f32 v12, v12\n\t"
    "v_mfma_f32_16x16x32_bf16 v[0:3], a[148:151], v[164:167], v[0:3]\n\t"
    "ds_read_b64_tr_b16 a[188:189], v135 offset:57344\n\t"
    "v_add_f32 v146, v146, v11\n\t"
    "v_mfma_f32_16x16x32_bf16 v[0:3], a[152:155], v[168:171], v[0:3]\n\t"
    "s_mov_b32 m0, s57\n\t"
    "v_exp_f32 v13, v13\n\t"
    "v_mfma_f32_16x16x32_bf16 v[0:3], a[156:159], v[172:175], v[0:3]\n\t"
    "ds_read_b64_tr_b16 a[190:191], v135 offset:61440\n\t"
    "v_cvt_pk_bf16_f32 v33, v10, v11\n\t"
    "v_mfma_f32_16x16x32_bf16 a[32:35], a[176:179], v[48:51], a[32:35]\n\t"
    "s_add_u32 s51, s51, 16384\n\t"
    "v_exp_f32 v14, v14\n\t"
    "v_mfma_f32_16x16x32_bf16 a[32:35], a[180:183], v[52:55], a[32:35]\n\t"
    "ds_read_b128 a[132:135], v129 offset:8192\n\t"
    "v_cvt_pk_bf16_f32 v40, v12, v13\n\t"
    "v_mfma_f32_16x16x32_bf16 a[36:39], a[176:179], v[56:59], a[36:39]\n\t"
    "v_exp_f32 v15, v15\n\t"
    "v_mfma_f32_16x16x32_bf16 a[36:39], a[180:183], v[60:63], a[36:39]\n\t"
    "s_waitcnt lgkmcnt(3)\n\t"
    "buffer_load_dwordx4 v228, s[44:47], s51 offen offset:0 lds\n\t"
    "v_mfma_f32_16x16x32_bf16 v[4:7], a[144:147], v[176:179], v[84:87]\n\t"
    "v_exp_f32 v0, v0\n\t"
    "v_mfma_f32_16x16x32_bf16 v[4:7], a[148:151], v[180:183], v[4:7]\n\t"
    "ds_read_b64_tr_b16 a[160:161], v136 offset:49152\n\t"
    "v_cvt_pk_bf16_f32 v41, v14, v15\n\t"
    "v_mfma_f32_16x16x32_bf16 v[4:7], a[152:155], v[184:187], v[4:7]\n\t"
    "v_exp_f32 v1, v1\n\t"
    "v_mfma_f32_16x16x32_bf16 v[4:7], a[156:159], v[188:191], v[4:7]\n\t"
    "ds_read_b64_tr_b16 a[162:163], v136 offset:53248\n\t"
    "v_add_f32 v144, v144, v0\n\t"
    "v_mfma_f32_16x16x32_bf16 a[40:43], a[176:179], v[64:67], a[40:43]\n\t"
    "v_exp_f32 v2, v2\n\t"
    "v_mfma_f32_16x16x32_bf16 a[40:43], a[180:183], v[68:71], a[40:43]\n\t"
    "ds_read_b128 a[136:139], v130 offset:8192\n\t"
    "v_cvt_pk_bf16_f32 v18, v0, v1\n\t"
    "v_mfma_f32_16x16x32_bf16 a[44:47], a[176:179], v[72:75], a[44:47]\n\t"
    "v_exp_f32 v3, v3\n\t"
    "v_mfma_f32_16x16x32_bf16 a[44:47], a[180:183], v[76:79], a[44:47]\n\t"
    "s_waitcnt lgkmcnt(3)\n\t"
    "v_add_f32 v144, v144, v1\n\t"
    "v_add_f32 v144, v144, v2\n\t"
    "s_waitcnt vmcnt(4)\n\t"
    "v_mfma_f32_16x16x32_bf16 a[192:195], v[232:235], v[96:99], a[192:195]\n\t"
    "ds_read_b64_tr_b16 v[240:241], v231 offset:0\n\t"
    "v_mfma_f32_16x16x32_bf16 a[192:195], v[236:239], v[100:103], a[192:195]\n\t"
    "ds_read_b64_tr_b16 v[242:243], v231 offset:2048\n\t"
    "v_mfma_f32_16x16x32_bf16 a[200:203], v[232:235], v[104:107], a[200:203]\n\t"
    "ds_read_b64_tr_b16 v[244:245], v231 offset:4096\n\t"
    "v_mfma_f32_16x16x32_bf16 a[200:203], v[236:239], v[108:111], a[200:203]\n\t"
    "ds_read_b64_tr_b16 v[246:247], v231 offset:6144\n\t"
    "v_mfma_f32_16x16x32_bf16 a[208:211], v[232:235], v[112:115], a[208:211]\n\t"
    "v_mfma_f32_16x16x32_bf16 a[208:211], v[236:239], v[116:119], a[208:211]\n\t"
    "v_mfma_f32_16x16x32_bf16 a[216:219], v[232:235], v[120:123], a[216:219]\n\t"
    "v_mfma_f32_16x16x32_bf16 a[216:219], v[236:239], v[124:127], a[216:219]\n\t"
    "s_waitcnt lgkmcnt(0)\n\t"
    "v_mfma_f32_16x16x32_bf16 a[196:199], v[240:243], v[96:99], a[196:199]\n\t"
    "ds_read_b64_tr_b16 v[232:233], v248 offset:0\n\t"
    "v_mfma_f32_16x16x32_bf16 a[196:199], v[244:247], v[100:103], a[196:199]\n\t"
    "ds_read_b64_tr_b16 v[234:235], v248 offset:2048\n\t"
    "v_mfma_f32_16x16x32_bf16 a[204:207], v[240:243], v[104:107], a[204:207]\n\t"
    "ds_read_b64_tr_b16 v[236:237], v248 offset:4096\n\t"
    "v_mfma_f32_16x16x32_bf16 a[204:207], v[244:247], v[108:111], a[204:207]\n\t"
    "ds_read_b64_tr_b16 v[238:239], v248 offset:6144\n\t"
    "v_mfma_f32_16x16x32_bf16 a[212:215], v[240:243], v[112:115], a[212:215]\n\t"
    "v_mfma_f32_16x16x32_bf16 a[212:215], v[244:247], v[116:119], a[212:215]\n\t"
    "v_mfma_f32_16x16x32_bf16 a[220:223], v[240:243], v[120:123], a[220:223]\n\t"
    "v_mfma_f32_16x16x32_bf16 a[220:223], v[244:247], v[124:127], a[220:223]\n\t"
    "s_waitcnt lgkmcnt(0)\n\t"
    "v_mfma_f32_16x16x32_bf16 a[224:227], v[232:235], v[96:99], a[224:227]\n\t"
    "ds_read_b64_tr_b16 v[240:241], v249 offset:0\n\t"
    "v_mfma_f32_16x16x32_bf16 a[224:227], v[236:239], v[100:103], a[224:227]\n\t"
    "ds_read_b64_tr_b16 v[242:243], v249 offset:2048\n\t"
    "v_mfma_f32_16x16x32_bf16 a[232:235], v[232:235], v[104:107], a[232:235]\n\t"
    "ds_read_b64_tr_b16 v[244:245], v249 offset:4096\n\t"
    "v_mfma_f32_16x16x32_bf16 a[232:235], v[236:239], v[108:111], a[232:235]\n\t"
    "ds_read_b64_tr_b16 v[246:247], v249 offset:6144\n\t"
    "v_mfma_f32_16x16x32_bf16 a[240:243], v[232:235], v[112:115], a[240:243]\n\t"
    "v_mfma_f32_16x16x32_bf16 a[240:243], v[236:239], v[116:119], a[240:243]\n\t"
    "v_mfma_f32_16x16x32_bf16 a[248:251], v[232:235], v[120:123], a[248:251]\n\t"
    "v_mfma_f32_16x16x32_bf16 a[248:251], v[236:239], v[124:127], a[248:251]\n\t"
    "s_waitcnt lgkmcnt(0)\n\t"
    "v_mfma_f32_16x16x32_bf16 a[228:231], v[240:243], v[96:99], a[228:231]\n\t"
    "s_add_u32 m0, s95, 131072\n\t"
    "s_add_u32 s76, s74, 131072\n\t"
    "buffer_load_dwordx4 v159, s[60:63], s76 offen lds\n\t"
    "v_mfma_f32_16x16x32_bf16 a[228:231], v[244:247], v[100:103], a[228:231]\n\t"
    "s_add_u32 m0, s95, 132096\n\t"
    "s_add_u32 s76, s74, 139264\n\t"
    "buffer_load_dwordx4 v159, s[60:63], s76 offen lds\n\t"
    "v_mfma_f32_16x16x32_bf16 a[236:239], v[240:243], v[104:107], a[236:239]\n\t"
    "s_add_u32 m0, s95, 133120\n\t"
    "s_add_u32 s76, s74, 147456\n\t"
    "buffer_load_dwordx4 v159, s[60:63], s76 offen lds\n\t"
    "v_mfma_f32_16x16x32_bf16 a[236:239], v[244:247], v[108:111], a[236:239]\n\t"
    "s_add_u32 m0, s95, 134144\n\t"
    "s_add_u32 s76, s74, 155648\n\t"
    "buffer_load_dwordx4 v159, s[60:63], s76 offen lds\n\t"
    "v_mfma_f32_16x16x32_bf16 a[244:247], v[240:243], v[112:115], a[244:247]\n\t"
    "s_add_u32 m0, s95, 135168\n\t"
    "s_add_u32 s76, s74, 163840\n\t"
    "buffer_load_dwordx4 v159, s[60:63], s76 offen lds\n\t"
    "v_mfma_f32_16x16x32_bf16 a[244:247], v[244:247], v[116:119], a[244:247]\n\t"
    "s_add_u32 m0, s95, 136192\n\t"
    "s_add_u32 s76, s74, 172032\n\t"
    "buffer_load_dwordx4 v159, s[60:63], s76 offen lds\n\t"
    "v_mfma_f32_16x16x32_bf16 a[252:255], v[240:243], v[120:123], a[252:255]\n\t"
    "s_add_u32 m0, s95, 137216\n\t"
    "s_add_u32 s76, s74, 180224\n\t"
    "buffer_load_dwordx4 v159, s[60:63], s76 offen lds\n\t"
    "v_mfma_f32_16x16x32_bf16 a[252:255], v[244:247], v[124:127], a[252:255]\n\t"
    "s_add_u32 m0, s95, 138240\n\t"
    "s_add_u32 s76, s74, 188416\n\t"
    "buffer_load_dwordx4 v159, s[60:63], s76 offen lds\n\t"
    "v_mfma_f32_16x16x32_bf16 v[8:11], a[144:147], v[192:195], v[88:91]\n\t"
    "v_exp_f32 v4, v4\n\t"
    "v_mfma_f32_16x16x32_bf16 v[8:11], a[148:151], v[196:199], v[8:11]\n\t"
    "ds_read_b64_tr_b16 a[164:165], v136 offset:57344\n\t"
    "v_add_f32 v144, v144, v3\n\t"
    "v_mfma_f32_16x16x32_bf16 v[8:11], a[152:155], v[200:203], v[8:11]\n\t"
    "s_mov_b32 m0, s57\n\t"
    "v_exp_f32 v5, v5\n\t"
    "v_mfma_f32_16x16x32_bf16 v[8:11], a[156:159], v[204:207], v[8:11]\n\t"
    "ds_read_b64_tr_b16 a[166:167], v136 offset:61440\n\t"
    "v_cvt_pk_bf16_f32 v19, v2, v3\n\t"
    "v_mfma_f32_16x16x32_bf16 a[48:51], a[184:187], v[48:51], a[48:51]\n\t"
    "v_exp_f32 v6, v6\n\t"
    "v_mfma_f32_16x16x32_bf16 a[48:51], a[188:191], v[52:55], a[48:51]\n\t"
    "ds_read_b128 a[140:143], v131 offset:8192\n\t"
    "v_cvt_pk_bf16_f32 v26, v4, v5\n\t"
    "v_mfma_f32_16x16x32_bf16 a[52:55], a[184:187], v[56:59], a[52:55]\n\t"
    "v_exp_f32 v7, v7\n\t"
    "v_mfma_f32_16x16x32_bf16 a[52:55], a[188:191], v[60:63], a[52:55]\n\t"
    "s_waitcnt lgkmcnt(3)\n\t"
    "buffer_load_dwordx4 v229, s[44:47], s51 offen offset:1024 lds\n\t"
    "v_mfma_f32_16x16x32_bf16 v[12:15], a[144:147], v[208:211], v[92:95]\n\t"
    "v_exp_f32 v8, v8\n\t"
    "v_mfma_f32_16x16x32_bf16 v[12:15], a[148:151], v[212:215], v[12:15]\n\t"
    "ds_read_b64_tr_b16 a[168:169], v137 offset:49152\n\t"
    "v_cvt_pk_bf16_f32 v27, v6, v7\n\t"
    "v_mfma_f32_16x16x32_bf16 v[12:15], a[152:155], v[216:219], v[12:15]\n\t"
    "v_exp_f32 v9, v9\n\t"
    "v_mfma_f32_16x16x32_bf16 v[12:15], a[156:159], v[220:223], v[12:15]\n\t"
    "ds_read_b64_tr_b16 a[170:171], v137 offset:53248\n\t"
    "v_add_f32 v146, v146, v8\n\t"
    "v_mfma_f32_16x16x32_bf16 a[56:59], a[184:187], v[64:67], a[56:59]\n\t"
    "v_exp_f32 v10, v10\n\t"
    "v_mfma_f32_16x16x32_bf16 a[56:59], a[188:191], v[68:71], a[56:59]\n\t"
    "ds_read_b128 a[144:147], v128 offset:12288\n\t"
    "v_cvt_pk_bf16_f32 v34, v8, v9\n\t"
    "v_mfma_f32_16x16x32_bf16 a[60:63], a[184:187], v[72:75], a[60:63]\n\t"
    "v_exp_f32 v11, v11\n\t"
    "v_mfma_f32_16x16x32_bf16 a[60:63], a[188:191], v[76:79], a[60:63]\n\t"
    "s_waitcnt lgkmcnt(3)\n\t"
    "v_add_f32 v146, v146, v9\n\t"
    "v_add_f32 v146, v146, v10\n\t"
    "s_add_u32 s77, s72, 4096\n\t"
    "buffer_load_dwordx4 v[96:99], v255, s[68:71], s77 offen offset:0 sc1\n\t"
    "buffer_load_dwordx4 v[100:103], v255, s[68:71], s77 offen offset:1024 sc1\n\t"
    "s_add_u32 s77, s72, 61440\n\t"
    "buffer_load_dwordx4 v[104:107], v255, s[68:71], s77 offen offset:0 sc1\n\t"
    "v_mfma_f32_16x16x32_bf16 v[0:3], a[128:131], v[160:163], v[80:83]\n\t"
    "v_exp_f32 v12, v12\n\t"
    "v_mfma_f32_16x16x32_bf16 v[0:3], a[132:135], v[164:167], v[0:3]\n\t"
    "ds_read_b64_tr_b16 a[172:173], v137 offset:57344\n\t"
    "v_add_f32 v146, v146, v11\n\t"
    "v_mfma_f32_16x16x32_bf16 v[0:3], a[136:139], v[168:171], v[0:3]\n\t"
    "s_mov_b32 m0, s57\n\t"
    "v_exp_f32 v13, v13\n\t"
    "v_mfma_f32_16x16x32_bf16 v[0:3], a[140:143], v[172:175], v[0:3]\n\t"
    "ds_read_b64_tr_b16 a[174:175], v137 offset:61440\n\t"
    "v_cvt_pk_bf16_f32 v35, v10, v11\n\t"
    "v_mfma_f32_16x16x32_bf16 a[64:67], a[160:163], v[48:51], a[64:67]\n\t"
    "v_exp_f32 v14, v14\n\t"
    "v_mfma_f32_16x16x32_bf16 a[64:67], a[164:167], v[52:55], a[64:67]\n\t"
    "ds_read_b128 a[148:151], v129 offset:12288\n\t"
    "v_cvt_pk_bf16_f32 v42, v12, v13\n\t"
    "v_mfma_f32_16x16x32_bf16 a[68:71], a[160:163], v[56:59], a[68:71]\n\t"
    "v_exp_f32 v15, v15\n\t"
    "v_mfma_f32_16x16x32_bf16 a[68:71], a[164:167], v[60:63], a[68:71]\n\t"
    "s_waitcnt lgkmcnt(3)\n\t"
    "buffer_load_dwordx4 v228, s[44:47], s51 offen offset:2048 lds\n\t"
    "v_mfma_f32_16x16x32_bf16 v[4:7], a[128:131], v[176:179], v[84:87]\n\t"
    "v_exp_f32 v0, v0\n\t"
    "v_mfma_f32_16x16x32_bf16 v[4:7], a[132:135], v[180:183], v[4:7]\n\t"
    "ds_read_b64_tr_b16 a[176:177], v138 offset:49152\n\t"
    "v_cvt_pk_bf16_f32 v43, v14, v15\n\t"
    "v_mfma_f32_16x16x32_bf16 v[4:7], a[136:139], v[184:187], v[4:7]\n\t"
    "v_exp_f32 v1, v1\n\t"
    "v_mfma_f32_16x16x32_bf16 v[4:7], a[140:143], v[188:191], v[4:7]\n\t"
    "ds_read_b64_tr_b16 a[178:179], v138 offset:53248\n\t"
    "v_add_f32 v144, v144, v0\n\t"
    "v_mfma_f32_16x16x32_bf16 a[72:75], a[160:163], v[64:67], a[72:75]\n\t"
    "v_exp_f32 v2, v2\n\t"
    "v_mfma_f32_16x16x32_bf16 a[72:75], a[164:167], v[68:71], a[72:75]\n\t"
    "ds_read_b128 a[152:155], v130 offset:12288\n\t"
    "v_cvt_pk_bf16_f32 v20, v0, v1\n\t"
    "v_mfma_f32_16x16x32_bf16 a[76:79], a[160:163], v[72:75], a[76:79]\n\t"
    "v_exp_f32 v3, v3\n\t"
    "v_mfma_f32_16x16x32_bf16 a[76:79], a[164:167], v[76:79], a[76:79]\n\t"
    "s_waitcnt lgkmcnt(3)\n\t"
    "v_add_f32 v144, v144, v1\n\t"
    "v_add_f32 v144, v144, v2\n\t"
    "buffer_load_dwordx4 v[108:111], v255, s[68:71], s77 offen offset:1024 sc1\n\t"
    "s_add_u32 s77, s72, 118784\n\t"
    "buffer_load_dwordx4 v[112:115], v255, s[68:71], s77 offen offset:0 sc1\n\t"
    "buffer_load_dwordx4 v[116:119], v255, s[68:71], s77 offen offset:1024 sc1\n\t"
    "v_mfma_f32_16x16x32_bf16 v[8:11], a[128:131], v[192:195], v[88:91]\n\t"
    "v_exp_f32 v4, v4\n\t"
    "v_mfma_f32_16x16x32_bf16 v[8:11], a[132:135], v[196:199], v[8:11]\n\t"
    "ds_read_b64_tr_b16 a[180:181], v138 offset:57344\n\t"
    "v_add_f32 v144, v144, v3\n\t"
    "v_mfma_f32_16x16x32_bf16 v[8:11], a[136:139], v[200:203], v[8:11]\n\t"
    "s_mov_b32 m0, s57\n\t"
    "v_exp_f32 v5, v5\n\t"
    "v_mfma_f32_16x16x32_bf16 v[8:11], a[140:143], v[204:207], v[8:11]\n\t"
    "ds_read_b64_tr_b16 a[182:183], v138 offset:61440\n\t"
    "v_cvt_pk_bf16_f32 v21, v2, v3\n\t"
    "v_mfma_f32_16x16x32_bf16 a[80:83], a[168:171], v[48:51], a[80:83]\n\t"
    "v_exp_f32 v6, v6\n\t"
    "v_mfma_f32_16x16x32_bf16 a[80:83], a[172:175], v[52:55], a[80:83]\n\t"
    "ds_read_b128 a[156:159], v131 offset:12288\n\t"
    "v_cvt_pk_bf16_f32 v28, v4, v5\n\t"
    "v_mfma_f32_16x16x32_bf16 a[84:87], a[168:171], v[56:59], a[84:87]\n\t"
    "v_exp_f32 v7, v7\n\t"
    "v_mfma_f32_16x16x32_bf16 a[84:87], a[172:175], v[60:63], a[84:87]\n\t"
    "s_waitcnt lgkmcnt(3)\n\t"
    "buffer_load_dwordx4 v229, s[44:47], s51 offen offset:3072 lds\n\t"
    "v_mfma_f32_16x16x32_bf16 v[12:15], a[128:131], v[208:211], v[92:95]\n\t"
    "s_waitcnt vmcnt(27)\n\t"
    "v_exp_f32 v8, v8\n\t"
    "v_mfma_f32_16x16x32_bf16 v[12:15], a[132:135], v[212:215], v[12:15]\n\t"
    "ds_read_b64_tr_b16 a[184:185], v139 offset:49152\n\t"
    "v_cvt_pk_bf16_f32 v29, v6, v7\n\t"
    "v_mfma_f32_16x16x32_bf16 v[12:15], a[136:139], v[216:219], v[12:15]\n\t"
    "s_barrier\n\t"
    "v_exp_f32 v9, v9\n\t"
    "v_mfma_f32_16x16x32_bf16 v[12:15], a[140:143], v[220:223], v[12:15]\n\t"
    "ds_read_b64_tr_b16 a[186:187], v139 offset:53248\n\t"
    "v_add_f32 v146, v146, v8\n\t"
    "v_mfma_f32_16x16x32_bf16 a[88:91], a[168:171], v[64:67], a[88:91]\n\t"
    "v_exp_f32 v10, v10\n\t"
    "v_mfma_f32_16x16x32_bf16 a[88:91], a[172:175], v[68:71], a[88:91]\n\t"
    "ds_read_b128 a[128:131], v128 offset:16384\n\t"
    "v_cvt_pk_bf16_f32 v36, v8, v9\n\t"
    "v_mfma_f32_16x16x32_bf16 a[92:95], a[168:171], v[72:75], a[92:95]\n\t"
    "v_exp_f32 v11, v11\n\t"
    "v_mfma_f32_16x16x32_bf16 a[92:95], a[172:175], v[76:79], a[92:95]\n\t"
    "s_waitcnt lgkmcnt(3)\n\t"
    "v_add_f32 v146, v146, v9\n\t"
    "v_add_f32 v146, v146, v10\n\t"
    "s_add_u32 s77, s72, 176128\n\t"
    "buffer_load_dwordx4 v[120:123], v255, s[68:71], s77 offen offset:0 sc1\n\t"
    "buffer_load_dwordx4 v[124:127], v255, s[68:71], s77 offen offset:1024 sc1\n\t"
    "v_mfma_f32_16x16x32_bf16 v[0:3], a[144:147], v[160:163], v[80:83]\n\t"
    "v_exp_f32 v12, v12\n\t"
    "v_mfma_f32_16x16x32_bf16 v[0:3], a[148:151], v[164:167], v[0:3]\n\t"
    "ds_read_b64_tr_b16 a[188:189], v139 offset:57344\n\t"
    "v_add_f32 v146, v146, v11\n\t"
    "v_mfma_f32_16x16x32_bf16 v[0:3], a[152:155], v[168:171], v[0:3]\n\t"
    "s_mov_b32 m0, s55\n\t"
    "v_exp_f32 v13, v13\n\t"
    "v_mfma_f32_16x16x32_bf16 v[0:3], a[156:159], v[172:175], v[0:3]\n\t"
    "ds_read_b64_tr_b16 a[190:191], v139 offset:61440\n\t"
    "v_cvt_pk_bf16_f32 v37, v10, v11\n\t"
    "v_mfma_f32_16x16x32_bf16 a[96:99], a[176:179], v[48:51], a[96:99]\n\t"
    "s_add_u32 s48, s48, 16384\n\t"
    "v_exp_f32 v14, v14\n\t"
    "v_mfma_f32_16x16x32_bf16 a[96:99], a[180:183], v[52:55], a[96:99]\n\t"
    "ds_read_b128 a[132:135], v129 offset:16384\n\t"
    "v_cvt_pk_bf16_f32 v44, v12, v13\n\t"
    "v_mfma_f32_16x16x32_bf16 a[100:103], a[176:179], v[56:59], a[100:103]\n\t"
    "v_exp_f32 v15, v15\n\t"
    "v_mfma_f32_16x16x32_bf16 a[100:103], a[180:183], v[60:63], a[100:103]\n\t"
    "s_waitcnt lgkmcnt(3)\n\t"
    "buffer_load_dwordx4 v224, s[40:43], s48 offen offset:0 lds\n\t"
    "v_mfma_f32_16x16x32_bf16 v[4:7], a[144:147], v[176:179], v[84:87]\n\t"
    "v_exp_f32 v0, v0\n\t"
    "v_mfma_f32_16x16x32_bf16 v[4:7], a[148:151], v[180:183], v[4:7]\n\t"
    "ds_read_b64_tr_b16 a[160:161], v132 offset:0\n\t"
    "v_cvt_pk_bf16_f32 v45, v14, v15\n\t"
    "v_mfma_f32_16x16x32_bf16 v[4:7], a[152:155], v[184:187], v[4:7]\n\t"
    "v_exp_f32 v1, v1\n\t"
    "v_mfma_f32_16x16x32_bf16 v[4:7], a[156:159], v[188:191], v[4:7]\n\t"
    "ds_read_b64_tr_b16 a[162:163], v132 offset:4096\n\t"
    "v_add_f32 v144, v144, v0\n\t"
    "v_mfma_f32_16x16x32_bf16 a[104:107], a[176:179], v[64:67], a[104:107]\n\t"
    "v_exp_f32 v2, v2\n\t"
    "v_mfma_f32_16x16x32_bf16 a[104:107], a[180:183], v[68:71], a[104:107]\n\t"
    "ds_read_b128 a[136:139], v130 offset:16384\n\t"
    "v_cvt_pk_bf16_f32 v22, v0, v1\n\t"
    "v_mfma_f32_16x16x32_bf16 a[108:111], a[176:179], v[72:75], a[108:111]\n\t"
    "v_exp_f32 v3, v3\n\t"
    "v_mfma_f32_16x16x32_bf16 a[108:111], a[180:183], v[76:79], a[108:111]\n\t"
    "s_waitcnt lgkmcnt(3)\n\t"
    "v_add_f32 v144, v144, v1\n\t"
    "v_add_f32 v144, v144, v2\n\t"
    "v_mfma_f32_16x16x32_bf16 v[8:11], a[144:147], v[192:195], v[88:91]\n\t"
    "v_exp_f32 v4, v4\n\t"
    "v_mfma_f32_16x16x32_bf16 v[8:11], a[148:151], v[196:199], v[8:11]\n\t"
    "ds_read_b64_tr_b16 a[164:165], v132 offset:8192\n\t"
    "v_add_f32 v144, v144, v3\n\t"
    "v_mfma_f32_16x16x32_bf16 v[8:11], a[152:155], v[200:203], v[8:11]\n\t"
    "s_mov_b32 m0, s55\n\t"
    "v_exp_f32 v5, v5\n\t"
    "v_mfma_f32_16x16x32_bf16 v[8:11], a[156:159], v[204:207], v[8:11]\n\t"
    "ds_read_b64_tr_b16 a[166:167], v132 offset:12288\n\t"
    "v_cvt_pk_bf16_f32 v23, v2, v3\n\t"
    "v_mfma_f32_16x16x32_bf16 a[112:115], a[184:187], v[48:51], a[112:115]\n\t"
    "v_exp_f32 v6, v6\n\t"
    "v_mfma_f32_16x16x32_bf16 a[112:115], a[188:191], v[52:55], a[112:115]\n\t"
    "ds_read_b128 a[140:143], v131 offset:16384\n\t"
    "v_cvt_pk_bf16_f32 v30, v4, v5\n\t"
    "v_mfma_f32_16x16x32_bf16 a[116:119], a[184:187], v[56:59], a[116:119]\n\t"
    "v_exp_f32 v7, v7\n\t"
    "v_mfma_f32_16x16x32_bf16 a[116:119], a[188:191], v[60:63], a[116:119]\n\t"
    "s_waitcnt lgkmcnt(3)\n\t"
    "buffer_load_dwordx4 v225, s[40:43], s48 offen offset:1024 lds\n\t"
    "v_mfma_f32_16x16x32_bf16 v[12:15], a[144:147], v[208:211], v[92:95]\n\t"
    "v_exp_f32 v8, v8\n\t"
    "v_mfma_f32_16x16x32_bf16 v[12:15], a[148:151], v[212:215], v[12:15]\n\t"
    "ds_read_b64_tr_b16 a[168:169], v133 offset:0\n\t"
    "v_cvt_pk_bf16_f32 v31, v6, v7\n\t"
    "v_mfma_f32_16x16x32_bf16 v[12:15], a[152:155], v[216:219], v[12:15]\n\t"
    "v_exp_f32 v9, v9\n\t"
    "v_mfma_f32_16x16x32_bf16 v[12:15], a[156:159], v[220:223], v[12:15]\n\t"
    "ds_read_b64_tr_b16 a[170:171], v133 offset:4096\n\t"
    "v_add_f32 v146, v146, v8\n\t"
    "v_mfma_f32_16x16x32_bf16 a[120:123], a[184:187], v[64:67], a[120:123]\n\t"
    "v_exp_f32 v10, v10\n\t"
    "v_mfma_f32_16x16x32_bf16 a[120:123], a[188:191], v[68:71], a[120:123]\n\t"
    "ds_read_b128 a[144:147], v128 offset:20480\n\t"
    "v_cvt_pk_bf16_f32 v38, v8, v9\n\t"
    "v_mfma_f32_16x16x32_bf16 a[124:127], a[184:187], v[72:75], a[124:127]\n\t"
    "v_exp_f32 v11, v11\n\t"
    "v_mfma_f32_16x16x32_bf16 a[124:127], a[188:191], v[76:79], a[124:127]\n\t"
    "s_waitcnt lgkmcnt(3)\n\t"
    "v_add_f32 v146, v146, v9\n\t"
    "v_add_f32 v146, v146, v10\n\t"
    "v_mfma_f32_16x16x32_bf16 v[0:3], a[128:131], v[160:163], v[80:83]\n\t"
    "v_exp_f32 v12, v12\n\t"
    "v_mfma_f32_16x16x32_bf16 v[0:3], a[132:135], v[164:167], v[0:3]\n\t"
    "ds_read_b64_tr_b16 a[172:173], v133 offset:8192\n\t"
    "v_add_f32 v146, v146, v11\n\t"
    "v_mfma_f32_16x16x32_bf16 v[0:3], a[136:139], v[168:171], v[0:3]\n\t"
    "s_mov_b32 m0, s55\n\t"
    "v_exp_f32 v13, v13\n\t"
    "v_mfma_f32_16x16x32_bf16 v[0:3], a[140:143], v[172:175], v[0:3]\n\t"
    "ds_read_b64_tr_b16 a[174:175], v133 offset:12288\n\t"
    "v_cvt_pk_bf16_f32 v39, v10, v11\n\t"
    "v_mfma_f32_16x16x32_bf16 a[0:3], a[160:163], v[16:19], a[0:3]\n\t"
    "v_exp_f32 v14, v14\n\t"
    "v_mfma_f32_16x16x32_bf16 a[0:3], a[164:167], v[20:23], a[0:3]\n\t"
    "ds_read_b128 a[148:151], v129 offset:20480\n\t"
    "v_cvt_pk_bf16_f32 v46, v12, v13\n\t"
    "v_mfma_f32_16x16x32_bf16 a[4:7], a[160:163], v[24:27], a[4:7]\n\t"
    "v_exp_f32 v15, v15\n\t"
    "v_mfma_f32_16x16x32_bf16 a[4:7], a[164:167], v[28:31], a[4:7]\n\t"
    "s_waitcnt lgkmcnt(3)\n\t"
    "buffer_load_dwordx4 v226, s[40:43], s48 offen offset:2048 lds\n\t"
    "v_mfma_f32_16x16x32_bf16 v[4:7], a[128:131], v[176:179], v[84:87]\n\t"
    "v_exp_f32 v0, v0\n\t"
    "v_mfma_f32_16x16x32_bf16 v[4:7], a[132:135], v[180:183], v[4:7]\n\t"
    "ds_read_b64_tr_b16 a[176:177], v134 offset:0\n\t"
    "v_cvt_pk_bf16_f32 v47, v14, v15\n\t"
    "v_mfma_f32_16x16x32_bf16 v[4:7], a[136:139], v[184:187], v[4:7]\n\t"
    "v_exp_f32 v1, v1\n\t"
    "v_mfma_f32_16x16x32_bf16 v[4:7], a[140:143], v[188:191], v[4:7]\n\t"
    "ds_read_b64_tr_b16 a[178:179], v134 offset:4096\n\t"
    "v_add_f32 v144, v144, v0\n\t"
    "v_mfma_f32_16x16x32_bf16 a[8:11], a[160:163], v[32:35], a[8:11]\n\t"
    "v_exp_f32 v2, v2\n\t"
    "v_mfma_f32_16x16x32_bf16 a[8:11], a[164:167], v[36:39], a[8:11]\n\t"
    "ds_read_b128 a[152:155], v130 offset:20480\n\t"
    "v_cvt_pk_bf16_f32 v48, v0, v1\n\t"
    "v_mfma_f32_16x16x32_bf16 a[12:15], a[160:163], v[40:43], a[12:15]\n\t"
    "v_exp_f32 v3, v3\n\t"
    "v_mfma_f32_16x16x32_bf16 a[12:15], a[164:167], v[44:47], a[12:15]\n\t"
    "s_waitcnt lgkmcnt(3)\n\t"
    "v_add_f32 v144, v144, v1\n\t"
    "v_add_f32 v144, v144, v2\n\t"
    "v_mfma_f32_16x16x32_bf16 v[148:151], v[140:143], v[24:27], v[148:151]\n\t"
    "v_mfma_f32_16x16x32_bf16 v[148:151], v[140:143], v[28:31], v[148:151]\n\t"
    "s_waitcnt vmcnt(14)\n\t"
    "ds_read_b64_tr_b16 v[232:233], v230 offset:0\n\t"
    "ds_read_b64_tr_b16 v[234:235], v230 offset:2048\n\t"
    "ds_read_b64_tr_b16 v[236:237], v230 offset:4096\n\t"
    "ds_read_b64_tr_b16 v[238:239], v230 offset:6144\n\t"
    "v_mfma_f32_16x16x32_bf16 v[8:11], a[128:131], v[192:195], v[88:91]\n\t"
    "v_exp_f32 v4, v4\n\t"
    "v_mfma_f32_16x16x32_bf16 v[8:11], a[132:135], v[196:199], v[8:11]\n\t"
    "ds_read_b64_tr_b16 a[180:181], v134 offset:8192\n\t"
    "v_add_f32 v144, v144, v3\n\t"
    "v_mfma_f32_16x16x32_bf16 v[8:11], a[136:139], v[200:203], v[8:11]\n\t"
    "s_mov_b32 m0, s55\n\t"
    "v_exp_f32 v5, v5\n\t"
    "v_mfma_f32_16x16x32_bf16 v[8:11], a[140:143], v[204:207], v[8:11]\n\t"
    "ds_read_b64_tr_b16 a[182:183], v134 offset:12288\n\t"
    "v_cvt_pk_bf16_f32 v49, v2, v3\n\t"
    "v_mfma_f32_16x16x32_bf16 a[16:19], a[168:171], v[16:19], a[16:19]\n\t"
    "v_exp_f32 v6, v6\n\t"
    "v_mfma_f32_16x16x32_bf16 a[16:19], a[172:175], v[20:23], a[16:19]\n\t"
    "ds_read_b128 a[156:159], v131 offset:20480\n\t"
    "v_cvt_pk_bf16_f32 v56, v4, v5\n\t"
    "v_mfma_f32_16x16x32_bf16 a[20:23], a[168:171], v[24:27], a[20:23]\n\t"
    "v_exp_f32 v7, v7\n\t"
    "v_mfma_f32_16x16x32_bf16 a[20:23], a[172:175], v[28:31], a[20:23]\n\t"
    "s_waitcnt lgkmcnt(3)\n\t"
    "buffer_load_dwordx4 v227, s[40:43], s48 offen offset:3072 lds\n\t"
    "v_mfma_f32_16x16x32_bf16 v[12:15], a[128:131], v[208:211], v[92:95]\n\t"
    "v_exp_f32 v8, v8\n\t"
    "v_mfma_f32_16x16x32_bf16 v[12:15], a[132:135], v[212:215], v[12:15]\n\t"
    "ds_read_b64_tr_b16 a[184:185], v135 offset:0\n\t"
    "v_cvt_pk_bf16_f32 v57, v6, v7\n\t"
    "v_mfma_f32_16x16x32_bf16 v[12:15], a[136:139], v[216:219], v[12:15]\n\t"
    "v_exp_f32 v9, v9\n\t"
    "v_mfma_f32_16x16x32_bf16 v[12:15], a[140:143], v[220:223], v[12:15]\n\t"
    "ds_read_b64_tr_b16 a[186:187], v135 offset:4096\n\t"
    "v_add_f32 v146, v146, v8\n\t"
    "v_mfma_f32_16x16x32_bf16 a[24:27], a[168:171], v[32:35], a[24:27]\n\t"
    "v_exp_f32 v10, v10\n\t"
    "v_mfma_f32_16x16x32_bf16 a[24:27], a[172:175], v[36:39], a[24:27]\n\t"
    "ds_read_b128 a[128:131], v128 offset:24576\n\t"
    "v_cvt_pk_bf16_f32 v64, v8, v9\n\t"
    "v_mfma_f32_16x16x32_bf16 a[28:31], a[168:171], v[40:43], a[28:31]\n\t"
    "v_exp_f32 v11, v11\n\t"
    "v_mfma_f32_16x16x32_bf16 a[28:31], a[172:175], v[44:47], a[28:31]\n\t"
    "s_waitcnt lgkmcnt(3)\n\t"
    "v_add_f32 v146, v146, v9\n\t"
    "v_add_f32 v146, v146, v10\n\t"
    "v_mfma_f32_16x16x32_bf16 v[152:155], v[140:143], v[40:43], v[152:155]\n\t"
    "v_mfma_f32_16x16x32_bf16 v[152:155], v[140:143], v[44:47], v[152:155]\n\t"
    "s_waitcnt vmcnt(4)\n\t"
    "v_mfma_f32_16x16x32_bf16 a[192:195], v[232:235], v[96:99], a[192:195]\n\t"
    "ds_read_b64_tr_b16 v[240:241], v231 offset:0\n\t"
    "v_mfma_f32_16x16x32_bf16 a[192:195], v[236:239], v[100:103], a[192:195]\n\t"
    "ds_read_b64_tr_b16 v[242:243], v231 offset:2048\n\t"
    "v_mfma_f32_16x16x32_bf16 a[200:203], v[232:235], v[104:107], a[200:203]\n\t"
    "ds_read_b64_tr_b16 v[244:245], v231 offset:4096\n\t"
    "v_mfma_f32_16x16x32_bf16 a[200:203], v[236:239], v[108:111], a[200:203]\n\t"
    "ds_read_b64_tr_b16 v[246:247], v231 offset:6144\n\t"
    "v_mfma_f32_16x16x32_bf16 a[208:211], v[232:235], v[112:115], a[208:211]\n\t"
    "v_mfma_f32_16x16x32_bf16 a[208:211], v[236:239], v[116:119], a[208:211]\n\t"
    "v_mfma_f32_16x16x32_bf16 a[216:219], v[232:235], v[120:123], a[216:219]\n\t"
    "v_mfma_f32_16x16x32_bf16 a[216:219], v[236:239], v[124:127], a[216:219]\n\t"
    "s_waitcnt lgkmcnt(0)\n\t"
    "v_mfma_f32_16x16x32_bf16 a[196:199], v[240:243], v[96:99], a[196:199]\n\t"
    "ds_read_b64_tr_b16 v[232:233], v248 offset:0\n\t"
    "v_mfma_f32_16x16x32_bf16 a[196:199], v[244:247], v[100:103], a[196:199]\n\t"
    "ds_read_b64_tr_b16 v[234:235], v248 offset:2048\n\t"
    "v_mfma_f32_16x16x32_bf16 a[204:207], v[240:243], v[104:107], a[204:207]\n\t"
    "ds_read_b64_tr_b16 v[236:237], v248 offset:4096\n\t"
    "v_mfma_f32_16x16x32_bf16 a[204:207], v[244:247], v[108:111], a[204:207]\n\t"
    "ds_read_b64_tr_b16 v[238:239], v248 offset:6144\n\t"
    "v_mfma_f32_16x16x32_bf16 a[212:215], v[240:243], v[112:115], a[212:215]\n\t"
    "v_mfma_f32_16x16x32_bf16 a[212:215], v[244:247], v[116:119], a[212:215]\n\t"
    "v_mfma_f32_16x16x32_bf16 a[220:223], v[240:243], v[120:123], a[220:223]\n\t"
    "v_mfma_f32_16x16x32_bf16 a[220:223], v[244:247], v[124:127], a[220:223]\n\t"
    "s_waitcnt lgkmcnt(0)\n\t"
    "v_mfma_f32_16x16x32_bf16 a[224:227], v[232:235], v[96:99], a[224:227]\n\t"
    "ds_read_b64_tr_b16 v[240:241], v249 offset:0\n\t"
    "v_mfma_f32_16x16x32_bf16 a[224:227], v[236:239], v[100:103], a[224:227]\n\t"
    "ds_read_b64_tr_b16 v[242:243], v249 offset:2048\n\t"
    "v_mfma_f32_16x16x32_bf16 a[232:235], v[232:235], v[104:107], a[232:235]\n\t"
    "ds_read_b64_tr_b16 v[244:245], v249 offset:4096\n\t"
    "v_mfma_f32_16x16x32_bf16 a[232:235], v[236:239], v[108:111], a[232:235]\n\t"
    "ds_read_b64_tr_b16 v[246:247], v249 offset:6144\n\t"
    "v_mfma_f32_16x16x32_bf16 a[240:243], v[232:235], v[112:115], a[240:243]\n\t"
    "v_mfma_f32_16x16x32_bf16 a[240:243], v[236:239], v[116:119], a[240:243]\n\t"
    "v_mfma_f32_16x16x32_bf16 a[248:251], v[232:235], v[120:123], a[248:251]\n\t"
    "v_mfma_f32_16x16x32_bf16 a[248:251], v[236:239], v[124:127], a[248:251]\n\t"
    "s_waitcnt lgkmcnt(0)\n\t"
    "v_mfma_f32_16x16x32_bf16 a[228:231], v[240:243], v[96:99], a[228:231]\n\t"
    "s_add_u32 m0, s95, 131072\n\t"
    "s_add_u32 s76, s74, 196608\n\t"
    "buffer_load_dwordx4 v159, s[60:63], s76 offen lds\n\t"
    "v_mfma_f32_16x16x32_bf16 a[228:231], v[244:247], v[100:103], a[228:231]\n\t"
    "s_add_u32 m0, s95, 132096\n\t"
    "s_add_u32 s76, s74, 204800\n\t"
    "buffer_load_dwordx4 v159, s[60:63], s76 offen lds\n\t"
    "v_mfma_f32_16x16x32_bf16 a[236:239], v[240:243], v[104:107], a[236:239]\n\t"
    "s_add_u32 m0, s95, 133120\n\t"
    "s_add_u32 s76, s74, 212992\n\t"
    "buffer_load_dwordx4 v159, s[60:63], s76 offen lds\n\t"
    "v_mfma_f32_16x16x32_bf16 a[236:239], v[244:247], v[108:111], a[236:239]\n\t"
    "s_add_u32 m0, s95, 134144\n\t"
    "s_add_u32 s76, s74, 221184\n\t"
    "buffer_load_dwordx4 v159, s[60:63], s76 offen lds\n\t"
    "v_mfma_f32_16x16x32_bf16 a[244:247], v[240:243], v[112:115], a[244:247]\n\t"
    "s_add_u32 m0, s95, 135168\n\t"
    "s_add_u32 s76, s74, 229376\n\t"
    "buffer_load_dwordx4 v159, s[60:63], s76 offen lds\n\t"
    "v_mfma_f32_16x16x32_bf16 a[244:247], v[244:247], v[116:119], a[244:247]\n\t"
    "s_add_u32 m0, s95, 136192\n\t"
    "s_add_u32 s76, s74, 237568\n\t"
    "buffer_load_dwordx4 v159, s[60:63], s76 offen lds\n\t"
    "v_mfma_f32_16x16x32_bf16 a[252:255], v[240:243], v[120:123], a[252:255]\n\t"
    "s_add_u32 m0, s95, 137216\n\t"
    "s_add_u32 s76, s74, 245760\n\t"
    "buffer_load_dwordx4 v159, s[60:63], s76 offen lds\n\t"
    "v_mfma_f32_16x16x32_bf16 a[252:255], v[244:247], v[124:127], a[252:255]\n\t"
    "s_add_u32 m0, s95, 138240\n\t"
    "s_add_u32 s76, s74, 253952\n\t"
    "buffer_load_dwordx4 v159, s[60:63], s76 offen lds\n\t"
    "v_mfma_f32_16x16x32_bf16 v[0:3], a[144:147], v[160:163], v[80:83]\n\t"
    "v_exp_f32 v12, v12\n\t"
    "v_mfma_f32_16x16x32_bf16 v[0:3], a[148:151], v[164:167], v[0:3]\n\t"
    "ds_read_b64_tr_b16 a[188:189], v135 offset:8192\n\t"
    "v_add_f32 v146, v146, v11\n\t"
    "v_mfma_f32_16x16x32_bf16 v[0:3], a[152:155], v[168:171], v[0:3]\n\t"
    "s_mov_b32 m0, s58\n\t"
    "v_exp_f32 v13, v13\n\t"
    "v_mfma_f32_16x16x32_bf16 v[0:3], a[156:159], v[172:175], v[0:3]\n\t"
    "ds_read_b64_tr_b16 a[190:191], v135 offset:12288\n\t"
    "v_cvt_pk_bf16_f32 v65, v10, v11\n\t"
    "v_mfma_f32_16x16x32_bf16 a[32:35], a[176:179], v[16:19], a[32:35]\n\t"
    "s_add_u32 s51, s51, 16384\n\t"
    "v_exp_f32 v14, v14\n\t"
    "v_mfma_f32_16x16x32_bf16 a[32:35], a[180:183], v[20:23], a[32:35]\n\t"
    "ds_read_b128 a[132:135], v129 offset:24576\n\t"
    "v_cvt_pk_bf16_f32 v72, v12, v13\n\t"
    "v_mfma_f32_16x16x32_bf16 a[36:39], a[176:179], v[24:27], a[36:39]\n\t"
    "v_exp_f32 v15, v15\n\t"
    "v_mfma_f32_16x16x32_bf16 a[36:39], a[180:183], v[28:31], a[36:39]\n\t"
    "s_waitcnt lgkmcnt(3)\n\t"
    "buffer_load_dwordx4 v228, s[44:47], s51 offen offset:0 lds\n\t"
    "v_mfma_f32_16x16x32_bf16 v[4:7], a[144:147], v[176:179], v[84:87]\n\t"
    "v_exp_f32 v0, v0\n\t"
    "v_mfma_f32_16x16x32_bf16 v[4:7], a[148:151], v[180:183], v[4:7]\n\t"
    "ds_read_b64_tr_b16 a[160:161], v136 offset:0\n\t"
    "v_cvt_pk_bf16_f32 v73, v14, v15\n\t"
    "v_mfma_f32_16x16x32_bf16 v[4:7], a[152:155], v[184:187], v[4:7]\n\t"
    "v_exp_f32 v1, v1\n\t"
    "v_mfma_f32_16x16x32_bf16 v[4:7], a[156:159], v[188:191], v[4:7]\n\t"
    "ds_read_b64_tr_b16 a[162:163], v136 offset:4096\n\t"
    "v_add_f32 v144, v144, v0\n\t"
    "v_mfma_f32_16x16x32_bf16 a[40:43], a[176:179], v[32:35], a[40:43]\n\t"
    "v_exp_f32 v2, v2\n\t"
    "v_mfma_f32_16x16x32_bf16 a[40:43], a[180:183], v[36:39], a[40:43]\n\t"
    "ds_read_b128 a[136:139], v130 offset:24576\n\t"
    "v_cvt_pk_bf16_f32 v50, v0, v1\n\t"
    "v_mfma_f32_16x16x32_bf16 a[44:47], a[176:179], v[40:43], a[44:47]\n\t"
    "v_exp_f32 v3, v3\n\t"
    "v_mfma_f32_16x16x32_bf16 a[44:47], a[180:183], v[44:47], a[44:47]\n\t"
    "s_waitcnt lgkmcnt(3)\n\t"
    "v_add_f32 v144, v144, v1\n\t"
    "v_add_f32 v144, v144, v2\n\t"
    "s_add_u32 s77, s72, 6144\n\t"
    "buffer_load_dwordx4 v[96:99], v255, s[68:71], s77 offen offset:0 sc1\n\t"
    "buffer_load_dwordx4 v[100:103], v255, s[68:71], s77 offen offset:1024 sc1\n\t"
    "s_add_u32 s77, s72, 63488\n\t"
    "buffer_load_dwordx4 v[104:107], v255, s[68:71], s77 offen offset:0 sc1\n\t"
    "v_mfma_f32_16x16x32_bf16 v[8:11], a[144:147], v[192:195], v[88:91]\n\t"
    "v_exp_f32 v4, v4\n\t"
    "v_mfma_f32_16x16x32_bf16 v[8:11], a[148:151], v[196:199], v[8:11]\n\t"
    "ds_read_b64_tr_b16 a[164:165], v136 offset:8192\n\t"
    "v_add_f32 v144, v144, v3\n\t"
    "v_mfma_f32_16x16x32_bf16 v[8:11], a[152:155], v[200:203], v[8:11]\n\t"
    "s_mov_b32 m0, s58\n\t"
    "v_exp_f32 v5, v5\n\t"
    "v_mfma_f32_16x16x32_bf16 v[8:11], a[156:159], v[204:207], v[8:11]\n\t"
    "ds_read_b64_tr_b16 a[166:167], v136 offset:12288\n\t"
    "v_cvt_pk_bf16_f32 v51, v2, v3\n\t"
    "v_mfma_f32_16x16x32_bf16 a[48:51], a[184:187], v[16:19], a[48:51]\n\t"
    "v_exp_f32 v6, v6\n\t"
    "v_mfma_f32_16x16x32_bf16 a[48:51], a[188:191], v[20:23], a[48:51]\n\t"
    "ds_read_b128 a[140:143], v131 offset:24576\n\t"
    "v_cvt_pk_bf16_f32 v58, v4, v5\n\t"
    "v_mfma_f32_16x16x32_bf16 a[52:55], a[184:187], v[24:27], a[52:55]\n\t"
    "v_exp_f32 v7, v7\n\t"
    "v_mfma_f32_16x16x32_bf16 a[52:55], a[188:191], v[28:31], a[52:55]\n\t"
    "s_waitcnt lgkmcnt(3)\n\t"
    "buffer_load_dwordx4 v229, s[44:47], s51 offen offset:1024 lds\n\t"
    "v_mfma_f32_16x16x32_bf16 v[12:15], a[144:147], v[208:211], v[92:95]\n\t"
    "v_exp_f32 v8, v8\n\t"
    "v_mfma_f32_16x16x32_bf16 v[12:15], a[148:151], v[212:215], v[12:15]\n\t"
    "ds_read_b64_tr_b16 a[168:169], v137 offset:0\n\t"
    "v_cvt_pk_bf16_f32 v59, v6, v7\n\t"
    "v_mfma_f32_16x16x32_bf16 v[12:15], a[152:155], v[216:219], v[12:15]\n\t"
    "v_exp_f32 v9, v9\n\t"
    "v_mfma_f32_16x16x32_bf16 v[12:15], a[156:159], v[220:223], v[12:15]\n\t"
    "ds_read_b64_tr_b16 a[170:171], v137 offset:4096\n\t"
    "v_add_f32 v146, v146, v8\n\t"
    "v_mfma_f32_16x16x32_bf16 a[56:59], a[184:187], v[32:35], a[56:59]\n\t"
    "v_exp_f32 v10, v10\n\t"
    "v_mfma_f32_16x16x32_bf16 a[56:59], a[188:191], v[36:39], a[56:59]\n\t"
    "ds_read_b128 a[144:147], v128 offset:28672\n\t"
    "v_cvt_pk_bf16_f32 v66, v8, v9\n\t"
    "v_mfma_f32_16x16x32_bf16 a[60:63], a[184:187], v[40:43], a[60:63]\n\t"
    "v_exp_f32 v11, v11\n\t"
    "v_mfma_f32_16x16x32_bf16 a[60:63], a[188:191], v[44:47], a[60:63]\n\t"
    "s_waitcnt lgkmcnt(3)\n\t"
    "v_add_f32 v146, v146, v9\n\t"
    "v_add_f32 v146, v146, v10\n\t"
    "buffer_load_dwordx4 v[108:111], v255, s[68:71], s77 offen offset:1024 sc1\n\t"
    "s_add_u32 s77, s72, 120832\n\t"
    "buffer_load_dwordx4 v[112:115], v255, s[68:71], s77 offen offset:0 sc1\n\t"
    "buffer_load_dwordx4 v[116:119], v255, s[68:71], s77 offen offset:1024 sc1\n\t"
    "v_mfma_f32_16x16x32_bf16 v[0:3], a[128:131], v[160:163], v[80:83]\n\t"
    "v_exp_f32 v12, v12\n\t"
    "v_mfma_f32_16x16x32_bf16 v[0:3], a[132:135], v[164:167], v[0:3]\n\t"
    "ds_read_b64_tr_b16 a[172:173], v137 offset:8192\n\t"
    "v_add_f32 v146, v146, v11\n\t"
    "v_mfma_f32_16x16x32_bf16 v[0:3], a[136:139], v[168:171], v[0:3]\n\t"
    "s_mov_b32 m0, s58\n\t"
    "v_exp_f32 v13, v13\n\t"
    "v_mfma_f32_16x16x32_bf16 v[0:3], a[140:143], v[172:175], v[0:3]\n\t"
    "ds_read_b64_tr_b16 a[174:175], v137 offset:12288\n\t"
    "v_cvt_pk_bf16_f32 v67, v10, v11\n\t"
    "v_mfma_f32_16x16x32_bf16 a[64:67], a[160:163], v[16:19], a[64:67]\n\t"
    "v_exp_f32 v14, v14\n\t"
    "v_mfma_f32_16x16x32_bf16 a[64:67], a[164:167], v[20:23], a[64:67]\n\t"
    "ds_read_b128 a[148:151], v129 offset:28672\n\t"
    "v_cvt_pk_bf16_f32 v74, v12, v13\n\t"
    "v_mfma_f32_16x16x32_bf16 a[68:71], a[160:163], v[24:27], a[68:71]\n\t"
    "v_exp_f32 v15, v15\n\t"
    "v_mfma_f32_16x16x32_bf16 a[68:71], a[164:167], v[28:31], a[68:71]\n\t"
    "s_waitcnt lgkmcnt(3)\n\t"
    "buffer_load_dwordx4 v228, s[44:47], s51 offen offset:2048 lds\n\t"
    "v_mfma_f32_16x16x32_bf16 v[4:7], a[128:131], v[176:179], v[84:87]\n\t"
    "v_exp_f32 v0, v0\n\t"
    "v_mfma_f32_16x16x32_bf16 v[4:7], a[132:135], v[180:183], v[4:7]\n\t"
    "ds_read_b64_tr_b16 a[176:177], v138 offset:0\n\t"
    "v_cvt_pk_bf16_f32 v75, v14, v15\n\t"
    "v_mfma_f32_16x16x32_bf16 v[4:7], a[136:139], v[184:187], v[4:7]\n\t"
    "v_exp_f32 v1, v1\n\t"
    "v_mfma_f32_16x16x32_bf16 v[4:7], a[140:143], v[188:191], v[4:7]\n\t"
    "ds_read_b64_tr_b16 a[178:179], v138 offset:4096\n\t"
    "v_add_f32 v144, v144, v0\n\t"
    "v_mfma_f32_16x16x32_bf16 a[72:75], a[160:163], v[32:35], a[72:75]\n\t"
    "v_exp_f32 v2, v2\n\t"
    "v_mfma_f32_16x16x32_bf16 a[72:75], a[164:167], v[36:39], a[72:75]\n\t"
    "ds_read_b128 a[152:155], v130 offset:28672\n\t"
    "v_cvt_pk_bf16_f32 v52, v0, v1\n\t"
    "v_mfma_f32_16x16x32_bf16 a[76:79], a[160:163], v[40:43], a[76:79]\n\t"
    "v_exp_f32 v3, v3\n\t"
    "v_mfma_f32_16x16x32_bf16 a[76:79], a[164:167], v[44:47], a[76:79]\n\t"
    "s_waitcnt lgkmcnt(3)\n\t"
    "v_add_f32 v144, v144, v1\n\t"
    "v_add_f32 v144, v144, v2\n\t"
    "s_add_u32 s77, s72, 178176\n\t"
    "buffer_load_dwordx4 v[120:123], v255, s[68:71], s77 offen offset:0 sc1\n\t"
    "buffer_load_dwordx4 v[124:127], v255, s[68:71], s77 offen offset:1024 sc1\n\t"
    "v_mfma_f32_16x16x32_bf16 v[8:11], a[128:131], v[192:195], v[88:91]\n\t"
    "v_exp_f32 v4, v4\n\t"
    "v_mfma_f32_16x16x32_bf16 v[8:11], a[132:135], v[196:199], v[8:11]\n\t"
    "ds_read_b64_tr_b16 a[180:181], v138 offset:8192\n\t"
    "v_add_f32 v144, v144, v3\n\t"
    "v_mfma_f32_16x16x32_bf16 v[8:11], a[136:139], v[200:203], v[8:11]\n\t"
    "s_mov_b32 m0, s58\n\t"
    "v_exp_f32 v5, v5\n\t"
    "v_mfma_f32_16x16x32_bf16 v[8:11], a[140:143], v[204:207], v[8:11]\n\t"
    "ds_read_b64_tr_b16 a[182:183], v138 offset:12288\n\t"
    "v_cvt_pk_bf16_f32 v53, v2, v3\n\t"
    "v_mfma_f32_16x16x32_bf16 a[80:83], a[168:171], v[16:19], a[80:83]\n\t"
    "v_exp_f32 v6, v6\n\t"
    "v_mfma_f32_16x16x32_bf16 a[80:83], a[172:175], v[20:23], a[80:83]\n\t"
    "ds_read_b128 a[156:159], v131 offset:28672\n\t"
    "v_cvt_pk_bf16_f32 v60, v4, v5\n\t"
    "v_mfma_f32_16x16x32_bf16 a[84:87], a[168:171], v[24:27], a[84:87]\n\t"
    "v_exp_f32 v7, v7\n\t"
    "v_mfma_f32_16x16x32_bf16 a[84:87], a[172:175], v[28:31], a[84:87]\n\t"
    "s_waitcnt lgkmcnt(3)\n\t"
    "buffer_load_dwordx4 v229, s[44:47], s51 offen offset:3072 lds\n\t"
    "v_mfma_f32_16x16x32_bf16 v[12:15], a[128:131], v[208:211], v[92:95]\n\t"
    "s_waitcnt vmcnt(26)\n\t"
    "v_exp_f32 v8, v8\n\t"
    "v_mfma_f32_16x16x32_bf16 v[12:15], a[132:135], v[212:215], v[12:15]\n\t"
    "ds_read_b64_tr_b16 a[184:185], v139 offset:0\n\t"
    "v_cvt_pk_bf16_f32 v61, v6, v7\n\t"
    "v_mfma_f32_16x16x32_bf16 v[12:15], a[136:139], v[216:219], v[12:15]\n\t"
    "s_barrier\n\t"
    "v_exp_f32 v9, v9\n\t"
    "v_mfma_f32_16x16x32_bf16 v[12:15], a[140:143], v[220:223], v[12:15]\n\t"
    "ds_read_b64_tr_b16 a[186:187], v139 offset:4096\n\t"
    "v_add_f32 v146, v146, v8\n\t"
    "v_mfma_f32_16x16x32_bf16 a[88:91], a[168:171], v[32:35], a[88:91]\n\t"
    "v_exp_f32 v10, v10\n\t"
    "v_mfma_f32_16x16x32_bf16 a[88:91], a[172:175], v[36:39], a[88:91]\n\t"
    "ds_read_b128 a[128:131], v128 offset:32768\n\t"
    "v_cvt_pk_bf16_f32 v68, v8, v9\n\t"
    "v_mfma_f32_16x16x32_bf16 a[92:95], a[168:171], v[40:43], a[92:95]\n\t"
    "v_exp_f32 v11, v11\n\t"
    "v_mfma_f32_16x16x32_bf16 a[92:95], a[172:175], v[44:47], a[92:95]\n\t"
    "s_waitcnt lgkmcnt(3)\n\t"
    "v_add_f32 v146, v146, v9\n\t"
    "v_add_f32 v146, v146, v10\n\t"
    "v_mfma_f32_16x16x32_bf16 v[0:3], a[144:147], v[160:163], v[80:83]\n\t"
    "v_exp_f32 v12, v12\n\t"
    "v_mfma_f32_16x16x32_bf16 v[0:3], a[148:151], v[164:167], v[0:3]\n\t"
    "ds_read_b64_tr_b16 a[188:189], v139 offset:8192\n\t"
    "v_add_f32 v146, v146, v11\n\t"
    "v_mfma_f32_16x16x32_bf16 v[0:3], a[152:155], v[168:171], v[0:3]\n\t"
    "v_exp_f32 v13, v13\n\t"
    "v_mfma_f32_16x16x32_bf16 v[0:3], a[156:159], v[172:175], v[0:3]\n\t"
    "ds_read_b64_tr_b16 a[190:191], v139 offset:12288\n\t"
    "v_cvt_pk_bf16_f32 v69, v10, v11\n\t"
    "v_mfma_f32_16x16x32_bf16 a[96:99], a[176:179], v[16:19], a[96:99]\n\t"
    "v_exp_f32 v14, v14\n\t"
    "v_mfma_f32_16x16x32_bf16 a[96:99], a[180:183], v[20:23], a[96:99]\n\t"
    "ds_read_b128 a[132:135], v129 offset:32768\n\t"
    "v_cvt_pk_bf16_f32 v76, v12, v13\n\t"
    "v_mfma_f32_16x16x32_bf16 a[100:103], a[176:179], v[24:27], a[100:103]\n\t"
    "v_exp_f32 v15, v15\n\t"
    "v_mfma_f32_16x16x32_bf16 a[100:103], a[180:183], v[28:31], a[100:103]\n\t"
    "s_waitcnt lgkmcnt(3)\n\t"
    "s_add_u32 m0, s80, 0\n\t"
    "s_add_u32 s76, s93, 0\n\t"
    "buffer_load_dwordx4 v254, s[64:67], s76 offen lds\n\t"
    "v_mfma_f32_16x16x32_bf16 v[4:7], a[144:147], v[176:179], v[84:87]\n\t"
    "v_exp_f32 v0, v0\n\t"
    "v_mfma_f32_16x16x32_bf16 v[4:7], a[148:151], v[180:183], v[4:7]\n\t"
    "ds_read_b64_tr_b16 a[160:161], v132 offset:16384\n\t"
    "v_cvt_pk_bf16_f32 v77, v14, v15\n\t"
    "v_mfma_f32_16x16x32_bf16 v[4:7], a[152:155], v[184:187], v[4:7]\n\t"
    "v_exp_f32 v1, v1\n\t"
    "v_mfma_f32_16x16x32_bf16 v[4:7], a[156:159], v[188:191], v[4:7]\n\t"
    "ds_read_b64_tr_b16 a[162:163], v132 offset:20480\n\t"
    "v_add_f32 v144, v144, v0\n\t"
    "v_mfma_f32_16x16x32_bf16 a[104:107], a[176:179], v[32:35], a[104:107]\n\t"
    "v_exp_f32 v2, v2\n\t"
    "v_mfma_f32_16x16x32_bf16 a[104:107], a[180:183], v[36:39], a[104:107]\n\t"
    "ds_read_b128 a[136:139], v130 offset:32768\n\t"
    "v_cvt_pk_bf16_f32 v54, v0, v1\n\t"
    "v_mfma_f32_16x16x32_bf16 a[108:111], a[176:179], v[40:43], a[108:111]\n\t"
    "v_exp_f32 v3, v3\n\t"
    "v_mfma_f32_16x16x32_bf16 a[108:111], a[180:183], v[44:47], a[108:111]\n\t"
    "s_waitcnt lgkmcnt(3)\n\t"
    "v_add_f32 v144, v144, v1\n\t"
    "v_add_f32 v144, v144, v2\n\t"
    "s_add_u32 m0, s80, 4096\n\t"
    "s_add_u32 s76, s93, 16384\n\t"
    "buffer_load_dwordx4 v254, s[64:67], s76 offen lds\n\t"
    "s_add_u32 m0, s80, 8192\n\t"
    "s_add_u32 s76, s93, 32768\n\t"
    "buffer_load_dwordx4 v254, s[64:67], s76 offen lds\n\t"
    "s_add_u32 m0, s80, 12288\n\t"
    "s_add_u32 s76, s93, 49152\n\t"
    "buffer_load_dwordx4 v254, s[64:67], s76 offen lds\n\t"
    "v_mfma_f32_16x16x32_bf16 v[8:11], a[144:147], v[192:195], v[88:91]\n\t"
    "v_exp_f32 v4, v4\n\t"
    "v_mfma_f32_16x16x32_bf16 v[8:11], a[148:151], v[196:199], v[8:11]\n\t"
    "ds_read_b64_tr_b16 a[164:165], v132 offset:24576\n\t"
    "v_add_f32 v144, v144, v3\n\t"
    "v_mfma_f32_16x16x32_bf16 v[8:11], a[152:155], v[200:203], v[8:11]\n\t"
    "v_exp_f32 v5, v5\n\t"
    "v_mfma_f32_16x16x32_bf16 v[8:11], a[156:159], v[204:207], v[8:11]\n\t"
    "ds_read_b64_tr_b16 a[166:167], v132 offset:28672\n\t"
    "v_cvt_pk_bf16_f32 v55, v2, v3\n\t"
    "v_mfma_f32_16x16x32_bf16 a[112:115], a[184:187], v[16:19], a[112:115]\n\t"
    "v_exp_f32 v6, v6\n\t"
    "v_mfma_f32_16x16x32_bf16 a[112:115], a[188:191], v[20:23], a[112:115]\n\t"
    "ds_read_b128 a[140:143], v131 offset:32768\n\t"
    "v_cvt_pk_bf16_f32 v62, v4, v5\n\t"
    "v_mfma_f32_16x16x32_bf16 a[116:119], a[184:187], v[24:27], a[116:119]\n\t"
    "v_exp_f32 v7, v7\n\t"
    "v_mfma_f32_16x16x32_bf16 a[116:119], a[188:191], v[28:31], a[116:119]\n\t"
    "s_waitcnt lgkmcnt(3)\n\t"
    "v_mfma_f32_16x16x32_bf16 v[12:15], a[144:147], v[208:211], v[92:95]\n\t"
    "v_exp_f32 v8, v8\n\t"
    "v_mfma_f32_16x16x32_bf16 v[12:15], a[148:151], v[212:215], v[12:15]\n\t"
    "ds_read_b64_tr_b16 a[168:169], v133 offset:16384\n\t"
    "v_cvt_pk_bf16_f32 v63, v6, v7\n\t"
    "v_mfma_f32_16x16x32_bf16 v[12:15], a[152:155], v[216:219], v[12:15]\n\t"
    "v_exp_f32 v9, v9\n\t"
    "v_mfma_f32_16x16x32_bf16 v[12:15], a[156:159], v[220:223], v[12:15]\n\t"
    "ds_read_b64_tr_b16 a[170:171], v133 offset:20480\n\t"
    "v_add_f32 v146, v146, v8\n\t"
    "v_mfma_f32_16x16x32_bf16 a[120:123], a[184:187], v[32:35], a[120:123]\n\t"
    "v_exp_f32 v10, v10\n\t"
    "v_mfma_f32_16x16x32_bf16 a[120:123], a[188:191], v[36:39], a[120:123]\n\t"
    "ds_read_b128 a[144:147], v128 offset:36864\n\t"
    "v_cvt_pk_bf16_f32 v70, v8, v9\n\t"
    "v_mfma_f32_16x16x32_bf16 a[124:127], a[184:187], v[40:43], a[124:127]\n\t"
    "v_exp_f32 v11, v11\n\t"
    "v_mfma_f32_16x16x32_bf16 a[124:127], a[188:191], v[44:47], a[124:127]\n\t"
    "s_waitcnt lgkmcnt(3)\n\t"
    "v_add_f32 v146, v146, v9\n\t"
    "v_add_f32 v146, v146, v10\n\t"
    "s_waitcnt vmcnt(16)\n\t"
    "ds_read_b64_tr_b16 v[232:233], v230 offset:0\n\t"
    "ds_read_b64_tr_b16 v[234:235], v230 offset:2048\n\t"
    "ds_read_b64_tr_b16 v[236:237], v230 offset:4096\n\t"
    "ds_read_b64_tr_b16 v[238:239], v230 offset:6144\n\t"
    "v_mfma_f32_16x16x32_bf16 v[0:3], a[128:131], v[160:163], v[80:83]\n\t"
    "v_exp_f32 v12, v12\n\t"
    "v_mfma_f32_16x16x32_bf16 v[0:3], a[132:135], v[164:167], v[0:3]\n\t"
    "ds_read_b64_tr_b16 a[172:173], v133 offset:24576\n\t"
    "v_add_f32 v146, v146, v11\n\t"
    "v_mfma_f32_16x16x32_bf16 v[0:3], a[136:139], v[168:171], v[0:3]\n\t"
    "v_exp_f32 v13, v13\n\t"
    "v_mfma_f32_16x16x32_bf16 v[0:3], a[140:143], v[172:175], v[0:3]\n\t"
    "ds_read_b64_tr_b16 a[174:175], v133 offset:28672\n\t"
    "v_cvt_pk_bf16_f32 v71, v10, v11\n\t"
    "v_mfma_f32_16x16x32_bf16 a[0:3], a[160:163], v[48:51], a[0:3]\n\t"
    "v_exp_f32 v14, v14\n\t"
    "v_mfma_f32_16x16x32_bf16 a[0:3], a[164:167], v[52:55], a[0:3]\n\t"
    "ds_read_b128 a[148:151], v129 offset:36864\n\t"
    "v_cvt_pk_bf16_f32 v78, v12, v13\n\t"
    "v_mfma_f32_16x16x32_bf16 a[4:7], a[160:163], v[56:59], a[4:7]\n\t"
    "v_exp_f32 v15, v15\n\t"
    "v_mfma_f32_16x16x32_bf16 a[4:7], a[164:167], v[60:63], a[4:7]\n\t"
    "s_waitcnt lgkmcnt(3)\n\t"
    "v_mfma_f32_16x16x32_bf16 v[4:7], a[128:131], v[176:179], v[84:87]\n\t"
    "v_exp_f32 v0, v0\n\t"
    "v_mfma_f32_16x16x32_bf16 v[4:7], a[132:135], v[180:183], v[4:7]\n\t"
    "ds_read_b64_tr_b16 a[176:177], v134 offset:16384\n\t"
    "v_cvt_pk_bf16_f32 v79, v14, v15\n\t"
    "v_mfma_f32_16x16x32_bf16 v[4:7], a[136:139], v[184:187], v[4:7]\n\t"
    "v_exp_f32 v1, v1\n\t"
    "v_mfma_f32_16x16x32_bf16 v[4:7], a[140:143], v[188:191], v[4:7]\n\t"
    "ds_read_b64_tr_b16 a[178:179], v134 offset:20480\n\t"
    "v_add_f32 v144, v144, v0\n\t"
    "v_mfma_f32_16x16x32_bf16 a[8:11], a[160:163], v[64:67], a[8:11]\n\t"
    "v_exp_f32 v2, v2\n\t"
    "v_mfma_f32_16x16x32_bf16 a[8:11], a[164:167], v[68:71], a[8:11]\n\t"
    "ds_read_b128 a[152:155], v130 offset:36864\n\t"
    "v_cvt_pk_bf16_f32 v16, v0, v1\n\t"
    "v_mfma_f32_16x16x32_bf16 a[12:15], a[160:163], v[72:75], a[12:15]\n\t"
    "v_exp_f32 v3, v3\n\t"
    "v_mfma_f32_16x16x32_bf16 a[12:15], a[164:167], v[76:79], a[12:15]\n\t"
    "s_waitcnt lgkmcnt(3)\n\t"
    "v_add_f32 v144, v144, v1\n\t"
    "v_add_f32 v144, v144, v2\n\t"
    "v_mfma_f32_16x16x32_bf16 v[148:151], v[140:143], v[56:59], v[148:151]\n\t"
    "v_mfma_f32_16x16x32_bf16 v[148:151], v[140:143], v[60:63], v[148:151]\n\t"
    "s_waitcnt vmcnt(5)\n\t"
    "v_mfma_f32_16x16x32_bf16 a[192:195], v[232:235], v[96:99], a[192:195]\n\t"
    "ds_read_b64_tr_b16 v[240:241], v231 offset:0\n\t"
    "v_mfma_f32_16x16x32_bf16 a[192:195], v[236:239], v[100:103], a[192:195]\n\t"
    "ds_read_b64_tr_b16 v[242:243], v231 offset:2048\n\t"
    "v_mfma_f32_16x16x32_bf16 a[200:203], v[232:235], v[104:107], a[200:203]\n\t"
    "ds_read_b64_tr_b16 v[244:245], v231 offset:4096\n\t"
    "v_mfma_f32_16x16x32_bf16 a[200:203], v[236:239], v[108:111], a[200:203]\n\t"
    "ds_read_b64_tr_b16 v[246:247], v231 offset:6144\n\t"
    "v_mfma_f32_16x16x32_bf16 a[208:211], v[232:235], v[112:115], a[208:211]\n\t"
    "v_mfma_f32_16x16x32_bf16 a[208:211], v[236:239], v[116:119], a[208:211]\n\t"
    "v_mfma_f32_16x16x32_bf16 a[216:219], v[232:235], v[120:123], a[216:219]\n\t"
    "v_mfma_f32_16x16x32_bf16 a[216:219], v[236:239], v[124:127], a[216:219]\n\t"
    "s_waitcnt lgkmcnt(0)\n\t"
    "v_mfma_f32_16x16x32_bf16 a[196:199], v[240:243], v[96:99], a[196:199]\n\t"
    "ds_read_b64_tr_b16 v[232:233], v248 offset:0\n\t"
    "v_mfma_f32_16x16x32_bf16 a[196:199], v[244:247], v[100:103], a[196:199]\n\t"
    "ds_read_b64_tr_b16 v[234:235], v248 offset:2048\n\t"
    "v_mfma_f32_16x16x32_bf16 a[204:207], v[240:243], v[104:107], a[204:207]\n\t"
    "ds_read_b64_tr_b16 v[236:237], v248 offset:4096\n\t"
    "v_mfma_f32_16x16x32_bf16 a[204:207], v[244:247], v[108:111], a[204:207]\n\t"
    "ds_read_b64_tr_b16 v[238:239], v248 offset:6144\n\t"
    "v_mfma_f32_16x16x32_bf16 a[212:215], v[240:243], v[112:115], a[212:215]\n\t"
    "v_mfma_f32_16x16x32_bf16 a[212:215], v[244:247], v[116:119], a[212:215]\n\t"
    "v_mfma_f32_16x16x32_bf16 a[220:223], v[240:243], v[120:123], a[220:223]\n\t"
    "v_mfma_f32_16x16x32_bf16 a[220:223], v[244:247], v[124:127], a[220:223]\n\t"
    "s_waitcnt lgkmcnt(0)\n\t"
    "v_mfma_f32_16x16x32_bf16 a[224:227], v[232:235], v[96:99], a[224:227]\n\t"
    "ds_read_b64_tr_b16 v[240:241], v249 offset:0\n\t"
    "v_mfma_f32_16x16x32_bf16 a[224:227], v[236:239], v[100:103], a[224:227]\n\t"
    "ds_read_b64_tr_b16 v[242:243], v249 offset:2048\n\t"
    "v_mfma_f32_16x16x32_bf16 a[232:235], v[232:235], v[104:107], a[232:235]\n\t"
    "ds_read_b64_tr_b16 v[244:245], v249 offset:4096\n\t"
    "v_mfma_f32_16x16x32_bf16 a[232:235], v[236:239], v[108:111], a[232:235]\n\t"
    "ds_read_b64_tr_b16 v[246:247], v249 offset:6144\n\t"
    "v_mfma_f32_16x16x32_bf16 a[240:243], v[232:235], v[112:115], a[240:243]\n\t"
    "v_mfma_f32_16x16x32_bf16 a[240:243], v[236:239], v[116:119], a[240:243]\n\t"
    "v_mfma_f32_16x16x32_bf16 a[248:251], v[232:235], v[120:123], a[248:251]\n\t"
    "v_mfma_f32_16x16x32_bf16 a[248:251], v[236:239], v[124:127], a[248:251]\n\t"
    "s_waitcnt lgkmcnt(0)\n\t"
    "v_mfma_f32_16x16x32_bf16 a[228:231], v[240:243], v[96:99], a[228:231]\n\t"
    "s_add_u32 m0, s95, 131072\n\t"
    "s_add_u32 s76, s74, 262144\n\t"
    "buffer_load_dwordx4 v159, s[60:63], s76 offen lds\n\t"
    "v_mfma_f32_16x16x32_bf16 a[228:231], v[244:247], v[100:103], a[228:231]\n\t"
    "s_add_u32 m0, s95, 132096\n\t"
    "s_add_u32 s76, s74, 270336\n\t"
    "buffer_load_dwordx4 v159, s[60:63], s76 offen lds\n\t"
    "v_mfma_f32_16x16x32_bf16 a[236:239], v[240:243], v[104:107], a[236:239]\n\t"
    "s_add_u32 m0, s95, 133120\n\t"
    "s_add_u32 s76, s74, 278528\n\t"
    "buffer_load_dwordx4 v159, s[60:63], s76 offen lds\n\t"
    "v_mfma_f32_16x16x32_bf16 a[236:239], v[244:247], v[108:111], a[236:239]\n\t"
    "s_add_u32 m0, s95, 134144\n\t"
    "s_add_u32 s76, s74, 286720\n\t"
    "buffer_load_dwordx4 v159, s[60:63], s76 offen lds\n\t"
    "v_mfma_f32_16x16x32_bf16 a[244:247], v[240:243], v[112:115], a[244:247]\n\t"
    "s_add_u32 m0, s95, 135168\n\t"
    "s_add_u32 s76, s74, 294912\n\t"
    "buffer_load_dwordx4 v159, s[60:63], s76 offen lds\n\t"
    "v_mfma_f32_16x16x32_bf16 a[244:247], v[244:247], v[116:119], a[244:247]\n\t"
    "s_add_u32 m0, s95, 136192\n\t"
    "s_add_u32 s76, s74, 303104\n\t"
    "buffer_load_dwordx4 v159, s[60:63], s76 offen lds\n\t"
    "v_mfma_f32_16x16x32_bf16 a[252:255], v[240:243], v[120:123], a[252:255]\n\t"
    "s_add_u32 m0, s95, 137216\n\t"
    "s_add_u32 s76, s74, 311296\n\t"
    "buffer_load_dwordx4 v159, s[60:63], s76 offen lds\n\t"
    "v_mfma_f32_16x16x32_bf16 a[252:255], v[244:247], v[124:127], a[252:255]\n\t"
    "s_add_u32 m0, s95, 138240\n\t"
    "s_add_u32 s76, s74, 319488\n\t"
    "buffer_load_dwordx4 v159, s[60:63], s76 offen lds\n\t"
    "v_mfma_f32_16x16x32_bf16 v[8:11], a[128:131], v[192:195], v[88:91]\n\t"
    "v_exp_f32 v4, v4\n\t"
    "v_mfma_f32_16x16x32_bf16 v[8:11], a[132:135], v[196:199], v[8:11]\n\t"
    "ds_read_b64_tr_b16 a[180:181], v134 offset:24576\n\t"
    "v_add_f32 v144, v144, v3\n\t"
    "v_mfma_f32_16x16x32_bf16 v[8:11], a[136:139], v[200:203], v[8:11]\n\t"
    "v_exp_f32 v5, v5\n\t"
    "v_mfma_f32_16x16x32_bf16 v[8:11], a[140:143], v[204:207], v[8:11]\n\t"
    "ds_read_b64_tr_b16 a[182:183], v134 offset:28672\n\t"
    "v_cvt_pk_bf16_f32 v17, v2, v3\n\t"
    "v_mfma_f32_16x16x32_bf16 a[16:19], a[168:171], v[48:51], a[16:19]\n\t"
    "v_exp_f32 v6, v6\n\t"
    "v_mfma_f32_16x16x32_bf16 a[16:19], a[172:175], v[52:55], a[16:19]\n\t"
    "ds_read_b128 a[156:159], v131 offset:36864\n\t"
    "v_cvt_pk_bf16_f32 v24, v4, v5\n\t"
    "v_mfma_f32_16x16x32_bf16 a[20:23], a[168:171], v[56:59], a[20:23]\n\t"
    "v_exp_f32 v7, v7\n\t"
    "v_mfma_f32_16x16x32_bf16 a[20:23], a[172:175], v[60:63], a[20:23]\n\t"
    "s_waitcnt lgkmcnt(3)\n\t"
    "s_add_u32 s77, s72, 8192\n\t"
    "buffer_load_dwordx4 v[96:99], v255, s[68:71], s77 offen offset:0 sc1\n\t"
    "v_mfma_f32_16x16x32_bf16 v[12:15], a[128:131], v[208:211], v[92:95]\n\t"
    "v_exp_f32 v8, v8\n\t"
    "v_mfma_f32_16x16x32_bf16 v[12:15], a[132:135], v[212:215], v[12:15]\n\t"
    "ds_read_b64_tr_b16 a[184:185], v135 offset:16384\n\t"
    "v_cvt_pk_bf16_f32 v25, v6, v7\n\t"
    "v_mfma_f32_16x16x32_bf16 v[12:15], a[136:139], v[216:219], v[12:15]\n\t"
    "v_exp_f32 v9, v9\n\t"
    "v_mfma_f32_16x16x32_bf16 v[12:15], a[140:143], v[220:223], v[12:15]\n\t"
    "ds_read_b64_tr_b16 a[186:187], v135 offset:20480\n\t"
    "v_add_f32 v146, v146, v8\n\t"
    "v_mfma_f32_16x16x32_bf16 a[24:27], a[168:171], v[64:67], a[24:27]\n\t"
    "v_exp_f32 v10, v10\n\t"
    "v_mfma_f32_16x16x32_bf16 a[24:27], a[172:175], v[68:71], a[24:27]\n\t"
    "ds_read_b128 a[128:131], v128 offset:40960\n\t"
    "v_cvt_pk_bf16_f32 v32, v8, v9\n\t"
    "v_mfma_f32_16x16x32_bf16 a[28:31], a[168:171], v[72:75], a[28:31]\n\t"
    "v_exp_f32 v11, v11\n\t"
    "v_mfma_f32_16x16x32_bf16 a[28:31], a[172:175], v[76:79], a[28:31]\n\t"
    "s_waitcnt lgkmcnt(3)\n\t"
    "v_add_f32 v146, v146, v9\n\t"
    "v_add_f32 v146, v146, v10\n\t"
    "buffer_load_dwordx4 v[100:103], v255, s[68:71], s77 offen offset:1024 sc1\n\t"
    "s_add_u32 s77, s72, 65536\n\t"
    "buffer_load_dwordx4 v[104:107], v255, s[68:71], s77 offen offset:0 sc1\n\t"
    "buffer_load_dwordx4 v[108:111], v255, s[68:71], s77 offen offset:1024 sc1\n\t"
    "v_mfma_f32_16x16x32_bf16 v[152:155], v[140:143], v[72:75], v[152:155]\n\t"
    "v_mfma_f32_16x16x32_bf16 v[152:155], v[140:143], v[76:79], v[152:155]\n\t"
    "v_mfma_f32_16x16x32_bf16 v[0:3], a[144:147], v[160:163], v[80:83]\n\t"
    "v_exp_f32 v12, v12\n\t"
    "v_mfma_f32_16x16x32_bf16 v[0:3], a[148:151], v[164:167], v[0:3]\n\t"
    "ds_read_b64_tr_b16 a[188:189], v135 offset:24576\n\t"
    "v_add_f32 v146, v146, v11\n\t"
    "v_mfma_f32_16x16x32_bf16 v[0:3], a[152:155], v[168:171], v[0:3]\n\t"
    "s_mov_b32 m0, s59\n\t"
    "v_exp_f32 v13, v13\n\t"
    "v_mfma_f32_16x16x32_bf16 v[0:3], a[156:159], v[172:175], v[0:3]\n\t"
    "ds_read_b64_tr_b16 a[190:191], v135 offset:28672\n\t"
    "v_cvt_pk_bf16_f32 v33, v10, v11\n\t"
    "v_mfma_f32_16x16x32_bf16 a[32:35], a[176:179], v[48:51], a[32:35]\n\t"
    "s_add_u32 s51, s51, 16384\n\t"
    "v_exp_f32 v14, v14\n\t"
    "v_mfma_f32_16x16x32_bf16 a[32:35], a[180:183], v[52:55], a[32:35]\n\t"
    "ds_read_b128 a[132:135], v129 offset:40960\n\t"
    "v_cvt_pk_bf16_f32 v40, v12, v13\n\t"
    "v_mfma_f32_16x16x32_bf16 a[36:39], a[176:179], v[56:59], a[36:39]\n\t"
    "v_exp_f32 v15, v15\n\t"
    "v_mfma_f32_16x16x32_bf16 a[36:39], a[180:183], v[60:63], a[36:39]\n\t"
    "s_waitcnt lgkmcnt(3)\n\t"
    "buffer_load_dwordx4 v228, s[44:47], s51 offen offset:0 lds\n\t"
    "v_mfma_f32_16x16x32_bf16 v[4:7], a[144:147], v[176:179], v[84:87]\n\t"
    "v_exp_f32 v0, v0\n\t"
    "v_mfma_f32_16x16x32_bf16 v[4:7], a[148:151], v[180:183], v[4:7]\n\t"
    "ds_read_b64_tr_b16 a[160:161], v136 offset:16384\n\t"
    "v_cvt_pk_bf16_f32 v41, v14, v15\n\t"
    "v_mfma_f32_16x16x32_bf16 v[4:7], a[152:155], v[184:187], v[4:7]\n\t"
    "v_exp_f32 v1, v1\n\t"
    "v_mfma_f32_16x16x32_bf16 v[4:7], a[156:159], v[188:191], v[4:7]\n\t"
    "ds_read_b64_tr_b16 a[162:163], v136 offset:20480\n\t"
    "v_add_f32 v144, v144, v0\n\t"
    "v_mfma_f32_16x16x32_bf16 a[40:43], a[176:179], v[64:67], a[40:43]\n\t"
    "v_exp_f32 v2, v2\n\t"
    "v_mfma_f32_16x16x32_bf16 a[40:43], a[180:183], v[68:71], a[40:43]\n\t"
    "ds_read_b128 a[136:139], v130 offset:40960\n\t"
    "v_cvt_pk_bf16_f32 v18, v0, v1\n\t"
    "v_mfma_f32_16x16x32_bf16 a[44:47], a[176:179], v[72:75], a[44:47]\n\t"
    "v_exp_f32 v3, v3\n\t"
    "v_mfma_f32_16x16x32_bf16 a[44:47], a[180:183], v[76:79], a[44:47]\n\t"
    "s_waitcnt lgkmcnt(3)\n\t"
    "v_add_f32 v144, v144, v1\n\t"
    "v_add_f32 v144, v144, v2\n\t"
    "s_add_u32 s77, s72, 122880\n\t"
    "buffer_load_dwordx4 v[112:115], v255, s[68:71], s77 offen offset:0 sc1\n\t"
    "buffer_load_dwordx4 v[116:119], v255, s[68:71], s77 offen offset:1024 sc1\n\t"
    "s_add_u32 s77, s72, 180224\n\t"
    "buffer_load_dwordx4 v[120:123], v255, s[68:71], s77 offen offset:0 sc1\n\t"
    "v_mfma_f32_16x16x32_bf16 v[8:11], a[144:147], v[192:195], v[88:91]\n\t"
    "v_exp_f32 v4, v4\n\t"
    "v_mfma_f32_16x16x32_bf16 v[8:11], a[148:151], v[196:199], v[8:11]\n\t"
    "ds_read_b64_tr_b16 a[164:165], v136 offset:24576\n\t"
    "v_add_f32 v144, v144, v3\n\t"
    "v_mfma_f32_16x16x32_bf16 v[8:11], a[152:155], v[200:203], v[8:11]\n\t"
    "s_mov_b32 m0, s59\n\t"
    "v_exp_f32 v5, v5\n\t"
    "v_mfma_f32_16x16x32_bf16 v[8:11], a[156:159], v[204:207], v[8:11]\n\t"
    "ds_read_b64_tr_b16 a[166:167], v136 offset:28672\n\t"
    "v_cvt_pk_bf16_f32 v19, v2, v3\n\t"
    "v_mfma_f32_16x16x32_bf16 a[48:51], a[184:187], v[48:51], a[48:51]\n\t"
    "v_exp_f32 v6, v6\n\t"
    "v_mfma_f32_16x16x32_bf16 a[48:51], a[188:191], v[52:55], a[48:51]\n\t"
    "ds_read_b128 a[140:143], v131 offset:40960\n\t"
    "v_cvt_pk_bf16_f32 v26, v4, v5\n\t"
    "v_mfma_f32_16x16x32_bf16 a[52:55], a[184:187], v[56:59], a[52:55]\n\t"
    "v_exp_f32 v7, v7\n\t"
    "v_mfma_f32_16x16x32_bf16 a[52:55], a[188:191], v[60:63], a[52:55]\n\t"
    "s_waitcnt lgkmcnt(3)\n\t"
    "buffer_load_dwordx4 v229, s[44:47], s51 offen offset:1024 lds\n\t"
    "v_mfma_f32_16x16x32_bf16 v[12:15], a[144:147], v[208:211], v[92:95]\n\t"
    "v_exp_f32 v8, v8\n\t"
    "v_mfma_f32_16x16x32_bf16 v[12:15], a[148:151], v[212:215], v[12:15]\n\t"
    "ds_read_b64_tr_b16 a[168:169], v137 offset:16384\n\t"
    "v_cvt_pk_bf16_f32 v27, v6, v7\n\t"
    "v_mfma_f32_16x16x32_bf16 v[12:15], a[152:155], v[216:219], v[12:15]\n\t"
    "v_exp_f32 v9, v9\n\t"
    "v_mfma_f32_16x16x32_bf16 v[12:15], a[156:159], v[220:223], v[12:15]\n\t"
    "ds_read_b64_tr_b16 a[170:171], v137 offset:20480\n\t"
    "v_add_f32 v146, v146, v8\n\t"
    "v_mfma_f32_16x16x32_bf16 a[56:59], a[184:187], v[64:67], a[56:59]\n\t"
    "v_exp_f32 v10, v10\n\t"
    "v_mfma_f32_16x16x32_bf16 a[56:59], a[188:191], v[68:71], a[56:59]\n\t"
    "ds_read_b128 a[144:147], v128 offset:45056\n\t"
    "v_cvt_pk_bf16_f32 v34, v8, v9\n\t"
    "v_mfma_f32_16x16x32_bf16 a[60:63], a[184:187], v[72:75], a[60:63]\n\t"
    "v_exp_f32 v11, v11\n\t"
    "v_mfma_f32_16x16x32_bf16 a[60:63], a[188:191], v[76:79], a[60:63]\n\t"
    "s_waitcnt lgkmcnt(3)\n\t"
    "v_add_f32 v146, v146, v9\n\t"
    "v_add_f32 v146, v146, v10\n\t"
    "buffer_load_dwordx4 v[124:127], v255, s[68:71], s77 offen offset:1024 sc1\n\t"
    "v_mfma_f32_16x16x32_bf16 v[0:3], a[128:131], v[160:163], v[80:83]\n\t"
    "v_exp_f32 v12, v12\n\t"
    "v_mfma_f32_16x16x32_bf16 v[0:3], a[132:135], v[164:167], v[0:3]\n\t"
    "ds_read_b64_tr_b16 a[172:173], v137 offset:24576\n\t"
    "v_add_f32 v146, v146, v11\n\t"
    "v_mfma_f32_16x16x32_bf16 v[0:3], a[136:139], v[168:171], v[0:3]\n\t"
    "s_mov_b32 m0, s59\n\t"
    "v_exp_f32 v13, v13\n\t"
    "v_mfma_f32_16x16x32_bf16 v[0:3], a[140:143], v[172:175], v[0:3]\n\t"
    "ds_read_b64_tr_b16 a[174:175], v137 offset:28672\n\t"
    "v_cvt_pk_bf16_f32 v35, v10, v11\n\t"
    "v_mfma_f32_16x16x32_bf16 a[64:67], a[160:163], v[48:51], a[64:67]\n\t"
    "v_exp_f32 v14, v14\n\t"
    "v_mfma_f32_16x16x32_bf16 a[64:67], a[164:167], v[52:55], a[64:67]\n\t"
    "ds_read_b128 a[148:151], v129 offset:45056\n\t"
    "v_cvt_pk_bf16_f32 v42, v12, v13\n\t"
    "v_mfma_f32_16x16x32_bf16 a[68:71], a[160:163], v[56:59], a[68:71]\n\t"
    "v_exp_f32 v15, v15\n\t"
    "v_mfma_f32_16x16x32_bf16 a[68:71], a[164:167], v[60:63], a[68:71]\n\t"
    "s_waitcnt lgkmcnt(3)\n\t"
    "buffer_load_dwordx4 v228, s[44:47], s51 offen offset:2048 lds\n\t"
    "v_mfma_f32_16x16x32_bf16 v[4:7], a[128:131], v[176:179], v[84:87]\n\t"
    "v_exp_f32 v0, v0\n\t"
    "v_mfma_f32_16x16x32_bf16 v[4:7], a[132:135], v[180:183], v[4:7]\n\t"
    "ds_read_b64_tr_b16 a[176:177], v138 offset:16384\n\t"
    "v_cvt_pk_bf16_f32 v43, v14, v15\n\t"
    "v_mfma_f32_16x16x32_bf16 v[4:7], a[136:139], v[184:187], v[4:7]\n\t"
    "v_exp_f32 v1, v1\n\t"
    "v_mfma_f32_16x16x32_bf16 v[4:7], a[140:143], v[188:191], v[4:7]\n\t"
    "ds_read_b64_tr_b16 a[178:179], v138 offset:20480\n\t"
    "v_add_f32 v144, v144, v0\n\t"
    "v_mfma_f32_16x16x32_bf16 a[72:75], a[160:163], v[64:67], a[72:75]\n\t"
    "v_exp_f32 v2, v2\n\t"
    "v_mfma_f32_16x16x32_bf16 a[72:75], a[164:167], v[68:71], a[72:75]\n\t"
    "ds_read_b128 a[152:155], v130 offset:45056\n\t"
    "v_cvt_pk_bf16_f32 v20, v0, v1\n\t"
    "v_mfma_f32_16x16x32_bf16 a[76:79], a[160:163], v[72:75], a[76:79]\n\t"
    "v_exp_f32 v3, v3\n\t"
    "v_mfma_f32_16x16x32_bf16 a[76:79], a[164:167], v[76:79], a[76:79]\n\t"
    "s_waitcnt lgkmcnt(3)\n\t"
    "v_add_f32 v144, v144, v1\n\t"
    "v_add_f32 v144, v144, v2\n\t"
    "v_mfma_f32_16x16x32_bf16 v[8:11], a[128:131], v[192:195], v[88:91]\n\t"
    "v_exp_f32 v4, v4\n\t"
    "v_mfma_f32_16x16x32_bf16 v[8:11], a[132:135], v[196:199], v[8:11]\n\t"
    "ds_read_b64_tr_b16 a[180:181], v138 offset:24576\n\t"
    "v_add_f32 v144, v144, v3\n\t"
    "v_mfma_f32_16x16x32_bf16 v[8:11], a[136:139], v[200:203], v[8:11]\n\t"
    "s_mov_b32 m0, s59\n\t"
    "v_exp_f32 v5, v5\n\t"
    "v_mfma_f32_16x16x32_bf16 v[8:11], a[140:143], v[204:207], v[8:11]\n\t"
    "ds_read_b64_tr_b16 a[182:183], v138 offset:28672\n\t"
    "v_cvt_pk_bf16_f32 v21, v2, v3\n\t"
    "v_mfma_f32_16x16x32_bf16 a[80:83], a[168:171], v[48:51], a[80:83]\n\t"
    "v_exp_f32 v6, v6\n\t"
    "v_mfma_f32_16x16x32_bf16 a[80:83], a[172:175], v[52:55], a[80:83]\n\t"
    "ds_read_b128 a[156:159], v131 offset:45056\n\t"
    "v_cvt_pk_bf16_f32 v28, v4, v5\n\t"
    "v_mfma_f32_16x16x32_bf16 a[84:87], a[168:171], v[56:59], a[84:87]\n\t"
    "v_exp_f32 v7, v7\n\t"
    "v_mfma_f32_16x16x32_bf16 a[84:87], a[172:175], v[60:63], a[84:87]\n\t"
    "s_waitcnt lgkmcnt(3)\n\t"
    "buffer_load_dwordx4 v229, s[44:47], s51 offen offset:3072 lds\n\t"
    "v_mfma_f32_16x16x32_bf16 v[12:15], a[128:131], v[208:211], v[92:95]\n\t"
    "s_waitcnt vmcnt(24)\n\t"
    "v_exp_f32 v8, v8\n\t"
    "v_mfma_f32_16x16x32_bf16 v[12:15], a[132:135], v[212:215], v[12:15]\n\t"
    "ds_read_b64_tr_b16 a[184:185], v139 offset:16384\n\t"
    "v_cvt_pk_bf16_f32 v29, v6, v7\n\t"
    "v_mfma_f32_16x16x32_bf16 v[12:15], a[136:139], v[216:219], v[12:15]\n\t"
    "s_barrier\n\t"
    "v_exp_f32 v9, v9\n\t"
    "v_mfma_f32_16x16x32_bf16 v[12:15], a[140:143], v[220:223], v[12:15]\n\t"
    "ds_read_b64_tr_b16 a[186:187], v139 offset:20480\n\t"
    "v_add_f32 v146, v146, v8\n\t"
    "v_mfma_f32_16x16x32_bf16 a[88:91], a[168:171], v[64:67], a[88:91]\n\t"
    "v_exp_f32 v10, v10\n\t"
    "v_mfma_f32_16x16x32_bf16 a[88:91], a[172:175], v[68:71], a[88:91]\n\t"
    "ds_read_b128 a[128:131], v128 offset:49152\n\t"
    "v_cvt_pk_bf16_f32 v36, v8, v9\n\t"
    "v_mfma_f32_16x16x32_bf16 a[92:95], a[168:171], v[72:75], a[92:95]\n\t"
    "v_exp_f32 v11, v11\n\t"
    "v_mfma_f32_16x16x32_bf16 a[92:95], a[172:175], v[76:79], a[92:95]\n\t"
    "s_waitcnt lgkmcnt(3)\n\t"
    "v_add_f32 v146, v146, v9\n\t"
    "v_add_f32 v146, v146, v10\n\t"
    "v_mfma_f32_16x16x32_bf16 v[0:3], a[144:147], v[160:163], v[80:83]\n\t"
    "v_exp_f32 v12, v12\n\t"
    "v_mfma_f32_16x16x32_bf16 v[0:3], a[148:151], v[164:167], v[0:3]\n\t"
    "ds_read_b64_tr_b16 a[188:189], v139 offset:24576\n\t"
    "v_add_f32 v146, v146, v11\n\t"
    "v_mfma_f32_16x16x32_bf16 v[0:3], a[152:155], v[168:171], v[0:3]\n\t"
    "v_exp_f32 v13, v13\n\t"
    "v_mfma_f32_16x16x32_bf16 v[0:3], a[156:159], v[172:175], v[0:3]\n\t"
    "ds_read_b64_tr_b16 a[190:191], v139 offset:28672\n\t"
    "v_cvt_pk_bf16_f32 v37, v10, v11\n\t"
    "v_mfma_f32_16x16x32_bf16 a[96:99], a[176:179], v[48:51], a[96:99]\n\t"
    "v_exp_f32 v14, v14\n\t"
    "v_mfma_f32_16x16x32_bf16 a[96:99], a[180:183], v[52:55], a[96:99]\n\t"
    "ds_read_b128 a[132:135], v129 offset:49152\n\t"
    "v_cvt_pk_bf16_f32 v44, v12, v13\n\t"
    "v_mfma_f32_16x16x32_bf16 a[100:103], a[176:179], v[56:59], a[100:103]\n\t"
    "v_exp_f32 v15, v15\n\t"
    "s_add_u32 s53, %[h], 1\n\t"
    "s_and_b32 s53, s53, 3\n\t"
    "s_lshl_b32 s53, s53, 17\n\t"
    "s_add_u32 s53, s53, s75\n\t"
    "s_add_u32 s54, %[h], 2\n\t"
    "s_and_b32 s54, s54, 3\n\t"
    "s_lshl_b32 s54, s54, 17\n\t"
    "s_add_u32 s54, s54, s75\n\t"
    "s_add_u32 s55, %[h], 3\n\t"
    "s_and_b32 s55, s55, 3\n\t"
    "s_lshl_b32 s55, s55, 17\n\t"
    "s_add_u32 s55, s55, s75\n\t"
    "s_add_u32 s56, %[h], 1\n\t"
    "s_and_b32 s56, s56, 3\n\t"
    "s_lshl_b32 s56, s56, 12\n\t"
    "s_add_u32 s56, s56, s73\n\t"
    "s_add_u32 s57, %[h], 2\n\t"
    "s_and_b32 s57, s57, 3\n\t"
    "s_lshl_b32 s57, s57, 12\n\t"
    "s_add_u32 s57, s57, s73\n\t"
    "s_add_u32 s58, %[h], 3\n\t"
    "s_and_b32 s58, s58, 3\n\t"
    "s_lshl_b32 s58, s58, 12\n\t"
    "s_add_u32 s58, s58, s73\n\t"
    "v_mfma_f32_16x16x32_bf16 a[100:103], a[180:183], v[60:63], a[100:103]\n\t"
    "s_waitcnt lgkmcnt(3)\n\t"
    "s_add_u32 m0, s80, 16384\n\t"
    "s_add_u32 s76, s93, 65536\n\t"
    "buffer_load_dwordx4 v254, s[64:67], s76 offen lds\n\t"
    "v_mfma_f32_16x16x32_bf16 v[4:7], a[144:147], v[176:179], v[84:87]\n\t"
    "v_exp_f32 v0, v0\n\t"
    "v_mfma_f32_16x16x32_bf16 v[4:7], a[148:151], v[180:183], v[4:7]\n\t"
    "ds_read_b64_tr_b16 a[160:161], v132 offset:32768\n\t"
    "v_cvt_pk_bf16_f32 v45, v14, v15\n\t"
    "v_mfma_f32_16x16x32_bf16 v[4:7], a[152:155], v[184:187], v[4:7]\n\t"
    "v_exp_f32 v1, v1\n\t"
    "v_mfma_f32_16x16x32_bf16 v[4:7], a[156:159], v[188:191], v[4:7]\n\t"
    "ds_read_b64_tr_b16 a[162:163], v132 offset:36864\n\t"
    "v_add_f32 v144, v144, v0\n\t"
    "v_mfma_f32_16x16x32_bf16 a[104:107], a[176:179], v[64:67], a[104:107]\n\t"
    "v_exp_f32 v2, v2\n\t"
    "v_mfma_f32_16x16x32_bf16 a[104:107], a[180:183], v[68:71], a[104:107]\n\t"
    "ds_read_b128 a[136:139], v130 offset:49152\n\t"
    "v_cvt_pk_bf16_f32 v22, v0, v1\n\t"
    "v_mfma_f32_16x16x32_bf16 a[108:111], a[176:179], v[72:75], a[108:111]\n\t"
    "v_exp_f32 v3, v3\n\t"
    "v_mfma_f32_16x16x32_bf16 a[108:111], a[180:183], v[76:79], a[108:111]\n\t"
    "s_waitcnt lgkmcnt(3)\n\t"
    "v_add_f32 v144, v144, v1\n\t"
    "v_add_f32 v144, v144, v2\n\t"
    "s_add_u32 m0, s80, 20480\n\t"
    "s_add_u32 s76, s93, 81920\n\t"
    "buffer_load_dwordx4 v254, s[64:67], s76 offen lds\n\t"
    "s_add_u32 m0, s80, 24576\n\t"
    "s_add_u32 s76, s93, 98304\n\t"
    "buffer_load_dwordx4 v254, s[64:67], s76 offen lds\n\t"
    "s_add_u32 m0, s80, 28672\n\t"
    "s_add_u32 s76, s93, 114688\n\t"
    "buffer_load_dwordx4 v254, s[64:67], s76 offen lds\n\t"
    "s_waitcnt vmcnt(16)\n\t"
    "ds_read_b64_tr_b16 v[232:233], v230 offset:0\n\t"
    "ds_read_b64_tr_b16 v[234:235], v230 offset:2048\n\t"
    "ds_read_b64_tr_b16 v[236:237], v230 offset:4096\n\t"
    "ds_read_b64_tr_b16 v[238:239], v230 offset:6144\n\t"
    "v_mfma_f32_16x16x32_bf16 v[8:11], a[144:147], v[192:195], v[88:91]\n\t"
    "v_exp_f32 v4, v4\n\t"
    "v_mfma_f32_16x16x32_bf16 v[8:11], a[148:151], v[196:199], v[8:11]\n\t"
    "ds_read_b64_tr_b16 a[164:165], v132 offset:40960\n\t"
    "v_add_f32 v144, v144, v3\n\t"
    "v_mfma_f32_16x16x32_bf16 v[8:11], a[152:155], v[200:203], v[8:11]\n\t"
    "v_exp_f32 v5, v5\n\t"
    "v_mfma_f32_16x16x32_bf16 v[8:11], a[156:159], v[204:207], v[8:11]\n\t"
    "ds_read_b64_tr_b16 a[166:167], v132 offset:45056\n\t"
    "v_cvt_pk_bf16_f32 v23, v2, v3\n\t"
    "v_mfma_f32_16x16x32_bf16 a[112:115], a[184:187], v[48:51], a[112:115]\n\t"
    "v_exp_f32 v6, v6\n\t"
    "v_mfma_f32_16x16x32_bf16 a[112:115], a[188:191], v[52:55], a[112:115]\n\t"
    "ds_read_b128 a[140:143], v131 offset:49152\n\t"
    "v_cvt_pk_bf16_f32 v30, v4, v5\n\t"
    "v_mfma_f32_16x16x32_bf16 a[116:119], a[184:187], v[56:59], a[116:119]\n\t"
    "v_exp_f32 v7, v7\n\t"
    "v_mfma_f32_16x16x32_bf16 a[116:119], a[188:191], v[60:63], a[116:119]\n\t"
    "s_waitcnt lgkmcnt(3)\n\t"
    "s_add_u32 m0, s80, 65536\n\t"
    "s_add_u32 s76, s53, 0\n\t"
    "buffer_load_dwordx4 v254, s[64:67], s76 offen lds\n\t"
    "v_mfma_f32_16x16x32_bf16 v[12:15], a[144:147], v[208:211], v[92:95]\n\t"
    "v_exp_f32 v8, v8\n\t"
    "v_mfma_f32_16x16x32_bf16 v[12:15], a[148:151], v[212:215], v[12:15]\n\t"
    "ds_read_b64_tr_b16 a[168:169], v133 offset:32768\n\t"
    "v_cvt_pk_bf16_f32 v31, v6, v7\n\t"
    "v_mfma_f32_16x16x32_bf16 v[12:15], a[152:155], v[216:219], v[12:15]\n\t"
    "v_exp_f32 v9, v9\n\t"
    "v_mfma_f32_16x16x32_bf16 v[12:15], a[156:159], v[220:223], v[12:15]\n\t"
    "ds_read_b64_tr_b16 a[170:171], v133 offset:36864\n\t"
    "v_add_f32 v146, v146, v8\n\t"
    "v_mfma_f32_16x16x32_bf16 a[120:123], a[184:187], v[64:67], a[120:123]\n\t"
    "v_exp_f32 v10, v10\n\t"
    "v_mfma_f32_16x16x32_bf16 a[120:123], a[188:191], v[68:71], a[120:123]\n\t"
    "ds_read_b128 a[144:147], v128 offset:53248\n\t"
    "v_cvt_pk_bf16_f32 v38, v8, v9\n\t"
    "v_mfma_f32_16x16x32_bf16 a[124:127], a[184:187], v[72:75], a[124:127]\n\t"
    "v_exp_f32 v11, v11\n\t"
    "v_mfma_f32_16x16x32_bf16 a[124:127], a[188:191], v[76:79], a[124:127]\n\t"
    "s_waitcnt lgkmcnt(3)\n\t"
    "v_add_f32 v146, v146, v9\n\t"
    "v_add_f32 v146, v146, v10\n\t"
    "s_add_u32 m0, s80, 69632\n\t"
    "s_add_u32 s76, s53, 16384\n\t"
    "buffer_load_dwordx4 v254, s[64:67], s76 offen lds\n\t"
    "s_add_u32 m0, s80, 73728\n\t"
    "s_add_u32 s76, s53, 32768\n\t"
    "buffer_load_dwordx4 v254, s[64:67], s76 offen lds\n\t"
    "s_add_u32 m0, s80, 77824\n\t"
    "s_add_u32 s76, s53, 49152\n\t"
    "buffer_load_dwordx4 v254, s[64:67], s76 offen lds\n\t"
    "s_waitcnt vmcnt(10)\n\t"
    "v_mfma_f32_16x16x32_bf16 a[192:195], v[232:235], v[96:99], a[192:195]\n\t"
    "ds_read_b64_tr_b16 v[240:241], v231 offset:0\n\t"
    "v_mfma_f32_16x16x32_bf16 a[192:195], v[236:239], v[100:103], a[192:195]\n\t"
    "ds_read_b64_tr_b16 v[242:243], v231 offset:2048\n\t"
    "v_mfma_f32_16x16x32_bf16 a[200:203], v[232:235], v[104:107], a[200:203]\n\t"
    "ds_read_b64_tr_b16 v[244:245], v231 offset:4096\n\t"
    "v_mfma_f32_16x16x32_bf16 a[200:203], v[236:239], v[108:111], a[200:203]\n\t"
    "ds_read_b64_tr_b16 v[246:247], v231 offset:6144\n\t"
    "v_mfma_f32_16x16x32_bf16 a[208:211], v[232:235], v[112:115], a[208:211]\n\t"
    "v_mfma_f32_16x16x32_bf16 a[208:211], v[236:239], v[116:119], a[208:211]\n\t"
    "v_mfma_f32_16x16x32_bf16 a[216:219], v[232:235], v[120:123], a[216:219]\n\t"
    "v_mfma_f32_16x16x32_bf16 a[216:219], v[236:239], v[124:127], a[216:219]\n\t"
    "s_waitcnt lgkmcnt(0)\n\t"
    "v_mfma_f32_16x16x32_bf16 a[196:199], v[240:243], v[96:99], a[196:199]\n\t"
    "ds_read_b64_tr_b16 v[232:233], v248 offset:0\n\t"
    "v_mfma_f32_16x16x32_bf16 a[196:199], v[244:247], v[100:103], a[196:199]\n\t"
    "ds_read_b64_tr_b16 v[234:235], v248 offset:2048\n\t"
    "v_mfma_f32_16x16x32_bf16 a[204:207], v[240:243], v[104:107], a[204:207]\n\t"
    "ds_read_b64_tr_b16 v[236:237], v248 offset:4096\n\t"
    "v_mfma_f32_16x16x32_bf16 a[204:207], v[244:247], v[108:111], a[204:207]\n\t"
    "ds_read_b64_tr_b16 v[238:239], v248 offset:6144\n\t"
    "v_mfma_f32_16x16x32_bf16 a[212:215], v[240:243], v[112:115], a[212:215]\n\t"
    "v_mfma_f32_16x16x32_bf16 a[212:215], v[244:247], v[116:119], a[212:215]\n\t"
    "v_mfma_f32_16x16x32_bf16 a[220:223], v[240:243], v[120:123], a[220:223]\n\t"
    "v_mfma_f32_16x16x32_bf16 a[220:223], v[244:247], v[124:127], a[220:223]\n\t"
    "s_waitcnt lgkmcnt(0)\n\t"
    "v_mfma_f32_16x16x32_bf16 a[224:227], v[232:235], v[96:99], a[224:227]\n\t"
    "ds_read_b64_tr_b16 v[240:241], v249 offset:0\n\t"
    "v_mfma_f32_16x16x32_bf16 a[224:227], v[236:239], v[100:103], a[224:227]\n\t"
    "ds_read_b64_tr_b16 v[242:243], v249 offset:2048\n\t"
    "v_mfma_f32_16x16x32_bf16 a[232:235], v[232:235], v[104:107], a[232:235]\n\t"
    "ds_read_b64_tr_b16 v[244:245], v249 offset:4096\n\t"
    "v_mfma_f32_16x16x32_bf16 a[232:235], v[236:239], v[108:111], a[232:235]\n\t"
    "ds_read_b64_tr_b16 v[246:247], v249 offset:6144\n\t"
    "v_mfma_f32_16x16x32_bf16 a[240:243], v[232:235], v[112:115], a[240:243]\n\t"
    "v_mfma_f32_16x16x32_bf16 a[240:243], v[236:239], v[116:119], a[240:243]\n\t"
    "v_mfma_f32_16x16x32_bf16 a[248:251], v[232:235], v[120:123], a[248:251]\n\t"
    "v_mfma_f32_16x16x32_bf16 a[248:251], v[236:239], v[124:127], a[248:251]\n\t"
    "s_waitcnt lgkmcnt(0)\n\t"
    "v_mfma_f32_16x16x32_bf16 a[228:231], v[240:243], v[96:99], a[228:231]\n\t"
    "s_add_u32 m0, s95, 131072\n\t"
    "s_add_u32 s76, s74, 327680\n\t"
    "buffer_load_dwordx4 v159, s[60:63], s76 offen lds\n\t"
    "v_mfma_f32_16x16x32_bf16 a[228:231], v[244:247], v[100:103], a[228:231]\n\t"
    "s_add_u32 m0, s95, 132096\n\t"
    "s_add_u32 s76, s74, 335872\n\t"
    "buffer_load_dwordx4 v159, s[60:63], s76 offen lds\n\t"
    "v_mfma_f32_16x16x32_bf16 a[236:239], v[240:243], v[104:107], a[236:239]\n\t"
    "s_add_u32 m0, s95, 133120\n\t"
    "s_add_u32 s76, s74, 344064\n\t"
    "buffer_load_dwordx4 v159, s[60:63], s76 offen lds\n\t"
    "v_mfma_f32_16x16x32_bf16 a[236:239], v[244:247], v[108:111], a[236:239]\n\t"
    "s_add_u32 m0, s95, 134144\n\t"
    "s_add_u32 s76, s74, 352256\n\t"
    "buffer_load_dwordx4 v159, s[60:63], s76 offen lds\n\t"
    "v_mfma_f32_16x16x32_bf16 a[244:247], v[240:243], v[112:115], a[244:247]\n\t"
    "s_add_u32 m0, s95, 135168\n\t"
    "s_add_u32 s76, s74, 360448\n\t"
    "buffer_load_dwordx4 v159, s[60:63], s76 offen lds\n\t"
    "v_mfma_f32_16x16x32_bf16 a[244:247], v[244:247], v[116:119], a[244:247]\n\t"
    "s_add_u32 m0, s95, 136192\n\t"
    "s_add_u32 s76, s74, 368640\n\t"
    "buffer_load_dwordx4 v159, s[60:63], s76 offen lds\n\t"
    "v_mfma_f32_16x16x32_bf16 a[252:255], v[240:243], v[120:123], a[252:255]\n\t"
    "s_add_u32 m0, s95, 137216\n\t"
    "s_add_u32 s76, s74, 376832\n\t"
    "buffer_load_dwordx4 v159, s[60:63], s76 offen lds\n\t"
    "v_mfma_f32_16x16x32_bf16 a[252:255], v[244:247], v[124:127], a[252:255]\n\t"
    "s_add_u32 m0, s95, 138240\n\t"
    "s_add_u32 s76, s74, 385024\n\t"
    "buffer_load_dwordx4 v159, s[60:63], s76 offen lds\n\t"
    "v_mfma_f32_16x16x32_bf16 v[0:3], a[128:131], v[160:163], v[80:83]\n\t"
    "v_exp_f32 v12, v12\n\t"
    "v_mfma_f32_16x16x32_bf16 v[0:3], a[132:135], v[164:167], v[0:3]\n\t"
    "ds_read_b64_tr_b16 a[172:173], v133 offset:40960\n\t"
    "v_add_f32 v146, v146, v11\n\t"
    "v_mfma_f32_16x16x32_bf16 v[0:3], a[136:139], v[168:171], v[0:3]\n\t"
    "v_exp_f32 v13, v13\n\t"
    "v_mfma_f32_16x16x32_bf16 v[0:3], a[140:143], v[172:175], v[0:3]\n\t"
    "ds_read_b64_tr_b16 a[174:175], v133 offset:45056\n\t"
    "v_cvt_pk_bf16_f32 v39, v10, v11\n\t"
    "v_mfma_f32_16x16x32_bf16 a[0:3], a[160:163], v[16:19], a[0:3]\n\t"
    "v_exp_f32 v14, v14\n\t"
    "v_mfma_f32_16x16x32_bf16 a[0:3], a[164:167], v[20:23], a[0:3]\n\t"
    "ds_read_b128 a[148:151], v129 offset:53248\n\t"
    "v_cvt_pk_bf16_f32 v46, v12, v13\n\t"
    "v_mfma_f32_16x16x32_bf16 a[4:7], a[160:163], v[24:27], a[4:7]\n\t"
    "v_exp_f32 v15, v15\n\t"
    "v_mfma_f32_16x16x32_bf16 a[4:7], a[164:167], v[28:31], a[4:7]\n\t"
    "s_waitcnt lgkmcnt(3)\n\t"
    "s_add_u32 s77, s72, 10240\n\t"
    "buffer_load_dwordx4 v[96:99], v255, s[68:71], s77 offen offset:0 sc1\n\t"
    "v_mfma_f32_16x16x32_bf16 v[4:7], a[128:131], v[176:179], v[84:87]\n\t"
    "v_exp_f32 v0, v0\n\t"
    "v_mfma_f32_16x16x32_bf16 v[4:7], a[132:135], v[180:183], v[4:7]\n\t"
    "ds_read_b64_tr_b16 a[176:177], v134 offset:32768\n\t"
    "v_cvt_pk_bf16_f32 v47, v14, v15\n\t"
    "v_mfma_f32_16x16x32_bf16 v[4:7], a[136:139], v[184:187], v[4:7]\n\t"
    "v_exp_f32 v1, v1\n\t"
    "v_mfma_f32_16x16x32_bf16 v[4:7], a[140:143], v[188:191], v[4:7]\n\t"
    "ds_read_b64_tr_b16 a[178:179], v134 offset:36864\n\t"
    "v_add_f32 v144, v144, v0\n\t"
    "v_mfma_f32_16x16x32_bf16 a[8:11], a[160:163], v[32:35], a[8:11]\n\t"
    "v_exp_f32 v2, v2\n\t"
    "v_mfma_f32_16x16x32_bf16 a[8:11], a[164:167], v[36:39], a[8:11]\n\t"
    "ds_read_b128 a[152:155], v130 offset:53248\n\t"
    "v_cvt_pk_bf16_f32 v48, v0, v1\n\t"
    "v_mfma_f32_16x16x32_bf16 a[12:15], a[160:163], v[40:43], a[12:15]\n\t"
    "v_exp_f32 v3, v3\n\t"
    "v_mfma_f32_16x16x32_bf16 a[12:15], a[164:167], v[44:47], a[12:15]\n\t"
    "s_waitcnt lgkmcnt(3)\n\t"
    "v_add_f32 v144, v144, v1\n\t"
    "v_add_f32 v144, v144, v2\n\t"
    "buffer_load_dwordx4 v[100:103], v255, s[68:71], s77 offen offset:1024 sc1\n\t"
    "s_add_u32 s77, s72, 67584\n\t"
    "buffer_load_dwordx4 v[104:107], v255, s[68:71], s77 offen offset:0 sc1\n\t"
    "buffer_load_dwordx4 v[108:111], v255, s[68:71], s77 offen offset:1024 sc1\n\t"
    "v_mfma_f32_16x16x32_bf16 v[148:151], v[140:143], v[24:27], v[148:151]\n\t"
    "v_mfma_f32_16x16x32_bf16 v[148:151], v[140:143], v[28:31], v[148:151]\n\t"
    "v_mfma_f32_16x16x32_bf16 v[8:11], a[128:131], v[192:195], v[88:91]\n\t"
    "v_exp_f32 v4, v4\n\t"
    "v_mfma_f32_16x16x32_bf16 v[8:11], a[132:135], v[196:199], v[8:11]\n\t"
    "ds_read_b64_tr_b16 a[180:181], v134 offset:40960\n\t"
    "v_add_f32 v144, v144, v3\n\t"
    "v_mfma_f32_16x16x32_bf16 v[8:11], a[136:139], v[200:203], v[8:11]\n\t"
    "v_exp_f32 v5, v5\n\t"
    "v_mfma_f32_16x16x32_bf16 v[8:11], a[140:143], v[204:207], v[8:11]\n\t"
    "ds_read_b64_tr_b16 a[182:183], v134 offset:45056\n\t"
    "v_cvt_pk_bf16_f32 v49, v2, v3\n\t"
    "v_mfma_f32_16x16x32_bf16 a[16:19], a[168:171], v[16:19], a[16:19]\n\t"
    "v_exp_f32 v6, v6\n\t"
    "v_mfma_f32_16x16x32_bf16 a[16:19], a[172:175], v[20:23], a[16:19]\n\t"
    "ds_read_b128 a[156:159], v131 offset:53248\n\t"
    "v_cvt_pk_bf16_f32 v56, v4, v5\n\t"
    "v_mfma_f32_16x16x32_bf16 a[20:23], a[168:171], v[24:27], a[20:23]\n\t"
    "v_exp_f32 v7, v7\n\t"
    "v_mfma_f32_16x16x32_bf16 a[20:23], a[172:175], v[28:31], a[20:23]\n\t"
    "s_waitcnt lgkmcnt(3)\n\t"
    "s_add_u32 s77, s72, 124928\n\t"
    "buffer_load_dwordx4 v[112:115], v255, s[68:71], s77 offen offset:0 sc1\n\t"
    "v_mfma_f32_16x16x32_bf16 v[12:15], a[128:131], v[208:211], v[92:95]\n\t"
    "v_exp_f32 v8, v8\n\t"
    "v_mfma_f32_16x16x32_bf16 v[12:15], a[132:135], v[212:215], v[12:15]\n\t"
    "ds_read_b64_tr_b16 a[184:185], v135 offset:32768\n\t"
    "v_cvt_pk_bf16_f32 v57, v6, v7\n\t"
    "v_mfma_f32_16x16x32_bf16 v[12:15], a[136:139], v[216:219], v[12:15]\n\t"
    "v_exp_f32 v9, v9\n\t"
    "v_mfma_f32_16x16x32_bf16 v[12:15], a[140:143], v[220:223], v[12:15]\n\t"
    "ds_read_b64_tr_b16 a[186:187], v135 offset:36864\n\t"
    "v_add_f32 v146, v146, v8\n\t"
    "v_mfma_f32_16x16x32_bf16 a[24:27], a[168:171], v[32:35], a[24:27]\n\t"
    "v_exp_f32 v10, v10\n\t"
    "v_mfma_f32_16x16x32_bf16 a[24:27], a[172:175], v[36:39], a[24:27]\n\t"
    "ds_read_b128 a[128:131], v128 offset:57344\n\t"
    "v_cvt_pk_bf16_f32 v64, v8, v9\n\t"
    "v_mfma_f32_16x16x32_bf16 a[28:31], a[168:171], v[40:43], a[28:31]\n\t"
    "v_exp_f32 v11, v11\n\t"
    "v_mfma_f32_16x16x32_bf16 a[28:31], a[172:175], v[44:47], a[28:31]\n\t"
    "s_waitcnt lgkmcnt(3)\n\t"
    "v_add_f32 v146, v146, v9\n\t"
    "v_add_f32 v146, v146, v10\n\t"
    "buffer_load_dwordx4 v[116:119], v255, s[68:71], s77 offen offset:1024 sc1\n\t"
    "s_add_u32 s77, s72, 182272\n\t"
    "buffer_load_dwordx4 v[120:123], v255, s[68:71], s77 offen offset:0 sc1\n\t"
    "buffer_load_dwordx4 v[124:127], v255, s[68:71], s77 offen offset:1024 sc1\n\t"
    "v_mfma_f32_16x16x32_bf16 v[152:155], v[140:143], v[40:43], v[152:155]\n\t"
    "v_mfma_f32_16x16x32_bf16 v[152:155], v[140:143], v[44:47], v[152:155]\n\t"
    "v_mfma_f32_16x16x32_bf16 v[0:3], a[144:147], v[160:163], v[80:83]\n\t"
    "v_exp_f32 v12, v12\n\t"
    "v_mfma_f32_16x16x32_bf16 v[0:3], a[148:151], v[164:167], v[0:3]\n\t"
    "ds_read_b64_tr_b16 a[188:189], v135 offset:40960\n\t"
    "v_add_f32 v146, v146, v11\n\t"
    "v_mfma_f32_16x16x32_bf16 v[0:3], a[152:155], v[168:171], v[0:3]\n\t"
    "v_exp_f32 v13, v13\n\t"
    "v_mfma_f32_16x16x32_bf16 v[0:3], a[156:159], v[172:175], v[0:3]\n\t"
    "ds_read_b64_tr_b16 a[190:191], v135 offset:45056\n\t"
    "v_cvt_pk_bf16_f32 v65, v10, v11\n\t"
    "v_mfma_f32_16x16x32_bf16 a[32:35], a[176:179], v[16:19], a[32:35]\n\t"
    "v_exp_f32 v14, v14\n\t"
    "v_mfma_f32_16x16x32_bf16 a[32:35], a[180:183], v[20:23], a[32:35]\n\t"
    "ds_read_b128 a[132:135], v129 offset:57344\n\t"
    "v_cvt_pk_bf16_f32 v72, v12, v13\n\t"
    "v_mfma_f32_16x16x32_bf16 a[36:39], a[176:179], v[24:27], a[36:39]\n\t"
    "v_exp_f32 v15, v15\n\t"
    "v_mfma_f32_16x16x32_bf16 a[36:39], a[180:183], v[28:31], a[36:39]\n\t"
    "s_waitcnt lgkmcnt(3)\n\t"
    "v_mfma_f32_16x16x32_bf16 v[4:7], a[144:147], v[176:179], v[84:87]\n\t"
    "v_exp_f32 v0, v0\n\t"
    "v_mfma_f32_16x16x32_bf16 v[4:7], a[148:151], v[180:183], v[4:7]\n\t"
    "ds_read_b64_tr_b16 a[160:161], v136 offset:32768\n\t"
    "v_cvt_pk_bf16_f32 v73, v14, v15\n\t"
    "v_mfma_f32_16x16x32_bf16 v[4:7], a[152:155], v[184:187], v[4:7]\n\t"
    "v_exp_f32 v1, v1\n\t"
    "v_mfma_f32_16x16x32_bf16 v[4:7], a[156:159], v[188:191], v[4:7]\n\t"
    "ds_read_b64_tr_b16 a[162:163], v136 offset:36864\n\t"
    "v_add_f32 v144, v144, v0\n\t"
    "v_mfma_f32_16x16x32_bf16 a[40:43], a[176:179], v[32:35], a[40:43]\n\t"
    "v_exp_f32 v2, v2\n\t"
    "v_mfma_f32_16x16x32_bf16 a[40:43], a[180:183], v[36:39], a[40:43]\n\t"
    "ds_read_b128 a[136:139], v130 offset:57344\n\t"
    "v_cvt_pk_bf16_f32 v50, v0, v1\n\t"
    "v_mfma_f32_16x16x32_bf16 a[44:47], a[176:179], v[40:43], a[44:47]\n\t"
    "v_exp_f32 v3, v3\n\t"
    "v_mfma_f32_16x16x32_bf16 a[44:47], a[180:183], v[44:47], a[44:47]\n\t"
    "s_waitcnt lgkmcnt(3)\n\t"
    "v_add_f32 v144, v144, v1\n\t"
    "v_add_f32 v144, v144, v2\n\t"
    "v_mfma_f32_16x16x32_bf16 v[8:11], a[144:147], v[192:195], v[88:91]\n\t"
    "v_exp_f32 v4, v4\n\t"
    "v_mfma_f32_16x16x32_bf16 v[8:11], a[148:151], v[196:199], v[8:11]\n\t"
    "ds_read_b64_tr_b16 a[164:165], v136 offset:40960\n\t"
    "v_add_f32 v144, v144, v3\n\t"
    "v_mfma_f32_16x16x32_bf16 v[8:11], a[152:155], v[200:203], v[8:11]\n\t"
    "v_exp_f32 v5, v5\n\t"
    "v_mfma_f32_16x16x32_bf16 v[8:11], a[156:159], v[204:207], v[8:11]\n\t"
    "ds_read_b64_tr_b16 a[166:167], v136 offset:45056\n\t"
    "v_cvt_pk_bf16_f32 v51, v2, v3\n\t"
    "v_mfma_f32_16x16x32_bf16 a[48:51], a[184:187], v[16:19], a[48:51]\n\t"
    "v_exp_f32 v6, v6\n\t"
    "v_mfma_f32_16x16x32_bf16 a[48:51], a[188:191], v[20:23], a[48:51]\n\t"
    "ds_read_b128 a[140:143], v131 offset:57344\n\t"
    "v_cvt_pk_bf16_f32 v58, v4, v5\n\t"
    "v_mfma_f32_16x16x32_bf16 a[52:55], a[184:187], v[24:27], a[52:55]\n\t"
    "v_exp_f32 v7, v7\n\t"
    "v_mfma_f32_16x16x32_bf16 a[52:55], a[188:191], v[28:31], a[52:55]\n\t"
    "s_waitcnt lgkmcnt(3)\n\t"
    "v_mfma_f32_16x16x32_bf16 v[12:15], a[144:147], v[208:211], v[92:95]\n\t"
    "v_exp_f32 v8, v8\n\t"
    "v_mfma_f32_16x16x32_bf16 v[12:15], a[148:151], v[212:215], v[12:15]\n\t"
    "ds_read_b64_tr_b16 a[168:169], v137 offset:32768\n\t"
    "v_cvt_pk_bf16_f32 v59, v6, v7\n\t"
    "v_mfma_f32_16x16x32_bf16 v[12:15], a[152:155], v[216:219], v[12:15]\n\t"
    "v_exp_f32 v9, v9\n\t"
    "v_mfma_f32_16x16x32_bf16 v[12:15], a[156:159], v[220:223], v[12:15]\n\t"
    "ds_read_b64_tr_b16 a[170:171], v137 offset:36864\n\t"
    "v_add_f32 v146, v146, v8\n\t"
    "v_mfma_f32_16x16x32_bf16 a[56:59], a[184:187], v[32:35], a[56:59]\n\t"
    "v_exp_f32 v10, v10\n\t"
    "v_mfma_f32_16x16x32_bf16 a[56:59], a[188:191], v[36:39], a[56:59]\n\t"
    "ds_read_b128 a[144:147], v128 offset:61440\n\t"
    "v_cvt_pk_bf16_f32 v66, v8, v9\n\t"
    "v_mfma_f32_16x16x32_bf16 a[60:63], a[184:187], v[40:43], a[60:63]\n\t"
    "v_exp_f32 v11, v11\n\t"
    "v_mfma_f32_16x16x32_bf16 a[60:63], a[188:191], v[44:47], a[60:63]\n\t"
    "s_waitcnt lgkmcnt(3)\n\t"
    "v_add_f32 v146, v146, v9\n\t"
    "v_add_f32 v146, v146, v10\n\t"
    "v_mfma_f32_16x16x32_bf16 v[0:3], a[128:131], v[160:163], v[80:83]\n\t"
    "v_exp_f32 v12, v12\n\t"
    "v_mfma_f32_16x16x32_bf16 v[0:3], a[132:135], v[164:167], v[0:3]\n\t"
    "ds_read_b64_tr_b16 a[172:173], v137 offset:40960\n\t"
    "v_add_f32 v146, v146, v11\n\t"
    "v_mfma_f32_16x16x32_bf16 v[0:3], a[136:139], v[168:171], v[0:3]\n\t"
    "v_exp_f32 v13, v13\n\t"
    "v_mfma_f32_16x16x32_bf16 v[0:3], a[140:143], v[172:175], v[0:3]\n\t"
    "ds_read_b64_tr_b16 a[174:175], v137 offset:45056\n\t"
    "v_cvt_pk_bf16_f32 v67, v10, v11\n\t"
    "v_mfma_f32_16x16x32_bf16 a[64:67], a[160:163], v[16:19], a[64:67]\n\t"
    "v_exp_f32 v14, v14\n\t"
    "v_mfma_f32_16x16x32_bf16 a[64:67], a[164:167], v[20:23], a[64:67]\n\t"
    "ds_read_b128 a[148:151], v129 offset:61440\n\t"
    "v_cvt_pk_bf16_f32 v74, v12, v13\n\t"
    "v_mfma_f32_16x16x32_bf16 a[68:71], a[160:163], v[24:27], a[68:71]\n\t"
    "v_exp_f32 v15, v15\n\t"
    "v_mfma_f32_16x16x32_bf16 a[68:71], a[164:167], v[28:31], a[68:71]\n\t"
    "s_waitcnt lgkmcnt(3)\n\t"
    "v_mfma_f32_16x16x32_bf16 v[4:7], a[128:131], v[176:179], v[84:87]\n\t"
    "v_exp_f32 v0, v0\n\t"
    "v_mfma_f32_16x16x32_bf16 v[4:7], a[132:135], v[180:183], v[4:7]\n\t"
    "ds_read_b64_tr_b16 a[176:177], v138 offset:32768\n\t"
    "v_cvt_pk_bf16_f32 v75, v14, v15\n\t"
    "v_mfma_f32_16x16x32_bf16 v[4:7], a[136:139], v[184:187], v[4:7]\n\t"
    "v_exp_f32 v1, v1\n\t"
    "v_mfma_f32_16x16x32_bf16 v[4:7], a[140:143], v[188:191], v[4:7]\n\t"
    "ds_read_b64_tr_b16 a[178:179], v138 offset:36864\n\t"
    "v_add_f32 v144, v144, v0\n\t"
    "v_mfma_f32_16x16x32_bf16 a[72:75], a[160:163], v[32:35], a[72:75]\n\t"
    "v_exp_f32 v2, v2\n\t"
    "v_mfma_f32_16x16x32_bf16 a[72:75], a[164:167], v[36:39], a[72:75]\n\t"
    "ds_read_b128 a[152:155], v130 offset:61440\n\t"
    "v_cvt_pk_bf16_f32 v52, v0, v1\n\t"
    "v_mfma_f32_16x16x32_bf16 a[76:79], a[160:163], v[40:43], a[76:79]\n\t"
    "v_exp_f32 v3, v3\n\t"
    "v_mfma_f32_16x16x32_bf16 a[76:79], a[164:167], v[44:47], a[76:79]\n\t"
    "s_waitcnt lgkmcnt(3)\n\t"
    "v_add_f32 v144, v144, v1\n\t"
    "v_add_f32 v144, v144, v2\n\t"
    "v_mfma_f32_16x16x32_bf16 v[8:11], a[128:131], v[192:195], v[88:91]\n\t"
    "v_exp_f32 v4, v4\n\t"
    "v_mfma_f32_16x16x32_bf16 v[8:11], a[132:135], v[196:199], v[8:11]\n\t"
    "ds_read_b64_tr_b16 a[180:181], v138 offset:40960\n\t"
    "v_add_f32 v144, v144, v3\n\t"
    "v_mfma_f32_16x16x32_bf16 v[8:11], a[136:139], v[200:203], v[8:11]\n\t"
    "v_exp_f32 v5, v5\n\t"
    "v_mfma_f32_16x16x32_bf16 v[8:11], a[140:143], v[204:207], v[8:11]\n\t"
    "ds_read_b64_tr_b16 a[182:183], v138 offset:45056\n\t"
    "v_cvt_pk_bf16_f32 v53, v2, v3\n\t"
    "v_mfma_f32_16x16x32_bf16 a[80:83], a[168:171], v[16:19], a[80:83]\n\t"
    "v_exp_f32 v6, v6\n\t"
    "v_mfma_f32_16x16x32_bf16 a[80:83], a[172:175], v[20:23], a[80:83]\n\t"
    "ds_read_b128 a[156:159], v131 offset:61440\n\t"
    "v_cvt_pk_bf16_f32 v60, v4, v5\n\t"
    "v_mfma_f32_16x16x32_bf16 a[84:87], a[168:171], v[24:27], a[84:87]\n\t"
    "v_exp_f32 v7, v7\n\t"
    "v_mfma_f32_16x16x32_bf16 a[84:87], a[172:175], v[28:31], a[84:87]\n\t"
    "s_waitcnt lgkmcnt(3)\n\t"
    "v_mfma_f32_16x16x32_bf16 v[12:15], a[128:131], v[208:211], v[92:95]\n\t"
    "s_waitcnt vmcnt(24)\n\t"
    "v_exp_f32 v8, v8\n\t"
    "v_mfma_f32_16x16x32_bf16 v[12:15], a[132:135], v[212:215], v[12:15]\n\t"
    "ds_read_b64_tr_b16 a[184:185], v139 offset:32768\n\t"
    "v_cvt_pk_bf16_f32 v61, v6, v7\n\t"
    "v_mfma_f32_16x16x32_bf16 v[12:15], a[136:139], v[216:219], v[12:15]\n\t"
    "s_barrier\n\t"
    "v_exp_f32 v9, v9\n\t"
    "v_mfma_f32_16x16x32_bf16 v[12:15], a[140:143], v[220:223], v[12:15]\n\t"
    "ds_read_b64_tr_b16 a[186:187], v139 offset:36864\n\t"
    "v_add_f32 v146, v146, v8\n\t"
    "v_mfma_f32_16x16x32_bf16 a[88:91], a[168:171], v[32:35], a[88:91]\n\t"
    "v_exp_f32 v10, v10\n\t"
    "v_mfma_f32_16x16x32_bf16 a[88:91], a[172:175], v[36:39], a[88:91]\n\t"
    "v_cvt_pk_bf16_f32 v68, v8, v9\n\t"
    "v_mfma_f32_16x16x32_bf16 a[92:95], a[168:171], v[40:43], a[92:95]\n\t"
    "v_exp_f32 v11, v11\n\t"
    "v_mfma_f32_16x16x32_bf16 a[92:95], a[172:175], v[44:47], a[92:95]\n\t"
    "s_waitcnt lgkmcnt(2)\n\t"
    "v_add_f32 v146, v146, v9\n\t"
    "v_add_f32 v146, v146, v10\n\t"
    "s_waitcnt vmcnt(8)\n\t"
    "ds_read_b64_tr_b16 v[232:233], v230 offset:0\n\t"
    "ds_read_b64_tr_b16 v[234:235], v230 offset:2048\n\t"
    "ds_read_b64_tr_b16 v[236:237], v230 offset:4096\n\t"
    "ds_read_b64_tr_b16 v[238:239], v230 offset:6144\n\t"
    "v_mfma_f32_16x16x32_bf16 v[0:3], a[144:147], v[160:163], v[80:83]\n\t"
    "v_exp_f32 v12, v12\n\t"
    "v_mfma_f32_16x16x32_bf16 v[0:3], a[148:151], v[164:167], v[0:3]\n\t"
    "ds_read_b64_tr_b16 a[188:189], v139 offset:40960\n\t"
    "v_add_f32 v146, v146, v11\n\t"
    "v_mfma_f32_16x16x32_bf16 v[0:3], a[152:155], v[168:171], v[0:3]\n\t"
    "v_exp_f32 v13, v13\n\t"
    "v_mfma_f32_16x16x32_bf16 v[0:3], a[156:159], v[172:175], v[0:3]\n\t"
    "ds_read_b64_tr_b16 a[190:191], v139 offset:45056\n\t"
    "v_cvt_pk_bf16_f32 v69, v10, v11\n\t"
    "v_mfma_f32_16x16x32_bf16 a[96:99], a[176:179], v[16:19], a[96:99]\n\t"
    "v_exp_f32 v14, v14\n\t"
    "v_mfma_f32_16x16x32_bf16 a[96:99], a[180:183], v[20:23], a[96:99]\n\t"
    "v_cvt_pk_bf16_f32 v76, v12, v13\n\t"
    "v_mfma_f32_16x16x32_bf16 a[100:103], a[176:179], v[24:27], a[100:103]\n\t"
    "v_exp_f32 v15, v15\n\t"
    "v_mfma_f32_16x16x32_bf16 a[100:103], a[180:183], v[28:31], a[100:103]\n\t"
    "s_waitcnt lgkmcnt(2)\n\t"
    "s_add_u32 m0, s80, 81920\n\t"
    "s_add_u32 s76, s53, 65536\n\t"
    "buffer_load_dwordx4 v254, s[64:67], s76 offen lds\n\t"
    "v_mfma_f32_16x16x32_bf16 v[4:7], a[144:147], v[176:179], v[84:87]\n\t"
    "v_exp_f32 v0, v0\n\t"
    "v_mfma_f32_16x16x32_bf16 v[4:7], a[148:151], v[180:183], v[4:7]\n\t"
    "ds_read_b64_tr_b16 a[160:161], v132 offset:49152\n\t"
    "v_cvt_pk_bf16_f32 v77, v14, v15\n\t"
    "v_mfma_f32_16x16x32_bf16 v[4:7], a[152:155], v[184:187], v[4:7]\n\t"
    "v_exp_f32 v1, v1\n\t"
    "v_mfma_f32_16x16x32_bf16 v[4:7], a[156:159], v[188:191], v[4:7]\n\t"
    "ds_read_b64_tr_b16 a[162:163], v132 offset:53248\n\t"
    "v_add_f32 v144, v144, v0\n\t"
    "v_mfma_f32_16x16x32_bf16 a[104:107], a[176:179], v[32:35], a[104:107]\n\t"
    "v_exp_f32 v2, v2\n\t"
    "v_mfma_f32_16x16x32_bf16 a[104:107], a[180:183], v[36:39], a[104:107]\n\t"
    "v_cvt_pk_bf16_f32 v54, v0, v1\n\t"
    "v_mfma_f32_16x16x32_bf16 a[108:111], a[176:179], v[40:43], a[108:111]\n\t"
    "v_exp_f32 v3, v3\n\t"
    "v_mfma_f32_16x16x32_bf16 a[108:111], a[180:183], v[44:47], a[108:111]\n\t"
    "s_waitcnt lgkmcnt(2)\n\t"
    "v_add_f32 v144, v144, v1\n\t"
    "v_add_f32 v144, v144, v2\n\t"
    "s_add_u32 m0, s80, 86016\n\t"
    "s_add_u32 s76, s53, 81920\n\t"
    "buffer_load_dwordx4 v254, s[64:67], s76 offen lds\n\t"
    "s_add_u32 m0, s80, 90112\n\t"
    "s_add_u32 s76, s53, 98304\n\t"
    "buffer_load_dwordx4 v254, s[64:67], s76 offen lds\n\t"
    "s_add_u32 m0, s80, 94208\n\t"
    "s_add_u32 s76, s53, 114688\n\t"
    "buffer_load_dwordx4 v254, s[64:67], s76 offen lds\n\t"
    "s_waitcnt vmcnt(4)\n\t"
    "v_mfma_f32_16x16x32_bf16 a[192:195], v[232:235], v[96:99], a[192:195]\n\t"
    "ds_read_b64_tr_b16 v[240:241], v231 offset:0\n\t"
    "v_mfma_f32_16x16x32_bf16 a[192:195], v[236:239], v[100:103], a[192:195]\n\t"
    "ds_read_b64_tr_b16 v[242:243], v231 offset:2048\n\t"
    "v_mfma_f32_16x16x32_bf16 a[200:203], v[232:235], v[104:107], a[200:203]\n\t"
    "ds_read_b64_tr_b16 v[244:245], v231 offset:4096\n\t"
    "v_mfma_f32_16x16x32_bf16 a[200:203], v[236:239], v[108:111], a[200:203]\n\t"
    "ds_read_b64_tr_b16 v[246:247], v231 offset:6144\n\t"
    "v_mfma_f32_16x16x32_bf16 a[208:211], v[232:235], v[112:115], a[208:211]\n\t"
    "v_mfma_f32_16x16x32_bf16 a[208:211], v[236:239], v[116:119], a[208:211]\n\t"
    "v_mfma_f32_16x16x32_bf16 a[216:219], v[232:235], v[120:123], a[216:219]\n\t"
    "v_mfma_f32_16x16x32_bf16 a[216:219], v[236:239], v[124:127], a[216:219]\n\t"
    "s_waitcnt lgkmcnt(0)\n\t"
    "v_mfma_f32_16x16x32_bf16 a[196:199], v[240:243], v[96:99], a[196:199]\n\t"
    "ds_read_b64_tr_b16 v[232:233], v248 offset:0\n\t"
    "v_mfma_f32_16x16x32_bf16 a[196:199], v[244:247], v[100:103], a[196:199]\n\t"
    "ds_read_b64_tr_b16 v[234:235], v248 offset:2048\n\t"
    "v_mfma_f32_16x16x32_bf16 a[204:207], v[240:243], v[104:107], a[204:207]\n\t"
    "ds_read_b64_tr_b16 v[236:237], v248 offset:4096\n\t"
    "v_mfma_f32_16x16x32_bf16 a[204:207], v[244:247], v[108:111], a[204:207]\n\t"
    "ds_read_b64_tr_b16 v[238:239], v248 offset:6144\n\t"
    "v_mfma_f32_16x16x32_bf16 a[212:215], v[240:243], v[112:115], a[212:215]\n\t"
    "v_mfma_f32_16x16x32_bf16 a[212:215], v[244:247], v[116:119], a[212:215]\n\t"
    "v_mfma_f32_16x16x32_bf16 a[220:223], v[240:243], v[120:123], a[220:223]\n\t"
    "v_mfma_f32_16x16x32_bf16 a[220:223], v[244:247], v[124:127], a[220:223]\n\t"
    "s_waitcnt lgkmcnt(0)\n\t"
    "v_mfma_f32_16x16x32_bf16 a[224:227], v[232:235], v[96:99], a[224:227]\n\t"
    "ds_read_b64_tr_b16 v[240:241], v249 offset:0\n\t"
    "v_mfma_f32_16x16x32_bf16 a[224:227], v[236:239], v[100:103], a[224:227]\n\t"
    "ds_read_b64_tr_b16 v[242:243], v249 offset:2048\n\t"
    "v_mfma_f32_16x16x32_bf16 a[232:235], v[232:235], v[104:107], a[232:235]\n\t"
    "ds_read_b64_tr_b16 v[244:245], v249 offset:4096\n\t"
    "v_mfma_f32_16x16x32_bf16 a[232:235], v[236:239], v[108:111], a[232:235]\n\t"
    "ds_read_b64_tr_b16 v[246:247], v249 offset:6144\n\t"
    "v_mfma_f32_16x16x32_bf16 a[240:243], v[232:235], v[112:115], a[240:243]\n\t"
    "v_mfma_f32_16x16x32_bf16 a[240:243], v[236:239], v[116:119], a[240:243]\n\t"
    "v_mfma_f32_16x16x32_bf16 a[248:251], v[232:235], v[120:123], a[248:251]\n\t"
    "v_mfma_f32_16x16x32_bf16 a[248:251], v[236:239], v[124:127], a[248:251]\n\t"
    "s_waitcnt lgkmcnt(0)\n\t"
    "v_mfma_f32_16x16x32_bf16 a[228:231], v[240:243], v[96:99], a[228:231]\n\t"
    "s_add_u32 m0, s95, 131072\n\t"
    "s_add_u32 s76, s74, 393216\n\t"
    "buffer_load_dwordx4 v159, s[60:63], s76 offen lds\n\t"
    "v_mfma_f32_16x16x32_bf16 a[228:231], v[244:247], v[100:103], a[228:231]\n\t"
    "s_add_u32 m0, s95, 132096\n\t"
    "s_add_u32 s76, s74, 401408\n\t"
    "buffer_load_dwordx4 v159, s[60:63], s76 offen lds\n\t"
    "v_mfma_f32_16x16x32_bf16 a[236:239], v[240:243], v[104:107], a[236:239]\n\t"
    "s_add_u32 m0, s95, 133120\n\t"
    "s_add_u32 s76, s74, 409600\n\t"
    "buffer_load_dwordx4 v159, s[60:63], s76 offen lds\n\t"
    "v_mfma_f32_16x16x32_bf16 a[236:239], v[244:247], v[108:111], a[236:239]\n\t"
    "s_add_u32 m0, s95, 134144\n\t"
    "s_add_u32 s76, s74, 417792\n\t"
    "buffer_load_dwordx4 v159, s[60:63], s76 offen lds\n\t"
    "v_mfma_f32_16x16x32_bf16 a[244:247], v[240:243], v[112:115], a[244:247]\n\t"
    "s_add_u32 m0, s95, 135168\n\t"
    "s_add_u32 s76, s74, 425984\n\t"
    "buffer_load_dwordx4 v159, s[60:63], s76 offen lds\n\t"
    "v_mfma_f32_16x16x32_bf16 a[244:247], v[244:247], v[116:119], a[244:247]\n\t"
    "s_add_u32 m0, s95, 136192\n\t"
    "s_add_u32 s76, s74, 434176\n\t"
    "buffer_load_dwordx4 v159, s[60:63], s76 offen lds\n\t"
    "v_mfma_f32_16x16x32_bf16 a[252:255], v[240:243], v[120:123], a[252:255]\n\t"
    "s_add_u32 m0, s95, 137216\n\t"
    "s_add_u32 s76, s74, 442368\n\t"
    "buffer_load_dwordx4 v159, s[60:63], s76 offen lds\n\t"
    "v_mfma_f32_16x16x32_bf16 a[252:255], v[244:247], v[124:127], a[252:255]\n\t"
    "s_add_u32 m0, s95, 138240\n\t"
    "s_add_u32 s76, s74, 450560\n\t"
    "buffer_load_dwordx4 v159, s[60:63], s76 offen lds\n\t"
    "v_mfma_f32_16x16x32_bf16 v[8:11], a[144:147], v[192:195], v[88:91]\n\t"
    "v_exp_f32 v4, v4\n\t"
    "v_mfma_f32_16x16x32_bf16 v[8:11], a[148:151], v[196:199], v[8:11]\n\t"
    "ds_read_b64_tr_b16 a[164:165], v132 offset:57344\n\t"
    "v_add_f32 v144, v144, v3\n\t"
    "v_mfma_f32_16x16x32_bf16 v[8:11], a[152:155], v[200:203], v[8:11]\n\t"
    "v_exp_f32 v5, v5\n\t"
    "v_mfma_f32_16x16x32_bf16 v[8:11], a[156:159], v[204:207], v[8:11]\n\t"
    "ds_read_b64_tr_b16 a[166:167], v132 offset:61440\n\t"
    "v_cvt_pk_bf16_f32 v55, v2, v3\n\t"
    "v_mfma_f32_16x16x32_bf16 a[112:115], a[184:187], v[16:19], a[112:115]\n\t"
    "v_exp_f32 v6, v6\n\t"
    "v_mfma_f32_16x16x32_bf16 a[112:115], a[188:191], v[20:23], a[112:115]\n\t"
    "v_cvt_pk_bf16_f32 v62, v4, v5\n\t"
    "v_mfma_f32_16x16x32_bf16 a[116:119], a[184:187], v[24:27], a[116:119]\n\t"
    "v_exp_f32 v7, v7\n\t"
    "v_mfma_f32_16x16x32_bf16 a[116:119], a[188:191], v[28:31], a[116:119]\n\t"
    "s_waitcnt lgkmcnt(2)\n\t"
    "s_add_u32 s77, s72, 12288\n\t"
    "buffer_load_dwordx4 v[96:99], v255, s[68:71], s77 offen offset:0 sc1\n\t"
    "v_mfma_f32_16x16x32_bf16 v[12:15], a[144:147], v[208:211], v[92:95]\n\t"
    "v_exp_f32 v8, v8\n\t"
    "v_mfma_f32_16x16x32_bf16 v[12:15], a[148:151], v[212:215], v[12:15]\n\t"
    "ds_read_b64_tr_b16 a[168:169], v133 offset:49152\n\t"
    "v_cvt_pk_bf16_f32 v63, v6, v7\n\t"
    "v_mfma_f32_16x16x32_bf16 v[12:15], a[152:155], v[216:219], v[12:15]\n\t"
    "v_exp_f32 v9, v9\n\t"
    "v_mfma_f32_16x16x32_bf16 v[12:15], a[156:159], v[220:223], v[12:15]\n\t"
    "ds_read_b64_tr_b16 a[170:171], v133 offset:53248\n\t"
    "v_add_f32 v146, v146, v8\n\t"
    "v_mfma_f32_16x16x32_bf16 a[120:123], a[184:187], v[32:35], a[120:123]\n\t"
    "v_exp_f32 v10, v10\n\t"
    "v_mfma_f32_16x16x32_bf16 a[120:123], a[188:191], v[36:39], a[120:123]\n\t"
    "v_cvt_pk_bf16_f32 v70, v8, v9\n\t"
    "v_mfma_f32_16x16x32_bf16 a[124:127], a[184:187], v[40:43], a[124:127]\n\t"
    "v_exp_f32 v11, v11\n\t"
    "v_mfma_f32_16x16x32_bf16 a[124:127], a[188:191], v[44:47], a[124:127]\n\t"
    "s_waitcnt lgkmcnt(2)\n\t"
    "v_add_f32 v146, v146, v9\n\t"
    "v_add_f32 v146, v146, v10\n\t"
    "buffer_load_dwordx4 v[100:103], v255, s[68:71], s77 offen offset:1024 sc1\n\t"
    "s_add_u32 s77, s72, 69632\n\t"
    "buffer_load_dwordx4 v[104:107], v255, s[68:71], s77 offen offset:0 sc1\n\t"
    "buffer_load_dwordx4 v[108:111], v255, s[68:71], s77 offen offset:1024 sc1\n\t"
    "s_nop 0\n\t"
    "v_exp_f32 v12, v12\n\t"
    "s_add_u32 s77, s72, 126976\n\t"
    "buffer_load_dwordx4 v[112:115], v255, s[68:71], s77 offen offset:0 sc1\n\t"
    "s_nop 0\n\t"
    "ds_read_b64_tr_b16 a[172:173], v133 offset:57344\n\t"
    "v_add_f32 v146, v146, v11\n\t"
    "s_nop 0\n\t"
    "v_exp_f32 v13, v13\n\t"
    "buffer_load_dwordx4 v[116:119], v255, s[68:71], s77 offen offset:1024 sc1\n\t"
    "s_nop 0\n\t"
    "ds_read_b64_tr_b16 a[174:175], v133 offset:61440\n\t"
    "v_cvt_pk_bf16_f32 v71, v10, v11\n\t"
    "v_mfma_f32_16x16x32_bf16 a[0:3], a[160:163], v[48:51], a[0:3]\n\t"
    "v_exp_f32 v14, v14\n\t"
    "v_mfma_f32_16x16x32_bf16 a[0:3], a[164:167], v[52:55], a[0:3]\n\t"
    "v_cvt_pk_bf16_f32 v78, v12, v13\n\t"
    "v_mfma_f32_16x16x32_bf16 a[4:7], a[160:163], v[56:59], a[4:7]\n\t"
    "v_exp_f32 v15, v15\n\t"
    "v_mfma_f32_16x16x32_bf16 a[4:7], a[164:167], v[60:63], a[4:7]\n\t"
    "s_waitcnt lgkmcnt(2)\n\t"
    "s_add_u32 s77, s72, 184320\n\t"
    "buffer_load_dwordx4 v[120:123], v255, s[68:71], s77 offen offset:0 sc1\n\t"
    "s_nop 0\n\t"
    "ds_read_b64_tr_b16 a[176:177], v134 offset:49152\n\t"
    "v_cvt_pk_bf16_f32 v79, v14, v15\n\t"
    "buffer_load_dwordx4 v[124:127], v255, s[68:71], s77 offen offset:1024 sc1\n\t"
    "s_nop 0\n\t"
    "ds_read_b64_tr_b16 a[178:179], v134 offset:53248\n\t"
    "v_mfma_f32_16x16x32_bf16 a[8:11], a[160:163], v[64:67], a[8:11]\n\t"
    "v_mfma_f32_16x16x32_bf16 a[8:11], a[164:167], v[68:71], a[8:11]\n\t"
    "v_mfma_f32_16x16x32_bf16 a[12:15], a[160:163], v[72:75], a[12:15]\n\t"
    "v_mfma_f32_16x16x32_bf16 a[12:15], a[164:167], v[76:79], a[12:15]\n\t"
    "s_waitcnt lgkmcnt(2)\n\t"
    "v_mfma_f32_16x16x32_bf16 v[148:151], v[140:143], v[56:59], v[148:151]\n\t"
    "v_mfma_f32_16x16x32_bf16 v[148:151], v[140:143], v[60:63], v[148:151]\n\t"
    "s_add_u32 m0, s80, 32768\n\t"
    "s_add_u32 s76, s55, 0\n\t"
    "buffer_load_dwordx4 v254, s[64:67], s76 offen lds\n\t"
    "s_nop 0\n\t"
    "ds_read_b64_tr_b16 a[180:181], v134 offset:57344\n\t"
    "s_add_u32 m0, s80, 36864\n\t"
    "s_add_u32 s76, s55, 16384\n\t"
    "buffer_load_dwordx4 v254, s[64:67], s76 offen lds\n\t"
    "s_nop 0\n\t"
    "ds_read_b64_tr_b16 a[182:183], v134 offset:61440\n\t"
    "v_mfma_f32_16x16x32_bf16 a[16:19], a[168:171], v[48:51], a[16:19]\n\t"
    "v_mfma_f32_16x16x32_bf16 a[16:19], a[172:175], v[52:55], a[16:19]\n\t"
    "v_mfma_f32_16x16x32_bf16 a[20:23], a[168:171], v[56:59], a[20:23]\n\t"
    "v_mfma_f32_16x16x32_bf16 a[20:23], a[172:175], v[60:63], a[20:23]\n\t"
    "s_waitcnt lgkmcnt(2)\n\t"
    "s_add_u32 m0, s80, 40960\n\t"
    "s_add_u32 s76, s55, 32768\n\t"
    "buffer_load_dwordx4 v254, s[64:67], s76 offen lds\n\t"
    "s_nop 0\n\t"
    "ds_read_b64_tr_b16 a[184:185], v135 offset:49152\n\t"
    "s_add_u32 m0, s80, 45056\n\t"
    "s_add_u32 s76, s55, 49152\n\t"
    "buffer_load_dwordx4 v254, s[64:67], s76 offen lds\n\t"
    "s_nop 0\n\t"
    "ds_read_b64_tr_b16 a[186:187], v135 offset:53248\n\t"
    "v_mfma_f32_16x16x32_bf16 a[24:27], a[168:171], v[64:67], a[24:27]\n\t"
    "v_mfma_f32_16x16x32_bf16 a[24:27], a[172:175], v[68:71], a[24:27]\n\t"
    "v_mfma_f32_16x16x32_bf16 a[28:31], a[168:171], v[72:75], a[28:31]\n\t"
    "v_mfma_f32_16x16x32_bf16 a[28:31], a[172:175], v[76:79], a[28:31]\n\t"
    "s_waitcnt lgkmcnt(2)\n\t"
    "v_mfma_f32_16x16x32_bf16 v[152:155], v[140:143], v[72:75], v[152:155]\n\t"
    "v_mfma_f32_16x16x32_bf16 v[152:155], v[140:143], v[76:79], v[152:155]\n\t"
    "s_nop 0\n\t"
    "ds_read_b64_tr_b16 a[188:189], v135 offset:57344\n\t"
    "s_nop 0\n\t"
    "ds_read_b64_tr_b16 a[190:191], v135 offset:61440\n\t"
    "v_mfma_f32_16x16x32_bf16 a[32:35], a[176:179], v[48:51], a[32:35]\n\t"
    "v_mfma_f32_16x16x32_bf16 a[32:35], a[180:183], v[52:55], a[32:35]\n\t"
    "v_mfma_f32_16x16x32_bf16 a[36:39], a[176:179], v[56:59], a[36:39]\n\t"
    "v_mfma_f32_16x16x32_bf16 a[36:39], a[180:183], v[60:63], a[36:39]\n\t"
    "s_waitcnt lgkmcnt(2)\n\t"
    "v_mov_b32 v144, v144\n\t"
    "v_mov_b32 v80, v144\n\t"
    "v_mov_b32 v145, v148\n\t"
    "v_mov_b32 v81, v148\n\t"
    "v_mov_b32 v146, v146\n\t"
    "v_mov_b32 v82, v146\n\t"
    "v_mov_b32 v147, v152\n\t"
    "v_mov_b32 v83, v152\n\t"
    "v_permlane32_swap_b32 v144, v80\n\t"
    "v_permlane32_swap_b32 v145, v81\n\t"
    "v_permlane32_swap_b32 v146, v82\n\t"
    "v_permlane32_swap_b32 v147, v83\n\t"
    "s_nop 0\n\t"
    "v_add_f32 v144, v144, v80\n\t"
    "v_add_f32 v145, v145, v81\n\t"
    "v_add_f32 v146, v146, v82\n\t"
    "v_add_f32 v147, v147, v83\n\t"
    "v_mov_b32 v80, v144\n\t"
    "v_mov_b32 v81, v145\n\t"
    "v_mov_b32 v82, v146\n\t"
    "v_mov_b32 v83, v147\n\t"
    "s_nop 0\n\t"
    "v_permlane16_swap_b32 v144, v80\n\t"
    "v_permlane16_swap_b32 v145, v81\n\t"
    "v_permlane16_swap_b32 v146, v82\n\t"
    "v_permlane16_swap_b32 v147, v83\n\t"
    "s_nop 0\n\t"
    "v_add_f32 v144, v144, v80\n\t"
    "v_add_f32 v145, v145, v81\n\t"
    "v_add_f32 v146, v146, v82\n\t"
    "v_add_f32 v147, v147, v83\n\t"
    "s_mov_b32 s76, 0x5f800000\n\t"
    "v_add_f32 v81, v144, v145\n\t"
    "v_add_f32 v82, v146, v147\n\t"
    "v_add_f32 v81, v81, v82\n\t"
    "v_cmp_nge_f32 vcc, s76, v81\n\t"
    "v_rcp_f32 v144, v144\n\t"
    "v_rcp_f32 v145, v145\n\t"
    "v_rcp_f32 v146, v146\n\t"
    "v_rcp_f32 v147, v147\n\t"
    "s_or_b32 s76, vcc_lo, vcc_hi\n\t"
    "v_mov_b32 v81, s76\n\t"
    "s_lshl_b32 s77, s50, 2\n\t"
    "s_add_u32 s77, s77, 49152\n\t"
    "v_mov_b32 v82, s77\n\t"
    "ds_write_b32 v82, v81\n\t"
    "v_accvgpr_read_b32 v80, a0\n\t"
    "v_accvgpr_read_b32 v81, a1\n\t"
    "s_nop 0\n\t"
    "ds_read_b64_tr_b16 a[160:161], v136 offset:49152\n\t"
    "v_accvgpr_read_b32 v82, a2\n\t"
    "v_accvgpr_read_b32 v83, a3\n\t"
    "v_accvgpr_read_b32 v84, a16\n\t"
    "v_accvgpr_read_b32 v85, a17\n\t"
    "s_nop 0\n\t"
    "ds_read_b64_tr_b16 a[162:163], v136 offset:53248\n\t"
    "v_accvgpr_read_b32 v86, a18\n\t"
    "v_accvgpr_read_b32 v87, a19\n\t"
    "v_mfma_f32_16x16x32_bf16 a[40:43], a[176:179], v[64:67], a[40:43]\n\t"
    "v_mul_f32 v80, v80, v144\n\t"
    "v_mul_f32 v81, v81, v144\n\t"
    "v_mfma_f32_16x16x32_bf16 a[40:43], a[180:183], v[68:71], a[40:43]\n\t"
    "v_mul_f32 v82, v82, v144\n\t"
    "v_mul_f32 v83, v83, v144\n\t"
    "v_mfma_f32_16x16x32_bf16 a[44:47], a[176:179], v[72:75], a[44:47]\n\t"
    "v_mul_f32 v84, v84, v144\n\t"
    "v_mul_f32 v85, v85, v144\n\t"
    "v_mfma_f32_16x16x32_bf16 a[44:47], a[180:183], v[76:79], a[44:47]\n\t"
    "s_waitcnt lgkmcnt(2)\n\t"
    "v_mul_f32 v86, v86, v144\n\t"
    "v_mul_f32 v87, v87, v144\n\t"
    "v_cvt_pk_bf16_f32 v0, v80, v81\n\t"
    "v_cvt_pk_bf16_f32 v1, v82, v83\n\t"
    "s_nop 0\n\t"
    "ds_read_b64_tr_b16 a[164:165], v136 offset:57344\n\t"
    "v_cvt_pk_bf16_f32 v2, v84, v85\n\t"
    "v_cvt_pk_bf16_f32 v3, v86, v87\n\t"
    "s_add_u32 s79, s85, 0\n\t"
    "buffer_store_dwordx4 v[0:3], v255, s[68:71], s79 offen sc1\n\t"
    "v_accvgpr_read_b32 v88, a4\n\t"
    "v_accvgpr_read_b32 v89, a5\n\t"
    "s_nop 0\n\t"
    "ds_read_b64_tr_b16 a[166:167], v136 offset:61440\n\t"
    "v_accvgpr_read_b32 v90, a6\n\t"
    "v_accvgpr_read_b32 v91, a7\n\t"
    "v_mfma_f32_16x16x32_bf16 a[48:51], a[184:187], v[48:51], a[48:51]\n\t"
    "v_accvgpr_read_b32 v92, a20\n\t"
    "v_accvgpr_read_b32 v93, a21\n\t"
    "v_mfma_f32_16x16x32_bf16 a[48:51], a[188:191], v[52:55], a[48:51]\n\t"
    "v_accvgpr_read_b32 v94, a22\n\t"
    "v_accvgpr_read_b32 v95, a23\n\t"
    "v_mfma_f32_16x16x32_bf16 a[52:55], a[184:187], v[56:59], a[52:55]\n\t"
    "v_mul_f32 v88, v88, v145\n\t"
    "v_mul_f32 v89, v89, v145\n\t"
    "v_mfma_f32_16x16x32_bf16 a[52:55], a[188:191], v[60:63], a[52:55]\n\t"
    "s_waitcnt lgkmcnt(2)\n\t"
    "v_mul_f32 v90, v90, v145\n\t"
    "v_mul_f32 v91, v91, v145\n\t"
    "v_mul_f32 v92, v92, v145\n\t"
    "v_mul_f32 v93, v93, v145\n\t"
    "s_nop 0\n\t"
    "ds_read_b64_tr_b16 a[168:169], v137 offset:49152\n\t"
    "v_mul_f32 v94, v94, v145\n\t"
    "v_mul_f32 v95, v95, v145\n\t"
    "v_cvt_pk_bf16_f32 v4, v88, v89\n\t"
    "v_cvt_pk_bf16_f32 v5, v90, v91\n\t"
    "s_nop 0\n\t"
    "ds_read_b64_tr_b16 a[170:171], v137 offset:53248\n\t"
    "v_cvt_pk_bf16_f32 v6, v92, v93\n\t"
    "v_cvt_pk_bf16_f32 v7, v94, v95\n\t"
    "s_add_u32 s79, s85, 131072\n\t"
    "buffer_store_dwordx4 v[4:7], v255, s[68:71], s79 offen sc1\n\t"
    "v_mfma_f32_16x16x32_bf16 a[56:59], a[184:187], v[64:67], a[56:59]\n\t"
    "v_accvgpr_read_b32 v80, a8\n\t"
    "v_accvgpr_read_b32 v81, a9\n\t"
    "v_mfma_f32_16x16x32_bf16 a[56:59], a[188:191], v[68:71], a[56:59]\n\t"
    "v_accvgpr_read_b32 v82, a10\n\t"
    "v_accvgpr_read_b32 v83, a11\n\t"
    "v_mfma_f32_16x16x32_bf16 a[60:63], a[184:187], v[72:75], a[60:63]\n\t"
    "v_accvgpr_read_b32 v84, a24\n\t"
    "v_accvgpr_read_b32 v85, a25\n\t"
    "v_mfma_f32_16x16x32_bf16 a[60:63], a[188:191], v[76:79], a[60:63]\n\t"
    "s_waitcnt lgkmcnt(2)\n\t"
    "v_accvgpr_read_b32 v86, a26\n\t"
    "v_accvgpr_read_b32 v87, a27\n\t"
    "v_mul_f32 v80, v80, v146\n\t"
    "v_mul_f32 v81, v81, v146\n\t"
    "s_nop 0\n\t"
    "ds_read_b64_tr_b16 a[172:173], v137 offset:57344\n\t"
    "v_mul_f32 v82, v82, v146\n\t"
    "v_mul_f32 v83, v83, v146\n\t"
    "v_mul_f32 v84, v84, v146\n\t"
    "v_mul_f32 v85, v85, v146\n\t"
    "s_nop 0\n\t"
    "ds_read_b64_tr_b16 a[174:175], v137 offset:61440\n\t"
    "v_mul_f32 v86, v86, v146\n\t"
    "v_mul_f32 v87, v87, v146\n\t"
    "v_mfma_f32_16x16x32_bf16 a[64:67], a[160:163], v[48:51], a[64:67]\n\t"
    "v_cvt_pk_bf16_f32 v8, v80, v81\n\t"
    "v_cvt_pk_bf16_f32 v9, v82, v83\n\t"
    "v_mfma_f32_16x16x32_bf16 a[64:67], a[164:167], v[52:55], a[64:67]\n\t"
    "v_cvt_pk_bf16_f32 v10, v84, v85\n\t"
    "v_cvt_pk_bf16_f32 v11, v86, v87\n\t"
    "s_add_u32 s79, s85, 262144\n\t"
    "buffer_store_dwordx4 v[8:11], v255, s[68:71], s79 offen sc1\n\t"
    "v_mfma_f32_16x16x32_bf16 a[68:71], a[160:163], v[56:59], a[68:71]\n\t"
    "v_accvgpr_read_b32 v88, a12\n\t"
    "v_accvgpr_read_b32 v89, a13\n\t"
    "v_mfma_f32_16x16x32_bf16 a[68:71], a[164:167], v[60:63], a[68:71]\n\t"
    "s_waitcnt lgkmcnt(2)\n\t"
    "v_accvgpr_read_b32 v90, a14\n\t"
    "v_accvgpr_read_b32 v91, a15\n\t"
    "v_accvgpr_read_b32 v92, a28\n\t"
    "v_accvgpr_read_b32 v93, a29\n\t"
    "s_nop 0\n\t"
    "ds_read_b64_tr_b16 a[176:177], v138 offset:49152\n\t"
    "v_accvgpr_read_b32 v94, a30\n\t"
    "v_accvgpr_read_b32 v95, a31\n\t"
    "v_mul_f32 v88, v88, v147\n\t"
    "v_mul_f32 v89, v89, v147\n\t"
    "s_nop 0\n\t"
    "ds_read_b64_tr_b16 a[178:179], v138 offset:53248\n\t"
    "v_mul_f32 v90, v90, v147\n\t"
    "v_mul_f32 v91, v91, v147\n\t"
    "v_mfma_f32_16x16x32_bf16 a[72:75], a[160:163], v[64:67], a[72:75]\n\t"
    "v_mul_f32 v92, v92, v147\n\t"
    "v_mul_f32 v93, v93, v147\n\t"
    "v_mfma_f32_16x16x32_bf16 a[72:75], a[164:167], v[68:71], a[72:75]\n\t"
    "v_mul_f32 v94, v94, v147\n\t"
    "v_mul_f32 v95, v95, v147\n\t"
    "v_mfma_f32_16x16x32_bf16 a[76:79], a[160:163], v[72:75], a[76:79]\n\t"
    "v_cvt_pk_bf16_f32 v12, v88, v89\n\t"
    "v_cvt_pk_bf16_f32 v13, v90, v91\n\t"
    "v_mfma_f32_16x16x32_bf16 a[76:79], a[164:167], v[76:79], a[76:79]\n\t"
    "s_waitcnt lgkmcnt(2)\n\t"
    "v_cvt_pk_bf16_f32 v14, v92, v93\n\t"
    "v_cvt_pk_bf16_f32 v15, v94, v95\n\t"
    "s_add_u32 s79, s85, 393216\n\t"
    "buffer_store_dwordx4 v[12:15], v255, s[68:71], s79 offen sc1\n\t"
    "v_accvgpr_read_b32 v80, a32\n\t"
    "v_accvgpr_read_b32 v81, a33\n\t"
    "s_nop 0\n\t"
    "ds_read_b64_tr_b16 a[180:181], v138 offset:57344\n\t"
    "v_accvgpr_read_b32 v82, a34\n\t"
    "v_accvgpr_read_b32 v83, a35\n\t"
    "v_accvgpr_read_b32 v84, a48\n\t"
    "v_accvgpr_read_b32 v85, a49\n\t"
    "s_nop 0\n\t"
    "ds_read_b64_tr_b16 a[182:183], v138 offset:61440\n\t"
    "v_accvgpr_read_b32 v86, a50\n\t"
    "v_accvgpr_read_b32 v87, a51\n\t"
    "v_mfma_f32_16x16x32_bf16 a[80:83], a[168:171], v[48:51], a[80:83]\n\t"
    "v_mul_f32 v80, v80, v144\n\t"
    "v_mul_f32 v81, v81, v144\n\t"
    "v_mfma_f32_16x16x32_bf16 a[80:83], a[172:175], v[52:55], a[80:83]\n\t"
    "v_mul_f32 v82, v82, v144\n\t"
    "v_mul_f32 v83, v83, v144\n\t"
    "v_mfma_f32_16x16x32_bf16 a[84:87], a[168:171], v[56:59], a[84:87]\n\t"
    "v_mul_f32 v84, v84, v144\n\t"
    "v_mul_f32 v85, v85, v144\n\t"
    "v_mfma_f32_16x16x32_bf16 a[84:87], a[172:175], v[60:63], a[84:87]\n\t"
    "s_waitcnt lgkmcnt(2)\n\t"
    "v_mul_f32 v86, v86, v144\n\t"
    "v_mul_f32 v87, v87, v144\n\t"
    "v_cvt_pk_bf16_f32 v16, v80, v81\n\t"
    "v_cvt_pk_bf16_f32 v17, v82, v83\n\t"
    "s_nop 0\n\t"
    "ds_read_b64_tr_b16 a[184:185], v139 offset:49152\n\t"
    "v_cvt_pk_bf16_f32 v18, v84, v85\n\t"
    "v_cvt_pk_bf16_f32 v19, v86, v87\n\t"
    "s_add_u32 s79, s85, 1024\n\t"
    "buffer_store_dwordx4 v[16:19], v255, s[68:71], s79 offen sc1\n\t"
    "v_accvgpr_read_b32 v88, a36\n\t"
    "v_accvgpr_read_b32 v89, a37\n\t"
    "s_nop 0\n\t"
    "ds_read_b64_tr_b16 a[186:187], v139 offset:53248\n\t"
    "v_accvgpr_read_b32 v90, a38\n\t"
    "v_accvgpr_read_b32 v91, a39\n\t"
    "v_mfma_f32_16x16x32_bf16 a[88:91], a[168:171], v[64:67], a[88:91]\n\t"
    "v_accvgpr_read_b32 v92, a52\n\t"
    "v_accvgpr_read_b32 v93, a53\n\t"
    "v_mfma_f32_16x16x32_bf16 a[88:91], a[172:175], v[68:71], a[88:91]\n\t"
    "v_accvgpr_read_b32 v94, a54\n\t"
    "v_accvgpr_read_b32 v95, a55\n\t"
    "v_mfma_f32_16x16x32_bf16 a[92:95], a[168:171], v[72:75], a[92:95]\n\t"
    "v_mul_f32 v88, v88, v145\n\t"
    "v_mul_f32 v89, v89, v145\n\t"
    "v_mfma_f32_16x16x32_bf16 a[92:95], a[172:175], v[76:79], a[92:95]\n\t"
    "s_waitcnt lgkmcnt(2)\n\t"
    "v_mul_f32 v90, v90, v145\n\t"
    "v_mul_f32 v91, v91, v145\n\t"
    "v_mul_f32 v92, v92, v145\n\t"
    "v_mul_f32 v93, v93, v145\n\t"
    "s_nop 0\n\t"
    "ds_read_b64_tr_b16 a[188:189], v139 offset:57344\n\t"
    "v_mul_f32 v94, v94, v145\n\t"
    "v_mul_f32 v95, v95, v145\n\t"
    "v_cvt_pk_bf16_f32 v20, v88, v89\n\t"
    "v_cvt_pk_bf16_f32 v21, v90, v91\n\t"
    "s_nop 0\n\t"
    "ds_read_b64_tr_b16 a[190:191], v139 offset:61440\n\t"
    "v_cvt_pk_bf16_f32 v22, v92, v93\n\t"
    "v_cvt_pk_bf16_f32 v23, v94, v95\n\t"
    "s_add_u32 s79, s85, 132096\n\t"
    "buffer_store_dwordx4 v[20:23], v255, s[68:71], s79 offen sc1\n\t"
    "v_mfma_f32_16x16x32_bf16 a[96:99], a[176:179], v[48:51], a[96:99]\n\t"
    "v_accvgpr_read_b32 v80, a40\n\t"
    "v_accvgpr_read_b32 v81, a41\n\t"
    "v_mfma_f32_16x16x32_bf16 a[96:99], a[180:183], v[52:55], a[96:99]\n\t"
    "v_accvgpr_read_b32 v82, a42\n\t"
    "v_accvgpr_read_b32 v83, a43\n\t"
    "v_mfma_f32_16x16x32_bf16 a[100:103], a[176:179], v[56:59], a[100:103]\n\t"
    "v_accvgpr_read_b32 v84, a56\n\t"
    "v_accvgpr_read_b32 v85, a57\n\t"
    "v_mfma_f32_16x16x32_bf16 a[100:103], a[180:183], v[60:63], a[100:103]\n\t"
    "s_waitcnt lgkmcnt(2)\n\t"
    "v_accvgpr_read_b32 v86, a58\n\t"
    "v_accvgpr_read_b32 v87, a59\n\t"
    "v_mul_f32 v80, v80, v146\n\t"
    "v_mul_f32 v81, v81, v146\n\t"
    "v_mul_f32 v82, v82, v146\n\t"
    "v_mul_f32 v83, v83, v146\n\t"
    "v_mul_f32 v84, v84, v146\n\t"
    "v_mul_f32 v85, v85, v146\n\t"
    "v_mul_f32 v86, v86, v146\n\t"
    "v_mul_f32 v87, v87, v146\n\t"
    "v_mfma_f32_16x16x32_bf16 a[104:107], a[176:179], v[64:67], a[104:107]\n\t"
    "v_cvt_pk_bf16_f32 v24, v80, v81\n\t"
    "v_cvt_pk_bf16_f32 v25, v82, v83\n\t"
    "v_mfma_f32_16x16x32_bf16 a[104:107], a[180:183], v[68:71], a[104:107]\n\t"
    "v_cvt_pk_bf16_f32 v26, v84, v85\n\t"
    "v_cvt_pk_bf16_f32 v27, v86, v87\n\t"
    "s_add_u32 s79, s85, 263168\n\t"
    "buffer_store_dwordx4 v[24:27], v255, s[68:71], s79 offen sc1\n\t"
    "v_mfma_f32_16x16x32_bf16 a[108:111], a[176:179], v[72:75], a[108:111]\n\t"
    "v_accvgpr_read_b32 v88, a44\n\t"
    "v_accvgpr_read_b32 v89, a45\n\t"
    "v_mfma_f32_16x16x32_bf16 a[108:111], a[180:183], v[76:79], a[108:111]\n\t"
    "s_waitcnt lgkmcnt(0)\n\t"
    "v_accvgpr_read_b32 v90, a46\n\t"
    "v_accvgpr_read_b32 v91, a47\n\t"
    "v_accvgpr_read_b32 v92, a60\n\t"
    "v_accvgpr_read_b32 v93, a61\n\t"
    "v_accvgpr_read_b32 v94, a62\n\t"
    "v_accvgpr_read_b32 v95, a63\n\t"
    "v_mul_f32 v88, v88, v147\n\t"
    "v_mul_f32 v89, v89, v147\n\t"
    "v_mul_f32 v90, v90, v147\n\t"
    "v_mul_f32 v91, v91, v147\n\t"
    "v_mfma_f32_16x16x32_bf16 a[112:115], a[184:187], v[48:51], a[112:115]\n\t"
    "v_mul_f32 v92, v92, v147\n\t"
    "v_mul_f32 v93, v93, v147\n\t"
    "v_mfma_f32_16x16x32_bf16 a[112:115], a[188:191], v[52:55], a[112:115]\n\t"
    "v_mul_f32 v94, v94, v147\n\t"
    "v_mul_f32 v95, v95, v147\n\t"
    "v_mfma_f32_16x16x32_bf16 a[116:119], a[184:187], v[56:59], a[116:119]\n\t"
    "v_cvt_pk_bf16_f32 v28, v88, v89\n\t"
    "v_cvt_pk_bf16_f32 v29, v90, v91\n\t"
    "v_mfma_f32_16x16x32_bf16 a[116:119], a[188:191], v[60:63], a[116:119]\n\t"
    "s_waitcnt lgkmcnt(0)\n\t"
    "v_cvt_pk_bf16_f32 v30, v92, v93\n\t"
    "v_cvt_pk_bf16_f32 v31, v94, v95\n\t"
    "s_add_u32 s79, s85, 394240\n\t"
    "buffer_store_dwordx4 v[28:31], v255, s[68:71], s79 offen sc1\n\t"
    "v_accvgpr_read_b32 v80, a64\n\t"
    "v_accvgpr_read_b32 v81, a65\n\t"
    "v_accvgpr_read_b32 v82, a66\n\t"
    "v_accvgpr_read_b32 v83, a67\n\t"
    "v_accvgpr_read_b32 v84, a80\n\t"
    "v_accvgpr_read_b32 v85, a81\n\t"
    "v_accvgpr_read_b32 v86, a82\n\t"
    "v_accvgpr_read_b32 v87, a83\n\t"
    "v_mfma_f32_16x16x32_bf16 a[120:123], a[184:187], v[64:67], a[120:123]\n\t"
    "v_mul_f32 v80, v80, v144\n\t"
    "v_mul_f32 v81, v81, v144\n\t"
    "v_mfma_f32_16x16x32_bf16 a[120:123], a[188:191], v[68:71], a[120:123]\n\t"
    "v_mul_f32 v82, v82, v144\n\t"
    "v_mul_f32 v83, v83, v144\n\t"
    "v_mfma_f32_16x16x32_bf16 a[124:127], a[184:187], v[72:75], a[124:127]\n\t"
    "v_mul_f32 v84, v84, v144\n\t"
    "v_mul_f32 v85, v85, v144\n\t"
    "v_mfma_f32_16x16x32_bf16 a[124:127], a[188:191], v[76:79], a[124:127]\n\t"
    "s_waitcnt lgkmcnt(0)\n\t"
    "v_mul_f32 v86, v86, v144\n\t"
    "v_mul_f32 v87, v87, v144\n\t"
    "s_nop 7\n\t"
    "v_cvt_pk_bf16_f32 v32, v80, v81\n\t"
    "v_cvt_pk_bf16_f32 v33, v82, v83\n\t"
    "v_cvt_pk_bf16_f32 v34, v84, v85\n\t"
    "v_cvt_pk_bf16_f32 v35, v86, v87\n\t"
    "s_add_u32 s79, s85, 2048\n\t"
    "buffer_store_dwordx4 v[32:35], v255, s[68:71], s79 offen sc1\n\t"
    "v_accvgpr_read_b32 v88, a68\n\t"
    "v_accvgpr_read_b32 v89, a69\n\t"
    "v_accvgpr_read_b32 v90, a70\n\t"
    "v_accvgpr_read_b32 v91, a71\n\t"
    "v_accvgpr_read_b32 v92, a84\n\t"
    "v_accvgpr_read_b32 v93, a85\n\t"
    "v_accvgpr_read_b32 v94, a86\n\t"
    "v_accvgpr_read_b32 v95, a87\n\t"
    "v_mul_f32 v88, v88, v145\n\t"
    "v_mul_f32 v89, v89, v145\n\t"
    "v_mul_f32 v90, v90, v145\n\t"
    "v_mul_f32 v91, v91, v145\n\t"
    "v_mul_f32 v92, v92, v145\n\t"
    "v_mul_f32 v93, v93, v145\n\t"
    "v_mul_f32 v94, v94, v145\n\t"
    "v_mul_f32 v95, v95, v145\n\t"
    "v_cvt_pk_bf16_f32 v36, v88, v89\n\t"
    "v_cvt_pk_bf16_f32 v37, v90, v91\n\t"
    "v_cvt_pk_bf16_f32 v38, v92, v93\n\t"
    "v_cvt_pk_bf16_f32 v39, v94, v95\n\t"
    "s_add_u32 s79, s85, 133120\n\t"
    "buffer_store_dwordx4 v[36:39], v255, s[68:71], s79 offen sc1\n\t"
    "v_accvgpr_read_b32 v80, a72\n\t"
    "v_accvgpr_read_b32 v81, a73\n\t"
    "v_accvgpr_read_b32 v82, a74\n\t"
    "v_accvgpr_read_b32 v83, a75\n\t"
    "v_accvgpr_read_b32 v84, a88\n\t"
    "v_accvgpr_read_b32 v85, a89\n\t"
    "v_accvgpr_read_b32 v86, a90\n\t"
    "v_accvgpr_read_b32 v87, a91\n\t"
    "v_mul_f32 v80, v80, v146\n\t"
    "v_mul_f32 v81, v81, v146\n\t"
    "v_mul_f32 v82, v82, v146\n\t"
    "v_mul_f32 v83, v83, v146\n\t"
    "v_mul_f32 v84, v84, v146\n\t"
    "v_mul_f32 v85, v85, v146\n\t"
    "v_mul_f32 v86, v86, v146\n\t"
    "v_mul_f32 v87, v87, v146\n\t"
    "v_cvt_pk_bf16_f32 v40, v80, v81\n\t"
    "v_cvt_pk_bf16_f32 v41, v82, v83\n\t"
    "v_cvt_pk_bf16_f32 v42, v84, v85\n\t"
    "v_cvt_pk_bf16_f32 v43, v86, v87\n\t"
    "s_add_u32 s79, s85, 264192\n\t"
    "buffer_store_dwordx4 v[40:43], v255, s[68:71], s79 offen sc1\n\t"
    "v_accvgpr_read_b32 v88, a76\n\t"
    "v_accvgpr_read_b32 v89, a77\n\t"
    "v_accvgpr_read_b32 v90, a78\n\t"
    "v_accvgpr_read_b32 v91, a79\n\t"
    "v_accvgpr_read_b32 v92, a92\n\t"
    "v_accvgpr_read_b32 v93, a93\n\t"
    "v_accvgpr_read_b32 v94, a94\n\t"
    "v_accvgpr_read_b32 v95, a95\n\t"
    "v_mul_f32 v88, v88, v147\n\t"
    "v_mul_f32 v89, v89, v147\n\t"
    "v_mul_f32 v90, v90, v147\n\t"
    "v_mul_f32 v91, v91, v147\n\t"
    "v_mul_f32 v92, v92, v147\n\t"
    "v_mul_f32 v93, v93, v147\n\t"
    "v_mul_f32 v94, v94, v147\n\t"
    "v_mul_f32 v95, v95, v147\n\t"
    "v_cvt_pk_bf16_f32 v44, v88, v89\n\t"
    "v_cvt_pk_bf16_f32 v45, v90, v91\n\t"
    "v_cvt_pk_bf16_f32 v46, v92, v93\n\t"
    "v_cvt_pk_bf16_f32 v47, v94, v95\n\t"
    "s_add_u32 s79, s85, 395264\n\t"
    "buffer_store_dwordx4 v[44:47], v255, s[68:71], s79 offen sc1\n\t"
    "v_accvgpr_read_b32 v80, a96\n\t"
    "v_accvgpr_read_b32 v81, a97\n\t"
    "v_accvgpr_read_b32 v82, a98\n\t"
    "v_accvgpr_read_b32 v83, a99\n\t"
    "v_accvgpr_read_b32 v84, a112\n\t"
    "v_accvgpr_read_b32 v85, a113\n\t"
    "v_accvgpr_read_b32 v86, a114\n\t"
    "v_accvgpr_read_b32 v87, a115\n\t"
    "v_mul_f32 v80, v80, v144\n\t"
    "v_mul_f32 v81, v81, v144\n\t"
    "v_mul_f32 v82, v82, v144\n\t"
    "v_mul_f32 v83, v83, v144\n\t"
    "v_mul_f32 v84, v84, v144\n\t"
    "v_mul_f32 v85, v85, v144\n\t"
    "v_mul_f32 v86, v86, v144\n\t"
    "v_mul_f32 v87, v87, v144\n\t"
    "v_cvt_pk_bf16_f32 v48, v80, v81\n\t"
    "v_cvt_pk_bf16_f32 v49, v82, v83\n\t"
    "v_cvt_pk_bf16_f32 v50, v84, v85\n\t"
    "v_cvt_pk_bf16_f32 v51, v86, v87\n\t"
    "s_add_u32 s79, s85, 3072\n\t"
    "buffer_store_dwordx4 v[48:51], v255, s[68:71], s79 offen sc1\n\t"
    "v_accvgpr_read_b32 v88, a100\n\t"
    "v_accvgpr_read_b32 v89, a101\n\t"
    "v_accvgpr_read_b32 v90, a102\n\t"
    "v_accvgpr_read_b32 v91, a103\n\t"
    "v_accvgpr_read_b32 v92, a116\n\t"
    "v_accvgpr_read_b32 v93, a117\n\t"
    "v_accvgpr_read_b32 v94, a118\n\t"
    "v_accvgpr_read_b32 v95, a119\n\t"
    "v_mul_f32 v88, v88, v145\n\t"
    "v_mul_f32 v89, v89, v145\n\t"
    "v_mul_f32 v90, v90, v145\n\t"
    "v_mul_f32 v91, v91, v145\n\t"
    "v_mul_f32 v92, v92, v145\n\t"
    "v_mul_f32 v93, v93, v145\n\t"
    "v_mul_f32 v94, v94, v145\n\t"
    "v_mul_f32 v95, v95, v145\n\t"
    "v_cvt_pk_bf16_f32 v52, v88, v89\n\t"
    "v_cvt_pk_bf16_f32 v53, v90, v91\n\t"
    "v_cvt_pk_bf16_f32 v54, v92, v93\n\t"
    "v_cvt_pk_bf16_f32 v55, v94, v95\n\t"
    "s_add_u32 s79, s85, 134144\n\t"
    "buffer_store_dwordx4 v[52:55], v255, s[68:71], s79 offen sc1\n\t"
    "v_accvgpr_read_b32 v80, a104\n\t"
    "v_accvgpr_read_b32 v81, a105\n\t"
    "v_accvgpr_read_b32 v82, a106\n\t"
    "v_accvgpr_read_b32 v83, a107\n\t"
    "v_accvgpr_read_b32 v84, a120\n\t"
    "v_accvgpr_read_b32 v85, a121\n\t"
    "v_accvgpr_read_b32 v86, a122\n\t"
    "v_accvgpr_read_b32 v87, a123\n\t"
    "v_mul_f32 v80, v80, v146\n\t"
    "v_mul_f32 v81, v81, v146\n\t"
    "v_mul_f32 v82, v82, v146\n\t"
    "v_mul_f32 v83, v83, v146\n\t"
    "v_mul_f32 v84, v84, v146\n\t"
    "v_mul_f32 v85, v85, v146\n\t"
    "v_mul_f32 v86, v86, v146\n\t"
    "v_mul_f32 v87, v87, v146\n\t"
    "v_cvt_pk_bf16_f32 v56, v80, v81\n\t"
    "v_cvt_pk_bf16_f32 v57, v82, v83\n\t"
    "v_cvt_pk_bf16_f32 v58, v84, v85\n\t"
    "v_cvt_pk_bf16_f32 v59, v86, v87\n\t"
    "s_add_u32 s79, s85, 265216\n\t"
    "buffer_store_dwordx4 v[56:59], v255, s[68:71], s79 offen sc1\n\t"
    "v_accvgpr_read_b32 v88, a108\n\t"
    "v_accvgpr_read_b32 v89, a109\n\t"
    "v_accvgpr_read_b32 v90, a110\n\t"
    "v_accvgpr_read_b32 v91, a111\n\t"
    "v_accvgpr_read_b32 v92, a124\n\t"
    "v_accvgpr_read_b32 v93, a125\n\t"
    "v_accvgpr_read_b32 v94, a126\n\t"
    "v_accvgpr_read_b32 v95, a127\n\t"
    "v_mul_f32 v88, v88, v147\n\t"
    "v_mul_f32 v89, v89, v147\n\t"
    "v_mul_f32 v90, v90, v147\n\t"
    "v_mul_f32 v91, v91, v147\n\t"
    "v_mul_f32 v92, v92, v147\n\t"
    "v_mul_f32 v93, v93, v147\n\t"
    "v_mul_f32 v94, v94, v147\n\t"
    "v_mul_f32 v95, v95, v147\n\t"
    "v_cvt_pk_bf16_f32 v60, v88, v89\n\t"
    "v_cvt_pk_bf16_f32 v61, v90, v91\n\t"
    "v_cvt_pk_bf16_f32 v62, v92, v93\n\t"
    "v_cvt_pk_bf16_f32 v63, v94, v95\n\t"
    "s_add_u32 s79, s85, 396288\n\t"
    "buffer_store_dwordx4 v[60:63], v255, s[68:71], s79 offen sc1\n\t"
    "s_waitcnt vmcnt(28)\n\t"
    "ds_read_b64_tr_b16 v[232:233], v230 offset:0\n\t"
    "ds_read_b64_tr_b16 v[234:235], v230 offset:2048\n\t"
    "ds_read_b64_tr_b16 v[236:237], v230 offset:4096\n\t"
    "ds_read_b64_tr_b16 v[238:239], v230 offset:6144\n\t"
    "s_waitcnt lgkmcnt(0)\n\t"
    "s_waitcnt vmcnt(20)\n\t"
    "v_mfma_f32_16x16x32_bf16 a[192:195], v[232:235], v[96:99], a[192:195]\n\t"
    "ds_read_b64_tr_b16 v[240:241], v231 offset:0\n\t"
    "v_mfma_f32_16x16x32_bf16 a[192:195], v[236:239], v[100:103], a[192:195]\n\t"
    "ds_read_b64_tr_b16 v[242:243], v231 offset:2048\n\t"
    "v_mfma_f32_16x16x32_bf16 a[200:203], v[232:235], v[104:107], a[200:203]\n\t"
    "ds_read_b64_tr_b16 v[244:245], v231 offset:4096\n\t"
    "v_mfma_f32_16x16x32_bf16 a[200:203], v[236:239], v[108:111], a[200:203]\n\t"
    "ds_read_b64_tr_b16 v[246:247], v231 offset:6144\n\t"
    "v_mfma_f32_16x16x32_bf16 a[208:211], v[232:235], v[112:115], a[208:211]\n\t"
    "v_mfma_f32_16x16x32_bf16 a[208:211], v[236:239], v[116:119], a[208:211]\n\t"
    "v_mfma_f32_16x16x32_bf16 a[216:219], v[232:235], v[120:123], a[216:219]\n\t"
    "v_mfma_f32_16x16x32_bf16 a[216:219], v[236:239], v[124:127], a[216:219]\n\t"
    "s_waitcnt lgkmcnt(0)\n\t"
    "v_mfma_f32_16x16x32_bf16 a[196:199], v[240:243], v[96:99], a[196:199]\n\t"
    "ds_read_b64_tr_b16 v[232:233], v248 offset:0\n\t"
    "v_mfma_f32_16x16x32_bf16 a[196:199], v[244:247], v[100:103], a[196:199]\n\t"
    "ds_read_b64_tr_b16 v[234:235], v248 offset:2048\n\t"
    "v_mfma_f32_16x16x32_bf16 a[204:207], v[240:243], v[104:107], a[204:207]\n\t"
    "ds_read_b64_tr_b16 v[236:237], v248 offset:4096\n\t"
    "v_mfma_f32_16x16x32_bf16 a[204:207], v[244:247], v[108:111], a[204:207]\n\t"
    "ds_read_b64_tr_b16 v[238:239], v248 offset:6144\n\t"
    "v_mfma_f32_16x16x32_bf16 a[212:215], v[240:243], v[112:115], a[212:215]\n\t"
    "v_mfma_f32_16x16x32_bf16 a[212:215], v[244:247], v[116:119], a[212:215]\n\t"
    "v_mfma_f32_16x16x32_bf16 a[220:223], v[240:243], v[120:123], a[220:223]\n\t"
    "v_mfma_f32_16x16x32_bf16 a[220:223], v[244:247], v[124:127], a[220:223]\n\t"
    "s_waitcnt lgkmcnt(0)\n\t"
    "v_mfma_f32_16x16x32_bf16 a[224:227], v[232:235], v[96:99], a[224:227]\n\t"
    "ds_read_b64_tr_b16 v[240:241], v249 offset:0\n\t"
    "v_mfma_f32_16x16x32_bf16 a[224:227], v[236:239], v[100:103], a[224:227]\n\t"
    "ds_read_b64_tr_b16 v[242:243], v249 offset:2048\n\t"
    "v_mfma_f32_16x16x32_bf16 a[232:235], v[232:235], v[104:107], a[232:235]\n\t"
    "ds_read_b64_tr_b16 v[244:245], v249 offset:4096\n\t"
    "v_mfma_f32_16x16x32_bf16 a[232:235], v[236:239], v[108:111], a[232:235]\n\t"
    "ds_read_b64_tr_b16 v[246:247], v249 offset:6144\n\t"
    "v_mfma_f32_16x16x32_bf16 a[240:243], v[232:235], v[112:115], a[240:243]\n\t"
    "v_mfma_f32_16x16x32_bf16 a[240:243], v[236:239], v[116:119], a[240:243]\n\t"
    "v_mfma_f32_16x16x32_bf16 a[248:251], v[232:235], v[120:123], a[248:251]\n\t"
    "v_mfma_f32_16x16x32_bf16 a[248:251], v[236:239], v[124:127], a[248:251]\n\t"
    "s_waitcnt lgkmcnt(0)\n\t"
    "v_mfma_f32_16x16x32_bf16 a[228:231], v[240:243], v[96:99], a[228:231]\n\t"
    "v_mfma_f32_16x16x32_bf16 a[228:231], v[244:247], v[100:103], a[228:231]\n\t"
    "v_mfma_f32_16x16x32_bf16 a[236:239], v[240:243], v[104:107], a[236:239]\n\t"
    "v_mfma_f32_16x16x32_bf16 a[236:239], v[244:247], v[108:111], a[236:239]\n\t"
    "v_mfma_f32_16x16x32_bf16 a[244:247], v[240:243], v[112:115], a[244:247]\n\t"
    "v_mfma_f32_16x16x32_bf16 a[244:247], v[244:247], v[116:119], a[244:247]\n\t"
    "v_mfma_f32_16x16x32_bf16 a[252:255], v[240:243], v[120:123], a[252:255]\n\t"
    "v_mfma_f32_16x16x32_bf16 a[252:255], v[244:247], v[124:127], a[252:255]\n\t"
    "s_waitcnt vmcnt(60) lgkmcnt(0)\n\t"
    "s_barrier\n\t"
    "v_mov_b32 v80, 49152\n\t"
    "ds_read_b128 v[84:87], v80\n\t"
    "s_waitcnt lgkmcnt(0)\n\t"
    "v_or_b32 v84, v84, v85\n\t"
    "v_or_b32 v86, v86, v87\n\t"
    "v_or_b32 v84, v84, v86\n\t"
    "s_nop 0\n\t"
    "v_readfirstlane_b32 s76, v84\n\t"
    "s_nop 3\n\t"
    "s_cmp_eq_u32 s76, 0\n\t"
    "s_cbranch_scc1 .Lsafe%=\n\t"
    "s_cmp_eq_u32 s91, 0\n\t"
    "s_cbranch_scc1 .Lfb%=\n\t"
    ".Lsafe%=:\n\t"
    "v_add_u32 v224, 4294901760, v132\n\t"
    "v_add_u32 v225, 4294901760, v133\n\t"
    "v_add_u32 v226, 4294901760, v134\n\t"
    "v_add_u32 v227, 4294901760, v135\n\t"
    "v_add_u32 v228, 4294901760, v136\n\t"
    "v_add_u32 v229, 4294901760, v137\n\t"
    "v_add_u32 v230, 4294901760, v138\n\t"
    "v_add_u32 v231, 4294901760, v139\n\t"
    "v_lshrrev_b32 v252, 8, v255\n\t"
    "v_lshlrev_b32 v252, 3, v252\n\t"
    "ds_read_b64_tr_b16 v[192:193], v224 offset:0\n\t"
    "ds_read_b64_tr_b16 v[194:195], v224 offset:4096\n\t"
    "ds_read_b64_tr_b16 v[196:197], v224 offset:8192\n\t"
    "ds_read_b64_tr_b16 v[198:199], v224 offset:12288\n\t"
    "ds_read_b64_tr_b16 v[200:201], v224 offset:16384\n\t"
    "ds_read_b64_tr_b16 v[202:203], v224 offset:20480\n\t"
    "ds_read_b64_tr_b16 v[204:205], v224 offset:24576\n\t"
    "ds_read_b64_tr_b16 v[206:207], v224 offset:28672\n\t"
    "s_waitcnt lgkmcnt(0)\n\t"
    "v_mfma_f32_16x16x32_bf16 v[64:67], v[192:195], v[0:3], 0\n\t"
    "ds_read_b64_tr_b16 v[208:209], v225 offset:0\n\t"
    "v_mfma_f32_16x16x32_bf16 v[64:67], v[196:199], v[16:19], v[64:67]\n\t"
    "ds_read_b64_tr_b16 v[210:211], v225 offset:4096\n\t"
    "v_mfma_f32_16x16x32_bf16 v[64:67], v[200:203], v[32:35], v[64:67]\n\t"
    "ds_read_b64_tr_b16 v[212:213], v225 offset:8192\n\t"
    "v_mfma_f32_16x16x32_bf16 v[64:67], v[204:207], v[48:51], v[64:67]\n\t"
    "ds_read_b64_tr_b16 v[214:215], v225 offset:12288\n\t"
    "v_mfma_f32_16x16x32_bf16 v[72:75], v[192:195], v[4:7], 0\n\t"
    "ds_read_b64_tr_b16 v[216:217], v225 offset:16384\n\t"
    "v_mfma_f32_16x16x32_bf16 v[72:75], v[196:199], v[20:23], v[72:75]\n\t"
    "ds_read_b64_tr_b16 v[218:219], v225 offset:20480\n\t"
    "v_mfma_f32_16x16x32_bf16 v[72:75], v[200:203], v[36:39], v[72:75]\n\t"
    "ds_read_b64_tr_b16 v[220:221], v225 offset:24576\n\t"
    "v_mfma_f32_16x16x32_bf16 v[72:75], v[204:207], v[52:55], v[72:75]\n\t"
    "ds_read_b64_tr_b16 v[222:223], v225 offset:28672\n\t"
    "v_mfma_f32_16x16x32_bf16 v[80:83], v[192:195], v[8:11], 0\n\t"
    "global_load_dwordx2 v[232:233], v252, s[88:89] offset:0\n\t"
    "v_mfma_f32_16x16x32_bf16 v[80:83], v[196:199], v[24:27], v[80:83]\n\t"
    "v_mfma_f32_16x16x32_bf16 v[80:83], v[200:203], v[40:43], v[80:83]\n\t"
    "global_load_dwordx2 v[234:235], v252, s[88:89] offset:32\n\t"
    "v_mfma_f32_16x16x32_bf16 v[80:83], v[204:207], v[56:59], v[80:83]\n\t"
    "v_mfma_f32_16x16x32_bf16 v[88:91], v[192:195], v[12:15], 0\n\t"
    "global_load_dwordx2 v[236:237], v252, s[88:89] offset:64\n\t"
    "v_mfma_f32_16x16x32_bf16 v[88:91], v[196:199], v[28:31], v[88:91]\n\t"
    "v_mfma_f32_16x16x32_bf16 v[88:91], v[200:203], v[44:47], v[88:91]\n\t"
    "global_load_dwordx2 v[238:239], v252, s[88:89] offset:96\n\t"
    "v_mfma_f32_16x16x32_bf16 v[88:91], v[204:207], v[60:63], v[88:91]\n\t"
    "s_waitcnt lgkmcnt(0)\n\t"
    "v_mfma_f32_16x16x32_bf16 v[68:71], v[208:211], v[0:3], 0\n\t"
    "ds_read_b64_tr_b16 v[192:193], v226 offset:0\n\t"
    "v_mfma_f32_16x16x32_bf16 v[68:71], v[212:215], v[16:19], v[68:71]\n\t"
    "ds_read_b64_tr_b16 v[194:195], v226 offset:4096\n\t"
    "v_mfma_f32_16x16x32_bf16 v[68:71], v[216:219], v[32:35], v[68:71]\n\t"
    "ds_read_b64_tr_b16 v[196:197], v226 offset:8192\n\t"
    "v_mfma_f32_16x16x32_bf16 v[68:71], v[220:223], v[48:51], v[68:71]\n\t"
    "ds_read_b64_tr_b16 v[198:199], v226 offset:12288\n\t"
    "v_mfma_f32_16x16x32_bf16 v[76:79], v[208:211], v[4:7], 0\n\t"
    "ds_read_b64_tr_b16 v[200:201], v226 offset:16384\n\t"
    "v_mfma_f32_16x16x32_bf16 v[76:79], v[212:215], v[20:23], v[76:79]\n\t"
    "ds_read_b64_tr_b16 v[202:203], v226 offset:20480\n\t"
    "v_mfma_f32_16x16x32_bf16 v[76:79], v[216:219], v[36:39], v[76:79]\n\t"
    "ds_read_b64_tr_b16 v[204:205], v226 offset:24576\n\t"
    "v_mfma_f32_16x16x32_bf16 v[76:79], v[220:223], v[52:55], v[76:79]\n\t"
    "ds_read_b64_tr_b16 v[206:207], v226 offset:28672\n\t"
    "v_mfma_f32_16x16x32_bf16 v[84:87], v[208:211], v[8:11], 0\n\t"
    "s_add_u32 m0, s80, 98304\n\t"
    "s_add_u32 s76, s54, 0\n\t"
    "buffer_load_dwordx4 v254, s[64:67], s76 offen lds\n\t"
    "v_mfma_f32_16x16x32_bf16 v[84:87], v[212:215], v[24:27], v[84:87]\n\t"
    "v_mfma_f32_16x16x32_bf16 v[84:87], v[216:219], v[40:43], v[84:87]\n\t"
    "s_add_u32 m0, s80, 102400\n\t"
    "s_add_u32 s76, s54, 16384\n\t"
    "buffer_load_dwordx4 v254, s[64:67], s76 offen lds\n\t"
    "v_mfma_f32_16x16x32_bf16 v[84:87], v[220:223], v[56:59], v[84:87]\n\t"
    "v_mfma_f32_16x16x32_bf16 v[92:95], v[208:211], v[12:15], 0\n\t"
    "s_add_u32 m0, s80, 106496\n\t"
    "s_add_u32 s76, s54, 32768\n\t"
    "buffer_load_dwordx4 v254, s[64:67], s76 offen lds\n\t"
    "v_mfma_f32_16x16x32_bf16 v[92:95], v[212:215], v[28:31], v[92:95]\n\t"
    "v_mfma_f32_16x16x32_bf16 v[92:95], v[216:219], v[44:47], v[92:95]\n\t"
    "s_add_u32 m0, s80, 110592\n\t"
    "s_add_u32 s76, s54, 49152\n\t"
    "buffer_load_dwordx4 v254, s[64:67], s76 offen lds\n\t"
    "v_mfma_f32_16x16x32_bf16 v[92:95], v[220:223], v[60:63], v[92:95]\n\t"
    "s_waitcnt lgkmcnt(0)\n\t"
    "v_mfma_f32_16x16x32_bf16 v[96:99], v[192:195], v[0:3], 0\n\t"
    "ds_read_b64_tr_b16 v[208:209], v227 offset:0\n\t"
    "v_mfma_f32_16x16x32_bf16 v[96:99], v[196:199], v[16:19], v[96:99]\n\t"
    "ds_read_b64_tr_b16 v[210:211], v227 offset:4096\n\t"
    "v_mfma_f32_16x16x32_bf16 v[96:99], v[200:203], v[32:35], v[96:99]\n\t"
    "ds_read_b64_tr_b16 v[212:213], v227 offset:8192\n\t"
    "v_mfma_f32_16x16x32_bf16 v[96:99], v[204:207], v[48:51], v[96:99]\n\t"
    "ds_read_b64_tr_b16 v[214:215], v227 offset:12288\n\t"
    "v_mfma_f32_16x16x32_bf16 v[104:107], v[192:195], v[4:7], 0\n\t"
    "ds_read_b64_tr_b16 v[216:217], v227 offset:16384\n\t"
    "v_mfma_f32_16x16x32_bf16 v[104:107], v[196:199], v[20:23], v[104:107]\n\t"
    "ds_read_b64_tr_b16 v[218:219], v227 offset:20480\n\t"
    "v_mfma_f32_16x16x32_bf16 v[104:107], v[200:203], v[36:39], v[104:107]\n\t"
    "ds_read_b64_tr_b16 v[220:221], v227 offset:24576\n\t"
    "v_mfma_f32_16x16x32_bf16 v[104:107], v[204:207], v[52:55], v[104:107]\n\t"
    "ds_read_b64_tr_b16 v[222:223], v227 offset:28672\n\t"
    "v_mfma_f32_16x16x32_bf16 v[112:115], v[192:195], v[8:11], 0\n\t"
    "s_add_u32 m0, s80, 114688\n\t"
    "s_add_u32 s76, s54, 65536\n\t"
    "buffer_load_dwordx4 v254, s[64:67], s76 offen lds\n\t"
    "s_waitcnt vmcnt(9)\n\t"
    "s_mov_b64 exec, 1\n\t"
    "buffer_atomic_add v251, v253, s[68:71], s86 offen\n\t"
    "s_mov_b64 exec, -1\n\t"
    "v_mfma_f32_16x16x32_bf16 v[112:115], v[196:199], v[24:27], v[112:115]\n\t"
    "v_mfma_f32_16x16x32_bf16 v[112:115], v[200:203], v[40:43], v[112:115]\n\t"
    "s_add_u32 m0, s80, 118784\n\t"
    "s_add_u32 s76, s54, 81920\n\t"
    "buffer_load_dwordx4 v254, s[64:67], s76 offen lds\n\t"
    "v_mfma_f32_16x16x32_bf16 v[112:115], v[204:207], v[56:59], v[112:115]\n\t"
    "v_mfma_f32_16x16x32_bf16 v[120:123], v[192:195], v[12:15], 0\n\t"
    "s_add_u32 m0, s80, 122880\n\t"
    "s_add_u32 s76, s54, 98304\n\t"
    "buffer_load_dwordx4 v254, s[64:67], s76 offen lds\n\t"
    "v_mfma_f32_16x16x32_bf16 v[120:123], v[196:199], v[28:31], v[120:123]\n\t"
    "v_mfma_f32_16x16x32_bf16 v[120:123], v[200:203], v[44:47], v[120:123]\n\t"
    "s_add_u32 m0, s80, 126976\n\t"
    "s_add_u32 s76, s54, 114688\n\t"
    "buffer_load_dwordx4 v254, s[64:67], s76 offen lds\n\t"
    "v_mfma_f32_16x16x32_bf16 v[120:123], v[204:207], v[60:63], v[120:123]\n\t"
    "s_waitcnt lgkmcnt(0)\n\t"
    "v_mfma_f32_16x16x32_bf16 v[100:103], v[208:211], v[0:3], 0\n\t"
    "ds_read_b64_tr_b16 v[192:193], v228 offset:0\n\t"
    "v_mfma_f32_16x16x32_bf16 v[100:103], v[212:215], v[16:19], v[100:103]\n\t"
    "ds_read_b64_tr_b16 v[194:195], v228 offset:4096\n\t"
    "v_mfma_f32_16x16x32_bf16 v[100:103], v[216:219], v[32:35], v[100:103]\n\t"
    "ds_read_b64_tr_b16 v[196:197], v228 offset:8192\n\t"
    "v_mfma_f32_16x16x32_bf16 v[100:103], v[220:223], v[48:51], v[100:103]\n\t"
    "ds_read_b64_tr_b16 v[198:199], v228 offset:12288\n\t"
    "v_mfma_f32_16x16x32_bf16 v[108:111], v[208:211], v[4:7], 0\n\t"
    "ds_read_b64_tr_b16 v[200:201], v228 offset:16384\n\t"
    "v_mfma_f32_16x16x32_bf16 v[108:111], v[212:215], v[20:23], v[108:111]\n\t"
    "ds_read_b64_tr_b16 v[202:203], v228 offset:20480\n\t"
    "v_mfma_f32_16x16x32_bf16 v[108:111], v[216:219], v[36:39], v[108:111]\n\t"
    "ds_read_b64_tr_b16 v[204:205], v228 offset:24576\n\t"
    "v_mfma_f32_16x16x32_bf16 v[108:111], v[220:223], v[52:55], v[108:111]\n\t"
    "ds_read_b64_tr_b16 v[206:207], v228 offset:28672\n\t"
    "v_mfma_f32_16x16x32_bf16 v[116:119], v[208:211], v[8:11], 0\n\t"
    "v_mfma_f32_16x16x32_bf16 v[116:119], v[212:215], v[24:27], v[116:119]\n\t"
    "v_mfma_f32_16x16x32_bf16 v[116:119], v[216:219], v[40:43], v[116:119]\n\t"
    "v_mfma_f32_16x16x32_bf16 v[116:119], v[220:223], v[56:59], v[116:119]\n\t"
    "v_mfma_f32_16x16x32_bf16 v[124:127], v[208:211], v[12:15], 0\n\t"
    "v_mfma_f32_16x16x32_bf16 v[124:127], v[212:215], v[28:31], v[124:127]\n\t"
    "v_mfma_f32_16x16x32_bf16 v[124:127], v[216:219], v[44:47], v[124:127]\n\t"
    "v_mfma_f32_16x16x32_bf16 v[124:127], v[220:223], v[60:63], v[124:127]\n\t"
    "s_waitcnt lgkmcnt(0)\n\t"
    "v_mfma_f32_16x16x32_bf16 v[128:131], v[192:195], v[0:3], 0\n\t"
    "ds_read_b64_tr_b16 v[208:209], v229 offset:0\n\t"
    "v_mfma_f32_16x16x32_bf16 v[128:131], v[196:199], v[16:19], v[128:131]\n\t"
    "ds_read_b64_tr_b16 v[210:211], v229 offset:4096\n\t"
    "v_mfma_f32_16x16x32_bf16 v[128:131], v[200:203], v[32:35], v[128:131]\n\t"
    "ds_read_b64_tr_b16 v[212:213], v229 offset:8192\n\t"
    "v_mfma_f32_16x16x32_bf16 v[128:131], v[204:207], v[48:51], v[128:131]\n\t"
    "ds_read_b64_tr_b16 v[214:215], v229 offset:12288\n\t"
    "v_mfma_f32_16x16x32_bf16 v[136:139], v[192:195], v[4:7], 0\n\t"
    "ds_read_b64_tr_b16 v[216:217], v229 offset:16384\n\t"
    "v_mfma_f32_16x16x32_bf16 v[136:139], v[196:199], v[20:23], v[136:139]\n\t"
    "ds_read_b64_tr_b16 v[218:219], v229 offset:20480\n\t"
    "v_mfma_f32_16x16x32_bf16 v[136:139], v[200:203], v[36:39], v[136:139]\n\t"
    "ds_read_b64_tr_b16 v[220:221], v229 offset:24576\n\t"
    "v_mfma_f32_16x16x32_bf16 v[136:139], v[204:207], v[52:55], v[136:139]\n\t"
    "ds_read_b64_tr_b16 v[222:223], v229 offset:28672\n\t"
    "v_mfma_f32_16x16x32_bf16 v[144:147], v[192:195], v[8:11], 0\n\t"
    "v_mfma_f32_16x16x32_bf16 v[144:147], v[196:199], v[24:27], v[144:147]\n\t"
    "v_mfma_f32_16x16x32_bf16 v[144:147], v[200:203], v[40:43], v[144:147]\n\t"
    "v_mfma_f32_16x16x32_bf16 v[144:147], v[204:207], v[56:59], v[144:147]\n\t"
    "v_mfma_f32_16x16x32_bf16 v[152:155], v[192:195], v[12:15], 0\n\t"
    "v_mfma_f32_16x16x32_bf16 v[152:155], v[196:199], v[28:31], v[152:155]\n\t"
    "v_mfma_f32_16x16x32_bf16 v[152:155], v[200:203], v[44:47], v[152:155]\n\t"
    "v_mfma_f32_16x16x32_bf16 v[152:155], v[204:207], v[60:63], v[152:155]\n\t"
    "s_waitcnt lgkmcnt(0)\n\t"
    "v_mfma_f32_16x16x32_bf16 v[132:135], v[208:211], v[0:3], 0\n\t"
    "ds_read_b64_tr_b16 v[192:193], v230 offset:0\n\t"
    "v_mfma_f32_16x16x32_bf16 v[132:135], v[212:215], v[16:19], v[132:135]\n\t"
    "ds_read_b64_tr_b16 v[194:195], v230 offset:4096\n\t"
    "v_mfma_f32_16x16x32_bf16 v[132:135], v[216:219], v[32:35], v[132:135]\n\t"
    "ds_read_b64_tr_b16 v[196:197], v230 offset:8192\n\t"
    "v_mfma_f32_16x16x32_bf16 v[132:135], v[220:223], v[48:51], v[132:135]\n\t"
    "ds_read_b64_tr_b16 v[198:199], v230 offset:12288\n\t"
    "v_mfma_f32_16x16x32_bf16 v[140:143], v[208:211], v[4:7], 0\n\t"
    "ds_read_b64_tr_b16 v[200:201], v230 offset:16384\n\t"
    "v_mfma_f32_16x16x32_bf16 v[140:143], v[212:215], v[20:23], v[140:143]\n\t"
    "ds_read_b64_tr_b16 v[202:203], v230 offset:20480\n\t"
    "v_mfma_f32_16x16x32_bf16 v[140:143], v[216:219], v[36:39], v[140:143]\n\t"
    "ds_read_b64_tr_b16 v[204:205], v230 offset:24576\n\t"
    "v_mfma_f32_16x16x32_bf16 v[140:143], v[220:223], v[52:55], v[140:143]\n\t"
    "ds_read_b64_tr_b16 v[206:207], v230 offset:28672\n\t"
    "v_mfma_f32_16x16x32_bf16 v[148:151], v[208:211], v[8:11], 0\n\t"
    "v_mfma_f32_16x16x32_bf16 v[148:151], v[212:215], v[24:27], v[148:151]\n\t"
    "v_mfma_f32_16x16x32_bf16 v[148:151], v[216:219], v[40:43], v[148:151]\n\t"
    "v_mfma_f32_16x16x32_bf16 v[148:151], v[220:223], v[56:59], v[148:151]\n\t"
    "v_mfma_f32_16x16x32_bf16 v[156:159], v[208:211], v[12:15], 0\n\t"
    "v_mfma_f32_16x16x32_bf16 v[156:159], v[212:215], v[28:31], v[156:159]\n\t"
    "v_mfma_f32_16x16x32_bf16 v[156:159], v[216:219], v[44:47], v[156:159]\n\t"
    "v_mfma_f32_16x16x32_bf16 v[156:159], v[220:223], v[60:63], v[156:159]\n\t"
    "s_waitcnt lgkmcnt(0)\n\t"
    "v_mfma_f32_16x16x32_bf16 v[160:163], v[192:195], v[0:3], 0\n\t"
    "ds_read_b64_tr_b16 v[208:209], v231 offset:0\n\t"
    "v_mfma_f32_16x16x32_bf16 v[160:163], v[196:199], v[16:19], v[160:163]\n\t"
    "ds_read_b64_tr_b16 v[210:211], v231 offset:4096\n\t"
    "v_mfma_f32_16x16x32_bf16 v[160:163], v[200:203], v[32:35], v[160:163]\n\t"
    "ds_read_b64_tr_b16 v[212:213], v231 offset:8192\n\t"
    "v_mfma_f32_16x16x32_bf16 v[160:163], v[204:207], v[48:51], v[160:163]\n\t"
    "ds_read_b64_tr_b16 v[214:215], v231 offset:12288\n\t"
    "v_mfma_f32_16x16x32_bf16 v[168:171], v[192:195], v[4:7], 0\n\t"
    "ds_read_b64_tr_b16 v[216:217], v231 offset:16384\n\t"
    "v_mfma_f32_16x16x32_bf16 v[168:171], v[196:199], v[20:23], v[168:171]\n\t"
    "ds_read_b64_tr_b16 v[218:219], v231 offset:20480\n\t"
    "v_mfma_f32_16x16x32_bf16 v[168:171], v[200:203], v[36:39], v[168:171]\n\t"
    "ds_read_b64_tr_b16 v[220:221], v231 offset:24576\n\t"
    "v_mfma_f32_16x16x32_bf16 v[168:171], v[204:207], v[52:55], v[168:171]\n\t"
    "ds_read_b64_tr_b16 v[222:223], v231 offset:28672\n\t"
    "v_mfma_f32_16x16x32_bf16 v[176:179], v[192:195], v[8:11], 0\n\t"
    "v_mfma_f32_16x16x32_bf16 v[176:179], v[196:199], v[24:27], v[176:179]\n\t"
    "v_mfma_f32_16x16x32_bf16 v[176:179], v[200:203], v[40:43], v[176:179]\n\t"
    "v_mfma_f32_16x16x32_bf16 v[176:179], v[204:207], v[56:59], v[176:179]\n\t"
    "v_mfma_f32_16x16x32_bf16 v[184:187], v[192:195], v[12:15], 0\n\t"
    "v_mfma_f32_16x16x32_bf16 v[184:187], v[196:199], v[28:31], v[184:187]\n\t"
    "v_mfma_f32_16x16x32_bf16 v[184:187], v[200:203], v[44:47], v[184:187]\n\t"
    "v_mfma_f32_16x16x32_bf16 v[184:187], v[204:207], v[60:63], v[184:187]\n\t"
    "s_waitcnt lgkmcnt(0)\n\t"
    "v_mfma_f32_16x16x32_bf16 v[164:167], v[208:211], v[0:3], 0\n\t"
    "v_mfma_f32_16x16x32_bf16 v[164:167], v[212:215], v[16:19], v[164:167]\n\t"
    "v_mfma_f32_16x16x32_bf16 v[164:167], v[216:219], v[32:35], v[164:167]\n\t"
    "v_mfma_f32_16x16x32_bf16 v[164:167], v[220:223], v[48:51], v[164:167]\n\t"
    "v_mfma_f32_16x16x32_bf16 v[172:175], v[208:211], v[4:7], 0\n\t"
    "v_mfma_f32_16x16x32_bf16 v[172:175], v[212:215], v[20:23], v[172:175]\n\t"
    "v_mfma_f32_16x16x32_bf16 v[172:175], v[216:219], v[36:39], v[172:175]\n\t"
    "v_mfma_f32_16x16x32_bf16 v[172:175], v[220:223], v[52:55], v[172:175]\n\t"
    "v_mfma_f32_16x16x32_bf16 v[180:183], v[208:211], v[8:11], 0\n\t"
    "v_mfma_f32_16x16x32_bf16 v[180:183], v[212:215], v[24:27], v[180:183]\n\t"
    "buffer_load_dword v252, v253, s[68:71], s86 offen sc1\n\t"
    "v_mfma_f32_16x16x32_bf16 v[180:183], v[216:219], v[40:43], v[180:183]\n\t"
    "v_mfma_f32_16x16x32_bf16 v[180:183], v[220:223], v[56:59], v[180:183]\n\t"
    "v_mfma_f32_16x16x32_bf16 v[188:191], v[208:211], v[12:15], 0\n\t"
    "v_mfma_f32_16x16x32_bf16 v[188:191], v[212:215], v[28:31], v[188:191]\n\t"
    "v_mfma_f32_16x16x32_bf16 v[188:191], v[216:219], v[44:47], v[188:191]\n\t"
    "v_mfma_f32_16x16x32_bf16 v[188:191], v[220:223], v[60:63], v[188:191]\n\t"
    "s_waitcnt lgkmcnt(0)\n\t"
    "s_waitcnt vmcnt(0)\n\t"
    "v_readfirstlane_b32 s78, v252\n\t"
    "s_nop 3\n\t"
    "s_cmp_ge_u32 s78, 4\n\t"
    "s_cbranch_scc1 .Lx%=_4\n\t"
    "s_mov_b32 s79, 0x8000\n\t"
    ".Lx%=_5:\n\t"
    "buffer_load_dword v252, v253, s[68:71], s86 offen sc1\n\t"
    "s_waitcnt vmcnt(0)\n\t"
    "v_readfirstlane_b32 s78, v252\n\t"
    "s_nop 3\n\t"
    "s_cmp_ge_u32 s78, 4\n\t"
    "s_cbranch_scc1 .Lx%=_6\n\t"
    "s_sleep 1\n\t"
    "s_sub_u32 s79, s79, 1\n\t"
    "s_cmp_lg_u32 s79, 0\n\t"
    "s_cbranch_scc1 .Lx%=_5\n\t"
    ".Lx%=_6:\n\t"
    ".Lx%=_4:\n\t"
    "v_add_u32 v192, s94, v255\n\t"
    "v_add_u32 v193, 131072, v192\n\t"
    "s_barrier\n\t"
    "ds_write_b128 v192, a[192:195] offset:0\n\t"
    "s_add_u32 m0, s80, 49152\n\t"
    "s_add_u32 s76, s55, 65536\n\t"
    "buffer_load_dwordx4 v254, s[64:67], s76 offen lds\n\t"
    "ds_write_b128 v192, a[196:199] offset:1024\n\t"
    "s_add_u32 m0, s80, 53248\n\t"
    "s_add_u32 s76, s55, 81920\n\t"
    "buffer_load_dwordx4 v254, s[64:67], s76 offen lds\n\t"
    "ds_write_b128 v192, a[224:227] offset:2048\n\t"
    "s_add_u32 m0, s80, 57344\n\t"
    "s_add_u32 s76, s55, 98304\n\t"
    "buffer_load_dwordx4 v254, s[64:67], s76 offen lds\n\t"
    "ds_write_b128 v192, a[228:231] offset:3072\n\t"
    "s_add_u32 m0, s80, 61440\n\t"
    "s_add_u32 s76, s55, 114688\n\t"
    "buffer_load_dwordx4 v254, s[64:67], s76 offen lds\n\t"
    "ds_write_b128 v192, a[200:203] offset:16384\n\t"
    "s_add_u32 s77, s57, 0\n\t"
    "buffer_load_dwordx4 a[0:3], v255, s[68:71], s77 offen offset:0 sc1\n\t"
    "ds_write_b128 v192, a[204:207] offset:17408\n\t"
    "buffer_load_dwordx4 a[4:7], v255, s[68:71], s77 offen offset:1024 sc1\n\t"
    "ds_write_b128 v192, a[232:235] offset:18432\n\t"
    "buffer_load_dwordx4 a[8:11], v255, s[68:71], s77 offen offset:2048 sc1\n\t"
    "ds_write_b128 v192, a[236:239] offset:19456\n\t"
    "buffer_load_dwordx4 a[12:15], v255, s[68:71], s77 offen offset:3072 sc1\n\t"
    "ds_write_b128 v193, a[208:211] offset:0\n\t"
    "s_add_u32 s77, s57, 131072\n\t"
    "buffer_load_dwordx4 a[16:19], v255, s[68:71], s77 offen offset:0 sc1\n\t"
    "ds_write_b128 v193, a[212:215] offset:1024\n\t"
    "buffer_load_dwordx4 a[20:23], v255, s[68:71], s77 offen offset:1024 sc1\n\t"
    "ds_write_b128 v193, a[240:243] offset:2048\n\t"
    "buffer_load_dwordx4 a[24:27], v255, s[68:71], s77 offen offset:2048 sc1\n\t"
    "ds_write_b128 v193, a[244:247] offset:3072\n\t"
    "buffer_load_dwordx4 a[28:31], v255, s[68:71], s77 offen offset:3072 sc1\n\t"
    "ds_write_b128 v193, a[216:219] offset:16384\n\t"
    "s_add_u32 s77, s57, 262144\n\t"
    "buffer_load_dwordx4 a[32:35], v255, s[68:71], s77 offen offset:0 sc1\n\t"
    "ds_write_b128 v193, a[220:223] offset:17408\n\t"
    "buffer_load_dwordx4 a[36:39], v255, s[68:71], s77 offen offset:1024 sc1\n\t"
    "ds_write_b128 v193, a[248:251] offset:18432\n\t"
    "buffer_load_dwordx4 a[40:43], v255, s[68:71], s77 offen offset:2048 sc1\n\t"
    "ds_write_b128 v193, a[252:255] offset:19456\n\t"
    "buffer_load_dwordx4 a[44:47], v255, s[68:71], s77 offen offset:3072 sc1\n\t"
    "s_lshr_b32 s76, s50, 1\n\t"
    "s_mul_i32 s76, s76, 131072\n\t"
    "s_and_b32 s78, s50, 1\n\t"
    "s_lshl_b32 s78, s78, 14\n\t"
    "s_add_u32 s76, s76, s78\n\t"
    "v_add_u32 v192, s76, v255\n\t"
    "s_waitcnt lgkmcnt(0)\n\t"
    "s_barrier\n\t"
    "ds_read_b128 v[0:3], v192 offset:0\n\t"
    "ds_read_b128 v[4:7], v192 offset:4096\n\t"
    "ds_read_b128 v[8:11], v192 offset:8192\n\t"
    "ds_read_b128 v[12:15], v192 offset:12288\n\t"
    "ds_read_b128 v[16:19], v192 offset:1024\n\t"
    "ds_read_b128 v[20:23], v192 offset:5120\n\t"
    "ds_read_b128 v[24:27], v192 offset:9216\n\t"
    "ds_read_b128 v[28:31], v192 offset:13312\n\t"
    "ds_read_b128 v[32:35], v192 offset:2048\n\t"
    "ds_read_b128 v[36:39], v192 offset:6144\n\t"
    "ds_read_b128 v[40:43], v192 offset:10240\n\t"
    "ds_read_b128 v[44:47], v192 offset:14336\n\t"
    "ds_read_b128 v[48:51], v192 offset:3072\n\t"
    "ds_read_b128 v[52:55], v192 offset:7168\n\t"
    "ds_read_b128 v[56:59], v192 offset:11264\n\t"
    "ds_read_b128 v[60:63], v192 offset:15360\n\t"
    "s_waitcnt vmcnt(27)\n\t"
    "v_lshlrev_b32 v194, 16, v232\n\t"
    "v_and_b32 v195, 0xffff0000, v232\n\t"
    "v_lshlrev_b32 v196, 16, v233\n\t"
    "v_and_b32 v197, 0xffff0000, v233\n\t"
    "v_lshlrev_b32 v198, 16, v234\n\t"
    "v_and_b32 v199, 0xffff0000, v234\n\t"
    "v_lshlrev_b32 v200, 16, v235\n\t"
    "v_and_b32 v201, 0xffff0000, v235\n\t"
    "v_lshlrev_b32 v202, 16, v236\n\t"
    "v_and_b32 v203, 0xffff0000, v236\n\t"
    "v_lshlrev_b32 v204, 16, v237\n\t"
    "v_and_b32 v205, 0xffff0000, v237\n\t"
    "v_lshlrev_b32 v206, 16, v238\n\t"
    "v_and_b32 v207, 0xffff0000, v238\n\t"
    "v_lshlrev_b32 v208, 16, v239\n\t"
    "v_and_b32 v209, 0xffff0000, v239\n\t"
    "s_waitcnt lgkmcnt(12)\n\t"
    "v_add_f32 v0, v0, v4\n\t"
    "v_add_f32 v1, v1, v5\n\t"
    "v_add_f32 v2, v2, v6\n\t"
    "v_add_f32 v3, v3, v7\n\t"
    "v_add_f32 v8, v8, v12\n\t"
    "v_add_f32 v9, v9, v13\n\t"
    "v_add_f32 v10, v10, v14\n\t"
    "v_add_f32 v11, v11, v15\n\t"
    "v_add_f32 v194, v194, v0\n\t"
    "v_add_f32 v195, v195, v1\n\t"
    "v_add_f32 v196, v196, v2\n\t"
    "v_add_f32 v197, v197, v3\n\t"
    "v_add_f32 v194, v194, v8\n\t"
    "v_add_f32 v195, v195, v9\n\t"
    "v_add_f32 v196, v196, v10\n\t"
    "v_add_f32 v197, v197, v11\n\t"
    "s_add_u32 s77, s57, 393216\n\t"
    "buffer_load_dwordx4 a[48:51], v255, s[68:71], s77 offen offset:0 sc1\n\t"
    "s_waitcnt lgkmcnt(8)\n\t"
    "v_add_f32 v16, v16, v20\n\t"
    "v_add_f32 v17, v17, v21\n\t"
    "v_add_f32 v18, v18, v22\n\t"
    "v_add_f32 v19, v19, v23\n\t"
    "v_add_f32 v24, v24, v28\n\t"
    "v_add_f32 v25, v25, v29\n\t"
    "v_add_f32 v26, v26, v30\n\t"
    "v_add_f32 v27, v27, v31\n\t"
    "v_add_f32 v198, v198, v16\n\t"
    "v_add_f32 v199, v199, v17\n\t"
    "v_add_f32 v200, v200, v18\n\t"
    "v_add_f32 v201, v201, v19\n\t"
    "v_add_f32 v198, v198, v24\n\t"
    "v_add_f32 v199, v199, v25\n\t"
    "v_add_f32 v200, v200, v26\n\t"
    "v_add_f32 v201, v201, v27\n\t"
    "buffer_load_dwordx4 a[52:55], v255, s[68:71], s77 offen offset:1024 sc1\n\t"
    "s_waitcnt lgkmcnt(4)\n\t"
    "v_add_f32 v32, v32, v36\n\t"
    "v_add_f32 v33, v33, v37\n\t"
    "v_add_f32 v34, v34, v38\n\t"
    "v_add_f32 v35, v35, v39\n\t"
    "v_add_f32 v40, v40, v44\n\t"
    "v_add_f32 v41, v41, v45\n\t"
    "v_add_f32 v42, v42, v46\n\t"
    "v_add_f32 v43, v43, v47\n\t"
    "v_add_f32 v202, v202, v32\n\t"
    "v_add_f32 v203, v203, v33\n\t"
    "v_add_f32 v204, v204, v34\n\t"
    "v_add_f32 v205, v205, v35\n\t"
    "v_add_f32 v202, v202, v40\n\t"
    "v_add_f32 v203, v203, v41\n\t"
    "v_add_f32 v204, v204, v42\n\t"
    "v_add_f32 v205, v205, v43\n\t"
    "buffer_load_dwordx4 a[56:59], v255, s[68:71], s77 offen offset:2048 sc1\n\t"
    "s_waitcnt lgkmcnt(0)\n\t"
    "v_add_f32 v48, v48, v52\n\t"
    "v_add_f32 v49, v49, v53\n\t"
    "v_add_f32 v50, v50, v54\n\t"
    "v_add_f32 v51, v51, v55\n\t"
    "v_add_f32 v56, v56, v60\n\t"
    "v_add_f32 v57, v57, v61\n\t"
    "v_add_f32 v58, v58, v62\n\t"
    "v_add_f32 v59, v59, v63\n\t"
    "v_add_f32 v206, v206, v48\n\t"
    "v_add_f32 v207, v207, v49\n\t"
    "v_add_f32 v208, v208, v50\n\t"
    "v_add_f32 v209, v209, v51\n\t"
    "v_add_f32 v206, v206, v56\n\t"
    "v_add_f32 v207, v207, v57\n\t"
    "v_add_f32 v208, v208, v58\n\t"
    "v_add_f32 v209, v209, v59\n\t"
    "buffer_load_dwordx4 a[60:63], v255, s[68:71], s77 offen offset:3072 sc1\n\t"
    "s_mov_b32 s78, 0xbfb8aa3b\n\t"
    "v_mul_f32 v0, s78, v194\n\t"
    "v_mul_f32 v1, s78, v195\n\t"
    "v_mul_f32 v2, s78, v196\n\t"
    "v_mul_f32 v3, s78, v197\n\t"
    "v_mul_f32 v4, s78, v198\n\t"
    "v_mul_f32 v5, s78, v199\n\t"
    "v_mul_f32 v6, s78, v200\n\t"
    "v_mul_f32 v7, s78, v201\n\t"
    "v_mul_f32 v8, s78, v202\n\t"
    "v_mul_f32 v9, s78, v203\n\t"
    "v_mul_f32 v10, s78, v204\n\t"
    "v_mul_f32 v11, s78, v205\n\t"
    "v_mul_f32 v12, s78, v206\n\t"
    "v_mul_f32 v13, s78, v207\n\t"
    "v_mul_f32 v14, s78, v208\n\t"
    "v_mul_f32 v15, s78, v209\n\t"
    "v_exp_f32 v0, v0\n\t"
    "v_exp_f32 v1, v1\n\t"
    "v_exp_f32 v2, v2\n\t"
    "v_exp_f32 v3, v3\n\t"
    "v_exp_f32 v4, v4\n\t"
    "v_exp_f32 v5, v5\n\t"
    "v_exp_f32 v6, v6\n\t"
    "v_exp_f32 v7, v7\n\t"
    "v_exp_f32 v8, v8\n\t"
    "v_exp_f32 v9, v9\n\t"
    "v_exp_f32 v10, v10\n\t"
    "v_exp_f32 v11, v11\n\t"
    "v_exp_f32 v12, v12\n\t"
    "v_exp_f32 v13, v13\n\t"
    "v_exp_f32 v14, v14\n\t"
    "v_exp_f32 v15, v15\n\t"
    "v_add_f32 v0, 1.0, v0\n\t"
    "v_add_f32 v1, 1.0, v1\n\t"
    "v_add_f32 v2, 1.0, v2\n\t"
    "v_add_f32 v3, 1.0, v3\n\t"
    "v_add_f32 v4, 1.0, v4\n\t"
    "v_add_f32 v5, 1.0, v5\n\t"
    "v_add_f32 v6, 1.0, v6\n\t"
    "v_add_f32 v7, 1.0, v7\n\t"
    "v_add_f32 v8, 1.0, v8\n\t"
    "v_add_f32 v9, 1.0, v9\n\t"
    "v_add_f32 v10, 1.0, v10\n\t"
    "v_add_f32 v11, 1.0, v11\n\t"
    "v_add_f32 v12, 1.0, v12\n\t"
    "v_add_f32 v13, 1.0, v13\n\t"
    "v_add_f32 v14, 1.0, v14\n\t"
    "v_add_f32 v15, 1.0, v15\n\t"
    "v_rcp_f32 v0, v0\n\t"
    "v_rcp_f32 v1, v1\n\t"
    "v_rcp_f32 v2, v2\n\t"
    "v_rcp_f32 v3, v3\n\t"
    "v_rcp_f32 v4, v4\n\t"
    "v_rcp_f32 v5, v5\n\t"
    "v_rcp_f32 v6, v6\n\t"
    "v_rcp_f32 v7, v7\n\t"
    "v_rcp_f32 v8, v8\n\t"
    "v_rcp_f32 v9, v9\n\t"
    "v_rcp_f32 v10, v10\n\t"
    "v_rcp_f32 v11, v11\n\t"
    "v_rcp_f32 v12, v12\n\t"
    "v_rcp_f32 v13, v13\n\t"
    "v_rcp_f32 v14, v14\n\t"
    "v_rcp_f32 v15, v15\n\t"
    "s_nop 0\n\t"
    "v_mul_f32 v194, v194, v0\n\t"
    "v_mul_f32 v195, v195, v1\n\t"
    "v_mul_f32 v196, v196, v2\n\t"
    "v_mul_f32 v197, v197, v3\n\t"
    "v_mul_f32 v198, v198, v4\n\t"
    "v_mul_f32 v199, v199, v5\n\t"
    "v_mul_f32 v200, v200, v6\n\t"
    "v_mul_f32 v201, v201, v7\n\t"
    "v_mul_f32 v202, v202, v8\n\t"
    "v_mul_f32 v203, v203, v9\n\t"
    "v_mul_f32 v204, v204, v10\n\t"
    "v_mul_f32 v205, v205, v11\n\t"
    "v_mul_f32 v206, v206, v12\n\t"
    "v_mul_f32 v207, v207, v13\n\t"
    "v_mul_f32 v208, v208, v14\n\t"
    "v_mul_f32 v209, v209, v15\n\t"
    "v_cvt_pk_bf16_f32 v210, v194, v195\n\t"
    "v_cvt_pk_bf16_f32 v211, v196, v197\n\t"
    "v_cvt_pk_bf16_f32 v212, v198, v199\n\t"
    "v_cvt_pk_bf16_f32 v213, v200, v201\n\t"
    "v_cvt_pk_bf16_f32 v214, v202, v203\n\t"
    "v_cvt_pk_bf16_f32 v215, v204, v205\n\t"
    "v_cvt_pk_bf16_f32 v216, v206, v207\n\t"
    "v_cvt_pk_bf16_f32 v217, v208, v209\n\t"
    "s_add_u32 s79, s100, 26345472\n\t"
    "buffer_store_dwordx4 v[210:213], v255, s[68:71], s79 offen offset:0 sc1\n\t"
    "buffer_store_dwordx4 v[214:217], v255, s[68:71], s79 offen offset:1024 sc1\n\t"
    "s_nop 1\n\t"
    "s_waitcnt vmcnt(18)\n\t"
    "s_barrier\n\t"
    "s_waitcnt vmcnt(2)\n\t"
    "v_add_u32 v232, 65536, v224\n\t"
    "ds_read_b64_tr_b16 v[192:193], v232 offset:32768\n\t"
    "ds_read_b64_tr_b16 v[194:195], v232 offset:36864\n\t"
    "ds_read_b64_tr_b16 v[196:197], v232 offset:40960\n\t"
    "ds_read_b64_tr_b16 v[198:199], v232 offset:45056\n\t"
    "ds_read_b64_tr_b16 v[200:201], v232 offset:49152\n\t"
    "ds_read_b64_tr_b16 v[202:203], v232 offset:53248\n\t"
    "ds_read_b64_tr_b16 v[204:205], v232 offset:57344\n\t"
    "ds_read_b64_tr_b16 v[206:207], v232 offset:61440\n\t"
    "s_waitcnt lgkmcnt(0)\n\t"
    "v_mfma_f32_16x16x32_bf16 v[64:67], v[192:195], a[0:3], v[64:67]\n\t"
    "v_add_u32 v234, 65536, v225\n\t"
    "ds_read_b64_tr_b16 v[208:209], v234 offset:32768\n\t"
    "v_mfma_f32_16x16x32_bf16 v[64:67], v[196:199], a[4:7], v[64:67]\n\t"
    "ds_read_b64_tr_b16 v[210:211], v234 offset:36864\n\t"
    "v_mfma_f32_16x16x32_bf16 v[64:67], v[200:203], a[8:11], v[64:67]\n\t"
    "ds_read_b64_tr_b16 v[212:213], v234 offset:40960\n\t"
    "v_mfma_f32_16x16x32_bf16 v[64:67], v[204:207], a[12:15], v[64:67]\n\t"
    "ds_read_b64_tr_b16 v[214:215], v234 offset:45056\n\t"
    "v_mfma_f32_16x16x32_bf16 v[72:75], v[192:195], a[16:19], v[72:75]\n\t"
    "ds_read_b64_tr_b16 v[216:217], v234 offset:49152\n\t"
    "v_mfma_f32_16x16x32_bf16 v[72:75], v[196:199], a[20:23], v[72:75]\n\t"
    "ds_read_b64_tr_b16 v[218:219], v234 offset:53248\n\t"
    "v_mfma_f32_16x16x32_bf16 v[72:75], v[200:203], a[24:27], v[72:75]\n\t"
    "ds_read_b64_tr_b16 v[220:221], v234 offset:57344\n\t"
    "v_mfma_f32_16x16x32_bf16 v[72:75], v[204:207], a[28:31], v[72:75]\n\t"
    "ds_read_b64_tr_b16 v[222:223], v234 offset:61440\n\t"
    "v_mfma_f32_16x16x32_bf16 v[80:83], v[192:195], a[32:35], v[80:83]\n\t"
    "s_add_u32 s77, s58, 0\n\t"
    "buffer_load_dwordx4 a[64:67], v255, s[68:71], s77 offen offset:0 sc1\n\t"
    "v_mfma_f32_16x16x32_bf16 v[80:83], v[196:199], a[36:39], v[80:83]\n\t"
    "v_mfma_f32_16x16x32_bf16 v[80:83], v[200:203], a[40:43], v[80:83]\n\t"
    "buffer_load_dwordx4 a[68:71], v255, s[68:71], s77 offen offset:1024 sc1\n\t"
    "v_mfma_f32_16x16x32_bf16 v[80:83], v[204:207], a[44:47], v[80:83]\n\t"
    "v_mfma_f32_16x16x32_bf16 v[88:91], v[192:195], a[48:51], v[88:91]\n\t"
    "buffer_load_dwordx4 a[72:75], v255, s[68:71], s77 offen offset:2048 sc1\n\t"
    "v_mfma_f32_16x16x32_bf16 v[88:91], v[196:199], a[52:55], v[88:91]\n\t"
    "v_mfma_f32_16x16x32_bf16 v[88:91], v[200:203], a[56:59], v[88:91]\n\t"
    "buffer_load_dwordx4 a[76:79], v255, s[68:71], s77 offen offset:3072 sc1\n\t"
    "v_mfma_f32_16x16x32_bf16 v[88:91], v[204:207], a[60:63], v[88:91]\n\t"
    "s_waitcnt lgkmcnt(0)\n\t"
    "v_mfma_f32_16x16x32_bf16 v[68:71], v[208:211], a[0:3], v[68:71]\n\t"
    "v_add_u32 v232, 65536, v226\n\t"
    "ds_read_b64_tr_b16 v[192:193], v232 offset:32768\n\t"
    "v_mfma_f32_16x16x32_bf16 v[68:71], v[212:215], a[4:7], v[68:71]\n\t"
    "ds_read_b64_tr_b16 v[194:195], v232 offset:36864\n\t"
    "v_mfma_f32_16x16x32_bf16 v[68:71], v[216:219], a[8:11], v[68:71]\n\t"
    "ds_read_b64_tr_b16 v[196:197], v232 offset:40960\n\t"
    "v_mfma_f32_16x16x32_bf16 v[68:71], v[220:223], a[12:15], v[68:71]\n\t"
    "ds_read_b64_tr_b16 v[198:199], v232 offset:45056\n\t"
    "v_mfma_f32_16x16x32_bf16 v[76:79], v[208:211], a[16:19], v[76:79]\n\t"
    "ds_read_b64_tr_b16 v[200:201], v232 offset:49152\n\t"
    "v_mfma_f32_16x16x32_bf16 v[76:79], v[212:215], a[20:23], v[76:79]\n\t"
    "ds_read_b64_tr_b16 v[202:203], v232 offset:53248\n\t"
    "v_mfma_f32_16x16x32_bf16 v[76:79], v[216:219], a[24:27], v[76:79]\n\t"
    "ds_read_b64_tr_b16 v[204:205], v232 offset:57344\n\t"
    "v_mfma_f32_16x16x32_bf16 v[76:79], v[220:223], a[28:31], v[76:79]\n\t"
    "ds_read_b64_tr_b16 v[206:207], v232 offset:61440\n\t"
    "v_mfma_f32_16x16x32_bf16 v[84:87], v[208:211], a[32:35], v[84:87]\n\t"
    "s_add_u32 s77, s58, 131072\n\t"
    "buffer_load_dwordx4 a[80:83], v255, s[68:71], s77 offen offset:0 sc1\n\t"
    "v_mfma_f32_16x16x32_bf16 v[84:87], v[212:215], a[36:39], v[84:87]\n\t"
    "v_mfma_f32_16x16x32_bf16 v[84:87], v[216:219], a[40:43], v[84:87]\n\t"
    "buffer_load_dwordx4 a[84:87], v255, s[68:71], s77 offen offset:1024 sc1\n\t"
    "v_mfma_f32_16x16x32_bf16 v[84:87], v[220:223], a[44:47], v[84:87]\n\t"
    "v_mfma_f32_16x16x32_bf16 v[92:95], v[208:211], a[48:51], v[92:95]\n\t"
    "buffer_load_dwordx4 a[88:91], v255, s[68:71], s77 offen offset:2048 sc1\n\t"
    "v_mfma_f32_16x16x32_bf16 v[92:95], v[212:215], a[52:55], v[92:95]\n\t"
    "v_mfma_f32_16x16x32_bf16 v[92:95], v[216:219], a[56:59], v[92:95]\n\t"
    "buffer_load_dwordx4 a[92:95], v255, s[68:71], s77 offen offset:3072 sc1\n\t"
    "v_mfma_f32_16x16x32_bf16 v[92:95], v[220:223], a[60:63], v[92:95]\n\t"
    "s_waitcnt lgkmcnt(0)\n\t"
    "s_waitcnt vmcnt(8)\n\t"
    "s_mov_b64 exec, 1\n\t"
    "buffer_atomic_add v251, v253, s[68:71], s92 offen\n\t"
    "s_mov_b64 exec, -1\n\t"
    "v_mfma_f32_16x16x32_bf16 v[96:99], v[192:195], a[0:3], v[96:99]\n\t"
    "v_add_u32 v234, 65536, v227\n\t"
    "ds_read_b64_tr_b16 v[208:209], v234 offset:32768\n\t"
    "v_mfma_f32_16x16x32_bf16 v[96:99], v[196:199], a[4:7], v[96:99]\n\t"
    "ds_read_b64_tr_b16 v[210:211], v234 offset:36864\n\t"
    "v_mfma_f32_16x16x32_bf16 v[96:99], v[200:203], a[8:11], v[96:99]\n\t"
    "ds_read_b64_tr_b16 v[212:213], v234 offset:40960\n\t"
    "v_mfma_f32_16x16x32_bf16 v[96:99], v[204:207], a[12:15], v[96:99]\n\t"
    "ds_read_b64_tr_b16 v[214:215], v234 offset:45056\n\t"
    "v_mfma_f32_16x16x32_bf16 v[104:107], v[192:195], a[16:19], v[104:107]\n\t"
    "ds_read_b64_tr_b16 v[216:217], v234 offset:49152\n\t"
    "v_mfma_f32_16x16x32_bf16 v[104:107], v[196:199], a[20:23], v[104:107]\n\t"
    "ds_read_b64_tr_b16 v[218:219], v234 offset:53248\n\t"
    "v_mfma_f32_16x16x32_bf16 v[104:107], v[200:203], a[24:27], v[104:107]\n\t"
    "ds_read_b64_tr_b16 v[220:221], v234 offset:57344\n\t"
    "v_mfma_f32_16x16x32_bf16 v[104:107], v[204:207], a[28:31], v[104:107]\n\t"
    "ds_read_b64_tr_b16 v[222:223], v234 offset:61440\n\t"
    "v_mfma_f32_16x16x32_bf16 v[112:115], v[192:195], a[32:35], v[112:115]\n\t"
    "s_add_u32 s77, s58, 262144\n\t"
    "buffer_load_dwordx4 a[96:99], v255, s[68:71], s77 offen offset:0 sc1\n\t"
    "v_mfma_f32_16x16x32_bf16 v[112:115], v[196:199], a[36:39], v[112:115]\n\t"
    "v_mfma_f32_16x16x32_bf16 v[112:115], v[200:203], a[40:43], v[112:115]\n\t"
    "buffer_load_dwordx4 a[100:103], v255, s[68:71], s77 offen offset:1024 sc1\n\t"
    "v_mfma_f32_16x16x32_bf16 v[112:115], v[204:207], a[44:47], v[112:115]\n\t"
    "v_mfma_f32_16x16x32_bf16 v[120:123], v[192:195], a[48:51], v[120:123]\n\t"
    "buffer_load_dwordx4 a[104:107], v255, s[68:71], s77 offen offset:2048 sc1\n\t"
    "v_mfma_f32_16x16x32_bf16 v[120:123], v[196:199], a[52:55], v[120:123]\n\t"
    "v_mfma_f32_16x16x32_bf16 v[120:123], v[200:203], a[56:59], v[120:123]\n\t"
    "buffer_load_dwordx4 a[108:111], v255, s[68:71], s77 offen offset:3072 sc1\n\t"
    "v_mfma_f32_16x16x32_bf16 v[120:123], v[204:207], a[60:63], v[120:123]\n\t"
    "s_waitcnt lgkmcnt(0)\n\t"
    "v_mfma_f32_16x16x32_bf16 v[100:103], v[208:211], a[0:3], v[100:103]\n\t"
    "v_add_u32 v232, 65536, v228\n\t"
    "ds_read_b64_tr_b16 v[192:193], v232 offset:32768\n\t"
    "v_mfma_f32_16x16x32_bf16 v[100:103], v[212:215], a[4:7], v[100:103]\n\t"
    "ds_read_b64_tr_b16 v[194:195], v232 offset:36864\n\t"
    "v_mfma_f32_16x16x32_bf16 v[100:103], v[216:219], a[8:11], v[100:103]\n\t"
    "ds_read_b64_tr_b16 v[196:197], v232 offset:40960\n\t"
    "v_mfma_f32_16x16x32_bf16 v[100:103], v[220:223], a[12:15], v[100:103]\n\t"
    "ds_read_b64_tr_b16 v[198:199], v232 offset:45056\n\t"
    "v_mfma_f32_16x16x32_bf16 v[108:111], v[208:211], a[16:19], v[108:111]\n\t"
    "ds_read_b64_tr_b16 v[200:201], v232 offset:49152\n\t"
    "v_mfma_f32_16x16x32_bf16 v[108:111], v[212:215], a[20:23], v[108:111]\n\t"
    "ds_read_b64_tr_b16 v[202:203], v232 offset:53248\n\t"
    "v_mfma_f32_16x16x32_bf16 v[108:111], v[216:219], a[24:27], v[108:111]\n\t"
    "ds_read_b64_tr_b16 v[204:205], v232 offset:57344\n\t"
    "v_mfma_f32_16x16x32_bf16 v[108:111], v[220:223], a[28:31], v[108:111]\n\t"
    "ds_read_b64_tr_b16 v[206:207], v232 offset:61440\n\t"
    "v_mfma_f32_16x16x32_bf16 v[116:119], v[208:211], a[32:35], v[116:119]\n\t"
    "s_add_u32 s77, s58, 393216\n\t"
    "buffer_load_dwordx4 a[112:115], v255, s[68:71], s77 offen offset:0 sc1\n\t"
    "v_mfma_f32_16x16x32_bf16 v[116:119], v[212:215], a[36:39], v[116:119]\n\t"
    "v_mfma_f32_16x16x32_bf16 v[116:119], v[216:219], a[40:43], v[116:119]\n\t"
    "buffer_load_dwordx4 a[116:119], v255, s[68:71], s77 offen offset:1024 sc1\n\t"
    "v_mfma_f32_16x16x32_bf16 v[116:119], v[220:223], a[44:47], v[116:119]\n\t"
    "v_mfma_f32_16x16x32_bf16 v[124:127], v[208:211], a[48:51], v[124:127]\n\t"
    "buffer_load_dwordx4 a[120:123], v255, s[68:71], s77 offen offset:2048 sc1\n\t"
    "v_mfma_f32_16x16x32_bf16 v[124:127], v[212:215], a[52:55], v[124:127]\n\t"
    "v_mfma_f32_16x16x32_bf16 v[124:127], v[216:219], a[56:59], v[124:127]\n\t"
    "buffer_load_dwordx4 a[124:127], v255, s[68:71], s77 offen offset:3072 sc1\n\t"
    "v_mfma_f32_16x16x32_bf16 v[124:127], v[220:223], a[60:63], v[124:127]\n\t"
    "s_waitcnt lgkmcnt(0)\n\t"
    "v_mfma_f32_16x16x32_bf16 v[128:131], v[192:195], a[0:3], v[128:131]\n\t"
    "v_add_u32 v234, 65536, v229\n\t"
    "ds_read_b64_tr_b16 v[208:209], v234 offset:32768\n\t"
    "v_mfma_f32_16x16x32_bf16 v[128:131], v[196:199], a[4:7], v[128:131]\n\t"
    "ds_read_b64_tr_b16 v[210:211], v234 offset:36864\n\t"
    "v_mfma_f32_16x16x32_bf16 v[128:131], v[200:203], a[8:11], v[128:131]\n\t"
    "ds_read_b64_tr_b16 v[212:213], v234 offset:40960\n\t"
    "v_mfma_f32_16x16x32_bf16 v[128:131], v[204:207], a[12:15], v[128:131]\n\t"
    "ds_read_b64_tr_b16 v[214:215], v234 offset:45056\n\t"
    "v_mfma_f32_16x16x32_bf16 v[136:139], v[192:195], a[16:19], v[136:139]\n\t"
    "ds_read_b64_tr_b16 v[216:217], v234 offset:49152\n\t"
    "v_mfma_f32_16x16x32_bf16 v[136:139], v[196:199], a[20:23], v[136:139]\n\t"
    "ds_read_b64_tr_b16 v[218:219], v234 offset:53248\n\t"
    "v_mfma_f32_16x16x32_bf16 v[136:139], v[200:203], a[24:27], v[136:139]\n\t"
    "ds_read_b64_tr_b16 v[220:221], v234 offset:57344\n\t"
    "v_mfma_f32_16x16x32_bf16 v[136:139], v[204:207], a[28:31], v[136:139]\n\t"
    "ds_read_b64_tr_b16 v[222:223], v234 offset:61440\n\t"
    "v_mfma_f32_16x16x32_bf16 v[144:147], v[192:195], a[32:35], v[144:147]\n\t"
    "s_add_u32 s77, s56, 0\n\t"
    "buffer_load_dwordx4 v[0:3], v255, s[68:71], s77 offen offset:0 sc1\n\t"
    "v_mfma_f32_16x16x32_bf16 v[144:147], v[196:199], a[36:39], v[144:147]\n\t"
    "v_mfma_f32_16x16x32_bf16 v[144:147], v[200:203], a[40:43], v[144:147]\n\t"
    "buffer_load_dwordx4 v[4:7], v255, s[68:71], s77 offen offset:1024 sc1\n\t"
    "v_mfma_f32_16x16x32_bf16 v[144:147], v[204:207], a[44:47], v[144:147]\n\t"
    "v_mfma_f32_16x16x32_bf16 v[152:155], v[192:195], a[48:51], v[152:155]\n\t"
    "buffer_load_dwordx4 v[8:11], v255, s[68:71], s77 offen offset:2048 sc1\n\t"
    "v_mfma_f32_16x16x32_bf16 v[152:155], v[196:199], a[52:55], v[152:155]\n\t"
    "v_mfma_f32_16x16x32_bf16 v[152:155], v[200:203], a[56:59], v[152:155]\n\t"
    "buffer_load_dwordx4 v[12:15], v255, s[68:71], s77 offen offset:3072 sc1\n\t"
    "v_mfma_f32_16x16x32_bf16 v[152:155], v[204:207], a[60:63], v[152:155]\n\t"
    "s_waitcnt lgkmcnt(0)\n\t"
    "v_mfma_f32_16x16x32_bf16 v[132:135], v[208:211], a[0:3], v[132:135]\n\t"
    "v_add_u32 v232, 65536, v230\n\t"
    "ds_read_b64_tr_b16 v[192:193], v232 offset:32768\n\t"
    "v_mfma_f32_16x16x32_bf16 v[132:135], v[212:215], a[4:7], v[132:135]\n\t"
    "ds_read_b64_tr_b16 v[194:195], v232 offset:36864\n\t"
    "v_mfma_f32_16x16x32_bf16 v[132:135], v[216:219], a[8:11], v[132:135]\n\t"
    "ds_read_b64_tr_b16 v[196:197], v232 offset:40960\n\t"
    "v_mfma_f32_16x16x32_bf16 v[132:135], v[220:223], a[12:15], v[132:135]\n\t"
    "ds_read_b64_tr_b16 v[198:199], v232 offset:45056\n\t"
    "v_mfma_f32_16x16x32_bf16 v[140:143], v[208:211], a[16:19], v[140:143]\n\t"
    "ds_read_b64_tr_b16 v[200:201], v232 offset:49152\n\t"
    "v_mfma_f32_16x16x32_bf16 v[140:143], v[212:215], a[20:23], v[140:143]\n\t"
    "ds_read_b64_tr_b16 v[202:203], v232 offset:53248\n\t"
    "v_mfma_f32_16x16x32_bf16 v[140:143], v[216:219], a[24:27], v[140:143]\n\t"
    "ds_read_b64_tr_b16 v[204:205], v232 offset:57344\n\t"
    "v_mfma_f32_16x16x32_bf16 v[140:143], v[220:223], a[28:31], v[140:143]\n\t"
    "ds_read_b64_tr_b16 v[206:207], v232 offset:61440\n\t"
    "v_mfma_f32_16x16x32_bf16 v[148:151], v[208:211], a[32:35], v[148:151]\n\t"
    "s_add_u32 s77, s56, 131072\n\t"
    "buffer_load_dwordx4 v[16:19], v255, s[68:71], s77 offen offset:0 sc1\n\t"
    "v_mfma_f32_16x16x32_bf16 v[148:151], v[212:215], a[36:39], v[148:151]\n\t"
    "v_mfma_f32_16x16x32_bf16 v[148:151], v[216:219], a[40:43], v[148:151]\n\t"
    "buffer_load_dwordx4 v[20:23], v255, s[68:71], s77 offen offset:1024 sc1\n\t"
    "v_mfma_f32_16x16x32_bf16 v[148:151], v[220:223], a[44:47], v[148:151]\n\t"
    "v_mfma_f32_16x16x32_bf16 v[156:159], v[208:211], a[48:51], v[156:159]\n\t"
    "buffer_load_dwordx4 v[24:27], v255, s[68:71], s77 offen offset:2048 sc1\n\t"
    "v_mfma_f32_16x16x32_bf16 v[156:159], v[212:215], a[52:55], v[156:159]\n\t"
    "v_mfma_f32_16x16x32_bf16 v[156:159], v[216:219], a[56:59], v[156:159]\n\t"
    "buffer_load_dwordx4 v[28:31], v255, s[68:71], s77 offen offset:3072 sc1\n\t"
    "v_mfma_f32_16x16x32_bf16 v[156:159], v[220:223], a[60:63], v[156:159]\n\t"
    "s_waitcnt lgkmcnt(0)\n\t"
    "v_mfma_f32_16x16x32_bf16 v[160:163], v[192:195], a[0:3], v[160:163]\n\t"
    "v_add_u32 v234, 65536, v231\n\t"
    "ds_read_b64_tr_b16 v[208:209], v234 offset:32768\n\t"
    "v_mfma_f32_16x16x32_bf16 v[160:163], v[196:199], a[4:7], v[160:163]\n\t"
    "ds_read_b64_tr_b16 v[210:211], v234 offset:36864\n\t"
    "v_mfma_f32_16x16x32_bf16 v[160:163], v[200:203], a[8:11], v[160:163]\n\t"
    "ds_read_b64_tr_b16 v[212:213], v234 offset:40960\n\t"
    "v_mfma_f32_16x16x32_bf16 v[160:163], v[204:207], a[12:15], v[160:163]\n\t"
    "ds_read_b64_tr_b16 v[214:215], v234 offset:45056\n\t"
    "v_mfma_f32_16x16x32_bf16 v[168:171], v[192:195], a[16:19], v[168:171]\n\t"
    "ds_read_b64_tr_b16 v[216:217], v234 offset:49152\n\t"
    "v_mfma_f32_16x16x32_bf16 v[168:171], v[196:199], a[20:23], v[168:171]\n\t"
    "ds_read_b64_tr_b16 v[218:219], v234 offset:53248\n\t"
    "v_mfma_f32_16x16x32_bf16 v[168:171], v[200:203], a[24:27], v[168:171]\n\t"
    "ds_read_b64_tr_b16 v[220:221], v234 offset:57344\n\t"
    "v_mfma_f32_16x16x32_bf16 v[168:171], v[204:207], a[28:31], v[168:171]\n\t"
    "ds_read_b64_tr_b16 v[222:223], v234 offset:61440\n\t"
    "v_mfma_f32_16x16x32_bf16 v[176:179], v[192:195], a[32:35], v[176:179]\n\t"
    "s_add_u32 s77, s56, 262144\n\t"
    "buffer_load_dwordx4 v[32:35], v255, s[68:71], s77 offen offset:0 sc1\n\t"
    "v_mfma_f32_16x16x32_bf16 v[176:179], v[196:199], a[36:39], v[176:179]\n\t"
    "v_mfma_f32_16x16x32_bf16 v[176:179], v[200:203], a[40:43], v[176:179]\n\t"
    "buffer_load_dwordx4 v[36:39], v255, s[68:71], s77 offen offset:1024 sc1\n\t"
    "v_mfma_f32_16x16x32_bf16 v[176:179], v[204:207], a[44:47], v[176:179]\n\t"
    "v_mfma_f32_16x16x32_bf16 v[184:187], v[192:195], a[48:51], v[184:187]\n\t"
    "buffer_load_dwordx4 v[40:43], v255, s[68:71], s77 offen offset:2048 sc1\n\t"
    "v_mfma_f32_16x16x32_bf16 v[184:187], v[196:199], a[52:55], v[184:187]\n\t"
    "v_mfma_f32_16x16x32_bf16 v[184:187], v[200:203], a[56:59], v[184:187]\n\t"
    "buffer_load_dwordx4 v[44:47], v255, s[68:71], s77 offen offset:3072 sc1\n\t"
    "v_mfma_f32_16x16x32_bf16 v[184:187], v[204:207], a[60:63], v[184:187]\n\t"
    "s_waitcnt lgkmcnt(0)\n\t"
    "v_mfma_f32_16x16x32_bf16 v[164:167], v[208:211], a[0:3], v[164:167]\n\t"
    "ds_read_b64_tr_b16 v[192:193], v224 offset:32768\n\t"
    "v_mfma_f32_16x16x32_bf16 v[164:167], v[212:215], a[4:7], v[164:167]\n\t"
    "ds_read_b64_tr_b16 v[194:195], v224 offset:36864\n\t"
    "v_mfma_f32_16x16x32_bf16 v[164:167], v[216:219], a[8:11], v[164:167]\n\t"
    "ds_read_b64_tr_b16 v[196:197], v224 offset:40960\n\t"
    "v_mfma_f32_16x16x32_bf16 v[164:167], v[220:223], a[12:15], v[164:167]\n\t"
    "ds_read_b64_tr_b16 v[198:199], v224 offset:45056\n\t"
    "v_mfma_f32_16x16x32_bf16 v[172:175], v[208:211], a[16:19], v[172:175]\n\t"
    "ds_read_b64_tr_b16 v[200:201], v224 offset:49152\n\t"
    "v_mfma_f32_16x16x32_bf16 v[172:175], v[212:215], a[20:23], v[172:175]\n\t"
    "ds_read_b64_tr_b16 v[202:203], v224 offset:53248\n\t"
    "v_mfma_f32_16x16x32_bf16 v[172:175], v[216:219], a[24:27], v[172:175]\n\t"
    "ds_read_b64_tr_b16 v[204:205], v224 offset:57344\n\t"
    "v_mfma_f32_16x16x32_bf16 v[172:175], v[220:223], a[28:31], v[172:175]\n\t"
    "ds_read_b64_tr_b16 v[206:207], v224 offset:61440\n\t"
    "v_mfma_f32_16x16x32_bf16 v[180:183], v[208:211], a[32:35], v[180:183]\n\t"
    "s_add_u32 s77, s56, 393216\n\t"
    "buffer_load_dwordx4 v[48:51], v255, s[68:71], s77 offen offset:0 sc1\n\t"
    "v_mfma_f32_16x16x32_bf16 v[180:183], v[212:215], a[36:39], v[180:183]\n\t"
    "v_mfma_f32_16x16x32_bf16 v[180:183], v[216:219], a[40:43], v[180:183]\n\t"
    "buffer_load_dwordx4 v[52:55], v255, s[68:71], s77 offen offset:1024 sc1\n\t"
    "v_mfma_f32_16x16x32_bf16 v[180:183], v[220:223], a[44:47], v[180:183]\n\t"
    "v_mfma_f32_16x16x32_bf16 v[188:191], v[208:211], a[48:51], v[188:191]\n\t"
    "buffer_load_dwordx4 v[56:59], v255, s[68:71], s77 offen offset:2048 sc1\n\t"
    "v_mfma_f32_16x16x32_bf16 v[188:191], v[212:215], a[52:55], v[188:191]\n\t"
    "v_mfma_f32_16x16x32_bf16 v[188:191], v[216:219], a[56:59], v[188:191]\n\t"
    "buffer_load_dwordx4 v[60:63], v255, s[68:71], s77 offen offset:3072 sc1\n\t"
    "v_mfma_f32_16x16x32_bf16 v[188:191], v[220:223], a[60:63], v[188:191]\n\t"
    "s_waitcnt lgkmcnt(0)\n\t"
    "s_waitcnt vmcnt(16)\n\t"
    "v_mfma_f32_16x16x32_bf16 v[64:67], v[192:195], a[64:67], v[64:67]\n\t"
    "ds_read_b64_tr_b16 v[208:209], v225 offset:32768\n\t"
    "v_mfma_f32_16x16x32_bf16 v[64:67], v[196:199], a[68:71], v[64:67]\n\t"
    "ds_read_b64_tr_b16 v[210:211], v225 offset:36864\n\t"
    "v_mfma_f32_16x16x32_bf16 v[64:67], v[200:203], a[72:75], v[64:67]\n\t"
    "ds_read_b64_tr_b16 v[212:213], v225 offset:40960\n\t"
    "v_mfma_f32_16x16x32_bf16 v[64:67], v[204:207], a[76:79], v[64:67]\n\t"
    "ds_read_b64_tr_b16 v[214:215], v225 offset:45056\n\t"
    "v_mfma_f32_16x16x32_bf16 v[72:75], v[192:195], a[80:83], v[72:75]\n\t"
    "ds_read_b64_tr_b16 v[216:217], v225 offset:49152\n\t"
    "v_mfma_f32_16x16x32_bf16 v[72:75], v[196:199], a[84:87], v[72:75]\n\t"
    "ds_read_b64_tr_b16 v[218:219], v225 offset:53248\n\t"
    "v_mfma_f32_16x16x32_bf16 v[72:75], v[200:203], a[88:91], v[72:75]\n\t"
    "ds_read_b64_tr_b16 v[220:221], v225 offset:57344\n\t"
    "v_mfma_f32_16x16x32_bf16 v[72:75], v[204:207], a[92:95], v[72:75]\n\t"
    "ds_read_b64_tr_b16 v[222:223], v225 offset:61440\n\t"
    "v_mfma_f32_16x16x32_bf16 v[80:83], v[192:195], a[96:99], v[80:83]\n\t"
    "v_mfma_f32_16x16x32_bf16 v[80:83], v[196:199], a[100:103], v[80:83]\n\t"
    "v_mfma_f32_16x16x32_bf16 v[80:83], v[200:203], a[104:107], v[80:83]\n\t"
    "v_mfma_f32_16x16x32_bf16 v[80:83], v[204:207], a[108:111], v[80:83]\n\t"
    "v_mfma_f32_16x16x32_bf16 v[88:91], v[192:195], a[112:115], v[88:91]\n\t"
    "v_mfma_f32_16x16x32_bf16 v[88:91], v[196:199], a[116:119], v[88:91]\n\t"
    "v_mfma_f32_16x16x32_bf16 v[88:91], v[200:203], a[120:123], v[88:91]\n\t"
    "v_mfma_f32_16x16x32_bf16 v[88:91], v[204:207], a[124:127], v[88:91]\n\t"
    "s_waitcnt lgkmcnt(0)\n\t"
    "v_mfma_f32_16x16x32_bf16 v[68:71], v[208:211], a[64:67], v[68:71]\n\t"
    "ds_read_b64_tr_b16 v[192:193], v226 offset:32768\n\t"
    "v_mfma_f32_16x16x32_bf16 v[68:71], v[212:215], a[68:71], v[68:71]\n\t"
    "ds_read_b64_tr_b16 v[194:195], v226 offset:36864\n\t"
    "v_mfma_f32_16x16x32_bf16 v[68:71], v[216:219], a[72:75], v[68:71]\n\t"
    "ds_read_b64_tr_b16 v[196:197], v226 offset:40960\n\t"
    "v_mfma_f32_16x16x32_bf16 v[68:71], v[220:223], a[76:79], v[68:71]\n\t"
    "ds_read_b64_tr_b16 v[198:199], v226 offset:45056\n\t"
    "v_mfma_f32_16x16x32_bf16 v[76:79], v[208:211], a[80:83], v[76:79]\n\t"
    "ds_read_b64_tr_b16 v[200:201], v226 offset:49152\n\t"
    "v_mfma_f32_16x16x32_bf16 v[76:79], v[212:215], a[84:87], v[76:79]\n\t"
    "ds_read_b64_tr_b16 v[202:203], v226 offset:53248\n\t"
    "v_mfma_f32_16x16x32_bf16 v[76:79], v[216:219], a[88:91], v[76:79]\n\t"
    "ds_read_b64_tr_b16 v[204:205], v226 offset:57344\n\t"
    "v_mfma_f32_16x16x32_bf16 v[76:79], v[220:223], a[92:95], v[76:79]\n\t"
    "ds_read_b64_tr_b16 v[206:207], v226 offset:61440\n\t"
    "v_mfma_f32_16x16x32_bf16 v[84:87], v[208:211], a[96:99], v[84:87]\n\t"
    "v_mfma_f32_16x16x32_bf16 v[84:87], v[212:215], a[100:103], v[84:87]\n\t"
    "v_mfma_f32_16x16x32_bf16 v[84:87], v[216:219], a[104:107], v[84:87]\n\t"
    "v_mfma_f32_16x16x32_bf16 v[84:87], v[220:223], a[108:111], v[84:87]\n\t"
    "v_mfma_f32_16x16x32_bf16 v[92:95], v[208:211], a[112:115], v[92:95]\n\t"
    "v_mfma_f32_16x16x32_bf16 v[92:95], v[212:215], a[116:119], v[92:95]\n\t"
    "v_mfma_f32_16x16x32_bf16 v[92:95], v[216:219], a[120:123], v[92:95]\n\t"
    "v_mfma_f32_16x16x32_bf16 v[92:95], v[220:223], a[124:127], v[92:95]\n\t"
    "s_waitcnt lgkmcnt(0)\n\t"
    "v_mfma_f32_16x16x32_bf16 v[96:99], v[192:195], a[64:67], v[96:99]\n\t"
    "ds_read_b64_tr_b16 v[208:209], v227 offset:32768\n\t"
    "v_mfma_f32_16x16x32_bf16 v[96:99], v[196:199], a[68:71], v[96:99]\n\t"
    "ds_read_b64_tr_b16 v[210:211], v227 offset:36864\n\t"
    "v_mfma_f32_16x16x32_bf16 v[96:99], v[200:203], a[72:75], v[96:99]\n\t"
    "ds_read_b64_tr_b16 v[212:213], v227 offset:40960\n\t"
    "v_mfma_f32_16x16x32_bf16 v[96:99], v[204:207], a[76:79], v[96:99]\n\t"
    "ds_read_b64_tr_b16 v[214:215], v227 offset:45056\n\t"
    "v_mfma_f32_16x16x32_bf16 v[104:107], v[192:195], a[80:83], v[104:107]\n\t"
    "ds_read_b64_tr_b16 v[216:217], v227 offset:49152\n\t"
    "v_mfma_f32_16x16x32_bf16 v[104:107], v[196:199], a[84:87], v[104:107]\n\t"
    "ds_read_b64_tr_b16 v[218:219], v227 offset:53248\n\t"
    "v_mfma_f32_16x16x32_bf16 v[104:107], v[200:203], a[88:91], v[104:107]\n\t"
    "ds_read_b64_tr_b16 v[220:221], v227 offset:57344\n\t"
    "v_mfma_f32_16x16x32_bf16 v[104:107], v[204:207], a[92:95], v[104:107]\n\t"
    "ds_read_b64_tr_b16 v[222:223], v227 offset:61440\n\t"
    "v_mfma_f32_16x16x32_bf16 v[112:115], v[192:195], a[96:99], v[112:115]\n\t"
    "v_mfma_f32_16x16x32_bf16 v[112:115], v[196:199], a[100:103], v[112:115]\n\t"
    "v_mfma_f32_16x16x32_bf16 v[112:115], v[200:203], a[104:107], v[112:115]\n\t"
    "v_mfma_f32_16x16x32_bf16 v[112:115], v[204:207], a[108:111], v[112:115]\n\t"
    "v_mfma_f32_16x16x32_bf16 v[120:123], v[192:195], a[112:115], v[120:123]\n\t"
    "v_mfma_f32_16x16x32_bf16 v[120:123], v[196:199], a[116:119], v[120:123]\n\t"
    "v_mfma_f32_16x16x32_bf16 v[120:123], v[200:203], a[120:123], v[120:123]\n\t"
    "v_mfma_f32_16x16x32_bf16 v[120:123], v[204:207], a[124:127], v[120:123]\n\t"
    "s_waitcnt lgkmcnt(0)\n\t"
    "v_mfma_f32_16x16x32_bf16 v[100:103], v[208:211], a[64:67], v[100:103]\n\t"
    "ds_read_b64_tr_b16 v[192:193], v228 offset:32768\n\t"
    "v_mfma_f32_16x16x32_bf16 v[100:103], v[212:215], a[68:71], v[100:103]\n\t"
    "ds_read_b64_tr_b16 v[194:195], v228 offset:36864\n\t"
    "v_mfma_f32_16x16x32_bf16 v[100:103], v[216:219], a[72:75], v[100:103]\n\t"
    "ds_read_b64_tr_b16 v[196:197], v228 offset:40960\n\t"
    "v_mfma_f32_16x16x32_bf16 v[100:103], v[220:223], a[76:79], v[100:103]\n\t"
    "ds_read_b64_tr_b16 v[198:199], v228 offset:45056\n\t"
    "v_mfma_f32_16x16x32_bf16 v[108:111], v[208:211], a[80:83], v[108:111]\n\t"
    "ds_read_b64_tr_b16 v[200:201], v228 offset:49152\n\t"
    "v_mfma_f32_16x16x32_bf16 v[108:111], v[212:215], a[84:87], v[108:111]\n\t"
    "ds_read_b64_tr_b16 v[202:203], v228 offset:53248\n\t"
    "v_mfma_f32_16x16x32_bf16 v[108:111], v[216:219], a[88:91], v[108:111]\n\t"
    "ds_read_b64_tr_b16 v[204:205], v228 offset:57344\n\t"
    "v_mfma_f32_16x16x32_bf16 v[108:111], v[220:223], a[92:95], v[108:111]\n\t"
    "ds_read_b64_tr_b16 v[206:207], v228 offset:61440\n\t"
    "v_mfma_f32_16x16x32_bf16 v[116:119], v[208:211], a[96:99], v[116:119]\n\t"
    "v_mfma_f32_16x16x32_bf16 v[116:119], v[212:215], a[100:103], v[116:119]\n\t"
    "v_mfma_f32_16x16x32_bf16 v[116:119], v[216:219], a[104:107], v[116:119]\n\t"
    "v_mfma_f32_16x16x32_bf16 v[116:119], v[220:223], a[108:111], v[116:119]\n\t"
    "v_mfma_f32_16x16x32_bf16 v[124:127], v[208:211], a[112:115], v[124:127]\n\t"
    "v_mfma_f32_16x16x32_bf16 v[124:127], v[212:215], a[116:119], v[124:127]\n\t"
    "v_mfma_f32_16x16x32_bf16 v[124:127], v[216:219], a[120:123], v[124:127]\n\t"
    "v_mfma_f32_16x16x32_bf16 v[124:127], v[220:223], a[124:127], v[124:127]\n\t"
    "s_waitcnt lgkmcnt(0)\n\t"
    "buffer_load_dword v252, v253, s[68:71], s92 offen sc1\n\t"
    "v_mfma_f32_16x16x32_bf16 v[128:131], v[192:195], a[64:67], v[128:131]\n\t"
    "ds_read_b64_tr_b16 v[208:209], v229 offset:32768\n\t"
    "v_mfma_f32_16x16x32_bf16 v[128:131], v[196:199], a[68:71], v[128:131]\n\t"
    "ds_read_b64_tr_b16 v[210:211], v229 offset:36864\n\t"
    "v_mfma_f32_16x16x32_bf16 v[128:131], v[200:203], a[72:75], v[128:131]\n\t"
    "ds_read_b64_tr_b16 v[212:213], v229 offset:40960\n\t"
    "v_mfma_f32_16x16x32_bf16 v[128:131], v[204:207], a[76:79], v[128:131]\n\t"
    "ds_read_b64_tr_b16 v[214:215], v229 offset:45056\n\t"
    "v_mfma_f32_16x16x32_bf16 v[136:139], v[192:195], a[80:83], v[136:139]\n\t"
    "ds_read_b64_tr_b16 v[216:217], v229 offset:49152\n\t"
    "v_mfma_f32_16x16x32_bf16 v[136:139], v[196:199], a[84:87], v[136:139]\n\t"
    "ds_read_b64_tr_b16 v[218:219], v229 offset:53248\n\t"
    "v_mfma_f32_16x16x32_bf16 v[136:139], v[200:203], a[88:91], v[136:139]\n\t"
    "ds_read_b64_tr_b16 v[220:221], v229 offset:57344\n\t"
    "v_mfma_f32_16x16x32_bf16 v[136:139], v[204:207], a[92:95], v[136:139]\n\t"
    "ds_read_b64_tr_b16 v[222:223], v229 offset:61440\n\t"
    "v_mfma_f32_16x16x32_bf16 v[144:147], v[192:195], a[96:99], v[144:147]\n\t"
    "v_mfma_f32_16x16x32_bf16 v[144:147], v[196:199], a[100:103], v[144:147]\n\t"
    "v_mfma_f32_16x16x32_bf16 v[144:147], v[200:203], a[104:107], v[144:147]\n\t"
    "v_mfma_f32_16x16x32_bf16 v[144:147], v[204:207], a[108:111], v[144:147]\n\t"
    "v_mfma_f32_16x16x32_bf16 v[152:155], v[192:195], a[112:115], v[152:155]\n\t"
    "v_mfma_f32_16x16x32_bf16 v[152:155], v[196:199], a[116:119], v[152:155]\n\t"
    "v_mfma_f32_16x16x32_bf16 v[152:155], v[200:203], a[120:123], v[152:155]\n\t"
    "v_mfma_f32_16x16x32_bf16 v[152:155], v[204:207], a[124:127], v[152:155]\n\t"
    "s_waitcnt lgkmcnt(0)\n\t"
    "v_mfma_f32_16x16x32_bf16 v[132:135], v[208:211], a[64:67], v[132:135]\n\t"
    "ds_read_b64_tr_b16 v[192:193], v230 offset:32768\n\t"
    "v_mfma_f32_16x16x32_bf16 v[132:135], v[212:215], a[68:71], v[132:135]\n\t"
    "ds_read_b64_tr_b16 v[194:195], v230 offset:36864\n\t"
    "v_mfma_f32_16x16x32_bf16 v[132:135], v[216:219], a[72:75], v[132:135]\n\t"
    "ds_read_b64_tr_b16 v[196:197], v230 offset:40960\n\t"
    "v_mfma_f32_16x16x32_bf16 v[132:135], v[220:223], a[76:79], v[132:135]\n\t"
    "ds_read_b64_tr_b16 v[198:199], v230 offset:45056\n\t"
    "v_mfma_f32_16x16x32_bf16 v[140:143], v[208:211], a[80:83], v[140:143]\n\t"
    "ds_read_b64_tr_b16 v[200:201], v230 offset:49152\n\t"
    "v_mfma_f32_16x16x32_bf16 v[140:143], v[212:215], a[84:87], v[140:143]\n\t"
    "ds_read_b64_tr_b16 v[202:203], v230 offset:53248\n\t"
    "v_mfma_f32_16x16x32_bf16 v[140:143], v[216:219], a[88:91], v[140:143]\n\t"
    "ds_read_b64_tr_b16 v[204:205], v230 offset:57344\n\t"
    "v_mfma_f32_16x16x32_bf16 v[140:143], v[220:223], a[92:95], v[140:143]\n\t"
    "ds_read_b64_tr_b16 v[206:207], v230 offset:61440\n\t"
    "v_mfma_f32_16x16x32_bf16 v[148:151], v[208:211], a[96:99], v[148:151]\n\t"
    "v_mfma_f32_16x16x32_bf16 v[148:151], v[212:215], a[100:103], v[148:151]\n\t"
    "v_mfma_f32_16x16x32_bf16 v[148:151], v[216:219], a[104:107], v[148:151]\n\t"
    "v_mfma_f32_16x16x32_bf16 v[148:151], v[220:223], a[108:111], v[148:151]\n\t"
    "v_mfma_f32_16x16x32_bf16 v[156:159], v[208:211], a[112:115], v[156:159]\n\t"
    "v_mfma_f32_16x16x32_bf16 v[156:159], v[212:215], a[116:119], v[156:159]\n\t"
    "v_mfma_f32_16x16x32_bf16 v[156:159], v[216:219], a[120:123], v[156:159]\n\t"
    "v_mfma_f32_16x16x32_bf16 v[156:159], v[220:223], a[124:127], v[156:159]\n\t"
    "s_waitcnt lgkmcnt(0)\n\t"
    "v_mfma_f32_16x16x32_bf16 v[160:163], v[192:195], a[64:67], v[160:163]\n\t"
    "ds_read_b64_tr_b16 v[208:209], v231 offset:32768\n\t"
    "v_mfma_f32_16x16x32_bf16 v[160:163], v[196:199], a[68:71], v[160:163]\n\t"
    "ds_read_b64_tr_b16 v[210:211], v231 offset:36864\n\t"
    "v_mfma_f32_16x16x32_bf16 v[160:163], v[200:203], a[72:75], v[160:163]\n\t"
    "ds_read_b64_tr_b16 v[212:213], v231 offset:40960\n\t"
    "v_mfma_f32_16x16x32_bf16 v[160:163], v[204:207], a[76:79], v[160:163]\n\t"
    "ds_read_b64_tr_b16 v[214:215], v231 offset:45056\n\t"
    "v_mfma_f32_16x16x32_bf16 v[168:171], v[192:195], a[80:83], v[168:171]\n\t"
    "ds_read_b64_tr_b16 v[216:217], v231 offset:49152\n\t"
    "v_mfma_f32_16x16x32_bf16 v[168:171], v[196:199], a[84:87], v[168:171]\n\t"
    "ds_read_b64_tr_b16 v[218:219], v231 offset:53248\n\t"
    "v_mfma_f32_16x16x32_bf16 v[168:171], v[200:203], a[88:91], v[168:171]\n\t"
    "ds_read_b64_tr_b16 v[220:221], v231 offset:57344\n\t"
    "v_mfma_f32_16x16x32_bf16 v[168:171], v[204:207], a[92:95], v[168:171]\n\t"
    "ds_read_b64_tr_b16 v[222:223], v231 offset:61440\n\t"
    "v_mfma_f32_16x16x32_bf16 v[176:179], v[192:195], a[96:99], v[176:179]\n\t"
    "v_mfma_f32_16x16x32_bf16 v[176:179], v[196:199], a[100:103], v[176:179]\n\t"
    "v_mfma_f32_16x16x32_bf16 v[176:179], v[200:203], a[104:107], v[176:179]\n\t"
    "v_mfma_f32_16x16x32_bf16 v[176:179], v[204:207], a[108:111], v[176:179]\n\t"
    "v_mfma_f32_16x16x32_bf16 v[184:187], v[192:195], a[112:115], v[184:187]\n\t"
    "v_mfma_f32_16x16x32_bf16 v[184:187], v[196:199], a[116:119], v[184:187]\n\t"
    "v_mfma_f32_16x16x32_bf16 v[184:187], v[200:203], a[120:123], v[184:187]\n\t"
    "v_mfma_f32_16x16x32_bf16 v[184:187], v[204:207], a[124:127], v[184:187]\n\t"
    "s_waitcnt lgkmcnt(0)\n\t"
    "s_waitcnt vmcnt(0)\n\t"
    "v_readfirstlane_b32 s78, v252\n\t"
    "s_nop 3\n\t"
    "s_cmp_ge_u32 s78, 8\n\t"
    "s_cbranch_scc1 .Lx%=_7\n\t"
    "s_mov_b32 s79, 0x8000\n\t"
    ".Lx%=_8:\n\t"
    "buffer_load_dword v252, v253, s[68:71], s92 offen sc1\n\t"
    "s_waitcnt vmcnt(0)\n\t"
    "v_readfirstlane_b32 s78, v252\n\t"
    "s_nop 3\n\t"
    "s_cmp_ge_u32 s78, 8\n\t"
    "s_cbranch_scc1 .Lx%=_9\n\t"
    "s_sleep 1\n\t"
    "s_sub_u32 s79, s79, 1\n\t"
    "s_cmp_lg_u32 s79, 0\n\t"
    "s_cbranch_scc1 .Lx%=_8\n\t"
    ".Lx%=_9:\n\t"
    ".Lx%=_7:\n\t"
    "s_add_u32 s76, s101, 26345472\n\t"
    "s_add_u32 s78, s76, 4096\n\t"
    "buffer_load_dwordx4 v[236:239], v255, s[68:71], s76 offen sc1\n\t"
    "buffer_load_dwordx4 v[240:243], v255, s[68:71], s78 offen sc1\n\t"
    "v_mfma_f32_16x16x32_bf16 v[164:167], v[208:211], a[64:67], v[164:167]\n\t"
    "v_add_u32 v232, 65536, v224\n\t"
    "ds_read_b64_tr_b16 v[192:193], v232 offset:0\n\t"
    "v_mfma_f32_16x16x32_bf16 v[164:167], v[212:215], a[68:71], v[164:167]\n\t"
    "ds_read_b64_tr_b16 v[194:195], v232 offset:4096\n\t"
    "v_mfma_f32_16x16x32_bf16 v[164:167], v[216:219], a[72:75], v[164:167]\n\t"
    "ds_read_b64_tr_b16 v[196:197], v232 offset:8192\n\t"
    "v_mfma_f32_16x16x32_bf16 v[164:167], v[220:223], a[76:79], v[164:167]\n\t"
    "ds_read_b64_tr_b16 v[198:199], v232 offset:12288\n\t"
    "v_mfma_f32_16x16x32_bf16 v[172:175], v[208:211], a[80:83], v[172:175]\n\t"
    "ds_read_b64_tr_b16 v[200:201], v232 offset:16384\n\t"
    "v_mfma_f32_16x16x32_bf16 v[172:175], v[212:215], a[84:87], v[172:175]\n\t"
    "ds_read_b64_tr_b16 v[202:203], v232 offset:20480\n\t"
    "v_mfma_f32_16x16x32_bf16 v[172:175], v[216:219], a[88:91], v[172:175]\n\t"
    "ds_read_b64_tr_b16 v[204:205], v232 offset:24576\n\t"
    "v_mfma_f32_16x16x32_bf16 v[172:175], v[220:223], a[92:95], v[172:175]\n\t"
    "ds_read_b64_tr_b16 v[206:207], v232 offset:28672\n\t"
    "v_mfma_f32_16x16x32_bf16 v[180:183], v[208:211], a[96:99], v[180:183]\n\t"
    "v_mfma_f32_16x16x32_bf16 v[180:183], v[212:215], a[100:103], v[180:183]\n\t"
    "v_mfma_f32_16x16x32_bf16 v[180:183], v[216:219], a[104:107], v[180:183]\n\t"
    "v_mfma_f32_16x16x32_bf16 v[180:183], v[220:223], a[108:111], v[180:183]\n\t"
    "v_mfma_f32_16x16x32_bf16 v[188:191], v[208:211], a[112:115], v[188:191]\n\t"
    "v_mfma_f32_16x16x32_bf16 v[188:191], v[212:215], a[116:119], v[188:191]\n\t"
    "v_mfma_f32_16x16x32_bf16 v[188:191], v[216:219], a[120:123], v[188:191]\n\t"
    "v_mfma_f32_16x16x32_bf16 v[188:191], v[220:223], a[124:127], v[188:191]\n\t"
    "s_waitcnt lgkmcnt(0)\n\t"
    "s_waitcnt vmcnt(4)\n\t"
    "v_mfma_f32_16x16x32_bf16 v[64:67], v[192:195], v[0:3], v[64:67]\n\t"
    "v_add_u32 v234, 65536, v225\n\t"
    "ds_read_b64_tr_b16 v[208:209], v234 offset:0\n\t"
    "v_mfma_f32_16x16x32_bf16 v[64:67], v[196:199], v[4:7], v[64:67]\n\t"
    "ds_read_b64_tr_b16 v[210:211], v234 offset:4096\n\t"
    "v_mfma_f32_16x16x32_bf16 v[64:67], v[200:203], v[8:11], v[64:67]\n\t"
    "ds_read_b64_tr_b16 v[212:213], v234 offset:8192\n\t"
    "v_mfma_f32_16x16x32_bf16 v[64:67], v[204:207], v[12:15], v[64:67]\n\t"
    "ds_read_b64_tr_b16 v[214:215], v234 offset:12288\n\t"
    "v_mfma_f32_16x16x32_bf16 v[72:75], v[192:195], v[16:19], v[72:75]\n\t"
    "ds_read_b64_tr_b16 v[216:217], v234 offset:16384\n\t"
    "v_mfma_f32_16x16x32_bf16 v[72:75], v[196:199], v[20:23], v[72:75]\n\t"
    "ds_read_b64_tr_b16 v[218:219], v234 offset:20480\n\t"
    "v_mfma_f32_16x16x32_bf16 v[72:75], v[200:203], v[24:27], v[72:75]\n\t"
    "ds_read_b64_tr_b16 v[220:221], v234 offset:24576\n\t"
    "v_mfma_f32_16x16x32_bf16 v[72:75], v[204:207], v[28:31], v[72:75]\n\t"
    "ds_read_b64_tr_b16 v[222:223], v234 offset:28672\n\t"
    "v_mfma_f32_16x16x32_bf16 v[80:83], v[192:195], v[32:35], v[80:83]\n\t"
    "v_mfma_f32_16x16x32_bf16 v[80:83], v[196:199], v[36:39], v[80:83]\n\t"
    "v_mfma_f32_16x16x32_bf16 v[80:83], v[200:203], v[40:43], v[80:83]\n\t"
    "v_mfma_f32_16x16x32_bf16 v[80:83], v[204:207], v[44:47], v[80:83]\n\t"
    "v_mfma_f32_16x16x32_bf16 v[88:91], v[192:195], v[48:51], v[88:91]\n\t"
    "v_mfma_f32_16x16x32_bf16 v[88:91], v[196:199], v[52:55], v[88:91]\n\t"
    "v_mfma_f32_16x16x32_bf16 v[88:91], v[200:203], v[56:59], v[88:91]\n\t"
    "v_mfma_f32_16x16x32_bf16 v[88:91], v[204:207], v[60:63], v[88:91]\n\t"
    "s_waitcnt lgkmcnt(0)\n\t"
    "v_mfma_f32_16x16x32_bf16 v[68:71], v[208:211], v[0:3], v[68:71]\n\t"
    "v_add_u32 v232, 65536, v226\n\t"
    "ds_read_b64_tr_b16 v[192:193], v232 offset:0\n\t"
    "v_mfma_f32_16x16x32_bf16 v[68:71], v[212:215], v[4:7], v[68:71]\n\t"
    "ds_read_b64_tr_b16 v[194:195], v232 offset:4096\n\t"
    "v_mfma_f32_16x16x32_bf16 v[68:71], v[216:219], v[8:11], v[68:71]\n\t"
    "ds_read_b64_tr_b16 v[196:197], v232 offset:8192\n\t"
    "v_mfma_f32_16x16x32_bf16 v[68:71], v[220:223], v[12:15], v[68:71]\n\t"
    "ds_read_b64_tr_b16 v[198:199], v232 offset:12288\n\t"
    "v_mfma_f32_16x16x32_bf16 v[76:79], v[208:211], v[16:19], v[76:79]\n\t"
    "ds_read_b64_tr_b16 v[200:201], v232 offset:16384\n\t"
    "v_mfma_f32_16x16x32_bf16 v[76:79], v[212:215], v[20:23], v[76:79]\n\t"
    "ds_read_b64_tr_b16 v[202:203], v232 offset:20480\n\t"
    "v_mfma_f32_16x16x32_bf16 v[76:79], v[216:219], v[24:27], v[76:79]\n\t"
    "ds_read_b64_tr_b16 v[204:205], v232 offset:24576\n\t"
    "v_mfma_f32_16x16x32_bf16 v[76:79], v[220:223], v[28:31], v[76:79]\n\t"
    "ds_read_b64_tr_b16 v[206:207], v232 offset:28672\n\t"
    "v_mfma_f32_16x16x32_bf16 v[84:87], v[208:211], v[32:35], v[84:87]\n\t"
    "v_mfma_f32_16x16x32_bf16 v[84:87], v[212:215], v[36:39], v[84:87]\n\t"
    "v_mfma_f32_16x16x32_bf16 v[84:87], v[216:219], v[40:43], v[84:87]\n\t"
    "v_mfma_f32_16x16x32_bf16 v[84:87], v[220:223], v[44:47], v[84:87]\n\t"
    "v_mfma_f32_16x16x32_bf16 v[92:95], v[208:211], v[48:51], v[92:95]\n\t"
    "v_mfma_f32_16x16x32_bf16 v[92:95], v[212:215], v[52:55], v[92:95]\n\t"
    "v_mfma_f32_16x16x32_bf16 v[92:95], v[216:219], v[56:59], v[92:95]\n\t"
    "v_mfma_f32_16x16x32_bf16 v[92:95], v[220:223], v[60:63], v[92:95]\n\t"
    "s_waitcnt lgkmcnt(0)\n\t"
    "v_mfma_f32_16x16x32_bf16 v[96:99], v[192:195], v[0:3], v[96:99]\n\t"
    "v_add_u32 v234, 65536, v227\n\t"
    "ds_read_b64_tr_b16 v[208:209], v234 offset:0\n\t"
    "v_mfma_f32_16x16x32_bf16 v[96:99], v[196:199], v[4:7], v[96:99]\n\t"
    "ds_read_b64_tr_b16 v[210:211], v234 offset:4096\n\t"
    "v_cvt_pk_bf16_f32 v64, v64, v65\n\t"
    "v_mfma_f32_16x16x32_bf16 v[96:99], v[200:203], v[8:11], v[96:99]\n\t"
    "ds_read_b64_tr_b16 v[212:213], v234 offset:8192\n\t"
    "v_cvt_pk_bf16_f32 v65, v66, v67\n\t"
    "v_mfma_f32_16x16x32_bf16 v[96:99], v[204:207], v[12:15], v[96:99]\n\t"
    "ds_read_b64_tr_b16 v[214:215], v234 offset:12288\n\t"
    "v_cvt_pk_bf16_f32 v66, v68, v69\n\t"
    "v_mfma_f32_16x16x32_bf16 v[104:107], v[192:195], v[16:19], v[104:107]\n\t"
    "ds_read_b64_tr_b16 v[216:217], v234 offset:16384\n\t"
    "v_cvt_pk_bf16_f32 v67, v70, v71\n\t"
    "s_add_u32 s79, s81, 43122688\n\t"
    "buffer_store_dwordx4 v[64:67], v255, s[68:71], s79 offen sc1\n\t"
    "v_mfma_f32_16x16x32_bf16 v[104:107], v[196:199], v[20:23], v[104:107]\n\t"
    "ds_read_b64_tr_b16 v[218:219], v234 offset:20480\n\t"
    "v_cvt_pk_bf16_f32 v72, v72, v73\n\t"
    "v_mfma_f32_16x16x32_bf16 v[104:107], v[200:203], v[24:27], v[104:107]\n\t"
    "ds_read_b64_tr_b16 v[220:221], v234 offset:24576\n\t"
    "v_cvt_pk_bf16_f32 v73, v74, v75\n\t"
    "v_mfma_f32_16x16x32_bf16 v[104:107], v[204:207], v[28:31], v[104:107]\n\t"
    "ds_read_b64_tr_b16 v[222:223], v234 offset:28672\n\t"
    "v_cvt_pk_bf16_f32 v74, v76, v77\n\t"
    "v_mfma_f32_16x16x32_bf16 v[112:115], v[192:195], v[32:35], v[112:115]\n\t"
    "v_cvt_pk_bf16_f32 v75, v78, v79\n\t"
    "s_add_u32 s79, s81, 43123712\n\t"
    "buffer_store_dwordx4 v[72:75], v255, s[68:71], s79 offen sc1\n\t"
    "v_mfma_f32_16x16x32_bf16 v[112:115], v[196:199], v[36:39], v[112:115]\n\t"
    "v_cvt_pk_bf16_f32 v80, v80, v81\n\t"
    "v_cvt_pk_bf16_f32 v81, v82, v83\n\t"
    "v_mfma_f32_16x16x32_bf16 v[112:115], v[200:203], v[40:43], v[112:115]\n\t"
    "v_cvt_pk_bf16_f32 v82, v84, v85\n\t"
    "v_cvt_pk_bf16_f32 v83, v86, v87\n\t"
    "s_add_u32 s79, s81, 43188224\n\t"
    "buffer_store_dwordx4 v[80:83], v255, s[68:71], s79 offen sc1\n\t"
    "v_mfma_f32_16x16x32_bf16 v[112:115], v[204:207], v[44:47], v[112:115]\n\t"
    "v_cvt_pk_bf16_f32 v88, v88, v89\n\t"
    "v_cvt_pk_bf16_f32 v89, v90, v91\n\t"
    "v_mfma_f32_16x16x32_bf16 v[120:123], v[192:195], v[48:51], v[120:123]\n\t"
    "v_cvt_pk_bf16_f32 v90, v92, v93\n\t"
    "v_cvt_pk_bf16_f32 v91, v94, v95\n\t"
    "s_add_u32 s79, s81, 43189248\n\t"
    "buffer_store_dwordx4 v[88:91], v255, s[68:71], s79 offen sc1\n\t"
    "v_mfma_f32_16x16x32_bf16 v[120:123], v[196:199], v[52:55], v[120:123]\n\t"
    "v_mfma_f32_16x16x32_bf16 v[120:123], v[200:203], v[56:59], v[120:123]\n\t"
    "v_mfma_f32_16x16x32_bf16 v[120:123], v[204:207], v[60:63], v[120:123]\n\t"
    "s_waitcnt lgkmcnt(0)\n\t"
    "v_mfma_f32_16x16x32_bf16 v[100:103], v[208:211], v[0:3], v[100:103]\n\t"
    "v_add_u32 v232, 65536, v228\n\t"
    "ds_read_b64_tr_b16 v[192:193], v232 offset:0\n\t"
    "v_mfma_f32_16x16x32_bf16 v[100:103], v[212:215], v[4:7], v[100:103]\n\t"
    "ds_read_b64_tr_b16 v[194:195], v232 offset:4096\n\t"
    "v_mfma_f32_16x16x32_bf16 v[100:103], v[216:219], v[8:11], v[100:103]\n\t"
    "ds_read_b64_tr_b16 v[196:197], v232 offset:8192\n\t"
    "v_mfma_f32_16x16x32_bf16 v[100:103], v[220:223], v[12:15], v[100:103]\n\t"
    "ds_read_b64_tr_b16 v[198:199], v232 offset:12288\n\t"
    "v_mfma_f32_16x16x32_bf16 v[108:111], v[208:211], v[16:19], v[108:111]\n\t"
    "ds_read_b64_tr_b16 v[200:201], v232 offset:16384\n\t"
    "v_mfma_f32_16x16x32_bf16 v[108:111], v[212:215], v[20:23], v[108:111]\n\t"
    "ds_read_b64_tr_b16 v[202:203], v232 offset:20480\n\t"
    "v_mfma_f32_16x16x32_bf16 v[108:111], v[216:219], v[24:27], v[108:111]\n\t"
    "ds_read_b64_tr_b16 v[204:205], v232 offset:24576\n\t"
    "v_mfma_f32_16x16x32_bf16 v[108:111], v[220:223], v[28:31], v[108:111]\n\t"
    "ds_read_b64_tr_b16 v[206:207], v232 offset:28672\n\t"
    "v_mfma_f32_16x16x32_bf16 v[116:119], v[208:211], v[32:35], v[116:119]\n\t"
    "v_mfma_f32_16x16x32_bf16 v[116:119], v[212:215], v[36:39], v[116:119]\n\t"
    "v_mfma_f32_16x16x32_bf16 v[116:119], v[216:219], v[40:43], v[116:119]\n\t"
    "v_mfma_f32_16x16x32_bf16 v[116:119], v[220:223], v[44:47], v[116:119]\n\t"
    "v_mfma_f32_16x16x32_bf16 v[124:127], v[208:211], v[48:51], v[124:127]\n\t"
    "v_mfma_f32_16x16x32_bf16 v[124:127], v[212:215], v[52:55], v[124:127]\n\t"
    "v_mfma_f32_16x16x32_bf16 v[124:127], v[216:219], v[56:59], v[124:127]\n\t"
    "v_mfma_f32_16x16x32_bf16 v[124:127], v[220:223], v[60:63], v[124:127]\n\t"
    "s_waitcnt lgkmcnt(0)\n\t"
    "v_mfma_f32_16x16x32_bf16 v[128:131], v[192:195], v[0:3], v[128:131]\n\t"
    "v_add_u32 v234, 65536, v229\n\t"
    "ds_read_b64_tr_b16 v[208:209], v234 offset:0\n\t"
    "v_mfma_f32_16x16x32_bf16 v[128:131], v[196:199], v[4:7], v[128:131]\n\t"
    "ds_read_b64_tr_b16 v[210:211], v234 offset:4096\n\t"
    "v_cvt_pk_bf16_f32 v96, v96, v97\n\t"
    "v_mfma_f32_16x16x32_bf16 v[128:131], v[200:203], v[8:11], v[128:131]\n\t"
    "ds_read_b64_tr_b16 v[212:213], v234 offset:8192\n\t"
    "v_cvt_pk_bf16_f32 v97, v98, v99\n\t"
    "v_mfma_f32_16x16x32_bf16 v[128:131], v[204:207], v[12:15], v[128:131]\n\t"
    "ds_read_b64_tr_b16 v[214:215], v234 offset:12288\n\t"
    "v_cvt_pk_bf16_f32 v98, v100, v101\n\t"
    "v_mfma_f32_16x16x32_bf16 v[136:139], v[192:195], v[16:19], v[136:139]\n\t"
    "ds_read_b64_tr_b16 v[216:217], v234 offset:16384\n\t"
    "v_cvt_pk_bf16_f32 v99, v102, v103\n\t"
    "s_add_u32 s79, s81, 43124736\n\t"
    "buffer_store_dwordx4 v[96:99], v255, s[68:71], s79 offen sc1\n\t"
    "v_mfma_f32_16x16x32_bf16 v[136:139], v[196:199], v[20:23], v[136:139]\n\t"
    "ds_read_b64_tr_b16 v[218:219], v234 offset:20480\n\t"
    "v_cvt_pk_bf16_f32 v104, v104, v105\n\t"
    "v_mfma_f32_16x16x32_bf16 v[136:139], v[200:203], v[24:27], v[136:139]\n\t"
    "ds_read_b64_tr_b16 v[220:221], v234 offset:24576\n\t"
    "v_cvt_pk_bf16_f32 v105, v106, v107\n\t"
    "v_mfma_f32_16x16x32_bf16 v[136:139], v[204:207], v[28:31], v[136:139]\n\t"
    "ds_read_b64_tr_b16 v[222:223], v234 offset:28672\n\t"
    "v_cvt_pk_bf16_f32 v106, v108, v109\n\t"
    "v_mfma_f32_16x16x32_bf16 v[144:147], v[192:195], v[32:35], v[144:147]\n\t"
    "v_cvt_pk_bf16_f32 v107, v110, v111\n\t"
    "s_add_u32 s79, s81, 43125760\n\t"
    "buffer_store_dwordx4 v[104:107], v255, s[68:71], s79 offen sc1\n\t"
    "v_mfma_f32_16x16x32_bf16 v[144:147], v[196:199], v[36:39], v[144:147]\n\t"
    "v_cvt_pk_bf16_f32 v112, v112, v113\n\t"
    "v_cvt_pk_bf16_f32 v113, v114, v115\n\t"
    "v_mfma_f32_16x16x32_bf16 v[144:147], v[200:203], v[40:43], v[144:147]\n\t"
    "v_cvt_pk_bf16_f32 v114, v116, v117\n\t"
    "v_cvt_pk_bf16_f32 v115, v118, v119\n\t"
    "s_add_u32 s79, s81, 43190272\n\t"
    "buffer_store_dwordx4 v[112:115], v255, s[68:71], s79 offen sc1\n\t"
    "v_mfma_f32_16x16x32_bf16 v[144:147], v[204:207], v[44:47], v[144:147]\n\t"
    "v_cvt_pk_bf16_f32 v120, v120, v121\n\t"
    "v_cvt_pk_bf16_f32 v121, v122, v123\n\t"
    "v_mfma_f32_16x16x32_bf16 v[152:155], v[192:195], v[48:51], v[152:155]\n\t"
    "v_cvt_pk_bf16_f32 v122, v124, v125\n\t"
    "v_cvt_pk_bf16_f32 v123, v126, v127\n\t"
    "s_add_u32 s79, s81, 43191296\n\t"
    "buffer_store_dwordx4 v[120:123], v255, s[68:71], s79 offen sc1\n\t"
    "v_mfma_f32_16x16x32_bf16 v[152:155], v[196:199], v[52:55], v[152:155]\n\t"
    "v_mfma_f32_16x16x32_bf16 v[152:155], v[200:203], v[56:59], v[152:155]\n\t"
    "v_mfma_f32_16x16x32_bf16 v[152:155], v[204:207], v[60:63], v[152:155]\n\t"
    "s_waitcnt lgkmcnt(0)\n\t"
    "v_mfma_f32_16x16x32_bf16 v[132:135], v[208:211], v[0:3], v[132:135]\n\t"
    "v_add_u32 v232, 65536, v230\n\t"
    "ds_read_b64_tr_b16 v[192:193], v232 offset:0\n\t"
    "v_mfma_f32_16x16x32_bf16 v[132:135], v[212:215], v[4:7], v[132:135]\n\t"
    "ds_read_b64_tr_b16 v[194:195], v232 offset:4096\n\t"
    "v_mfma_f32_16x16x32_bf16 v[132:135], v[216:219], v[8:11], v[132:135]\n\t"
    "ds_read_b64_tr_b16 v[196:197], v232 offset:8192\n\t"
    "v_mfma_f32_16x16x32_bf16 v[132:135], v[220:223], v[12:15], v[132:135]\n\t"
    "ds_read_b64_tr_b16 v[198:199], v232 offset:12288\n\t"
    "v_mfma_f32_16x16x32_bf16 v[140:143], v[208:211], v[16:19], v[140:143]\n\t"
    "ds_read_b64_tr_b16 v[200:201], v232 offset:16384\n\t"
    "v_mfma_f32_16x16x32_bf16 v[140:143], v[212:215], v[20:23], v[140:143]\n\t"
    "ds_read_b64_tr_b16 v[202:203], v232 offset:20480\n\t"
    "v_mfma_f32_16x16x32_bf16 v[140:143], v[216:219], v[24:27], v[140:143]\n\t"
    "ds_read_b64_tr_b16 v[204:205], v232 offset:24576\n\t"
    "v_mfma_f32_16x16x32_bf16 v[140:143], v[220:223], v[28:31], v[140:143]\n\t"
    "ds_read_b64_tr_b16 v[206:207], v232 offset:28672\n\t"
    "v_mfma_f32_16x16x32_bf16 v[148:151], v[208:211], v[32:35], v[148:151]\n\t"
    "v_mfma_f32_16x16x32_bf16 v[148:151], v[212:215], v[36:39], v[148:151]\n\t"
    "v_mfma_f32_16x16x32_bf16 v[148:151], v[216:219], v[40:43], v[148:151]\n\t"
    "v_mfma_f32_16x16x32_bf16 v[148:151], v[220:223], v[44:47], v[148:151]\n\t"
    "v_mfma_f32_16x16x32_bf16 v[156:159], v[208:211], v[48:51], v[156:159]\n\t"
    "v_mfma_f32_16x16x32_bf16 v[156:159], v[212:215], v[52:55], v[156:159]\n\t"
    "v_mfma_f32_16x16x32_bf16 v[156:159], v[216:219], v[56:59], v[156:159]\n\t"
    "v_mfma_f32_16x16x32_bf16 v[156:159], v[220:223], v[60:63], v[156:159]\n\t"
    "s_waitcnt lgkmcnt(0)\n\t"
    "v_mfma_f32_16x16x32_bf16 v[160:163], v[192:195], v[0:3], v[160:163]\n\t"
    "v_add_u32 v234, 65536, v231\n\t"
    "ds_read_b64_tr_b16 v[208:209], v234 offset:0\n\t"
    "v_mfma_f32_16x16x32_bf16 v[160:163], v[196:199], v[4:7], v[160:163]\n\t"
    "ds_read_b64_tr_b16 v[210:211], v234 offset:4096\n\t"
    "v_cvt_pk_bf16_f32 v128, v128, v129\n\t"
    "v_mfma_f32_16x16x32_bf16 v[160:163], v[200:203], v[8:11], v[160:163]\n\t"
    "ds_read_b64_tr_b16 v[212:213], v234 offset:8192\n\t"
    "v_cvt_pk_bf16_f32 v129, v130, v131\n\t"
    "v_mfma_f32_16x16x32_bf16 v[160:163], v[204:207], v[12:15], v[160:163]\n\t"
    "ds_read_b64_tr_b16 v[214:215], v234 offset:12288\n\t"
    "v_cvt_pk_bf16_f32 v130, v132, v133\n\t"
    "v_mfma_f32_16x16x32_bf16 v[168:171], v[192:195], v[16:19], v[168:171]\n\t"
    "ds_read_b64_tr_b16 v[216:217], v234 offset:16384\n\t"
    "v_cvt_pk_bf16_f32 v131, v134, v135\n\t"
    "s_add_u32 s79, s81, 43126784\n\t"
    "buffer_store_dwordx4 v[128:131], v255, s[68:71], s79 offen sc1\n\t"
    "v_mfma_f32_16x16x32_bf16 v[168:171], v[196:199], v[20:23], v[168:171]\n\t"
    "ds_read_b64_tr_b16 v[218:219], v234 offset:20480\n\t"
    "v_cvt_pk_bf16_f32 v136, v136, v137\n\t"
    "v_mfma_f32_16x16x32_bf16 v[168:171], v[200:203], v[24:27], v[168:171]\n\t"
    "ds_read_b64_tr_b16 v[220:221], v234 offset:24576\n\t"
    "v_cvt_pk_bf16_f32 v137, v138, v139\n\t"
    "v_mfma_f32_16x16x32_bf16 v[168:171], v[204:207], v[28:31], v[168:171]\n\t"
    "ds_read_b64_tr_b16 v[222:223], v234 offset:28672\n\t"
    "v_cvt_pk_bf16_f32 v138, v140, v141\n\t"
    "v_mfma_f32_16x16x32_bf16 v[176:179], v[192:195], v[32:35], v[176:179]\n\t"
    "v_cvt_pk_bf16_f32 v139, v142, v143\n\t"
    "s_add_u32 s79, s81, 43127808\n\t"
    "buffer_store_dwordx4 v[136:139], v255, s[68:71], s79 offen sc1\n\t"
    "v_mfma_f32_16x16x32_bf16 v[176:179], v[196:199], v[36:39], v[176:179]\n\t"
    "v_cvt_pk_bf16_f32 v144, v144, v145\n\t"
    "v_cvt_pk_bf16_f32 v145, v146, v147\n\t"
    "v_mfma_f32_16x16x32_bf16 v[176:179], v[200:203], v[40:43], v[176:179]\n\t"
    "v_cvt_pk_bf16_f32 v146, v148, v149\n\t"
    "v_cvt_pk_bf16_f32 v147, v150, v151\n\t"
    "s_add_u32 s79, s81, 43192320\n\t"
    "buffer_store_dwordx4 v[144:147], v255, s[68:71], s79 offen sc1\n\t"
    "v_mfma_f32_16x16x32_bf16 v[176:179], v[204:207], v[44:47], v[176:179]\n\t"
    "v_cvt_pk_bf16_f32 v152, v152, v153\n\t"
    "v_cvt_pk_bf16_f32 v153, v154, v155\n\t"
    "v_mfma_f32_16x16x32_bf16 v[184:187], v[192:195], v[48:51], v[184:187]\n\t"
    "v_cvt_pk_bf16_f32 v154, v156, v157\n\t"
    "v_cvt_pk_bf16_f32 v155, v158, v159\n\t"
    "s_add_u32 s79, s81, 43193344\n\t"
    "buffer_store_dwordx4 v[152:155], v255, s[68:71], s79 offen sc1\n\t"
    "v_mfma_f32_16x16x32_bf16 v[184:187], v[196:199], v[52:55], v[184:187]\n\t"
    "v_mfma_f32_16x16x32_bf16 v[184:187], v[200:203], v[56:59], v[184:187]\n\t"
    "v_mfma_f32_16x16x32_bf16 v[184:187], v[204:207], v[60:63], v[184:187]\n\t"
    "s_waitcnt lgkmcnt(0)\n\t"
    "v_mfma_f32_16x16x32_bf16 v[164:167], v[208:211], v[0:3], v[164:167]\n\t"
    "v_mfma_f32_16x16x32_bf16 v[164:167], v[212:215], v[4:7], v[164:167]\n\t"
    "v_mfma_f32_16x16x32_bf16 v[164:167], v[216:219], v[8:11], v[164:167]\n\t"
    "v_mfma_f32_16x16x32_bf16 v[164:167], v[220:223], v[12:15], v[164:167]\n\t"
    "v_mfma_f32_16x16x32_bf16 v[172:175], v[208:211], v[16:19], v[172:175]\n\t"
    "v_mfma_f32_16x16x32_bf16 v[172:175], v[212:215], v[20:23], v[172:175]\n\t"
    "v_mfma_f32_16x16x32_bf16 v[172:175], v[216:219], v[24:27], v[172:175]\n\t"
    "v_mfma_f32_16x16x32_bf16 v[172:175], v[220:223], v[28:31], v[172:175]\n\t"
    "v_mfma_f32_16x16x32_bf16 v[180:183], v[208:211], v[32:35], v[180:183]\n\t"
    "v_mfma_f32_16x16x32_bf16 v[180:183], v[212:215], v[36:39], v[180:183]\n\t"
    "v_mfma_f32_16x16x32_bf16 v[180:183], v[216:219], v[40:43], v[180:183]\n\t"
    "v_mfma_f32_16x16x32_bf16 v[180:183], v[220:223], v[44:47], v[180:183]\n\t"
    "v_mfma_f32_16x16x32_bf16 v[188:191], v[208:211], v[48:51], v[188:191]\n\t"
    "v_mfma_f32_16x16x32_bf16 v[188:191], v[212:215], v[52:55], v[188:191]\n\t"
    "v_mfma_f32_16x16x32_bf16 v[188:191], v[216:219], v[56:59], v[188:191]\n\t"
    "v_mfma_f32_16x16x32_bf16 v[188:191], v[220:223], v[60:63], v[188:191]\n\t"
    "s_waitcnt lgkmcnt(0)\n\t"
    "s_nop 7\n\t"
    "v_cvt_pk_bf16_f32 v160, v160, v161\n\t"
    "v_cvt_pk_bf16_f32 v161, v162, v163\n\t"
    "v_cvt_pk_bf16_f32 v162, v164, v165\n\t"
    "v_cvt_pk_bf16_f32 v163, v166, v167\n\t"
    "s_add_u32 s79, s81, 43128832\n\t"
    "buffer_store_dwordx4 v[160:163], v255, s[68:71], s79 offen sc1\n\t"
    "v_cvt_pk_bf16_f32 v168, v168, v169\n\t"
    "v_cvt_pk_bf16_f32 v169, v170, v171\n\t"
    "v_cvt_pk_bf16_f32 v170, v172, v173\n\t"
    "v_cvt_pk_bf16_f32 v171, v174, v175\n\t"
    "s_add_u32 s79, s81, 43129856\n\t"
    "buffer_store_dwordx4 v[168:171], v255, s[68:71], s79 offen sc1\n\t"
    "v_cvt_pk_bf16_f32 v176, v176, v177\n\t"
    "v_cvt_pk_bf16_f32 v177, v178, v179\n\t"
    "v_cvt_pk_bf16_f32 v178, v180, v181\n\t"
    "v_cvt_pk_bf16_f32 v179, v182, v183\n\t"
    "s_add_u32 s79, s81, 43194368\n\t"
    "buffer_store_dwordx4 v[176:179], v255, s[68:71], s79 offen sc1\n\t"
    "v_cvt_pk_bf16_f32 v184, v184, v185\n\t"
    "v_cvt_pk_bf16_f32 v185, v186, v187\n\t"
    "v_cvt_pk_bf16_f32 v186, v188, v189\n\t"
    "v_cvt_pk_bf16_f32 v187, v190, v191\n\t"
    "s_add_u32 s79, s81, 43195392\n\t"
    "buffer_store_dwordx4 v[184:187], v255, s[68:71], s79 offen sc1\n\t"
    "s_waitcnt vmcnt(4)\n\t"
    "s_mov_b64 exec, 1\n\t"
    "s_add_u32 s78, s87, 16384\n\t"
    "buffer_atomic_add v251, v253, s[68:71], s78 offen\n\t"
    "s_add_u32 s78, s87, 16448\n\t"
    "buffer_atomic_add v251, v253, s[68:71], s78 offen\n\t"
    "s_add_u32 s78, s87, 16400\n\t"
    "buffer_atomic_add v251, v253, s[68:71], s78 offen\n\t"
    "s_add_u32 s78, s87, 16464\n\t"
    "buffer_atomic_add v251, v253, s[68:71], s78 offen\n\t"
    "s_add_u32 s78, s87, 16416\n\t"
    "buffer_atomic_add v251, v253, s[68:71], s78 offen\n\t"
    "s_add_u32 s78, s87, 16480\n\t"
    "buffer_atomic_add v251, v253, s[68:71], s78 offen\n\t"
    "s_mov_b64 exec, -1\n\t"
    "s_load_dwordx2 s[40:41], %[ka], 48\n\t"
    "s_load_dwordx2 s[44:45], %[ka], 80\n\t"
    "s_mov_b32 s42, 2097152\n\t"
    "s_mov_b32 s43, 0x20000\n\t"
    "s_mov_b32 s46, 2097152\n\t"
    "s_mov_b32 s47, 0x20000\n\t"
    "v_lshrrev_b32 v225, 8, v255\n\t"
    "v_lshlrev_b32 v225, 3, v225\n\t"
    "v_bfe_u32 v224, v255, 4, 4\n\t"
    "v_lshl_add_u32 v224, v224, 10, v225\n\t"
    "s_waitcnt lgkmcnt(0)\n\t"
    "buffer_load_dwordx2 v[226:227], v224, s[40:43], s83 offen offset:0\n\t"
    "buffer_load_dwordx2 v[228:229], v224, s[40:43], s83 offen offset:32\n\t"
    "s_add_u32 s77, s83, 16384\n\t"
    "buffer_load_dwordx2 v[230:231], v224, s[40:43], s77 offen offset:0\n\t"
    "buffer_load_dwordx2 v[232:233], v224, s[40:43], s77 offen offset:32\n\t"
    "s_lshl_b32 s76, s50, 4\n\t"
    "s_add_u32 s90, s90, s76\n\t"
    "s_waitcnt vmcnt(10)\n\t"
    "s_mov_b64 exec, 1\n\t"
    "s_add_u32 s78, s87, 16432\n\t"
    "buffer_atomic_add v251, v253, s[68:71], s78 offen\n\t"
    "s_add_u32 s78, s87, 16496\n\t"
    "buffer_atomic_add v251, v253, s[68:71], s78 offen\n\t"
    "s_mov_b64 exec, -1\n\t"
    "s_waitcnt vmcnt(2)\n\t"
    "v_lshlrev_b32 v128, 16, v236\n\t"
    "v_and_b32 v129, 0xffff0000, v236\n\t"
    "v_lshlrev_b32 v130, 16, v237\n\t"
    "v_and_b32 v131, 0xffff0000, v237\n\t"
    "v_lshlrev_b32 v132, 16, v238\n\t"
    "v_and_b32 v133, 0xffff0000, v238\n\t"
    "v_lshlrev_b32 v134, 16, v239\n\t"
    "v_and_b32 v135, 0xffff0000, v239\n\t"
    "v_lshlrev_b32 v136, 16, v240\n\t"
    "v_and_b32 v137, 0xffff0000, v240\n\t"
    "v_lshlrev_b32 v138, 16, v241\n\t"
    "v_and_b32 v139, 0xffff0000, v241\n\t"
    "v_lshlrev_b32 v140, 16, v242\n\t"
    "v_and_b32 v141, 0xffff0000, v242\n\t"
    "v_lshlrev_b32 v142, 16, v243\n\t"
    "v_and_b32 v143, 0xffff0000, v243\n\t"
    "v_lshlrev_b32 v160, 16, v226\n\t"
    "v_and_b32 v161, 0xffff0000, v226\n\t"
    "v_lshlrev_b32 v162, 16, v227\n\t"
    "v_and_b32 v163, 0xffff0000, v227\n\t"
    "v_lshlrev_b32 v164, 16, v228\n\t"
    "v_and_b32 v165, 0xffff0000, v228\n\t"
    "v_lshlrev_b32 v166, 16, v229\n\t"
    "v_and_b32 v167, 0xffff0000, v229\n\t"
    "v_lshlrev_b32 v168, 16, v230\n\t"
    "v_and_b32 v169, 0xffff0000, v230\n\t"
    "v_lshlrev_b32 v170, 16, v231\n\t"
    "v_and_b32 v171, 0xffff0000, v231\n\t"
    "v_lshlrev_b32 v172, 16, v232\n\t"
    "v_and_b32 v173, 0xffff0000, v232\n\t"
    "v_lshlrev_b32 v174, 16, v233\n\t"
    "v_and_b32 v175, 0xffff0000, v233\n\t"
    "s_add_u32 s77, s90, 16384\n\t"
    "s_mov_b32 s79, 0x8000\n\t"
    ".Lx%=_10:\n\t"
    "buffer_load_dword v252, v253, s[68:71], s77 offen sc1\n\t"
    "s_waitcnt vmcnt(0)\n\t"
    "v_readfirstlane_b32 s78, v252\n\t"
    "s_nop 3\n\t"
    "s_cmp_ge_u32 s78, 8\n\t"
    "s_cbranch_scc1 .Lx%=_11\n\t"
    "s_sleep 1\n\t"
    "s_sub_u32 s79, s79, 1\n\t"
    "s_cmp_lg_u32 s79, 0\n\t"
    "s_cbranch_scc1 .Lx%=_10\n\t"
    ".Lx%=_11:\n\t"
    "s_add_u32 s78, s82, 43122688\n\t"
    "buffer_load_dwordx4 v[0:3], v255, s[68:71], s78 offen offset:0 sc1\n\t"
    "buffer_load_dwordx4 v[32:35], v255, s[68:71], s78 offen offset:1024 sc1\n\t"
    "s_add_u32 s78, s82, 43130880\n\t"
    "buffer_load_dwordx4 v[4:7], v255, s[68:71], s78 offen offset:0 sc1\n\t"
    "buffer_load_dwordx4 v[36:39], v255, s[68:71], s78 offen offset:1024 sc1\n\t"
    "s_add_u32 s78, s82, 43139072\n\t"
    "buffer_load_dwordx4 v[8:11], v255, s[68:71], s78 offen offset:0 sc1\n\t"
    "buffer_load_dwordx4 v[40:43], v255, s[68:71], s78 offen offset:1024 sc1\n\t"
    "s_add_u32 s78, s82, 43147264\n\t"
    "buffer_load_dwordx4 v[12:15], v255, s[68:71], s78 offen offset:0 sc1\n\t"
    "buffer_load_dwordx4 v[44:47], v255, s[68:71], s78 offen offset:1024 sc1\n\t"
    "s_add_u32 s78, s82, 43155456\n\t"
    "buffer_load_dwordx4 v[16:19], v255, s[68:71], s78 offen offset:0 sc1\n\t"
    "buffer_load_dwordx4 v[48:51], v255, s[68:71], s78 offen offset:1024 sc1\n\t"
    "s_add_u32 s78, s82, 43163648\n\t"
    "buffer_load_dwordx4 v[20:23], v255, s[68:71], s78 offen offset:0 sc1\n\t"
    "buffer_load_dwordx4 v[52:55], v255, s[68:71], s78 offen offset:1024 sc1\n\t"
    "s_add_u32 s78, s82, 43171840\n\t"
    "buffer_load_dwordx4 v[24:27], v255, s[68:71], s78 offen offset:0 sc1\n\t"
    "buffer_load_dwordx4 v[56:59], v255, s[68:71], s78 offen offset:1024 sc1\n\t"
    "s_add_u32 s78, s82, 43180032\n\t"
    "buffer_load_dwordx4 v[28:31], v255, s[68:71], s78 offen offset:0 sc1\n\t"
    "buffer_load_dwordx4 v[60:63], v255, s[68:71], s78 offen offset:1024 sc1\n\t"
    "s_mov_b32 s76, 0x00003f80\n\t"
    "s_mov_b32 s77, 0x3f800000\n\t"
    "s_waitcnt vmcnt(14)\n\t"
    "v_dot2_f32_bf16 v176, v0, s76, 0\n\t"
    "v_dot2_f32_bf16 v177, v0, s77, 0\n\t"
    "v_dot2_f32_bf16 v178, v1, s76, 0\n\t"
    "v_dot2_f32_bf16 v179, v1, s77, 0\n\t"
    "v_dot2_f32_bf16 v180, v2, s76, 0\n\t"
    "v_dot2_f32_bf16 v181, v2, s77, 0\n\t"
    "v_dot2_f32_bf16 v182, v3, s76, 0\n\t"
    "v_dot2_f32_bf16 v183, v3, s77, 0\n\t"
    "v_dot2_f32_bf16 v184, v32, s76, 0\n\t"
    "v_dot2_f32_bf16 v185, v32, s77, 0\n\t"
    "v_dot2_f32_bf16 v186, v33, s76, 0\n\t"
    "v_dot2_f32_bf16 v187, v33, s77, 0\n\t"
    "v_dot2_f32_bf16 v188, v34, s76, 0\n\t"
    "v_dot2_f32_bf16 v189, v34, s77, 0\n\t"
    "v_dot2_f32_bf16 v190, v35, s76, 0\n\t"
    "v_dot2_f32_bf16 v191, v35, s77, 0\n\t"
    "s_waitcnt vmcnt(12)\n\t"
    "v_dot2_f32_bf16 v176, v4, s76, v176\n\t"
    "v_dot2_f32_bf16 v177, v4, s77, v177\n\t"
    "v_dot2_f32_bf16 v178, v5, s76, v178\n\t"
    "v_dot2_f32_bf16 v179, v5, s77, v179\n\t"
    "v_dot2_f32_bf16 v180, v6, s76, v180\n\t"
    "v_dot2_f32_bf16 v181, v6, s77, v181\n\t"
    "v_dot2_f32_bf16 v182, v7, s76, v182\n\t"
    "v_dot2_f32_bf16 v183, v7, s77, v183\n\t"
    "v_dot2_f32_bf16 v184, v36, s76, v184\n\t"
    "v_dot2_f32_bf16 v185, v36, s77, v185\n\t"
    "v_dot2_f32_bf16 v186, v37, s76, v186\n\t"
    "v_dot2_f32_bf16 v187, v37, s77, v187\n\t"
    "v_dot2_f32_bf16 v188, v38, s76, v188\n\t"
    "v_dot2_f32_bf16 v189, v38, s77, v189\n\t"
    "v_dot2_f32_bf16 v190, v39, s76, v190\n\t"
    "v_dot2_f32_bf16 v191, v39, s77, v191\n\t"
    "s_waitcnt vmcnt(10)\n\t"
    "v_dot2_f32_bf16 v176, v8, s76, v176\n\t"
    "v_dot2_f32_bf16 v177, v8, s77, v177\n\t"
    "v_dot2_f32_bf16 v178, v9, s76, v178\n\t"
    "v_dot2_f32_bf16 v179, v9, s77, v179\n\t"
    "v_dot2_f32_bf16 v180, v10, s76, v180\n\t"
    "v_dot2_f32_bf16 v181, v10, s77, v181\n\t"
    "v_dot2_f32_bf16 v182, v11, s76, v182\n\t"
    "v_dot2_f32_bf16 v183, v11, s77, v183\n\t"
    "v_dot2_f32_bf16 v184, v40, s76, v184\n\t"
    "v_dot2_f32_bf16 v185, v40, s77, v185\n\t"
    "v_dot2_f32_bf16 v186, v41, s76, v186\n\t"
    "v_dot2_f32_bf16 v187, v41, s77, v187\n\t"
    "v_dot2_f32_bf16 v188, v42, s76, v188\n\t"
    "v_dot2_f32_bf16 v189, v42, s77, v189\n\t"
    "v_dot2_f32_bf16 v190, v43, s76, v190\n\t"
    "v_dot2_f32_bf16 v191, v43, s77, v191\n\t"
    "s_waitcnt vmcnt(8)\n\t"
    "v_dot2_f32_bf16 v176, v12, s76, v176\n\t"
    "v_dot2_f32_bf16 v177, v12, s77, v177\n\t"
    "v_dot2_f32_bf16 v178, v13, s76, v178\n\t"
    "v_dot2_f32_bf16 v179, v13, s77, v179\n\t"
    "v_dot2_f32_bf16 v180, v14, s76, v180\n\t"
    "v_dot2_f32_bf16 v181, v14, s77, v181\n\t"
    "v_dot2_f32_bf16 v182, v15, s76, v182\n\t"
    "v_dot2_f32_bf16 v183, v15, s77, v183\n\t"
    "v_dot2_f32_bf16 v184, v44, s76, v184\n\t"
    "v_dot2_f32_bf16 v185, v44, s77, v185\n\t"
    "v_dot2_f32_bf16 v186, v45, s76, v186\n\t"
    "v_dot2_f32_bf16 v187, v45, s77, v187\n\t"
    "v_dot2_f32_bf16 v188, v46, s76, v188\n\t"
    "v_dot2_f32_bf16 v189, v46, s77, v189\n\t"
    "v_dot2_f32_bf16 v190, v47, s76, v190\n\t"
    "v_dot2_f32_bf16 v191, v47, s77, v191\n\t"
    "s_waitcnt vmcnt(6)\n\t"
    "v_dot2_f32_bf16 v176, v16, s76, v176\n\t"
    "v_dot2_f32_bf16 v177, v16, s77, v177\n\t"
    "v_dot2_f32_bf16 v178, v17, s76, v178\n\t"
    "v_dot2_f32_bf16 v179, v17, s77, v179\n\t"
    "v_dot2_f32_bf16 v180, v18, s76, v180\n\t"
    "v_dot2_f32_bf16 v181, v18, s77, v181\n\t"
    "v_dot2_f32_bf16 v182, v19, s76, v182\n\t"
    "v_dot2_f32_bf16 v183, v19, s77, v183\n\t"
    "v_dot2_f32_bf16 v184, v48, s76, v184\n\t"
    "v_dot2_f32_bf16 v185, v48, s77, v185\n\t"
    "v_dot2_f32_bf16 v186, v49, s76, v186\n\t"
    "v_dot2_f32_bf16 v187, v49, s77, v187\n\t"
    "v_dot2_f32_bf16 v188, v50, s76, v188\n\t"
    "v_dot2_f32_bf16 v189, v50, s77, v189\n\t"
    "v_dot2_f32_bf16 v190, v51, s76, v190\n\t"
    "v_dot2_f32_bf16 v191, v51, s77, v191\n\t"
    "s_waitcnt vmcnt(4)\n\t"
    "v_dot2_f32_bf16 v176, v20, s76, v176\n\t"
    "v_dot2_f32_bf16 v177, v20, s77, v177\n\t"
    "v_dot2_f32_bf16 v178, v21, s76, v178\n\t"
    "v_dot2_f32_bf16 v179, v21, s77, v179\n\t"
    "v_dot2_f32_bf16 v180, v22, s76, v180\n\t"
    "v_dot2_f32_bf16 v181, v22, s77, v181\n\t"
    "v_dot2_f32_bf16 v182, v23, s76, v182\n\t"
    "v_dot2_f32_bf16 v183, v23, s77, v183\n\t"
    "v_dot2_f32_bf16 v184, v52, s76, v184\n\t"
    "v_dot2_f32_bf16 v185, v52, s77, v185\n\t"
    "v_dot2_f32_bf16 v186, v53, s76, v186\n\t"
    "v_dot2_f32_bf16 v187, v53, s77, v187\n\t"
    "v_dot2_f32_bf16 v188, v54, s76, v188\n\t"
    "v_dot2_f32_bf16 v189, v54, s77, v189\n\t"
    "v_dot2_f32_bf16 v190, v55, s76, v190\n\t"
    "v_dot2_f32_bf16 v191, v55, s77, v191\n\t"
    "s_waitcnt vmcnt(2)\n\t"
    "v_dot2_f32_bf16 v176, v24, s76, v176\n\t"
    "v_dot2_f32_bf16 v177, v24, s77, v177\n\t"
    "v_dot2_f32_bf16 v178, v25, s76, v178\n\t"
    "v_dot2_f32_bf16 v179, v25, s77, v179\n\t"
    "v_dot2_f32_bf16 v180, v26, s76, v180\n\t"
    "v_dot2_f32_bf16 v181, v26, s77, v181\n\t"
    "v_dot2_f32_bf16 v182, v27, s76, v182\n\t"
    "v_dot2_f32_bf16 v183, v27, s77, v183\n\t"
    "v_dot2_f32_bf16 v184, v56, s76, v184\n\t"
    "v_dot2_f32_bf16 v185, v56, s77, v185\n\t"
    "v_dot2_f32_bf16 v186, v57, s76, v186\n\t"
    "v_dot2_f32_bf16 v187, v57, s77, v187\n\t"
    "v_dot2_f32_bf16 v188, v58, s76, v188\n\t"
    "v_dot2_f32_bf16 v189, v58, s77, v189\n\t"
    "v_dot2_f32_bf16 v190, v59, s76, v190\n\t"
    "v_dot2_f32_bf16 v191, v59, s77, v191\n\t"
    "s_waitcnt vmcnt(0)\n\t"
    "v_dot2_f32_bf16 v176, v28, s76, v176\n\t"
    "v_dot2_f32_bf16 v177, v28, s77, v177\n\t"
    "v_dot2_f32_bf16 v178, v29, s76, v178\n\t"
    "v_dot2_f32_bf16 v179, v29, s77, v179\n\t"
    "v_dot2_f32_bf16 v180, v30, s76, v180\n\t"
    "v_dot2_f32_bf16 v181, v30, s77, v181\n\t"
    "v_dot2_f32_bf16 v182, v31, s76, v182\n\t"
    "v_dot2_f32_bf16 v183, v31, s77, v183\n\t"
    "v_dot2_f32_bf16 v184, v60, s76, v184\n\t"
    "v_dot2_f32_bf16 v185, v60, s77, v185\n\t"
    "v_dot2_f32_bf16 v186, v61, s76, v186\n\t"
    "v_dot2_f32_bf16 v187, v61, s77, v187\n\t"
    "v_dot2_f32_bf16 v188, v62, s76, v188\n\t"
    "v_dot2_f32_bf16 v189, v62, s77, v189\n\t"
    "v_dot2_f32_bf16 v190, v63, s76, v190\n\t"
    "v_dot2_f32_bf16 v191, v63, s77, v191\n\t"
    "s_nop 2\n\t"
    "v_fma_f32 v160, v176, v128, v160\n\t"
    "v_fma_f32 v161, v177, v129, v161\n\t"
    "v_fma_f32 v162, v178, v130, v162\n\t"
    "v_fma_f32 v163, v179, v131, v163\n\t"
    "v_fma_f32 v164, v180, v132, v164\n\t"
    "v_fma_f32 v165, v181, v133, v165\n\t"
    "v_fma_f32 v166, v182, v134, v166\n\t"
    "v_fma_f32 v167, v183, v135, v167\n\t"
    "v_fma_f32 v168, v184, v136, v168\n\t"
    "v_fma_f32 v169, v185, v137, v169\n\t"
    "v_fma_f32 v170, v186, v138, v170\n\t"
    "v_fma_f32 v171, v187, v139, v171\n\t"
    "v_fma_f32 v172, v188, v140, v172\n\t"
    "v_fma_f32 v173, v189, v141, v173\n\t"
    "v_fma_f32 v174, v190, v142, v174\n\t"
    "v_fma_f32 v175, v191, v143, v175\n\t"
    "v_cvt_pk_bf16_f32 v144, v160, v161\n\t"
    "v_cvt_pk_bf16_f32 v145, v162, v163\n\t"
    "v_cvt_pk_bf16_f32 v146, v164, v165\n\t"
    "v_cvt_pk_bf16_f32 v147, v166, v167\n\t"
    "v_cvt_pk_bf16_f32 v148, v168, v169\n\t"
    "v_cvt_pk_bf16_f32 v149, v170, v171\n\t"
    "v_cvt_pk_bf16_f32 v150, v172, v173\n\t"
    "v_cvt_pk_bf16_f32 v151, v174, v175\n\t"
    "buffer_store_dwordx2 v[144:145], v224, s[44:47], s83 offen offset:0\n\t"
    "buffer_store_dwordx2 v[146:147], v224, s[44:47], s83 offen offset:32\n\t"
    "s_add_u32 s77, s83, 16384\n\t"
    "buffer_store_dwordx2 v[148:149], v224, s[44:47], s77 offen offset:0\n\t"
    "buffer_store_dwordx2 v[150:151], v224, s[44:47], s77 offen offset:32\n\t"
    "s_waitcnt vmcnt(0)\n\t"
    "s_branch .Lend%=\n\t"
    ".Lfb%=:\n\t"
    "s_waitcnt vmcnt(0) lgkmcnt(0)\n\t"
    "s_barrier\n\t"
    "s_mov_b32 s91, 1\n\t"
    "v_bfe_u32 v96, v255, 4, 4\n\t"
    "v_lshrrev_b32 v97, 8, v255\n\t"
    "v_or_b32 v98, 0, v97\n\t"
    "v_xor_b32 v98, v98, v96\n\t"
    "v_lshlrev_b32 v98, 4, v98\n\t"
    "v_lshl_add_u32 v224, v97, 8, v98\n\t"
    "v_or_b32 v98, 4, v97\n\t"
    "v_xor_b32 v98, v98, v96\n\t"
    "v_lshlrev_b32 v98, 4, v98\n\t"
    "v_lshl_add_u32 v225, v97, 8, v98\n\t"
    "v_or_b32 v98, 8, v97\n\t"
    "v_xor_b32 v98, v98, v96\n\t"
    "v_lshlrev_b32 v98, 4, v98\n\t"
    "v_lshl_add_u32 v226, v97, 8, v98\n\t"
    "v_or_b32 v98, 12, v97\n\t"
    "v_xor_b32 v98, v98, v96\n\t"
    "v_lshlrev_b32 v98, 4, v98\n\t"
    "v_lshl_add_u32 v227, v97, 8, v98\n\t"
    "v_lshrrev_b32 v98, 1, v96\n\t"
    "v_or_b32 v99, 0, v97\n\t"
    "v_xor_b32 v98, v98, v99\n\t"
    "v_lshlrev_b32 v98, 5, v98\n\t"
    "v_and_b32 v99, 1, v96\n\t"
    "v_lshl_add_u32 v98, v99, 4, v98\n\t"
    "v_lshl_add_u32 v228, v97, 8, v98\n\t"
    "v_lshrrev_b32 v98, 1, v96\n\t"
    "v_or_b32 v99, 4, v97\n\t"
    "v_xor_b32 v98, v98, v99\n\t"
    "v_lshlrev_b32 v98, 5, v98\n\t"
    "v_and_b32 v99, 1, v96\n\t"
    "v_lshl_add_u32 v98, v99, 4, v98\n\t"
    "v_lshl_add_u32 v229, v97, 8, v98\n\t"
    "s_mov_b32 s48, s94\n\t"
    "s_mov_b32 s51, s94\n\t"
    "s_add_u32 s52, s94, 0\n\t"
    "s_add_u32 s53, s94, 16384\n\t"
    "s_add_u32 s54, s94, 32768\n\t"
    "s_add_u32 s55, s94, 49152\n\t"
    "s_add_u32 s56, s94, 65536\n\t"
    "s_add_u32 s57, s94, 81920\n\t"
    "s_add_u32 s58, s94, 98304\n\t"
    "s_add_u32 s59, s94, 114688\n\t"
    "v_mov_b32 v156, 0xff800000\n\t"
    "v_mov_b32 v157, 0xff800000\n\t"
    "v_mov_b32 v158, 0xff800000\n\t"
    "v_mov_b32 v159, 0xff800000\n\t"
    "s_mov_b32 s49, 32\n\t"
    ".Lscan%=:\n\t"
    "s_mov_b32 m0, s52\n\t"
    "s_nop 0\n\t"
    "buffer_load_dwordx4 v224, s[40:43], s48 offen offset:0 lds\n\t"
    "buffer_load_dwordx4 v225, s[40:43], s48 offen offset:1024 lds\n\t"
    "buffer_load_dwordx4 v226, s[40:43], s48 offen offset:2048 lds\n\t"
    "buffer_load_dwordx4 v227, s[40:43], s48 offen offset:3072 lds\n\t"
    "s_add_u32 s48, s48, 16384\n\t"
    "s_waitcnt vmcnt(0)\n\t"
    "s_barrier\n\t"
    "ds_read_b128 a[128:131], v128 offset:0\n\t"
    "ds_read_b128 a[132:135], v129 offset:0\n\t"
    "ds_read_b128 a[136:139], v130 offset:0\n\t"
    "ds_read_b128 a[140:143], v131 offset:0\n\t"
    "s_waitcnt lgkmcnt(0)\n\t"
    "v_mfma_f32_16x16x32_bf16 v[0:3], a[128:131], v[160:163], 0\n\t"
    "v_mfma_f32_16x16x32_bf16 v[0:3], a[132:135], v[164:167], v[0:3]\n\t"
    "v_mfma_f32_16x16x32_bf16 v[0:3], a[136:139], v[168:171], v[0:3]\n\t"
    "v_mfma_f32_16x16x32_bf16 v[0:3], a[140:143], v[172:175], v[0:3]\n\t"
    "v_mfma_f32_16x16x32_bf16 v[4:7], a[128:131], v[176:179], 0\n\t"
    "v_mfma_f32_16x16x32_bf16 v[4:7], a[132:135], v[180:183], v[4:7]\n\t"
    "v_mfma_f32_16x16x32_bf16 v[4:7], a[136:139], v[184:187], v[4:7]\n\t"
    "v_mfma_f32_16x16x32_bf16 v[4:7], a[140:143], v[188:191], v[4:7]\n\t"
    "v_mfma_f32_16x16x32_bf16 v[8:11], a[128:131], v[192:195], 0\n\t"
    "v_mfma_f32_16x16x32_bf16 v[8:11], a[132:135], v[196:199], v[8:11]\n\t"
    "v_mfma_f32_16x16x32_bf16 v[8:11], a[136:139], v[200:203], v[8:11]\n\t"
    "v_mfma_f32_16x16x32_bf16 v[8:11], a[140:143], v[204:207], v[8:11]\n\t"
    "v_mfma_f32_16x16x32_bf16 v[12:15], a[128:131], v[208:211], 0\n\t"
    "v_mfma_f32_16x16x32_bf16 v[12:15], a[132:135], v[212:215], v[12:15]\n\t"
    "v_mfma_f32_16x16x32_bf16 v[12:15], a[136:139], v[216:219], v[12:15]\n\t"
    "v_mfma_f32_16x16x32_bf16 v[12:15], a[140:143], v[220:223], v[12:15]\n\t"
    "s_nop 7\n\t"
    "v_max3_f32 v156, v0, v1, v156\n\t"
    "v_max3_f32 v156, v2, v3, v156\n\t"
    "v_max3_f32 v157, v4, v5, v157\n\t"
    "v_max3_f32 v157, v6, v7, v157\n\t"
    "v_max3_f32 v158, v8, v9, v158\n\t"
    "v_max3_f32 v158, v10, v11, v158\n\t"
    "v_max3_f32 v159, v12, v13, v159\n\t"
    "v_max3_f32 v159, v14, v15, v159\n\t"
    "s_nop 1\n\t"
    "ds_read_b128 a[144:147], v128 offset:4096\n\t"
    "ds_read_b128 a[148:151], v129 offset:4096\n\t"
    "ds_read_b128 a[152:155], v130 offset:4096\n\t"
    "ds_read_b128 a[156:159], v131 offset:4096\n\t"
    "s_waitcnt lgkmcnt(0)\n\t"
    "v_mfma_f32_16x16x32_bf16 v[0:3], a[144:147], v[160:163], 0\n\t"
    "v_mfma_f32_16x16x32_bf16 v[0:3], a[148:151], v[164:167], v[0:3]\n\t"
    "v_mfma_f32_16x16x32_bf16 v[0:3], a[152:155], v[168:171], v[0:3]\n\t"
    "v_mfma_f32_16x16x32_bf16 v[0:3], a[156:159], v[172:175], v[0:3]\n\t"
    "v_mfma_f32_16x16x32_bf16 v[4:7], a[144:147], v[176:179], 0\n\t"
    "v_mfma_f32_16x16x32_bf16 v[4:7], a[148:151], v[180:183], v[4:7]\n\t"
    "v_mfma_f32_16x16x32_bf16 v[4:7], a[152:155], v[184:187], v[4:7]\n\t"
    "v_mfma_f32_16x16x32_bf16 v[4:7], a[156:159], v[188:191], v[4:7]\n\t"
    "v_mfma_f32_16x16x32_bf16 v[8:11], a[144:147], v[192:195], 0\n\t"
    "v_mfma_f32_16x16x32_bf16 v[8:11], a[148:151], v[196:199], v[8:11]\n\t"
    "v_mfma_f32_16x16x32_bf16 v[8:11], a[152:155], v[200:203], v[8:11]\n\t"
    "v_mfma_f32_16x16x32_bf16 v[8:11], a[156:159], v[204:207], v[8:11]\n\t"
    "v_mfma_f32_16x16x32_bf16 v[12:15], a[144:147], v[208:211], 0\n\t"
    "v_mfma_f32_16x16x32_bf16 v[12:15], a[148:151], v[212:215], v[12:15]\n\t"
    "v_mfma_f32_16x16x32_bf16 v[12:15], a[152:155], v[216:219], v[12:15]\n\t"
    "v_mfma_f32_16x16x32_bf16 v[12:15], a[156:159], v[220:223], v[12:15]\n\t"
    "s_nop 7\n\t"
    "v_max3_f32 v156, v0, v1, v156\n\t"
    "v_max3_f32 v156, v2, v3, v156\n\t"
    "v_max3_f32 v157, v4, v5, v157\n\t"
    "v_max3_f32 v157, v6, v7, v157\n\t"
    "v_max3_f32 v158, v8, v9, v158\n\t"
    "v_max3_f32 v158, v10, v11, v158\n\t"
    "v_max3_f32 v159, v12, v13, v159\n\t"
    "v_max3_f32 v159, v14, v15, v159\n\t"
    "s_nop 1\n\t"
    "ds_read_b128 a[128:131], v128 offset:8192\n\t"
    "ds_read_b128 a[132:135], v129 offset:8192\n\t"
    "ds_read_b128 a[136:139], v130 offset:8192\n\t"
    "ds_read_b128 a[140:143], v131 offset:8192\n\t"
    "s_waitcnt lgkmcnt(0)\n\t"
    "v_mfma_f32_16x16x32_bf16 v[0:3], a[128:131], v[160:163], 0\n\t"
    "v_mfma_f32_16x16x32_bf16 v[0:3], a[132:135], v[164:167], v[0:3]\n\t"
    "v_mfma_f32_16x16x32_bf16 v[0:3], a[136:139], v[168:171], v[0:3]\n\t"
    "v_mfma_f32_16x16x32_bf16 v[0:3], a[140:143], v[172:175], v[0:3]\n\t"
    "v_mfma_f32_16x16x32_bf16 v[4:7], a[128:131], v[176:179], 0\n\t"
    "v_mfma_f32_16x16x32_bf16 v[4:7], a[132:135], v[180:183], v[4:7]\n\t"
    "v_mfma_f32_16x16x32_bf16 v[4:7], a[136:139], v[184:187], v[4:7]\n\t"
    "v_mfma_f32_16x16x32_bf16 v[4:7], a[140:143], v[188:191], v[4:7]\n\t"
    "v_mfma_f32_16x16x32_bf16 v[8:11], a[128:131], v[192:195], 0\n\t"
    "v_mfma_f32_16x16x32_bf16 v[8:11], a[132:135], v[196:199], v[8:11]\n\t"
    "v_mfma_f32_16x16x32_bf16 v[8:11], a[136:139], v[200:203], v[8:11]\n\t"
    "v_mfma_f32_16x16x32_bf16 v[8:11], a[140:143], v[204:207], v[8:11]\n\t"
    "v_mfma_f32_16x16x32_bf16 v[12:15], a[128:131], v[208:211], 0\n\t"
    "v_mfma_f32_16x16x32_bf16 v[12:15], a[132:135], v[212:215], v[12:15]\n\t"
    "v_mfma_f32_16x16x32_bf16 v[12:15], a[136:139], v[216:219], v[12:15]\n\t"
    "v_mfma_f32_16x16x32_bf16 v[12:15], a[140:143], v[220:223], v[12:15]\n\t"
    "s_nop 7\n\t"
    "v_max3_f32 v156, v0, v1, v156\n\t"
    "v_max3_f32 v156, v2, v3, v156\n\t"
    "v_max3_f32 v157, v4, v5, v157\n\t"
    "v_max3_f32 v157, v6, v7, v157\n\t"
    "v_max3_f32 v158, v8, v9, v158\n\t"
    "v_max3_f32 v158, v10, v11, v158\n\t"
    "v_max3_f32 v159, v12, v13, v159\n\t"
    "v_max3_f32 v159, v14, v15, v159\n\t"
    "s_nop 1\n\t"
    "ds_read_b128 a[144:147], v128 offset:12288\n\t"
    "ds_read_b128 a[148:151], v129 offset:12288\n\t"
    "ds_read_b128 a[152:155], v130 offset:12288\n\t"
    "ds_read_b128 a[156:159], v131 offset:12288\n\t"
    "s_waitcnt lgkmcnt(0)\n\t"
    "v_mfma_f32_16x16x32_bf16 v[0:3], a[144:147], v[160:163], 0\n\t"
    "v_mfma_f32_16x16x32_bf16 v[0:3], a[148:151], v[164:167], v[0:3]\n\t"
    "v_mfma_f32_16x16x32_bf16 v[0:3], a[152:155], v[168:171], v[0:3]\n\t"
    "v_mfma_f32_16x16x32_bf16 v[0:3], a[156:159], v[172:175], v[0:3]\n\t"
    "v_mfma_f32_16x16x32_bf16 v[4:7], a[144:147], v[176:179], 0\n\t"
    "v_mfma_f32_16x16x32_bf16 v[4:7], a[148:151], v[180:183], v[4:7]\n\t"
    "v_mfma_f32_16x16x32_bf16 v[4:7], a[152:155], v[184:187], v[4:7]\n\t"
    "v_mfma_f32_16x16x32_bf16 v[4:7], a[156:159], v[188:191], v[4:7]\n\t"
    "v_mfma_f32_16x16x32_bf16 v[8:11], a[144:147], v[192:195], 0\n\t"
    "v_mfma_f32_16x16x32_bf16 v[8:11], a[148:151], v[196:199], v[8:11]\n\t"
    "v_mfma_f32_16x16x32_bf16 v[8:11], a[152:155], v[200:203], v[8:11]\n\t"
    "v_mfma_f32_16x16x32_bf16 v[8:11], a[156:159], v[204:207], v[8:11]\n\t"
    "v_mfma_f32_16x16x32_bf16 v[12:15], a[144:147], v[208:211], 0\n\t"
    "v_mfma_f32_16x16x32_bf16 v[12:15], a[148:151], v[212:215], v[12:15]\n\t"
    "v_mfma_f32_16x16x32_bf16 v[12:15], a[152:155], v[216:219], v[12:15]\n\t"
    "v_mfma_f32_16x16x32_bf16 v[12:15], a[156:159], v[220:223], v[12:15]\n\t"
    "s_nop 7\n\t"
    "v_max3_f32 v156, v0, v1, v156\n\t"
    "v_max3_f32 v156, v2, v3, v156\n\t"
    "v_max3_f32 v157, v4, v5, v157\n\t"
    "v_max3_f32 v157, v6, v7, v157\n\t"
    "v_max3_f32 v158, v8, v9, v158\n\t"
    "v_max3_f32 v158, v10, v11, v158\n\t"
    "v_max3_f32 v159, v12, v13, v159\n\t"
    "v_max3_f32 v159, v14, v15, v159\n\t"
    "s_nop 1\n\t"
    "s_barrier\n\t"
    "s_sub_u32 s49, s49, 1\n\t"
    "s_cmp_lg_u32 s49, 0\n\t"
    "s_cbranch_scc1 .Lscan%=\n\t"
    "v_mov_b32 v96, v156\n\t"
    "v_mov_b32 v97, v157\n\t"
    "v_mov_b32 v98, v158\n\t"
    "v_mov_b32 v99, v159\n\t"
    "s_nop 0\n\t"
    "v_permlane32_swap_b32 v156, v96\n\t"
    "v_permlane32_swap_b32 v157, v97\n\t"
    "v_permlane32_swap_b32 v158, v98\n\t"
    "v_permlane32_swap_b32 v159, v99\n\t"
    "s_nop 0\n\t"
    "v_max_f32 v156, v156, v96\n\t"
    "v_max_f32 v157, v157, v97\n\t"
    "v_max_f32 v158, v158, v98\n\t"
    "v_max_f32 v159, v159, v99\n\t"
    "v_mov_b32 v96, v156\n\t"
    "v_mov_b32 v97, v157\n\t"
    "v_mov_b32 v98, v158\n\t"
    "v_mov_b32 v99, v159\n\t"
    "s_nop 0\n\t"
    "v_permlane16_swap_b32 v156, v96\n\t"
    "v_permlane16_swap_b32 v157, v97\n\t"
    "v_permlane16_swap_b32 v158, v98\n\t"
    "v_permlane16_swap_b32 v159, v99\n\t"
    "s_nop 0\n\t"
    "v_max_f32 v156, v156, v96\n\t"
    "v_max_f32 v157, v157, v97\n\t"
    "v_max_f32 v158, v158, v98\n\t"
    "v_max_f32 v159, v159, v99\n\t"
    "s_mov_b32 s48, s94\n\t"
    "s_mov_b32 s51, s94\n\t"
    "s_add_u32 s52, s94, 0\n\t"
    "s_add_u32 s53, s94, 16384\n\t"
    "s_add_u32 s54, s94, 32768\n\t"
    "s_add_u32 s55, s94, 49152\n\t"
    "s_add_u32 s56, s94, 65536\n\t"
    "s_add_u32 s57, s94, 81920\n\t"
    "s_add_u32 s58, s94, 98304\n\t"
    "s_add_u32 s59, s94, 114688\n\t"
    "s_mov_b32 m0, s52\n\t"
    "s_nop 0\n\t"
    "s_nop 0\n\t"
    "buffer_load_dwordx4 v224, s[40:43], s48 offen offset:0 lds\n\t"
    "s_mov_b32 m0, s52\n\t"
    "s_nop 0\n\t"
    "s_nop 0\n\t"
    "buffer_load_dwordx4 v225, s[40:43], s48 offen offset:1024 lds\n\t"
    "s_mov_b32 m0, s52\n\t"
    "s_nop 0\n\t"
    "s_nop 0\n\t"
    "buffer_load_dwordx4 v226, s[40:43], s48 offen offset:2048 lds\n\t"
    "s_mov_b32 m0, s52\n\t"
    "s_nop 0\n\t"
    "s_nop 0\n\t"
    "buffer_load_dwordx4 v227, s[40:43], s48 offen offset:3072 lds\n\t"
    "s_mov_b32 m0, s53\n\t"
    "s_add_u32 s48, s48, 16384\n\t"
    "s_nop 0\n\t"
    "buffer_load_dwordx4 v224, s[40:43], s48 offen offset:0 lds\n\t"
    "s_mov_b32 m0, s53\n\t"
    "s_nop 0\n\t"
    "s_nop 0\n\t"
    "buffer_load_dwordx4 v225, s[40:43], s48 offen offset:1024 lds\n\t"
    "s_mov_b32 m0, s53\n\t"
    "s_nop 0\n\t"
    "s_nop 0\n\t"
    "buffer_load_dwordx4 v226, s[40:43], s48 offen offset:2048 lds\n\t"
    "s_mov_b32 m0, s53\n\t"
    "s_nop 0\n\t"
    "s_nop 0\n\t"
    "buffer_load_dwordx4 v227, s[40:43], s48 offen offset:3072 lds\n\t"
    "s_mov_b32 m0, s56\n\t"
    "s_nop 0\n\t"
    "s_nop 0\n\t"
    "buffer_load_dwordx4 v228, s[44:47], s51 offen offset:0 lds\n\t"
    "s_mov_b32 m0, s56\n\t"
    "s_nop 0\n\t"
    "s_nop 0\n\t"
    "buffer_load_dwordx4 v229, s[44:47], s51 offen offset:1024 lds\n\t"
    "s_mov_b32 m0, s56\n\t"
    "s_nop 0\n\t"
    "s_nop 0\n\t"
    "buffer_load_dwordx4 v228, s[44:47], s51 offen offset:2048 lds\n\t"
    "s_mov_b32 m0, s56\n\t"
    "s_nop 0\n\t"
    "s_nop 0\n\t"
    "buffer_load_dwordx4 v229, s[44:47], s51 offen offset:3072 lds\n\t"
    "s_mov_b32 m0, s57\n\t"
    "s_add_u32 s51, s51, 16384\n\t"
    "s_nop 0\n\t"
    "buffer_load_dwordx4 v228, s[44:47], s51 offen offset:0 lds\n\t"
    "s_mov_b32 m0, s57\n\t"
    "s_nop 0\n\t"
    "s_nop 0\n\t"
    "buffer_load_dwordx4 v229, s[44:47], s51 offen offset:1024 lds\n\t"
    "s_mov_b32 m0, s57\n\t"
    "s_nop 0\n\t"
    "s_nop 0\n\t"
    "buffer_load_dwordx4 v228, s[44:47], s51 offen offset:2048 lds\n\t"
    "s_mov_b32 m0, s57\n\t"
    "s_nop 0\n\t"
    "s_nop 0\n\t"
    "buffer_load_dwordx4 v229, s[44:47], s51 offen offset:3072 lds\n\t"
    "s_mov_b32 m0, s54\n\t"
    "s_add_u32 s48, s48, 16384\n\t"
    "s_nop 0\n\t"
    "buffer_load_dwordx4 v224, s[40:43], s48 offen offset:0 lds\n\t"
    "s_mov_b32 m0, s54\n\t"
    "s_nop 0\n\t"
    "s_nop 0\n\t"
    "buffer_load_dwordx4 v225, s[40:43], s48 offen offset:1024 lds\n\t"
    "s_mov_b32 m0, s54\n\t"
    "s_nop 0\n\t"
    "s_nop 0\n\t"
    "buffer_load_dwordx4 v226, s[40:43], s48 offen offset:2048 lds\n\t"
    "s_mov_b32 m0, s54\n\t"
    "s_nop 0\n\t"
    "s_nop 0\n\t"
    "buffer_load_dwordx4 v227, s[40:43], s48 offen offset:3072 lds\n\t"
    "v_mov_b32 v144, 0\n\t"
    "v_mov_b32 v146, 0\n\t"
    "s_waitcnt vmcnt(0)\n\t"
    "s_branch .Latt%=\n\t"
    ".Lend%=:\n\t"
    : [req] "+v"(reqv)
    : [ka] "s"(ka), [w] "s"(w), [h] "s"(h), [qb] "s"(qb), [bid] "s"(bid), [lds] "s"((unsigned)(size_t)lds)
    : "memory", "scc", "vcc", "v0","v1","v2","v3","v4","v5","v6","v7","v8","v9","v10","v11","v12","v13","v14","v15","v16","v17","v18","v19","v20","v21","v22","v23","v24","v25","v26","v27","v28","v29","v30","v31","v32","v33","v34","v35","v36","v37","v38","v39","v40","v41","v42","v43","v44","v45","v46","v47","v48","v49","v50","v51","v52","v53","v54","v55","v56","v57","v58","v59","v60","v61","v62","v63","v64","v65","v66","v67","v68","v69","v70","v71","v72","v73","v74","v75","v76","v77","v78","v79","v80","v81","v82","v83","v84","v85","v86","v87","v88","v89","v90","v91","v92","v93","v94","v95","v96","v97","v98","v99","v100","v101","v102","v103","v104","v105","v106","v107","v108","v109","v110","v111","v112","v113","v114","v115","v116","v117","v118","v119","v120","v121","v122","v123","v124","v125","v126","v127","v128","v129","v130","v131","v132","v133","v134","v135","v136","v137","v138","v139","v140","v141","v142","v143","v144","v145","v146","v147","v148","v149","v150","v151","v152","v153","v154","v155","v156","v157","v158","v159","v160","v161","v162","v163","v164","v165","v166","v167","v168","v169","v170","v171","v172","v173","v174","v175","v176","v177","v178","v179","v180","v181","v182","v183","v184","v185","v186","v187","v188","v189","v190","v191","v192","v193","v194","v195","v196","v197","v198","v199","v200","v201","v202","v203","v204","v205","v206","v207","v208","v209","v210","v211","v212","v213","v214","v215","v216","v217","v218","v219","v220","v221","v222","v223","v224","v225","v226","v227","v228","v229","v230","v231","v232","v233","v234","v235","v236","v237","v238","v239","v240","v241","v242","v243","v244","v245","v246","v247","v248","v249","v250","v251","v252","v253","v255","a0","a1","a2","a3","a4","a5","a6","a7","a8","a9","a10","a11","a12","a13","a14","a15","a16","a17","a18","a19","a20","a21","a22","a23","a24","a25","a26","a27","a28","a29","a30","a31","a32","a33","a34","a35","a36","a37","a38","a39","a40","a41","a42","a43","a44","a45","a46","a47","a48","a49","a50","a51","a52","a53","a54","a55","a56","a57","a58","a59","a60","a61","a62","a63","a64","a65","a66","a67","a68","a69","a70","a71","a72","a73","a74","a75","a76","a77","a78","a79","a80","a81","a82","a83","a84","a85","a86","a87","a88","a89","a90","a91","a92","a93","a94","a95","a96","a97","a98","a99","a100","a101","a102","a103","a104","a105","a106","a107","a108","a109","a110","a111","a112","a113","a114","a115","a116","a117","a118","a119","a120","a121","a122","a123","a124","a125","a126","a127","a128","a129","a130","a131","a132","a133","a134","a135","a136","a137","a138","a139","a140","a141","a142","a143","a144","a145","a146","a147","a148","a149","a150","a151","a152","a153","a154","a155","a156","a157","a158","a159","a160","a161","a162","a163","a164","a165","a166","a167","a168","a169","a170","a171","a172","a173","a174","a175","a176","a177","a178","a179","a180","a181","a182","a183","a184","a185","a186","a187","a188","a189","a190","a191","a192","a193","a194","a195","a196","a197","a198","a199","a200","a201","a202","a203","a204","a205","a206","a207","a208","a209","a210","a211","a212","a213","a214","a215","a216","a217","a218","a219","a220","a221","a222","a223","a224","a225","a226","a227","a228","a229","a230","a231","a232","a233","a234","a235","a236","a237","a238","a239","a240","a241","a242","a243","a244","a245","a246","a247","a248","a249","a250","a251","a252","a253","a254","a255","s40","s41","s42","s43","s44","s45","s46","s47","s48","s49","s50","s51","s52","s53","s54","s55","s56","s57","s58","s59","s60","s61","s62","s63","s64","s65","s66","s67","s68","s69","s70","s71","s72","s73","s74","s75","s76","s77","s78","s79","s80","s81","s82","s83","s84","s85","s86","s87","s88","s89","s90","s91","s92","s93","s94","s95","s96","s97","s98","s99","s100","s101");
}

extern "C" void kernel_launch(void* const* d_in, const int* in_sizes, int n_in, void* d_out, int out_size, void* d_ws, size_t ws_size,
                              hipStream_t stream) {
  const u16* x = (const u16*)d_in[0];
  const u16* kc = (const u16*)d_in[1];
  const u16* vc = (const u16*)d_in[2];
  const u16* wq = (const u16*)d_in[3];
  const u16* wo = (const u16*)d_in[4];
  const u16* w1 = (const u16*)d_in[6];
  const u16* b1 = (const u16*)d_in[7];
  const u16* w2 = (const u16*)d_in[8];
  const u16* b2 = (const u16*)d_in[9];
  char* ws = (char*)d_ws;
  k_main<<<256, 256, 0, stream>>>(kc, vc, wq, w1, b1, ws, x, wo, w2, b2, (u16*)d_out);
}
```
